```python
import torch

def _softmax(x, axis=-1):
    return torch.softmax(x, dim=axis)

def _gelu(x):
    return torch.nn.functional.gelu(x, approximate='tanh')

def _silu(x):
    return torch.nn.functional.silu(x)

def _sigmoid(x):
    return torch.sigmoid(x)

def _layernorm(x, axis=-1, eps=1e-05):
    m = x.mean(dim=axis, keepdim=True)
    v = x.var(dim=axis, keepdim=True, unbiased=False)
    return (x - m) / torch.sqrt(v + eps)

def _scatter_add(tgt, idx, src, axis=0):
    return tgt.clone().index_add_(axis, idx, src)

def _segment_sum(src, seg, n):
    out = torch.zeros((n,) + src.shape[1:], dtype=src.dtype)
    return out.index_add_(0, seg, src)

def _pad(x, pw):
    flat = tuple((v for pair in reversed(pw) for v in pair))
    return torch.nn.functional.pad(x, flat)
kernel_name = 'standalone__video_timesformer__vision'

def setup_inputs():

    _w = lambda a: (lambda t: t.to(torch.bfloat16) if t.is_floating_point() else t)(torch.as_tensor(a))
    T = 8
    p = 16
    HW = 512 // p * p
    N = (HW // p) ** 2
    C = 192
    return {'x': _w((lambda *s: torch.randn(*s, dtype=torch.bfloat16) * 0.1)(1, T, N, C)), 'wqkv_t': _w((lambda *s: torch.randn(*s, dtype=torch.bfloat16) * 0.1)(C, 3 * C)), 'wo_t': _w((lambda *s: torch.randn(*s, dtype=torch.bfloat16) * 0.1)(C, C)), 'wqkv_s': _w((lambda *s: torch.randn(*s, dtype=torch.bfloat16) * 0.1)(C, 3 * C)), 'wo_s': _w((lambda *s: torch.randn(*s, dtype=torch.bfloat16) * 0.05)(C, C))}

def reference(x, wqkv_t, wo_t, wqkv_s, wo_s):
    B, T, N, C = x.shape
    scale = C ** (-0.5)
    xt = (lambda x, *d: x.permute(*d))(x, 0, 2, 1, 3).reshape(B * N, T, C)
    h = _layernorm(xt, axis=-1)
    qkv = h @ wqkv_t
    q, k, v = (qkv[..., :C].clone(), qkv[..., C:2 * C].clone(), qkv[..., 2 * C:].clone())
    a = _softmax(torch.einsum('btc,bsc->bts', q, k) * scale, axis=-1)
    xt = xt + torch.einsum('bts,bsc->btc', a, v) @ wo_t
    x1 = (lambda x, *d: x.permute(*d))(xt.reshape(B, N, T, C), 0, 2, 1, 3)
    xs = x1.reshape(B * T, N, C)
    h = _layernorm(xs, axis=-1)
    qkv = h @ wqkv_s
    q, k, v = (qkv[..., :C].clone(), qkv[..., C:2 * C].clone(), qkv[..., 2 * C:].clone())
    a = _softmax(torch.einsum('bnc,bmc->bnm', q, k) * scale, axis=-1)
    xs = xs + torch.einsum('bnm,bmc->bnc', a, v) @ wo_s
    return xs.reshape(B, T, N, C)
```

```cpp
#include <hip/hip_runtime.h>
#include <hip/hip_bf16.h>

typedef short s16x4 __attribute__((ext_vector_type(4)));
typedef short s16x8 __attribute__((ext_vector_type(8)));
typedef __bf16 b16x8 __attribute__((ext_vector_type(8)));
typedef __bf16 b16x2 __attribute__((ext_vector_type(2)));
typedef float f32x2 __attribute__((ext_vector_type(2)));
typedef float f32x4 __attribute__((ext_vector_type(4)));
typedef float f32x16 __attribute__((ext_vector_type(16)));
typedef unsigned u32x2 __attribute__((ext_vector_type(2)));
typedef unsigned u32x4 __attribute__((ext_vector_type(4)));
typedef unsigned long long u64;
#define LAS __attribute__((address_space(3)))
#define DI __device__ __forceinline__

constexpr int TILE_B = 12288;
constexpr int FRAME_B = 393216;
constexpr size_t WS_H2 = 0, WS_X1 = 8 * FRAME_B, WS_WQK = 16 * FRAME_B, WS_WVO = WS_WQK + 73728;
constexpr float QSCALE = 0.07216878364870322f * 1.4426950408889634f;

DI float asf(unsigned u) { return __builtin_bit_cast(float, u); }
DI unsigned asu(float f) { return __builtin_bit_cast(unsigned, f); }
DI unsigned cvtpk(float lo, float hi) { f32x2 v = {lo, hi}; return __builtin_bit_cast(unsigned, __builtin_convertvector(v, b16x2)); }
DI float bflo(unsigned u) { return __builtin_bit_cast(float, u << 16); }
DI float bfhi(unsigned u) { return __builtin_bit_cast(float, u & 0xffff0000u); }
DI f32x16 mfma32(u32x4 a, u32x4 b, f32x16 c) { return __builtin_amdgcn_mfma_f32_32x32x16_bf16(__builtin_bit_cast(b16x8, a), __builtin_bit_cast(b16x8, b), c, 0, 0, 0); }
DI f32x4 mfma16(u32x4 a, u32x4 b, f32x4 c) { return __builtin_amdgcn_mfma_f32_16x16x32_bf16(__builtin_bit_cast(b16x8, a), __builtin_bit_cast(b16x8, b), c, 0, 0, 0); }
DI f32x4 mfma16k16(u32x2 a, u32x2 b, f32x4 c) { return __builtin_amdgcn_mfma_f32_16x16x16bf16_1k(__builtin_bit_cast(s16x4, a), __builtin_bit_cast(s16x4, b), c, 0, 0, 0); }
DI u32x2 pack4(f32x4 v) { u32x2 r = {cvtpk(v[0], v[1]), cvtpk(v[2], v[3])}; return r; }
DI u32x4 lds_r128(unsigned a) { return *(LAS u32x4*)(size_t)a; }
DI void lds_w128(unsigned a, u32x4 v) { *(LAS u32x4*)(size_t)a = v; }
DI void lds_w64(unsigned a, u32x2 v) { *(LAS u32x2*)(size_t)a = v; }
DI u32x2 lds_tr(unsigned a) { return __builtin_bit_cast(u32x2, __builtin_amdgcn_ds_read_tr16_b64_v4i16((LAS s16x4*)(size_t)a)); }
DI u32x4 lds_tr2(unsigned a0, unsigned a1) { u32x2 x = lds_tr(a0), y = lds_tr(a1); u32x4 r = {x[0], x[1], y[0], y[1]}; return r; }
DI void wg_barrier() { asm volatile("s_waitcnt lgkmcnt(0)\n\ts_barrier" ::: "memory"); }
template <int CTRL> DI float dpp_add(float v) { return v + __builtin_bit_cast(float, __builtin_amdgcn_update_dpp(0, __builtin_bit_cast(int, v), CTRL, 0xf, 0xf, true)); }
DI float xor16_add(float v) { unsigned u = __builtin_bit_cast(unsigned, v); auto r = __builtin_amdgcn_permlane16_swap(u, u, false, false); return asf(r[0]) + asf(r[1]); }
DI float xor16_max(float v) { unsigned u = __builtin_bit_cast(unsigned, v); auto r = __builtin_amdgcn_permlane16_swap(u, u, false, false); return fmaxf(asf(r[0]), asf(r[1])); }
DI unsigned fi_pos(unsigned r, unsigned c8) {
  return (((r >> 4) * 6 + (c8 >> 2)) * 64 + (c8 & 3) * 16 + ((r & 15) ^ ((c8 & 1) | (((r >> 3) & 1) << 1) | ((r >> 4) << 2)))) * 16;
}
DI void layernorm24(const float (&v)[24], u32x4 (&o)[3]) {
  float s = 0.f;
#pragma unroll
  for (int i = 0; i < 24; ++i) s += v[i];
  s = dpp_add<0x124>(s); s = dpp_add<0x128>(s); s = xor16_add(s);
  const float mu = s * (1.0f / 192.0f);
  float d[24], q = 0.f;
#pragma unroll
  for (int i = 0; i < 24; ++i) { d[i] = v[i] - mu; q = fmaf(d[i], d[i], q); }
  q = dpp_add<0x124>(q); q = dpp_add<0x128>(q); q = xor16_add(q);
  const float rs = __builtin_amdgcn_rsqf(q * (1.0f / 192.0f) + 1e-5f);
#pragma unroll
  for (int i = 0; i < 3; ++i)
#pragma unroll
    for (int e = 0; e < 4; ++e) o[i][e] = cvtpk(d[8 * i + 2 * e] * rs, d[8 * i + 2 * e + 1] * rs);
}

__global__ __launch_bounds__(64) void k_merge(const unsigned short* __restrict__ wqkv, const unsigned short* __restrict__ wo, char* __restrict__ ws) {
  const int b = blockIdx.x, m = b / 72, f = b % 72, tau = f / 6, ks = f % 6, l = threadIdx.x, i16 = l & 15, g = l >> 4;
  f32x4 d0 = {0, 0, 0, 0}, d1 = {0, 0, 0, 0};
  const int ra = 32 * ks + 8 * (i16 >> 2) + (i16 & 3);
#pragma unroll
  for (int kk = 0; kk < 6; ++kk) {
    const int j0 = 32 * kk + 8 * g;
    u32x4 a0 = *(const u32x4*)(wqkv + ra * 576 + (m ? 384 : 0) + j0);
    u32x4 a1 = *(const u32x4*)(wqkv + (ra + 4) * 576 + (m ? 384 : 0) + j0);
    u32x4 bb;
    if (m == 0) bb = *(const u32x4*)(wqkv + (16 * tau + i16) * 576 + 192 + j0);
    else {
#pragma unroll
      for (int e = 0; e < 4; ++e) bb[e] = (unsigned)wo[(j0 + 2 * e) * 192 + 16 * tau + i16] | ((unsigned)wo[(j0 + 2 * e + 1) * 192 + 16 * tau + i16] << 16);
    }
    d0 = mfma16(a0, bb, d0); d1 = mfma16(a1, bb, d1);
  }
  const float sc = m ? 1.0f : QSCALE;
  u32x4 o = {cvtpk(d0[0] * sc, d0[1] * sc), cvtpk(d0[2] * sc, d0[3] * sc), cvtpk(d1[0] * sc, d1[1] * sc), cvtpk(d1[2] * sc, d1[3] * sc)};
  *(u32x4*)(ws + WS_WQK + (size_t)b * 1024 + l * 16) = o;
}

constexpr int SLOT = 36864, K1_H = 4 * SLOT, K1_LDS = K1_H + TILE_B;
#define DMA9(voff0, voff1, voff2, voff3, voff4, voff5, voff6, voff7, voff8, sbase, ldsa)                       \
  asm volatile("s_mov_b32 m0, %10\n\ts_nop 0\n\t"                                                               \
               "global_load_lds_dwordx4 %0, %9\n\tglobal_load_lds_dwordx4 %1, %9 offset:1024\n\t"               \
               "global_load_lds_dwordx4 %2, %9 offset:2048\n\tglobal_load_lds_dwordx4 %3, %9 offset:3072\n\t"   \
               "s_add_u32 m0, m0, 0x1000\n\ts_nop 0\n\t"                                                        \
               "global_load_lds_dwordx4 %4, %9\n\tglobal_load_lds_dwordx4 %5, %9 offset:1024\n\t"               \
               "global_load_lds_dwordx4 %6, %9 offset:2048\n\tglobal_load_lds_dwordx4 %7, %9 offset:3072\n\t"   \
               "s_add_u32 m0, m0, 0x1000\n\ts_nop 0\n\tglobal_load_lds_dwordx4 %8, %9"                          \
               :: "v"(voff0), "v"(voff1), "v"(voff2), "v"(voff3), "v"(voff4), "v"(voff5), "v"(voff6), "v"(voff7), "v"(voff8), \
                  "s"(sbase), "s"(ldsa) : "memory", "scc")

__global__ __launch_bounds__(256, 1) void k_temporal(const char* __restrict__ x, const char* __restrict__ wqkv, const char* __restrict__ wo, char* __restrict__ ws) {
  extern __shared__ __attribute__((aligned(16))) char smem[];
  const unsigned lds0 = (unsigned)(size_t)(LAS char*)smem;
  const unsigned b = blockIdx.x, l = threadIdx.x & 63, w = __builtin_amdgcn_readfirstlane(threadIdx.x >> 6);
  const unsigned nl = l & 3, j = (l >> 2) & 7, tt = 2 * w + (l >> 5), r = nl * 8 + tt;
  u32x4 xr[3];
  {
    const unsigned xo = (tt * 1024 + 4 * b + nl) * 384 + j * 16;
    asm volatile("global_load_dwordx4 %0, %3, %4\n\tglobal_load_dwordx4 %1, %3, %4 offset:128\n\tglobal_load_dwordx4 %2, %3, %4 offset:256"
                 : "=&v"(xr[0]), "=&v"(xr[1]), "=&v"(xr[2]) : "v"(xo), "s"(x) : "memory");
  }
  const unsigned qa = (9216 * w + l * 16) ^ ((w & 1) << 6), qb = qa ^ 32, q5 = qa + 4096, q7 = qb + 4096;
  const unsigned q2 = l >= 16 ? qb : qa, q4 = l < 32 ? q7 : q5, q6 = l >= 48 ? q7 : q5, q8 = qb + 8192;
  const unsigned ldsw = lds0 + 9216 * w;
#define DMAQ(c) DMA9(qa, qa, q2, qb, q4, q5, q6, q7, q8, wqkv + (c) * SLOT, ldsw + ((c) & 3) * SLOT)
  DMAQ(0); DMAQ(1); DMAQ(2); DMAQ(3);
  unsigned oo[9];
#pragma unroll
  for (int i = 0; i < 9; ++i) {
    const unsigned row = 24 * w + (((8 * i + (l >> 3)) * 171) >> 9);
    oo[i] = ((9216 * w + l * 16) ^ (((row & 2) | ((row & 8) >> 1)) << 4)) + (i >> 2) * 4096;
  }
#define DMAO(c) DMA9(oo[0], oo[1], oo[2], oo[3], oo[4], oo[5], oo[6], oo[7], oo[8], wo + ((c) - 6) * SLOT, ldsw + ((c) & 3) * SLOT)
  asm volatile("s_waitcnt vmcnt(36)" : "+v"(xr[0]), "+v"(xr[1]), "+v"(xr[2]) :: "memory");
  {
    float v[24]; u32x4 h[3];
#pragma unroll
    for (int i = 0; i < 3; ++i)
#pragma unroll
      for (int e = 0; e < 4; ++e) { v[8 * i + 2 * e] = bflo(xr[i][e]); v[8 * i + 2 * e + 1] = bfhi(xr[i][e]); }
    layernorm24(v, h);
#pragma unroll
    for (int i = 0; i < 3; ++i) lds_w128(lds0 + K1_H + fi_pos(r, j + 8 * i), h[i]);
  }
  const unsigned i16 = l & 15, g = l >> 4;
  unsigned fr[2];
#pragma unroll
  for (int hf = 0; hf < 2; ++hf) fr[hf] = fi_pos(16 * hf + i16, g);
  const unsigned tq = i16 >> 2, tp = l & 3, phi = ((tq >> 1) & 1) | ((g & 1) << 1);
  unsigned tcol[3];
#pragma unroll
  for (int i = 0; i < 3; ++i) tcol[i] = (((3 * w + i) ^ phi) * 2 + (tp >> 1)) * 16 + 8 * (tp & 1);
  f32x4 acc[3][3][2];
#pragma unroll
  for (int m = 0; m < 3; ++m)
#pragma unroll
    for (int i = 0; i < 3; ++i) { acc[m][i][0] = f32x4{0, 0, 0, 0}; acc[m][i][1] = f32x4{0, 0, 0, 0}; }
#pragma unroll
  for (int ks = 0; ks < 6; ++ks) {
    if (ks == 0) asm volatile("s_waitcnt vmcnt(27) lgkmcnt(0)\n\ts_barrier" ::: "memory");
    else asm volatile("s_waitcnt vmcnt(18) lgkmcnt(0)\n\ts_barrier" ::: "memory");
    if (ks == 1) DMAQ(4);
    if (ks == 2) DMAQ(5);
    if (ks == 3) DMAO(6);
    if (ks == 4) DMAO(7);
    const u32x4 h0 = lds_r128(lds0 + K1_H + fr[0] + ks * 1024), h1 = lds_r128(lds0 + K1_H + fr[1] + ks * 1024);
    const unsigned base = lds0 + (ks & 3) * SLOT + (8 * g + tq) * 1152;
#pragma unroll
    for (int i = 0; i < 3; ++i)
#pragma unroll
      for (int m = 0; m < 3; ++m) {
        const u32x4 a = lds_tr2(base + tcol[i] + m * 384, base + tcol[i] + m * 384 + 4608);
        acc[m][i][0] = mfma16(a, h0, acc[m][i][0]);
        acc[m][i][1] = mfma16(a, h1, acc[m][i][1]);
      }
  }
  const unsigned VI = lds0, SP = lds0 + TILE_B, YI = lds0 + SLOT;
#pragma unroll
  for (int hf = 0; hf < 2; ++hf) {
    f32x4 s = {0, 0, 0, 0};
#pragma unroll
    for (int i = 0; i < 3; ++i) s = mfma16k16(pack4(acc[1][i][hf]), pack4(acc[0][i][hf]), s);
    lds_w128(SP + ((w * 2 + hf) * 64 + l) * 16, __builtin_bit_cast(u32x4, s));
#pragma unroll
    for (int i = 0; i < 3; ++i) lds_w64(VI + fi_pos(16 * hf + i16, 2 * (3 * w + i) + (g >> 1)) + 8 * (g & 1), pack4(acc[2][i][hf]));
  }
  asm volatile("s_waitcnt vmcnt(9) lgkmcnt(0)\n\ts_barrier" ::: "memory");
  u32x2 pt[2];
#pragma unroll
  for (int hf = 0; hf < 2; ++hf) {
    f32x4 s = __builtin_bit_cast(f32x4, lds_r128(SP + (hf * 64 + l) * 16));
#pragma unroll
    for (int ww = 1; ww < 4; ++ww) s += __builtin_bit_cast(f32x4, lds_r128(SP + ((ww * 2 + hf) * 64 + l) * 16));
    float mx = xor16_max(fmaxf(fmaxf(s[0], s[1]), fmaxf(s[2], s[3])));
    f32x4 e;
#pragma unroll
    for (int k = 0; k < 4; ++k) e[k] = __builtin_amdgcn_exp2f((s[k] - mx) * QSCALE);
    const float sum = xor16_add((e[0] + e[1]) + (e[2] + e[3]));
    const float inv = ((g >> 1) == (i16 >> 3)) ? __builtin_amdgcn_rcpf(sum) : 0.f;
    pt[hf] = pack4(e * inv);
  }
  f32x4 vp[3][2];
#pragma unroll
  for (int i = 0; i < 3; ++i) { vp[i][0] = f32x4{0, 0, 0, 0}; vp[i][1] = f32x4{0, 0, 0, 0}; }
#pragma unroll
  for (int ks = 0; ks < 6; ++ks) {
    if (ks == 3) asm volatile("s_waitcnt vmcnt(0)\n\ts_barrier" ::: "memory");
    const u32x4 v0 = lds_r128(VI + fr[0] + ks * 1024), v1 = lds_r128(VI + fr[1] + ks * 1024);
    const unsigned base = lds0 + (2 + ks / 3) * SLOT + (32 * (ks % 3) + 8 * g + tq) * 384;
#pragma unroll
    for (int i = 0; i < 3; ++i) {
      const u32x4 bw = lds_tr2(base + tcol[i], base + tcol[i] + 1536);
      vp[i][0] = mfma16(v0, bw, vp[i][0]);
      vp[i][1] = mfma16(v1, bw, vp[i][1]);
    }
  }
#pragma unroll
  for (int i = 0; i < 3; ++i)
#pragma unroll
    for (int hf = 0; hf < 2; ++hf) {
      const f32x4 y = mfma16k16(pack4(vp[i][hf]), pt[hf], f32x4{0, 0, 0, 0});
      lds_w64(YI + fi_pos(16 * hf + i16, 2 * (3 * w + i) + (g >> 1)) + 8 * (g & 1), pack4(y));
    }
  wg_barrier();
  {
    float v[24]; u32x4 x1[3], h[3];
#pragma unroll
    for (int i = 0; i < 3; ++i) {
      const u32x4 y = lds_r128(YI + fi_pos(r, j + 8 * i));
#pragma unroll
      for (int e = 0; e < 4; ++e) {
        x1[i][e] = cvtpk(bflo(xr[i][e]) + bflo(y[e]), bfhi(xr[i][e]) + bfhi(y[e]));
        v[8 * i + 2 * e] = bflo(x1[i][e]); v[8 * i + 2 * e + 1] = bfhi(x1[i][e]);
      }
    }
    layernorm24(v, h);
    const unsigned so = tt * FRAME_B + (b >> 3) * TILE_B + (j >> 1) * 1024 + (b & 7) * 128 + (j & 1) * 64 + nl * 16;
#pragma unroll
    for (int i = 0; i < 3; ++i) *(u32x4*)(ws + WS_H2 + so + i * 4096) = h[i];
#pragma unroll
    for (int i = 0; i < 3; ++i) *(u32x4*)(ws + WS_X1 + so + i * 4096) = x1[i];
  }
}

constexpr int K2_V = 0, K2_FI = 8 * TILE_B, K2_Q = K2_FI + TILE_B, K2_N = K2_Q + TILE_B, K2_LDS = K2_N + 2048;

__global__ __launch_bounds__(256, 1) void k_spatial(const char* __restrict__ ws, char* __restrict__ out) {
  extern __shared__ __attribute__((aligned(16))) char smem[];
  const unsigned lds0 = (unsigned)(size_t)(LAS char*)smem;
  const unsigned b = blockIdx.x, t = b & 7, qb = b >> 3, l = threadIdx.x & 63, w = __builtin_amdgcn_readfirstlane(threadIdx.x >> 6);
  const unsigned r32 = l & 31, hh = l >> 5, i16 = l & 15, g = l >> 4;
  const auto rsH = __builtin_amdgcn_make_buffer_rsrc((void*)(ws + WS_H2 + (size_t)t * FRAME_B), 0, FRAME_B, 0x00020000);
  const auto rsW = __builtin_amdgcn_make_buffer_rsrc((void*)(ws + WS_WQK), 0, 2 * 73728, 0x00020000);
  const unsigned kofs = (r32 >> 2) * 128 + hh * 64 + (r32 & 3) * 16;
  auto ldk = [&](u32x4 (&K)[12], unsigned i) {
    const unsigned tile = 8 * w + ((i + qb) & 7);
#pragma unroll
    for (int s = 0; s < 12; ++s) K[s] = __builtin_amdgcn_raw_buffer_load_b128(rsH, kofs + (s & 3) * 1024, tile * TILE_B + (s >> 2) * 4096, 0);
  };
  u32x4 hq[3], wq[18], wv[18], KA[12], KB[12];
#pragma unroll
  for (int i = 0; i < 3; ++i) hq[i] = __builtin_amdgcn_raw_buffer_load_b128(rsH, kofs + i * 1024, qb * TILE_B + w * 3072, 0);
#pragma unroll
  for (int i = 0; i < 18; ++i) wq[i] = __builtin_amdgcn_raw_buffer_load_b128(rsW, l * 16 + (i & 3) * 1024, w * 18432 + (i >> 2) * 4096, 0);
  ldk(KA, 0);
  ldk(KB, 1);
#pragma unroll
  for (int i = 0; i < 3; ++i) lds_w128(lds0 + K2_FI + fi_pos(r32, 2 * (3 * w + i) + hh), hq[i]);
  wg_barrier();
  unsigned fr[2];
#pragma unroll
  for (int hf = 0; hf < 2; ++hf) fr[hf] = lds0 + K2_FI + fi_pos(16 * hf + i16, g);
  auto gemm = [&](const u32x4 (&wf)[18], f32x4 (&d)[3][2]) {
#pragma unroll
    for (int i = 0; i < 3; ++i) { d[i][0] = f32x4{0, 0, 0, 0}; d[i][1] = f32x4{0, 0, 0, 0}; }
#pragma unroll
    for (int ks = 0; ks < 6; ++ks) {
      const u32x4 b0 = lds_r128(fr[0] + ks * 1024), b1 = lds_r128(fr[1] + ks * 1024);
#pragma unroll
      for (int i = 0; i < 3; ++i) { d[i][0] = mfma16(wf[i * 6 + ks], b0, d[i][0]); d[i][1] = mfma16(wf[i * 6 + ks], b1, d[i][1]); }
    }
  };
  {
    f32x4 d[3][2];
    gemm(wq, d);
    float n2[2];
#pragma unroll
    for (int hf = 0; hf < 2; ++hf) {
      n2[hf] = 0.f;
#pragma unroll
      for (int i = 0; i < 3; ++i) {
        lds_w64(lds0 + K2_Q + ((3 * w + i) * 64 + (g >> 1) * 32 + 16 * hf + i16) * 16 + 8 * (g & 1), pack4(d[i][hf]));
#pragma unroll
        for (int e = 0; e < 4; ++e) n2[hf] = fmaf(d[i][hf][e], d[i][hf][e], n2[hf]);
      }
    }
    auto rr = __builtin_amdgcn_permlane16_swap(asu(n2[0]), asu(n2[1]), false, false);
    *(LAS float*)(size_t)(lds0 + K2_N + l * 16 + w * 4) = asf(rr[0]) + asf(rr[1]);
  }
  wg_barrier();
  u32x4 Q[12];
#pragma unroll
  for (int s = 0; s < 12; ++s) Q[s] = lds_r128(lds0 + K2_Q + s * 1024 + l * 16);
  float mref;
  {
    const f32x4 a = __builtin_bit_cast(f32x4, lds_r128(lds0 + K2_N + r32 * 16)), c = __builtin_bit_cast(f32x4, lds_r128(lds0 + K2_N + 512 + r32 * 16));
    const f32x4 s = a + c;
    mref = __builtin_amdgcn_sqrtf(((s[0] + s[1]) + (s[2] + s[3])) * 192.0f) * 1.01f;
  }
  const unsigned vbuf = lds0 + K2_V + w * 2 * TILE_B;
  unsigned vw[4];
  {
    const unsigned f = ((r32 & 2) << 1) | ((r32 & 4) >> 1) | (r32 & 1);
#pragma unroll
    for (int s = 0; s < 4; ++s) vw[s] = vbuf + r32 * 384 + (((2 * s + hh) ^ f) * 16);
  }
  unsigned vr[2];
  {
    const unsigned tq = i16 >> 2, tp = l & 3, row = 4 * hh + tq, f = ((row & 2) << 1) | ((row & 4) >> 1) | (row & 1);
#pragma unroll
    for (int c = 0; c < 2; ++c) vr[c] = vbuf + row * 384 + (((4 * c + 2 * (g & 1) + (tp >> 1)) ^ f) * 16) + 8 * (tp & 1);
  }
  u32x4 sel;
  { const unsigned one = ((i16 & 3) == (g & 1)) ? 0x3f803f80u : 0u; sel = u32x4{one, one, one, one}; }
  f32x16 negm;
#pragma unroll
  for (int i = 0; i < 16; ++i) negm[i] = -mref;
  f32x16 O[6];
#pragma unroll
  for (int c = 0; c < 6; ++c)
#pragma unroll
    for (int i = 0; i < 16; ++i) O[c][i] = 0.f;
  f32x4 lsum = {0, 0, 0, 0};
  auto qk = [&](const u32x4 (&K)[12], int buf) {
    f32x16 s = mfma32(K[0], Q[0], negm);
#pragma unroll
    for (int k = 1; k < 12; ++k) s = mfma32(K[k], Q[k], s);
#pragma unroll
    for (int k = 0; k < 12; ++k) lds_w128(vw[k & 3] + (k >> 2) * 128 + buf * TILE_B, K[k]);
    return s;
  };
  auto pv = [&](const f32x16& s, int buf) {
    u32x4 P[2];
#pragma unroll
    for (int k = 0; k < 2; ++k)
#pragma unroll
      for (int e = 0; e < 4; ++e) P[k][e] = cvtpk(__builtin_amdgcn_exp2f(s[8 * k + 2 * e]), __builtin_amdgcn_exp2f(s[8 * k + 2 * e + 1]));
#pragma unroll
    for (int k = 0; k < 2; ++k) {
      lsum = mfma16(sel, P[k], lsum);
#pragma unroll
      for (int c = 0; c < 6; ++c) {
        const unsigned a = vr[c & 1] + (c >> 1) * 128 + 16 * k * 384 + buf * TILE_B;
        O[c] = mfma32(lds_tr2(a, a + 8 * 384), P[k], O[c]);
      }
    }
  };
  f32x16 SA = qk(KA, 0), SB;
  ldk(KA, 2);
#pragma unroll
  for (int it = 0; it < 4; ++it) {
    SB = qk(KB, 1);
    if (it < 3) ldk(KB, 2 * it + 3);
    else {
#pragma unroll
      for (int i = 0; i < 18; ++i) wv[i] = __builtin_amdgcn_raw_buffer_load_b128(rsW, l * 16 + (i & 3) * 1024, 73728 + w * 18432 + (i >> 2) * 4096, 0);
    }
    pv(SA, 0);
    if (it < 3) { SA = qk(KA, 0); if (it < 2) ldk(KA, 2 * it + 4); }
    pv(SB, 1);
  }
  const auto rsX = __builtin_amdgcn_make_buffer_rsrc((void*)(ws + WS_X1 + (size_t)t * FRAME_B), 0, FRAME_B, 0x00020000);
  u32x4 x1[3];
  const unsigned tok = 16 * (g & 1) + i16;
#pragma unroll
  for (int i = 0; i < 3; ++i) x1[i] = __builtin_amdgcn_raw_buffer_load_b128(rsX, (tok >> 2) * 128 + hh * 64 + (tok & 3) * 16 + i * 1024, qb * TILE_B + w * 3072, 0);
#pragma unroll
  for (int u = 0; u < 12; ++u) {
    u32x4 p;
#pragma unroll
    for (int e = 0; e < 4; ++e) p[e] = cvtpk(O[u >> 1][8 * (u & 1) + 2 * e], O[u >> 1][8 * (u & 1) + 2 * e + 1]);
    lds_w128(vbuf + u * 1024 + l * 16, p);
  }
  if (l < 16) { u32x2 ls = {asu(lsum[0]), asu(lsum[1])}; lds_w64(lds0 + K2_N + 1024 + (l * 4 + w) * 8, ls); }
  wg_barrier();
#pragma unroll
  for (int i = 0; i < 3; ++i) {
    float a[8];
#pragma unroll
    for (int ww = 0; ww < 4; ++ww) {
      const u32x4 p = lds_r128(lds0 + K2_V + ww * 2 * TILE_B + (3 * w + i) * 1024 + l * 16);
#pragma unroll
      for (int e = 0; e < 4; ++e) {
        if (ww == 0) { a[2 * e] = bflo(p[e]); a[2 * e + 1] = bfhi(p[e]); }
        else { a[2 * e] += bflo(p[e]); a[2 * e + 1] += bfhi(p[e]); }
      }
    }
    u32x2 lo = {cvtpk(a[0], a[1]), cvtpk(a[2], a[3])}, hi = {cvtpk(a[4], a[5]), cvtpk(a[6], a[7])};
    lds_w64(lds0 + K2_FI + fi_pos(r32, 2 * (3 * w + i)) + 8 * hh, lo);
    lds_w64(lds0 + K2_FI + fi_pos(r32, 2 * (3 * w + i) + 1) + 8 * hh, hi);
  }
  float linv[2];
  {
    const f32x4 a = __builtin_bit_cast(f32x4, lds_r128(lds0 + K2_N + 1024 + i16 * 32)), c = __builtin_bit_cast(f32x4, lds_r128(lds0 + K2_N + 1024 + i16 * 32 + 16));
    linv[0] = __builtin_amdgcn_rcpf((a[0] + a[2]) + (c[0] + c[2]));
    linv[1] = __builtin_amdgcn_rcpf((a[1] + a[3]) + (c[1] + c[3]));
  }
  wg_barrier();
  f32x4 d[3][2];
  gemm(wv, d);
#pragma unroll
  for (int i = 0; i < 3; ++i) {
    const u32x2 y0 = pack4(d[i][0] * linv[0]), y1 = pack4(d[i][1] * linv[1]);
    auto s0 = __builtin_amdgcn_permlane16_swap(y0[0], y1[0], false, false);
    auto s1 = __builtin_amdgcn_permlane16_swap(y0[1], y1[1], false, false);
    const u32x4 y = {s0[0], s1[0], s0[1], s1[1]};
    u32x4 o;
#pragma unroll
    for (int e = 0; e < 4; ++e) o[e] = cvtpk(bflo(x1[i][e]) + bflo(y[e]), bfhi(x1[i][e]) + bfhi(y[e]));
    *(u32x4*)(out + (size_t)(t * 1024 + qb * 32 + tok) * 384 + (2 * (3 * w + i) + hh) * 16) = o;
  }
}

extern "C" void kernel_launch(void* const* d_in, const int* in_sizes, int n_in, void* d_out, int out_size, void* d_ws, size_t ws_size, hipStream_t stream) {
  (void)hipFuncSetAttribute((const void*)k_temporal, hipFuncAttributeMaxDynamicSharedMemorySize, K1_LDS);
  (void)hipFuncSetAttribute((const void*)k_spatial, hipFuncAttributeMaxDynamicSharedMemorySize, K2_LDS);
  k_merge<<<144, 64, 0, stream>>>((const unsigned short*)d_in[3], (const unsigned short*)d_in[4], (char*)d_ws);
  k_temporal<<<256, 256, K1_LDS, stream>>>((const char*)d_in[0], (const char*)d_in[1], (const char*)d_in[2], (char*)d_ws);
  k_spatial<<<256, 256, K2_LDS, stream>>>((const char*)d_ws, (char*)d_out);
}
```

```cpp
#include <hip/hip_runtime.h>
#include <hip/hip_bf16.h>
#include <type_traits>

typedef short s16x4 __attribute__((ext_vector_type(4)));
typedef __bf16 b16x8 __attribute__((ext_vector_type(8)));
typedef __bf16 b16x2 __attribute__((ext_vector_type(2)));
typedef float f32x2 __attribute__((ext_vector_type(2)));
typedef float f32x4 __attribute__((ext_vector_type(4)));
typedef float f32x16 __attribute__((ext_vector_type(16)));
typedef unsigned u32x2 __attribute__((ext_vector_type(2)));
typedef unsigned u32x4 __attribute__((ext_vector_type(4)));
typedef unsigned long long u64;
#define LAS __attribute__((address_space(3)))
#define DI __device__ __forceinline__
#define SBAR() __builtin_amdgcn_sched_barrier(0)

constexpr int TILE_B = 12288;
constexpr int FRAME_B = 393216;
constexpr unsigned WS_H2 = 0, WS_X1 = 8 * FRAME_B, WS_WM = 16 * FRAME_B, WS_FLAG = WS_WM + 2 * 73728, WS_FLAG2 = WS_FLAG + 2048, WS_EPOCH = WS_FLAG2 + 2048, WS_END = WS_EPOCH + 256;
constexpr float QSCALE = 0.07216878364870322f * 1.4426950408889634f;
constexpr int SC1 = 16;
constexpr unsigned POISON = 0xffffffffu;
constexpr int SPIN_CAP = 1 << 16;

#define ST_DECL
#define ST(k)
#define ST_OUT(ws, k0, k1)

DI float asf(unsigned u) { return __builtin_bit_cast(float, u); }
DI unsigned asu(float f) { return __builtin_bit_cast(unsigned, f); }
DI unsigned cvtpk(float lo, float hi) { f32x2 v = {lo, hi}; return __builtin_bit_cast(unsigned, __builtin_convertvector(v, b16x2)); }
DI float bflo(unsigned u) { return asf(u << 16); }
DI float bfhi(unsigned u) { return asf(u & 0xffff0000u); }
DI f32x16 mfma32(u32x4 a, u32x4 b, f32x16 c) { return __builtin_amdgcn_mfma_f32_32x32x16_bf16(__builtin_bit_cast(b16x8, a), __builtin_bit_cast(b16x8, b), c, 0, 0, 0); }
DI f32x4 mfma16(u32x4 a, u32x4 b, f32x4 c) { return __builtin_amdgcn_mfma_f32_16x16x32_bf16(__builtin_bit_cast(b16x8, a), __builtin_bit_cast(b16x8, b), c, 0, 0, 0); }
DI f32x4 mfma16k16(u32x2 a, u32x2 b, f32x4 c) { return __builtin_amdgcn_mfma_f32_16x16x16bf16_1k(__builtin_bit_cast(s16x4, a), __builtin_bit_cast(s16x4, b), c, 0, 0, 0); }
DI u32x2 pack4(f32x4 v) { u32x2 r = {cvtpk(v[0], v[1]), cvtpk(v[2], v[3])}; return r; }
DI u32x4 lds_r128(unsigned a) { return *(LAS u32x4*)(size_t)a; }
DI f32x4 lds_r128f(unsigned a) { return *(LAS f32x4*)(size_t)a; }
DI u32x2 lds_r64(unsigned a) { return *(LAS u32x2*)(size_t)a; }
DI void lds_w128(unsigned a, u32x4 v) { *(LAS u32x4*)(size_t)a = v; }
DI void lds_w128f(unsigned a, f32x4 v) { *(LAS f32x4*)(size_t)a = v; }
DI void lds_w64(unsigned a, u32x2 v) { *(LAS u32x2*)(size_t)a = v; }
DI void lds_w32(unsigned a, float v) { *(LAS float*)(size_t)a = v; }
DI u32x2 lds_tr(unsigned a) { return __builtin_bit_cast(u32x2, __builtin_amdgcn_ds_read_tr16_b64_v4i16((LAS s16x4*)(size_t)a)); }
DI u32x4 lds_tr2(unsigned a0, unsigned a1) { u32x2 x = lds_tr(a0), y = lds_tr(a1); u32x4 r = {x[0], x[1], y[0], y[1]}; return r; }
#define BAR() asm volatile("s_waitcnt lgkmcnt(0)\n\ts_barrier" ::: "memory")
#define BARV(n) asm volatile("s_waitcnt vmcnt(" #n ") lgkmcnt(0)\n\ts_barrier" ::: "memory")
template <int CTRL> DI float dpp_add(float v) { return v + asf(__builtin_amdgcn_update_dpp(0u, asu(v), CTRL, 0xf, 0xf, true)); }
DI float xor16_add(float v) { auto r = __builtin_amdgcn_permlane16_swap(asu(v), asu(v), false, false); return asf(r[0]) + asf(r[1]); }
DI float xor16_max(float v) { auto r = __builtin_amdgcn_permlane16_swap(asu(v), asu(v), false, false); return fmaxf(asf(r[0]), asf(r[1])); }
DI unsigned umax3(unsigned a, unsigned b, unsigned c) { return max(max(a, b), c); }
template <int N> DI bool poisoned(const u32x4 (&v)[N]) {
  unsigned m = 0;
#pragma unroll
  for (int i = 0; i < N; ++i) { m = umax3(m, v[i][0], v[i][1]); m = umax3(m, v[i][2], v[i][3]); }
  return __any(m == POISON);
}
DI u32x4 unpoison(u32x4 v) { u32x4 r = {min(v[0], POISON - 1), min(v[1], POISON - 1), min(v[2], POISON - 1), min(v[3], POISON - 1)}; return r; }
DI unsigned fi_pos(unsigned r, unsigned c8) {
  return (((r >> 4) * 6 + (c8 >> 2)) * 64 + (c8 & 3) * 16 + ((r & 15) ^ ((c8 & 1) | (((r >> 3) & 1) << 1) | ((r >> 4) << 2)))) * 16;
}
DI void layernorm24(const float (&v)[24], u32x4 (&o)[3]) {
  float s0 = 0.f, s1 = 0.f;
#pragma unroll
  for (int i = 0; i < 24; i += 2) { s0 += v[i]; s1 += v[i + 1]; }
  float s = s0 + s1;
  s = dpp_add<0x124>(s); s = dpp_add<0x128>(s); s = xor16_add(s);
  const float mu = s * (1.0f / 192.0f);
  float d[24], q0 = 0.f, q1 = 0.f;
#pragma unroll
  for (int i = 0; i < 24; i += 2) { d[i] = v[i] - mu; d[i + 1] = v[i + 1] - mu; q0 = fmaf(d[i], d[i], q0); q1 = fmaf(d[i + 1], d[i + 1], q1); }
  float q = q0 + q1;
  q = dpp_add<0x124>(q); q = dpp_add<0x128>(q); q = xor16_add(q);
  const float rs = __builtin_amdgcn_rsqf(q * (1.0f / 192.0f) + 1e-5f);
#pragma unroll
  for (int i = 0; i < 3; ++i)
#pragma unroll
    for (int e = 0; e < 4; ++e) o[i][e] = cvtpk(d[8 * i + 2 * e] * rs, d[8 * i + 2 * e + 1] * rs);
}

constexpr unsigned SLOT = 36864, L_H = 4 * SLOT, L_PT = L_H + TILE_B, LDS_BYTES = 163840;
constexpr unsigned L_VI = 0, L_SP = TILE_B, L_YI = SLOT, L_MA = 3 * SLOT, L_MB = L_MA + TILE_B;
constexpr unsigned L_HQ = 30720, L_Q = L_H, L_FI = L_H, L_N = L_PT + 1024, L_L = L_N + 1024;

#define DMA4(v0, v1, v2, v3, sbase, ldsa)                                                                  \
  asm volatile("s_mov_b32 m0, %5\n\ts_nop 0\n\t"                                                            \
               "global_load_lds_dwordx4 %0, %4\n\tglobal_load_lds_dwordx4 %1, %4 offset:1024\n\t"           \
               "global_load_lds_dwordx4 %2, %4 offset:2048\n\tglobal_load_lds_dwordx4 %3, %4 offset:3072"   \
               :: "v"(v0), "v"(v1), "v"(v2), "v"(v3), "s"(sbase), "s"(ldsa) : "memory")
#define DMA1(v0, sbase, ldsa) \
  asm volatile("s_mov_b32 m0, %2\n\ts_nop 0\n\tglobal_load_lds_dwordx4 %0, %1" :: "v"(v0), "s"(sbase), "s"(ldsa) : "memory")
#define DMA9(o, sbase, ldsa) do { DMA4(o[0], o[1], o[2], o[3], sbase, ldsa); DMA4(o[4], o[5], o[6], o[7], sbase, (ldsa) + 4096); DMA1(o[8], sbase, (ldsa) + 8192); } while (0)

template <int OFF> DI void bdma(unsigned voff, u32x4 rs, unsigned soff) { asm volatile("buffer_load_dwordx4 %0, %1, %2 offen offset:%3 sc1 lds" :: "v"(voff), "s"(rs), "s"(soff), "i"(OFF) : "memory"); }
template <int OFF> DI void setm0(unsigned base) { asm volatile("s_add_u32 m0, %0, %1\n\ts_nop 0" :: "s"(base), "i"(OFF) : "memory", "scc"); }
template <int N> DI void waitvm() { asm volatile("s_waitcnt vmcnt(%0)" :: "i"(N) : "memory"); }
#define IC(x) std::integral_constant<int, (x)>{}
#define OPAQUE(v) asm volatile("" : "+v"(v))
DI bool tags_ok(u32x4 f, u32x2 tag) { return f[0] == tag[0] && f[1] == tag[1] && f[2] == tag[0] && f[3] == tag[1]; }

__global__ __launch_bounds__(512, 2) void k_timesformer(const char* __restrict__ x, const char* __restrict__ wqkv_t, const char* __restrict__ wo_t,
                                                        const char* __restrict__ wqkv_s, const char* __restrict__ wo_s, char* __restrict__ ws, char* __restrict__ out) {
  extern __shared__ __attribute__((aligned(16))) char smem[];
  const unsigned lds0 = (unsigned)(size_t)(LAS char*)smem;
  const unsigned b = blockIdx.x, w8 = __builtin_amdgcn_readfirstlane(threadIdx.x >> 6), w = w8 & 3;
  unsigned l = threadIdx.x & 63;
  const unsigned t = b & 7, qb = b >> 3;
  const auto rsWS = __builtin_amdgcn_make_buffer_rsrc((void*)ws, 0, WS_END, 0x00020000);
  ST_DECL;
#define LANE_IDS const unsigned i16 = l & 15, g = l >> 4, r32 = l & 31, hh = l >> 5, tq = i16 >> 2, tp = l & 3
#define FRAG_POS unsigned fr[2], dpos[3][2];                                                                        \
  _Pragma("unroll") for (int hf = 0; hf < 2; ++hf) { fr[hf] = lds0 + fi_pos(16 * hf + i16, g);                     \
    _Pragma("unroll") for (int i = 0; i < 3; ++i) dpos[i][hf] = lds0 + fi_pos(16 * hf + i16, 2 * (3 * w + i) + (g >> 1)) + 8 * (g & 1); }
  auto reduce_o = [&]() {
    const unsigned r32 = l & 31, hh = l >> 5;
#pragma unroll
    for (int i = 0; i < 3; ++i) {
      const unsigned hu = 3 * w8 + i;
      u32x2 p[4];
#pragma unroll
      for (int ww = 0; ww < 4; ++ww) p[ww] = lds_r64(lds0 + ww * SLOT + hu * 512 + l * 8);
      f32x4 a;
      a[0] = (bflo(p[0][0]) + bflo(p[1][0])) + (bflo(p[2][0]) + bflo(p[3][0]));
      a[1] = (bfhi(p[0][0]) + bfhi(p[1][0])) + (bfhi(p[2][0]) + bfhi(p[3][0]));
      a[2] = (bflo(p[0][1]) + bflo(p[1][1])) + (bflo(p[2][1]) + bflo(p[3][1]));
      a[3] = (bfhi(p[0][1]) + bfhi(p[1][1])) + (bfhi(p[2][1]) + bfhi(p[3][1]));
      lds_w64(lds0 + L_FI + fi_pos(r32, hu) + 8 * hh, pack4(a));
    }
  };
  auto row_sums = [&](float (&lt)[2]) {
    const f32x4 a = lds_r128f(lds0 + L_L + (l & 15) * 32), c = lds_r128f(lds0 + L_L + (l & 15) * 32 + 16);
    lt[0] = (a[0] + a[2]) + (c[0] + c[2]);
    lt[1] = (a[1] + a[3]) + (c[1] + c[3]);
    return (bool)__any(!(lt[0] >= 8.7e-19f && lt[1] >= 8.7e-19f));
  };

  if (w8 < 4) {
    ST(0);
    const unsigned nl = l & 3, j = (l >> 2) & 7, tt = 2 * w + (l >> 5), r = nl * 8 + tt;
    const auto rsX = __builtin_amdgcn_make_buffer_rsrc((void*)x, 0, 8 * FRAME_B, 0x00020000);
    u32x4 xr[3];
#pragma unroll
    for (int i = 0; i < 3; ++i) xr[i] = __builtin_amdgcn_raw_buffer_load_b128(rsX, (tt * 1024 + nl) * 384 + j * 16 + i * 128, b * 1536, 0);
    const u32x2 ep = __builtin_amdgcn_raw_buffer_load_b64(rsWS, 0, WS_EPOCH, SC1);
    SBAR();
    const unsigned so = tt * FRAME_B + (j >> 1) * 1024 + ((t ^ ((j >> 1) & 1)) * 128) + (((j & 1) ^ (t >> 2)) * 64) + nl * 16, sso = qb * TILE_B;
    const u32x4 pz = {POISON, POISON, POISON, POISON};
    __builtin_amdgcn_raw_buffer_store_b128(pz, rsWS, so, WS_H2 + sso, SC1);
    OPAQUE(l);
    LANE_IDS;
    FRAG_POS;
    const unsigned phi = ((tq >> 1) & 1) | ((g & 1) << 1);
    unsigned tcol[3], rpos[3];
#pragma unroll
    for (int i = 0; i < 3; ++i) { tcol[i] = (((3 * w + i) ^ phi) * 2 + (tp >> 1)) * 16 + 8 * (tp & 1); rpos[i] = lds0 + fi_pos(r, j + 8 * i); }
    SBAR();
    float xv[24];
    {
      u32x4 h[3];
#pragma unroll
      for (int i = 0; i < 3; ++i)
#pragma unroll
        for (int e = 0; e < 4; ++e) { xv[8 * i + 2 * e] = bflo(xr[i][e]); xv[8 * i + 2 * e + 1] = bfhi(xr[i][e]); }
      ST(1);
      layernorm24(xv, h);
#pragma unroll
      for (int i = 0; i < 3; ++i) lds_w128(rpos[i] + L_H, h[i]);
    }
    __builtin_amdgcn_raw_buffer_store_b128(pz, rsWS, so, WS_H2 + sso + 4096, SC1);
    ST(2);
    const u64 tag64 = (((u64)ep[1] << 32) | ep[0]) + 1;
    const u32x2 tag = {(unsigned)tag64, (unsigned)(tag64 >> 32)};
    f32x4 acc[3][3][2];
#pragma unroll
    for (int m = 0; m < 3; ++m)
#pragma unroll
      for (int i = 0; i < 3; ++i) { acc[m][i][0] = f32x4{0, 0, 0, 0}; acc[m][i][1] = f32x4{0, 0, 0, 0}; }
#pragma unroll
    for (int ks = 0; ks < 6; ++ks) {
      if (ks == 3) BARV(0); else BAR();
      if (ks == 0) ST(3);
      if (ks == 5) ST(4);
      if (ks == 0) __builtin_amdgcn_raw_buffer_store_b128(pz, rsWS, so, WS_H2 + sso + 8192, SC1);
      if (ks == 3 && w == 0 && l == 0) __builtin_amdgcn_raw_buffer_store_b64(tag, rsWS, 0, WS_FLAG + b * 8, SC1);
      const u32x4 h0 = lds_r128(fr[0] + L_H + ks * 1024), h1 = lds_r128(fr[1] + L_H + ks * 1024);
      const unsigned base = lds0 + (ks & 3) * SLOT + (8 * g + tq) * 1152;
      u32x4 a[9];
#pragma unroll
      for (int m = 0; m < 3; ++m)
#pragma unroll
        for (int i = 0; i < 3; ++i) a[m * 3 + i] = lds_tr2(base + tcol[i] + m * 384, base + tcol[i] + m * 384 + 4608);
      SBAR();
#pragma unroll
      for (int m = 0; m < 3; ++m)
#pragma unroll
        for (int i = 0; i < 3; ++i) {
          acc[m][i][0] = mfma16(a[m * 3 + i], h0, acc[m][i][0]);
          acc[m][i][1] = mfma16(a[m * 3 + i], h1, acc[m][i][1]);
        }
      if (ks < 5) SBAR();
    }
#pragma unroll
    for (int hf = 0; hf < 2; ++hf) {
      f32x4 s = {0, 0, 0, 0};
#pragma unroll
      for (int i = 0; i < 3; ++i) s = mfma16k16(pack4(acc[1][i][hf]), pack4(acc[0][i][hf]), s);
#pragma unroll
      for (int i = 0; i < 3; ++i) lds_w64(dpos[i][hf] + L_VI, pack4(acc[2][i][hf]));
      lds_w128f(lds0 + L_SP + ((w * 2 + hf) * 64 + l) * 16, s);
    }
    ST(5);
    BAR();
    f32x4 vp[3][2];
#pragma unroll
    for (int i = 0; i < 3; ++i) { vp[i][0] = f32x4{0, 0, 0, 0}; vp[i][1] = f32x4{0, 0, 0, 0}; }
    u32x4 vf[6][2];
    u32x2 pt[2];
#pragma unroll
    for (int c = 0; c < 2; ++c) {
      if (c == 1) { BAR(); ST(6); }
      u32x4 bw[9];
#pragma unroll
      for (int k = 0; k < 3; ++k) {
        if (c == 0) { vf[k][0] = lds_r128(fr[0] + L_VI + k * 1024); vf[k][1] = lds_r128(fr[1] + L_VI + k * 1024); }
#pragma unroll
        for (int i = 0; i < 3; ++i) {
          const unsigned base = lds0 + (2 + c) * SLOT + (32 * k + 8 * g + tq) * 384 + tcol[i];
          bw[k * 3 + i] = lds_tr2(base, base + 1536);
        }
      }
      if (c == 0) {
#pragma unroll
        for (int k = 3; k < 6; ++k) { vf[k][0] = lds_r128(fr[0] + L_VI + k * 1024); vf[k][1] = lds_r128(fr[1] + L_VI + k * 1024); }
      } else { pt[0] = lds_r64(lds0 + L_PT + l * 8); pt[1] = lds_r64(lds0 + L_PT + 512 + l * 8); }
      SBAR();
#pragma unroll
      for (int k = 0; k < 3; ++k)
#pragma unroll
        for (int i = 0; i < 3; ++i) {
          vp[i][0] = mfma16(vf[3 * c + k][0], bw[k * 3 + i], vp[i][0]);
          vp[i][1] = mfma16(vf[3 * c + k][1], bw[k * 3 + i], vp[i][1]);
        }
      if (c == 0) SBAR();
    }
#pragma unroll
    for (int i = 0; i < 3; ++i)
#pragma unroll
      for (int hf = 0; hf < 2; ++hf) lds_w64(dpos[i][hf] + L_YI, pack4(mfma16k16(pack4(vp[i][hf]), pt[hf], f32x4{0, 0, 0, 0})));
    BAR();
    ST(7);
    {
      u32x4 x1[3], h[3];
#pragma unroll
      for (int i = 0; i < 3; ++i) {
        const u32x4 y = lds_r128(rpos[i] + L_YI);
#pragma unroll
        for (int e = 0; e < 4; ++e) { xv[8 * i + 2 * e] += bflo(y[e]); xv[8 * i + 2 * e + 1] += bfhi(y[e]); }
      }
      layernorm24(xv, h);
#pragma unroll
      for (int i = 0; i < 3; ++i) __builtin_amdgcn_raw_buffer_store_b128(unpoison(h[i]), rsWS, so, WS_H2 + sso + i * 4096, SC1);
      SBAR();
#pragma unroll
      for (int i = 0; i < 3; ++i) {
#pragma unroll
        for (int e = 0; e < 4; ++e) x1[i][e] = cvtpk(xv[8 * i + 2 * e], xv[8 * i + 2 * e + 1]);
        __builtin_amdgcn_raw_buffer_store_b128(x1[i], rsWS, so, WS_X1 + sso + i * 4096, SC1);
      }
    }
    ST(8);
    const unsigned ring = lds0 + w * SLOT;
    const unsigned kr0 = ring + (r32 >> 2) * 128 + (hh ^ (r32 >> 4)) * 64 + (r32 & 3) * 16, kr1 = kr0 ^ 128;
    const unsigned vr0 = ring + (g & 1) * 1024 + (hh ^ (g & 1)) * 128 + (tp >> 1) * 64 + tq * 16 + 8 * (tp & 1), vr1 = (vr0 ^ 64) + 512;
    const unsigned lo[3] = {l * 16, l * 16 + 4096, l * 16 + 8192};
    const u64 hbu = (u64)(ws + WS_H2 + (size_t)t * FRAME_B);
    const u32x4 rs = {(unsigned)hbu, (unsigned)(hbu >> 32) & 0xffffu, (unsigned)FRAME_B, 0x00020000u};
    unsigned toff[8];
#pragma unroll
    for (int i = 0; i < 8; ++i) toff[i] = (8 * w + ((i + qb) & 7)) * TILE_B;
    u32x4 sel;
    { const unsigned one = (tp == (g & 1)) ? 0x3f803f80u : 0u; sel = u32x4{one, one, one, one}; }
    auto piece = [&](auto Tc, auto Pc) {
      constexpr int T = decltype(Tc)::value, p = decltype(Pc)::value;
      if constexpr (T < 8) {
        if constexpr (p % 4 == 0) setm0<(T % 3) * TILE_B + p * 1024>(ring);
        bdma<(p % 4) * 1024>(lo[p / 4], rs, toff[T]);
      }
    };
#define P6(T, a) piece(IC(T), IC(a)); piece(IC(T), IC(a + 1)); piece(IC(T), IC(a + 2)); piece(IC(T), IC(a + 3)); piece(IC(T), IC(a + 4)); piece(IC(T), IC(a + 5));
    auto validate = [&](unsigned dst, unsigned off, const int n) {
      for (int sp = 0; sp < SPIN_CAP; ++sp) {
        unsigned m = 0;
#pragma unroll
        for (int k = 0; k < n; ++k) { const u32x4 v = lds_r128(dst + k * 1024 + lo[0]); m = umax3(m, v[0], v[1]); m = umax3(m, v[2], v[3]); }
        if (__builtin_expect(!__any(m == POISON), 1)) break;
#pragma unroll 1
        for (int k = 0; k < n; ++k) { setm0<0>(dst + k * 1024); bdma<0>(lo[0], rs, off + k * 1024); }
        waitvm<0>();
      }
    };
    BARV(3);
    ST(9);
    P6(0, 0) P6(0, 6)
    BARV(12);
    if (w == 0 && l == 0) __builtin_amdgcn_raw_buffer_store_b64(tag, rsWS, 0, WS_FLAG2 + b * 8, SC1);
    P6(1, 0) P6(1, 6) P6(2, 0)
    waitvm<18>();
    validate(ring, toff[0], 12);
    waitvm<6>();
    validate(ring + TILE_B, toff[1], 12);
    waitvm<0>();
    validate(ring + 2 * TILE_B, toff[2], 6);
    ST(10);
    BAR();
    u32x4 Q[12];
#pragma unroll
    for (int s = 0; s < 12; ++s) Q[s] = lds_r128(lds0 + L_Q + s * 1024 + lo[0]);
    float mref;
    {
      const f32x4 s = lds_r128f(lds0 + L_N + r32 * 16) + lds_r128f(lds0 + L_N + 512 + r32 * 16);
      mref = __builtin_amdgcn_sqrtf(((s[0] + s[1]) + (s[2] + s[3])) * 192.0f) * 1.01f;
    }
    constexpr int KW = 4, VW = 4;
#pragma unroll 1
    for (int attempt = 0;; ++attempt) {
      f32x16 S[2], O[6], negm;
      u32x4 K[12], V[12], P[2][2];
      f32x4 lsum = {0, 0, 0, 0};
      float pe[2][2];
#pragma unroll
      for (int i = 0; i < 16; ++i) negm[i] = -mref;
#define RK(k, T) K[k] = lds_r128((((k) & 1) ? kr1 : kr0) + ((T) % 3) * TILE_B + (k) * 1024);
#define RV(u, T) { const unsigned a_ = (((u) & 1) ? vr1 : vr0) + ((T) % 3) * TILE_B + ((u) >> 1) * 2048; V[u] = lds_tr2(a_, a_ + 256); }
      auto sm = [&](auto Tc, auto Hc, auto Gc) {
        constexpr int T = decltype(Tc)::value, H = decltype(Hc)::value, G = decltype(Gc)::value;
        if constexpr (G < 8) pe[(G >> 1) & 1][G & 1] = __builtin_amdgcn_exp2f(S[T & 1][8 * H + G]);
        if constexpr (G >= 2 && G <= 8 && G % 2 == 0) { constexpr int q = G / 2 - 1; P[T & 1][H][q] = cvtpk(pe[q & 1][0], pe[q & 1][1]); }
        if constexpr (G == 10) lsum = mfma16(sel, P[T & 1][H], lsum);
      };
      auto qkphase = [&](auto Ic) {
        constexpr int I = decltype(Ic)::value;
        auto step = [&](auto Kc) {
          constexpr int k = decltype(Kc)::value;
          if constexpr (I + 1 < 8) { S[(I + 1) & 1] = mfma32(K[k], Q[k], k == 0 ? negm : S[(I + 1) & 1]); if constexpr (k + KW < 12) RK(k + KW, I + 1) }
          if constexpr (I >= 0) { if constexpr (k & 1) piece(IC(I + 2), IC(6 + k / 2)); if constexpr (k < 11) sm(IC(I), IC(1), IC(k)); }
          if constexpr (I >= 0 && I < 7 && k >= 12 - VW) RV(k - (12 - VW), I)
          if constexpr (I == 7 && k < VW) RV(k, I)
          SBAR();
        };
        step(IC(0)); step(IC(1)); step(IC(2)); step(IC(3)); step(IC(4)); step(IC(5)); step(IC(6)); step(IC(7)); step(IC(8)); step(IC(9)); step(IC(10)); step(IC(11));
      };
      auto pvphase = [&](auto Ic) {
        constexpr int I = decltype(Ic)::value;
        auto step = [&](auto Uc) {
          constexpr int u = decltype(Uc)::value;
          if constexpr (I >= 0) {
            f32x16 z;
#pragma unroll
            for (int e = 0; e < 16; ++e) z[e] = 0.f;
            O[u >> 1] = mfma32(V[u], P[I & 1][u & 1], (I == 0 && !(u & 1)) ? z : O[u >> 1]);
            if constexpr (u & 1) piece(IC(I + 3), IC(u / 2));
            if constexpr (u + VW < 12) RV(u + VW, I)
          }
          if constexpr (I + 1 < 8 && u >= 1) sm(IC(I + 1), IC(0), IC(u - 1));
          if constexpr (I + 2 < 8 && I >= 0) {
            if constexpr (u == 12 - KW) waitvm<(I + 3 < 8 ? 6 + (12 - KW) / 2 : 6)>();
            if constexpr (u >= 12 - KW) RK(u - (12 - KW), I + 2)
            if constexpr (u == 11) waitvm<(I + 3 < 8 ? 6 : 0)>();
          }
          if constexpr (I < 0 && u < KW) RK(u, 1)
          if constexpr (I == 7 && u >= 3) {
            constexpr int un = u - 3;
#pragma unroll
            for (int hf = 0; hf < 2; ++hf) {
              const u32x2 pk = {cvtpk(O[un >> 1][8 * (un & 1) + 4 * hf], O[un >> 1][8 * (un & 1) + 4 * hf + 1]), cvtpk(O[un >> 1][8 * (un & 1) + 4 * hf + 2], O[un >> 1][8 * (un & 1) + 4 * hf + 3])};
              lds_w64(ring + (2 * un + hf) * 512 + l * 8, pk);
            }
          }
          SBAR();
        };
        step(IC(0)); step(IC(1)); step(IC(2)); step(IC(3)); step(IC(4)); step(IC(5)); step(IC(6)); step(IC(7)); step(IC(8)); step(IC(9)); step(IC(10)); step(IC(11));
      };
      setm0<2 * TILE_B + 4096>(ring);
      RK(0, 0) RK(1, 0) RK(2, 0) RK(3, 0)
      qkphase(IC(-1)); pvphase(IC(-1));
      qkphase(IC(0)); pvphase(IC(0)); qkphase(IC(1)); pvphase(IC(1)); qkphase(IC(2)); pvphase(IC(2)); qkphase(IC(3)); pvphase(IC(3));
      qkphase(IC(4)); pvphase(IC(4)); qkphase(IC(5)); pvphase(IC(5)); qkphase(IC(6)); pvphase(IC(6)); qkphase(IC(7)); pvphase(IC(7));
      if (l < 16) { const u32x2 ls = {asu(lsum[0]), asu(lsum[1])}; lds_w64(lds0 + L_L + (l * 4 + w) * 8, ls); }
#pragma unroll
      for (int un = 9; un < 12; ++un)
#pragma unroll
        for (int hf = 0; hf < 2; ++hf) {
          const u32x2 pk = {cvtpk(O[un >> 1][8 * (un & 1) + 4 * hf], O[un >> 1][8 * (un & 1) + 4 * hf + 1]), cvtpk(O[un >> 1][8 * (un & 1) + 4 * hf + 2], O[un >> 1][8 * (un & 1) + 4 * hf + 3])};
          lds_w64(ring + (2 * un + hf) * 512 + l * 8, pk);
        }
      ST(11);
      BAR();
      float lt[2];
      const bool redo = row_sums(lt);
      if (__builtin_expect(attempt > 0 || !redo, 1)) break;
      float mx = -3.0e38f;
#pragma unroll 1
      for (unsigned i = 0; i < 8; ++i) {
        const unsigned off = (8 * w + i) * TILE_B;
#pragma unroll 1
        for (int k = 0; k < 12; ++k) { setm0<0>(ring + k * 1024); bdma<0>(lo[0], rs, off + k * 1024); }
        waitvm<0>();
        validate(ring, off, 12);
        f32x16 s;
#pragma unroll
        for (int e = 0; e < 16; ++e) s[e] = 0.f;
#pragma unroll
        for (int k = 0; k < 12; ++k) s = mfma32(lds_r128(((k & 1) ? kr1 : kr0) + k * 1024), Q[k], s);
#pragma unroll
        for (int e = 0; e < 16; ++e) mx = fmaxf(mx, s[e]);
      }
      lds_w32(lds0 + L_N + (r32 * 8 + w * 2 + hh) * 4, mx);
      P6(0, 0) P6(0, 6) P6(1, 0) P6(1, 6) P6(2, 0)
      BARV(0);
      { const f32x4 c = lds_r128f(lds0 + L_N + r32 * 32), d = lds_r128f(lds0 + L_N + r32 * 32 + 16);
        mref = fmaxf(fmaxf(fmaxf(c[0], c[1]), fmaxf(c[2], c[3])), fmaxf(fmaxf(d[0], d[1]), fmaxf(d[2], d[3]))); }
    }
    reduce_o();
    BAR();
    ST_OUT(ws, 0, 12);
  } else {
    ST(12);
    const unsigned ldsw = lds0 + 9216 * w;
    unsigned qo[9];
    {
      const unsigned qa = (9216 * w + l * 16) ^ ((w & 1) << 6), qc = qa ^ 32;
      qo[0] = qa; qo[1] = qa; qo[2] = l >= 16 ? qc : qa; qo[3] = qc; qo[5] = qa + 4096; qo[7] = qc + 4096;
      qo[4] = l < 32 ? qo[7] : qo[5]; qo[6] = l >= 48 ? qo[7] : qo[5]; qo[8] = qc + 8192;
    }
#define DMAQ(c) DMA9(qo, wqkv_t + (c) * SLOT, ldsw + ((c) & 3) * SLOT)
    DMAQ(0);
    const bool merger = b < 144;
    const unsigned mm = b >= 72, mf = mm ? b - 72 : b, mtau = mf / 6, mks = mf % 6;
    if (merger) {
#pragma unroll
      for (int i = 0; i < 3; ++i) {
        const unsigned pos = (3 * w + i) * 64 + l, row = (pos * 2731) >> 16, ch = pos - row * 24;
        DMA1((32 * mks + row) * 1152 + mm * 768 + ((ch ^ (row & 7)) * 16), wqkv_s, lds0 + L_MA + (3 * w + i) * 1024);
      }
      if (w < 3) {
#pragma unroll
        for (int i = 0; i < 2; ++i) {
          const unsigned pos = (2 * w + i) * 64 + l, row = (pos * 2731) >> 16, ch = pos - row * 24;
          const unsigned o0 = (16 * mtau + row) * 1152 + 384 + ((ch ^ (row & 7)) * 16), o1 = (pos >> 1) * 384 + mtau * 32 + (pos & 1) * 16;
          DMA1(o0 + mm * (o1 - o0), mm ? wo_s : wqkv_s, lds0 + L_MB + (2 * w + i) * 1024);
        }
      }
    }
    DMAQ(1); DMAQ(2);
    OPAQUE(l);
    LANE_IDS;
    unsigned oo[9];
#pragma unroll
    for (int i = 0; i < 9; ++i) {
      const unsigned row = 24 * w + (((8 * i + (l >> 3)) * 171) >> 9);
      oo[i] = ((9216 * w + l * 16) ^ (((row & 2) | ((row & 8) >> 1)) << 4)) + (i >> 2) * 4096;
    }
#define DMAO(c) DMA9(oo, wo_t + ((c) - 6) * SLOT, ldsw + ((c) & 3) * SLOT)
    ST(13);
    BARV(18);
    if (merger && w == 0) {
      f32x4 d0 = {0, 0, 0, 0}, d1 = {0, 0, 0, 0};
      const unsigned ra = 8 * tq + tp;
      u32x4 bb[6];
      if (mm) {
#pragma unroll
        for (int kk = 0; kk < 6; ++kk) { const unsigned a = lds0 + L_MB + (32 * kk + 8 * g + tq) * 32 + tp * 8; bb[kk] = lds_tr2(a, a + 128); }
      } else {
#pragma unroll
        for (int kk = 0; kk < 6; ++kk) bb[kk] = lds_r128(lds0 + L_MB + i16 * 384 + (((4 * kk + g) ^ (i16 & 7)) * 16));
      }
#pragma unroll
      for (int kk = 0; kk < 6; ++kk) {
        const u32x4 a0 = lds_r128(lds0 + L_MA + ra * 384 + (((4 * kk + g) ^ (ra & 7)) * 16));
        const u32x4 a1 = lds_r128(lds0 + L_MA + (ra + 4) * 384 + (((4 * kk + g) ^ ((ra + 4) & 7)) * 16));
        d0 = mfma16(a0, bb[kk], d0); d1 = mfma16(a1, bb[kk], d1);
      }
      const float sc = mm ? 1.0f : QSCALE;
      const u32x2 o0 = pack4(d0 * sc), o1 = pack4(d1 * sc);
      const u32x4 o = {o0[0], o0[1], o1[0], o1[1]};
      __builtin_amdgcn_raw_buffer_store_b128(o, rsWS, l * 16, WS_WM + b * 1024, SC1);
    }
    const u32x2 ep = __builtin_amdgcn_raw_buffer_load_b64(rsWS, 0, WS_EPOCH, SC1);
    BARV(10);
    DMAQ(3); DMAQ(4);
    BARV(18);
    DMAQ(5);
    BARV(18);
    DMAO(6);
    BARV(18);
    DMAO(7);
    BARV(18);
    FRAG_POS;
    BARV(9);
    u32x4 fl[2];
    fl[0] = __builtin_amdgcn_raw_buffer_load_b128(rsWS, l * 16, WS_FLAG, SC1);
    fl[1] = __builtin_amdgcn_raw_buffer_load_b128(rsWS, l * 16 + 1024, WS_FLAG, SC1);
    if (w < 2) {
      f32x4 s = lds_r128f(lds0 + L_SP + (w * 64 + l) * 16);
#pragma unroll
      for (int ww = 1; ww < 4; ++ww) s += lds_r128f(lds0 + L_SP + ((ww * 2 + w) * 64 + l) * 16);
      const float mx = xor16_max(fmaxf(fmaxf(s[0], s[1]), fmaxf(s[2], s[3])));
      f32x4 e;
#pragma unroll
      for (int k = 0; k < 4; ++k) e[k] = __builtin_amdgcn_exp2f((s[k] - mx) * QSCALE);
      const float sum = xor16_add((e[0] + e[1]) + (e[2] + e[3]));
      const float inv = ((g >> 1) == (i16 >> 3)) ? __builtin_amdgcn_rcpf(sum) : 0.f;
      lds_w64(lds0 + L_PT + w * 512 + l * 8, pack4(e * inv));
    }
    BARV(2);
    const u64 tag64 = (((u64)ep[1] << 32) | ep[0]) + 1;
    const u32x2 tag = {(unsigned)tag64, (unsigned)(tag64 >> 32)};
    const bool early = __all(tags_ok(fl[0], tag) && tags_ok(fl[1], tag));
    u32x4 wq[18], wv[18];
    auto ldw = [&](u32x4 (&W)[18], const int m, const int i0, const int i1) {
#pragma unroll
      for (int i = i0; i < i1; ++i) W[i] = __builtin_amdgcn_raw_buffer_load_b128(rsWS, l * 16 + (i & 3) * 1024, WS_WM + m * 73728 + w * 18432 + (i >> 2) * 4096, SC1);
    };
    ldw(wq, 0, 0, 10);
    BAR();
    if (__builtin_expect(!early, 0)) {
      for (int sp = 0; sp < SPIN_CAP; ++sp) {
        asm volatile("" ::: "memory");
        fl[0] = __builtin_amdgcn_raw_buffer_load_b128(rsWS, l * 16, WS_FLAG, SC1);
        fl[1] = __builtin_amdgcn_raw_buffer_load_b128(rsWS, l * 16 + 1024, WS_FLAG, SC1);
        if (__all(tags_ok(fl[0], tag) && tags_ok(fl[1], tag))) break;
      }
      asm volatile("" ::: "memory");
      ldw(wq, 0, 0, 10);
    }
    if (b == 0 && w == 0 && l == 0) __builtin_amdgcn_raw_buffer_store_b64(tag, rsWS, 0, WS_EPOCH, SC1);
    ldw(wq, 0, 10, 18);
    ldw(wv, 1, 0, 18);
    ST(14);
    BAR();
    {
      u32x4 hq[3];
      for (int sp = 0; sp < SPIN_CAP; ++sp) {
        asm volatile("" ::: "memory");
#pragma unroll
        for (int i = 0; i < 3; ++i) hq[i] = __builtin_amdgcn_raw_buffer_load_b128(rsWS, l * 16 + i * 1024, WS_H2 + t * FRAME_B + qb * TILE_B + w * 3072, SC1);
        if (!poisoned(hq)) break;
      }
#pragma unroll
      for (int i = 0; i < 3; ++i) lds_w128(lds0 + w * SLOT + L_HQ + i * 1024 + l * 16, hq[i]);
    }
    ST(15);
    BAR();
    auto gemm = [&](const u32x4 (&wf)[18], const u32x4 (&bf)[6][2], f32x4 (&d)[3][2]) {
#pragma unroll
      for (int i = 0; i < 3; ++i) { d[i][0] = f32x4{0, 0, 0, 0}; d[i][1] = f32x4{0, 0, 0, 0}; }
#pragma unroll
      for (int ks = 0; ks < 6; ++ks)
#pragma unroll
        for (int i = 0; i < 3; ++i) { d[i][0] = mfma16(wf[i * 6 + ks], bf[ks][0], d[i][0]); d[i][1] = mfma16(wf[i * 6 + ks], bf[ks][1], d[i][1]); }
    };
    {
      u32x4 bf[6][2];
#pragma unroll
      for (int ks = 0; ks < 6; ++ks)
#pragma unroll
        for (int hf = 0; hf < 2; ++hf) {
          const unsigned pe_ = ((2 * ks) / 3) * SLOT + ((2 * ks) % 3) * 1024, po_ = ((2 * ks + 1) / 3) * SLOT + ((2 * ks + 1) % 3) * 1024;
          bf[ks][hf] = lds_r128(lds0 + L_HQ + (hh ? po_ : pe_) + ((4 * hf + tq) ^ hh) * 128 + ((g & 1) ^ hf) * 64 + tp * 16);
        }
      SBAR();
      f32x4 d[3][2];
      gemm(wq, bf, d);
      float n2[2];
#pragma unroll
      for (int hf = 0; hf < 2; ++hf) {
        n2[hf] = 0.f;
#pragma unroll
        for (int i = 0; i < 3; ++i) {
          lds_w64(lds0 + L_Q + ((3 * w + i) * 64 + (g >> 1) * 32 + 16 * hf + i16) * 16 + 8 * (g & 1), pack4(d[i][hf]));
#pragma unroll
          for (int e = 0; e < 4; ++e) n2[hf] = fmaf(d[i][hf][e], d[i][hf][e], n2[hf]);
        }
      }
      auto rr = __builtin_amdgcn_permlane16_swap(asu(n2[0]), asu(n2[1]), false, false);
      lds_w32(lds0 + L_N + l * 16 + w * 4, asf(rr[0]) + asf(rr[1]));
    }
    ST(16);
    BAR();
    __builtin_amdgcn_s_sleep(32);
    for (int sp = 0; sp < SPIN_CAP; ++sp) {
      asm volatile("" ::: "memory");
      const u32x4 f = __builtin_amdgcn_raw_buffer_load_b128(rsWS, (l & 3) * 16, WS_FLAG2 + qb * 64, SC1);
      if (__all(tags_ok(f, tag))) break;
      __builtin_amdgcn_s_sleep(8);
    }
    asm volatile("" ::: "memory");
    const unsigned tok = 16 * (g & 1) + i16;
    u32x4 x1[3];
#pragma unroll
    for (int k = 0; k < 3; ++k)
      x1[k] = __builtin_amdgcn_raw_buffer_load_b128(rsWS, ((tok >> 2) ^ ((w + k) & 1)) * 128 + (hh ^ (g & 1)) * 64 + tp * 16 + k * 1024, WS_X1 + t * FRAME_B + qb * TILE_B + w * 3072, SC1);
    float lt[2];
#pragma unroll 1
    for (int attempt = 0;; ++attempt) {
      BAR();
      const bool redo = row_sums(lt);
      if (__builtin_expect(attempt > 0 || !redo, 1)) break;
      BAR();
    }
    ST(17);
    reduce_o();
    const float linv[2] = {__builtin_amdgcn_rcpf(lt[0]), __builtin_amdgcn_rcpf(lt[1])};
    BAR();
    ST(18);
    u32x4 bf[6][2];
#pragma unroll
    for (int ks = 0; ks < 6; ++ks) { bf[ks][0] = lds_r128(fr[0] + L_FI + ks * 1024); bf[ks][1] = lds_r128(fr[1] + L_FI + ks * 1024); }
    SBAR();
    f32x4 d[3][2];
    gemm(wv, bf, d);
#pragma unroll
    for (int i = 0; i < 3; ++i) {
      const u32x2 y0 = pack4(d[i][0] * linv[0]), y1 = pack4(d[i][1] * linv[1]);
      auto s0 = __builtin_amdgcn_permlane16_swap(y0[0], y1[0], false, false);
      auto s1 = __builtin_amdgcn_permlane16_swap(y0[1], y1[1], false, false);
      const u32x4 y = {s0[0], s1[0], s0[1], s1[1]};
      u32x4 o;
#pragma unroll
      for (int e = 0; e < 4; ++e) o[e] = cvtpk(bflo(x1[i][e]) + bflo(y[e]), bfhi(x1[i][e]) + bfhi(y[e]));
      *(u32x4*)(out + (size_t)(t * 1024 + qb * 32 + tok) * 384 + (2 * (3 * w + i) + hh) * 16) = o;
    }
    ST(19);
    ST_OUT(ws, 12, 20);
  }
}

extern "C" void kernel_launch(void* const* d_in, const int* in_sizes, int n_in, void* d_out, int out_size, void* d_ws, size_t ws_size, hipStream_t stream) {
  (void)hipFuncSetAttribute((const void*)k_timesformer, hipFuncAttributeMaxDynamicSharedMemorySize, LDS_BYTES);
  k_timesformer<<<256, 512, LDS_BYTES, stream>>>((const char*)d_in[0], (const char*)d_in[1], (const char*)d_in[2], (const char*)d_in[3], (const char*)d_in[4],
                                                 (char*)d_ws, (char*)d_out);
}
```

```cpp
#include <hip/hip_runtime.h>
#include <hip/hip_bf16.h>
#include <type_traits>

typedef short s16x4 __attribute__((ext_vector_type(4)));
typedef __bf16 b16x8 __attribute__((ext_vector_type(8)));
typedef __bf16 b16x2 __attribute__((ext_vector_type(2)));
typedef float f32x2 __attribute__((ext_vector_type(2)));
typedef float f32x4 __attribute__((ext_vector_type(4)));
typedef float f32x16 __attribute__((ext_vector_type(16)));
typedef unsigned u32x2 __attribute__((ext_vector_type(2)));
typedef unsigned u32x4 __attribute__((ext_vector_type(4)));
typedef unsigned long long u64;
#define LAS __attribute__((address_space(3)))
#define DI __device__ __forceinline__
#define SBAR() __builtin_amdgcn_sched_barrier(0)

constexpr int TILE_B = 12288;
constexpr int FRAME_B = 393216;
constexpr unsigned WS_H2 = 0, WS_X1 = 8 * FRAME_B, WS_WM = 16 * FRAME_B, WS_FLAG = WS_WM + 2 * 73728, WS_FLAG2 = WS_FLAG + 2048, WS_EPOCH = WS_FLAG2 + 2048, WS_END = WS_EPOCH + 256;
constexpr float QSCALE = 0.07216878364870322f * 1.4426950408889634f;
constexpr int SC1 = 16;
constexpr unsigned POISON = 0xffffffffu;
constexpr int SPIN_CAP = 1 << 16;


constexpr int PROBE = 20; constexpr size_t WS_T0 = 32u << 20;
#define ST_DECL u64 st_ = 0
#define ST(k) do { if ((k) == PROBE || (k) == 0) { SBAR(); u64 t_; asm volatile("s_memrealtime %0\n\ts_waitcnt lgkmcnt(0)" : "=s"(t_) :: "memory"); SBAR(); \
    if ((k) == PROBE) st_ = t_; \
    if ((k) == 0 && threadIdx.x == 0) __hip_atomic_store((u64*)(ws + WS_T0) + blockIdx.x, t_, __ATOMIC_RELAXED, __HIP_MEMORY_SCOPE_AGENT); } } while (0)
#define ST_OUT(ws, k0, k1) do { if (PROBE >= (k0) && PROBE <= (k1)) { u64 m_ = ~0ull; \
    for (int q_ = 0; q_ < 4; ++q_) { u64 v_ = __hip_atomic_load((u64*)(ws + WS_T0) + (threadIdx.x & 63) * 4 + q_, __ATOMIC_RELAXED, __HIP_MEMORY_SCOPE_AGENT); m_ = v_ < m_ ? v_ : m_; } \
    for (int o_ = 32; o_; o_ >>= 1) { u64 v_ = __shfl_xor(m_, o_); m_ = v_ < m_ ? v_ : m_; } \
    long long d_ = (long long)(st_ - m_); if (d_ < 0) d_ = 0; if (d_ > 5000) d_ = 5000; \
    const u64 e_ = m_ + 4000 + 4 * d_; while (__builtin_amdgcn_s_memrealtime() < e_) __builtin_amdgcn_s_sleep(1); } } while (0)


DI float asf(unsigned u) { return __builtin_bit_cast(float, u); }
DI unsigned asu(float f) { return __builtin_bit_cast(unsigned, f); }
DI unsigned cvtpk(float lo, float hi) { f32x2 v = {lo, hi}; return __builtin_bit_cast(unsigned, __builtin_convertvector(v, b16x2)); }
DI float bflo(unsigned u) { return asf(u << 16); }
DI float bfhi(unsigned u) { return asf(u & 0xffff0000u); }
DI f32x16 mfma32(u32x4 a, u32x4 b, f32x16 c) { return __builtin_amdgcn_mfma_f32_32x32x16_bf16(__builtin_bit_cast(b16x8, a), __builtin_bit_cast(b16x8, b), c, 0, 0, 0); }
DI f32x4 mfma16(u32x4 a, u32x4 b, f32x4 c) { return __builtin_amdgcn_mfma_f32_16x16x32_bf16(__builtin_bit_cast(b16x8, a), __builtin_bit_cast(b16x8, b), c, 0, 0, 0); }
DI f32x4 mfma16k16(u32x2 a, u32x2 b, f32x4 c) { return __builtin_amdgcn_mfma_f32_16x16x16bf16_1k(__builtin_bit_cast(s16x4, a), __builtin_bit_cast(s16x4, b), c, 0, 0, 0); }
DI u32x2 pack4(f32x4 v) { u32x2 r = {cvtpk(v[0], v[1]), cvtpk(v[2], v[3])}; return r; }
DI u32x4 lds_r128(unsigned a) { return *(LAS u32x4*)(size_t)a; }
DI f32x4 lds_r128f(unsigned a) { return *(LAS f32x4*)(size_t)a; }
DI u32x2 lds_r64(unsigned a) { return *(LAS u32x2*)(size_t)a; }
DI void lds_w128(unsigned a, u32x4 v) { *(LAS u32x4*)(size_t)a = v; }
DI void lds_w128f(unsigned a, f32x4 v) { *(LAS f32x4*)(size_t)a = v; }
DI void lds_w64(unsigned a, u32x2 v) { *(LAS u32x2*)(size_t)a = v; }
DI void lds_w32(unsigned a, float v) { *(LAS float*)(size_t)a = v; }
DI u32x2 lds_tr(unsigned a) { return __builtin_bit_cast(u32x2, __builtin_amdgcn_ds_read_tr16_b64_v4i16((LAS s16x4*)(size_t)a)); }
DI u32x4 lds_tr2(unsigned a0, unsigned a1) { u32x2 x = lds_tr(a0), y = lds_tr(a1); u32x4 r = {x[0], x[1], y[0], y[1]}; return r; }
#define BAR() asm volatile("s_waitcnt lgkmcnt(0)\n\ts_barrier" ::: "memory")
#define BARV(n) asm volatile("s_waitcnt vmcnt(" #n ") lgkmcnt(0)\n\ts_barrier" ::: "memory")
template <int CTRL> DI float dpp_add(float v) { return v + asf(__builtin_amdgcn_update_dpp(0u, asu(v), CTRL, 0xf, 0xf, true)); }
DI float xor16_add(float v) { auto r = __builtin_amdgcn_permlane16_swap(asu(v), asu(v), false, false); return asf(r[0]) + asf(r[1]); }
DI float xor16_max(float v) { auto r = __builtin_amdgcn_permlane16_swap(asu(v), asu(v), false, false); return fmaxf(asf(r[0]), asf(r[1])); }
DI unsigned umax3(unsigned a, unsigned b, unsigned c) { return max(max(a, b), c); }
template <int N> DI bool poisoned(const u32x4 (&v)[N]) {
  unsigned m = 0;
#pragma unroll
  for (int i = 0; i < N; ++i) { m = umax3(m, v[i][0], v[i][1]); m = umax3(m, v[i][2], v[i][3]); }
  return __any(m == POISON);
}
DI u32x4 unpoison(u32x4 v) { u32x4 r = {min(v[0], POISON - 1), min(v[1], POISON - 1), min(v[2], POISON - 1), min(v[3], POISON - 1)}; return r; }
DI unsigned fi_pos(unsigned r, unsigned c8) {
  return (((r >> 4) * 6 + (c8 >> 2)) * 64 + (c8 & 3) * 16 + ((r & 15) ^ ((c8 & 1) | (((r >> 3) & 1) << 1) | ((r >> 4) << 2)))) * 16;
}
DI void layernorm24(const float (&v)[24], u32x4 (&o)[3]) {
  float s0 = 0.f, s1 = 0.f;
#pragma unroll
  for (int i = 0; i < 24; i += 2) { s0 += v[i]; s1 += v[i + 1]; }
  float s = s0 + s1;
  s = dpp_add<0x124>(s); s = dpp_add<0x128>(s); s = xor16_add(s);
  const float mu = s * (1.0f / 192.0f);
  float d[24], q0 = 0.f, q1 = 0.f;
#pragma unroll
  for (int i = 0; i < 24; i += 2) { d[i] = v[i] - mu; d[i + 1] = v[i + 1] - mu; q0 = fmaf(d[i], d[i], q0); q1 = fmaf(d[i + 1], d[i + 1], q1); }
  float q = q0 + q1;
  q = dpp_add<0x124>(q); q = dpp_add<0x128>(q); q = xor16_add(q);
  const float rs = __builtin_amdgcn_rsqf(q * (1.0f / 192.0f) + 1e-5f);
#pragma unroll
  for (int i = 0; i < 3; ++i)
#pragma unroll
    for (int e = 0; e < 4; ++e) o[i][e] = cvtpk(d[8 * i + 2 * e] * rs, d[8 * i + 2 * e + 1] * rs);
}

constexpr unsigned SLOT = 36864, L_H = 4 * SLOT, L_PT = L_H + TILE_B, LDS_BYTES = 163840;
constexpr unsigned L_VI = 0, L_SP = TILE_B, L_YI = SLOT, L_MA = 3 * SLOT, L_MB = L_MA + TILE_B;
constexpr unsigned L_HQ = 30720, L_Q = L_H, L_FI = L_H, L_N = L_PT + 1024, L_L = L_N + 1024;

#define DMA4(v0, v1, v2, v3, sbase, ldsa)                                                                  \
  asm volatile("s_mov_b32 m0, %5\n\ts_nop 0\n\t"                                                            \
               "global_load_lds_dwordx4 %0, %4\n\tglobal_load_lds_dwordx4 %1, %4 offset:1024\n\t"           \
               "global_load_lds_dwordx4 %2, %4 offset:2048\n\tglobal_load_lds_dwordx4 %3, %4 offset:3072"   \
               :: "v"(v0), "v"(v1), "v"(v2), "v"(v3), "s"(sbase), "s"(ldsa) : "memory")
#define DMA1(v0, sbase, ldsa) \
  asm volatile("s_mov_b32 m0, %2\n\ts_nop 0\n\tglobal_load_lds_dwordx4 %0, %1" :: "v"(v0), "s"(sbase), "s"(ldsa) : "memory")
#define DMA9(o, sbase, ldsa) do { DMA4(o[0], o[1], o[2], o[3], sbase, ldsa); DMA4(o[4], o[5], o[6], o[7], sbase, (ldsa) + 4096); DMA1(o[8], sbase, (ldsa) + 8192); } while (0)

template <int OFF> DI void bdma(unsigned voff, u32x4 rs, unsigned soff) { asm volatile("buffer_load_dwordx4 %0, %1, %2 offen offset:%3 sc1 lds" :: "v"(voff), "s"(rs), "s"(soff), "i"(OFF) : "memory"); }
template <int OFF> DI void setm0(unsigned base) { asm volatile("s_add_u32 m0, %0, %1\n\ts_nop 0" :: "s"(base), "i"(OFF) : "memory", "scc"); }
template <int N> DI void waitvm() { asm volatile("s_waitcnt vmcnt(%0)" :: "i"(N) : "memory"); }
#define IC(x) std::integral_constant<int, (x)>{}
#define OPAQUE(v) asm volatile("" : "+v"(v))
DI bool tags_ok(u32x4 f, u32x2 tag) { return f[0] == tag[0] && f[1] == tag[1] && f[2] == tag[0] && f[3] == tag[1]; }

__global__ __launch_bounds__(512, 2) void k_timesformer(const char* __restrict__ x, const char* __restrict__ wqkv_t, const char* __restrict__ wo_t,
                                                        const char* __restrict__ wqkv_s, const char* __restrict__ wo_s, char* __restrict__ ws, char* __restrict__ out) {
  extern __shared__ __attribute__((aligned(16))) char smem[];
  const unsigned lds0 = (unsigned)(size_t)(LAS char*)smem;
  const unsigned b = blockIdx.x, w8 = __builtin_amdgcn_readfirstlane(threadIdx.x >> 6), w = w8 & 3;
  unsigned l = threadIdx.x & 63;
  const unsigned t = b & 7, qb = b >> 3;
  const auto rsWS = __builtin_amdgcn_make_buffer_rsrc((void*)ws, 0, WS_END, 0x00020000);
  ST_DECL;
#define LANE_IDS const unsigned i16 = l & 15, g = l >> 4, r32 = l & 31, hh = l >> 5, tq = i16 >> 2, tp = l & 3
#define FRAG_POS unsigned fr[2], dpos[3][2];                                                                        \
  _Pragma("unroll") for (int hf = 0; hf < 2; ++hf) { fr[hf] = lds0 + fi_pos(16 * hf + i16, g);                     \
    _Pragma("unroll") for (int i = 0; i < 3; ++i) dpos[i][hf] = lds0 + fi_pos(16 * hf + i16, 2 * (3 * w + i) + (g >> 1)) + 8 * (g & 1); }
  auto reduce_o = [&]() {
    const unsigned r32 = l & 31, hh = l >> 5;
#pragma unroll
    for (int i = 0; i < 3; ++i) {
      const unsigned hu = 3 * w8 + i;
      u32x2 p[4];
#pragma unroll
      for (int ww = 0; ww < 4; ++ww) p[ww] = lds_r64(lds0 + ww * SLOT + hu * 512 + l * 8);
      f32x4 a;
      a[0] = (bflo(p[0][0]) + bflo(p[1][0])) + (bflo(p[2][0]) + bflo(p[3][0]));
      a[1] = (bfhi(p[0][0]) + bfhi(p[1][0])) + (bfhi(p[2][0]) + bfhi(p[3][0]));
      a[2] = (bflo(p[0][1]) + bflo(p[1][1])) + (bflo(p[2][1]) + bflo(p[3][1]));
      a[3] = (bfhi(p[0][1]) + bfhi(p[1][1])) + (bfhi(p[2][1]) + bfhi(p[3][1]));
      lds_w64(lds0 + L_FI + fi_pos(r32, hu) + 8 * hh, pack4(a));
    }
  };
  auto row_sums = [&](float (&lt)[2]) {
    const f32x4 a = lds_r128f(lds0 + L_L + (l & 15) * 32), c = lds_r128f(lds0 + L_L + (l & 15) * 32 + 16);
    lt[0] = (a[0] + a[2]) + (c[0] + c[2]);
    lt[1] = (a[1] + a[3]) + (c[1] + c[3]);
    return (bool)__any(!(lt[0] >= 8.7e-19f && lt[1] >= 8.7e-19f));
  };

  if (w8 < 4) {
    ST(0);
    const unsigned nl = l & 3, j = (l >> 2) & 7, tt = 2 * w + (l >> 5), r = nl * 8 + tt;
    const auto rsX = __builtin_amdgcn_make_buffer_rsrc((void*)x, 0, 8 * FRAME_B, 0x00020000);
    u32x4 xr[3];
#pragma unroll
    for (int i = 0; i < 3; ++i) xr[i] = __builtin_amdgcn_raw_buffer_load_b128(rsX, (tt * 1024 + nl) * 384 + j * 16 + i * 128, b * 1536, 0);
    const u32x2 ep = __builtin_amdgcn_raw_buffer_load_b64(rsWS, 0, WS_EPOCH, SC1);
    SBAR();
    const unsigned so = tt * FRAME_B + (j >> 1) * 1024 + ((t ^ ((j >> 1) & 1)) * 128) + (((j & 1) ^ (t >> 2)) * 64) + nl * 16, sso = qb * TILE_B;
    const u32x4 pz = {POISON, POISON, POISON, POISON};
    __builtin_amdgcn_raw_buffer_store_b128(pz, rsWS, so, WS_H2 + sso, SC1);
    OPAQUE(l);
    LANE_IDS;
    FRAG_POS;
    const unsigned phi = ((tq >> 1) & 1) | ((g & 1) << 1);
    unsigned tcol[3], rpos[3];
#pragma unroll
    for (int i = 0; i < 3; ++i) { tcol[i] = (((3 * w + i) ^ phi) * 2 + (tp >> 1)) * 16 + 8 * (tp & 1); rpos[i] = lds0 + fi_pos(r, j + 8 * i); }
    SBAR();
    float xv[24];
    {
      u32x4 h[3];
#pragma unroll
      for (int i = 0; i < 3; ++i)
#pragma unroll
        for (int e = 0; e < 4; ++e) { xv[8 * i + 2 * e] = bflo(xr[i][e]); xv[8 * i + 2 * e + 1] = bfhi(xr[i][e]); }
      ST(1);
      layernorm24(xv, h);
#pragma unroll
      for (int i = 0; i < 3; ++i) lds_w128(rpos[i] + L_H, h[i]);
    }
    __builtin_amdgcn_raw_buffer_store_b128(pz, rsWS, so, WS_H2 + sso + 4096, SC1);
    ST(2);
    const u64 tag64 = (((u64)ep[1] << 32) | ep[0]) + 1;
    const u32x2 tag = {(unsigned)tag64, (unsigned)(tag64 >> 32)};
    f32x4 acc[3][3][2];
#pragma unroll
    for (int m = 0; m < 3; ++m)
#pragma unroll
      for (int i = 0; i < 3; ++i) { acc[m][i][0] = f32x4{0, 0, 0, 0}; acc[m][i][1] = f32x4{0, 0, 0, 0}; }
#pragma unroll
    for (int ks = 0; ks < 6; ++ks) {
      if (ks == 3) BARV(0); else BAR();
      if (ks == 0) ST(3);
      if (ks == 5) ST(4);
      if (ks == 0) __builtin_amdgcn_raw_buffer_store_b128(pz, rsWS, so, WS_H2 + sso + 8192, SC1);
      if (ks == 3 && w == 0 && l == 0) __builtin_amdgcn_raw_buffer_store_b64(tag, rsWS, 0, WS_FLAG + b * 8, SC1);
      const u32x4 h0 = lds_r128(fr[0] + L_H + ks * 1024), h1 = lds_r128(fr[1] + L_H + ks * 1024);
      const unsigned base = lds0 + (ks & 3) * SLOT + (8 * g + tq) * 1152;
      u32x4 a[9];
#pragma unroll
      for (int m = 0; m < 3; ++m)
#pragma unroll
        for (int i = 0; i < 3; ++i) a[m * 3 + i] = lds_tr2(base + tcol[i] + m * 384, base + tcol[i] + m * 384 + 4608);
      SBAR();
#pragma unroll
      for (int m = 0; m < 3; ++m)
#pragma unroll
        for (int i = 0; i < 3; ++i) {
          acc[m][i][0] = mfma16(a[m * 3 + i], h0, acc[m][i][0]);
          acc[m][i][1] = mfma16(a[m * 3 + i], h1, acc[m][i][1]);
        }
      if (ks < 5) SBAR();
    }
#pragma unroll
    for (int hf = 0; hf < 2; ++hf) {
      f32x4 s = {0, 0, 0, 0};
#pragma unroll
      for (int i = 0; i < 3; ++i) s = mfma16k16(pack4(acc[1][i][hf]), pack4(acc[0][i][hf]), s);
#pragma unroll
      for (int i = 0; i < 3; ++i) lds_w64(dpos[i][hf] + L_VI, pack4(acc[2][i][hf]));
      lds_w128f(lds0 + L_SP + ((w * 2 + hf) * 64 + l) * 16, s);
    }
    ST(5);
    BAR();
    f32x4 vp[3][2];
#pragma unroll
    for (int i = 0; i < 3; ++i) { vp[i][0] = f32x4{0, 0, 0, 0}; vp[i][1] = f32x4{0, 0, 0, 0}; }
    u32x4 vf[6][2];
    u32x2 pt[2];
#pragma unroll
    for (int c = 0; c < 2; ++c) {
      if (c == 1) { BAR(); ST(6); }
      u32x4 bw[9];
#pragma unroll
      for (int k = 0; k < 3; ++k) {
        if (c == 0) { vf[k][0] = lds_r128(fr[0] + L_VI + k * 1024); vf[k][1] = lds_r128(fr[1] + L_VI + k * 1024); }
#pragma unroll
        for (int i = 0; i < 3; ++i) {
          const unsigned base = lds0 + (2 + c) * SLOT + (32 * k + 8 * g + tq) * 384 + tcol[i];
          bw[k * 3 + i] = lds_tr2(base, base + 1536);
        }
      }
      if (c == 0) {
#pragma unroll
        for (int k = 3; k < 6; ++k) { vf[k][0] = lds_r128(fr[0] + L_VI + k * 1024); vf[k][1] = lds_r128(fr[1] + L_VI + k * 1024); }
      } else { pt[0] = lds_r64(lds0 + L_PT + l * 8); pt[1] = lds_r64(lds0 + L_PT + 512 + l * 8); }
      SBAR();
#pragma unroll
      for (int k = 0; k < 3; ++k)
#pragma unroll
        for (int i = 0; i < 3; ++i) {
          vp[i][0] = mfma16(vf[3 * c + k][0], bw[k * 3 + i], vp[i][0]);
          vp[i][1] = mfma16(vf[3 * c + k][1], bw[k * 3 + i], vp[i][1]);
        }
      if (c == 0) SBAR();
    }
#pragma unroll
    for (int i = 0; i < 3; ++i)
#pragma unroll
      for (int hf = 0; hf < 2; ++hf) lds_w64(dpos[i][hf] + L_YI, pack4(mfma16k16(pack4(vp[i][hf]), pt[hf], f32x4{0, 0, 0, 0})));
    BAR();
    ST(7);
    {
      u32x4 x1[3], h[3];
#pragma unroll
      for (int i = 0; i < 3; ++i) {
        const u32x4 y = lds_r128(rpos[i] + L_YI);
#pragma unroll
        for (int e = 0; e < 4; ++e) { xv[8 * i + 2 * e] += bflo(y[e]); xv[8 * i + 2 * e + 1] += bfhi(y[e]); }
      }
      layernorm24(xv, h);
#pragma unroll
      for (int i = 0; i < 3; ++i) __builtin_amdgcn_raw_buffer_store_b128(unpoison(h[i]), rsWS, so, WS_H2 + sso + i * 4096, SC1);
      SBAR();
#pragma unroll
      for (int i = 0; i < 3; ++i) {
#pragma unroll
        for (int e = 0; e < 4; ++e) x1[i][e] = cvtpk(xv[8 * i + 2 * e], xv[8 * i + 2 * e + 1]);
        __builtin_amdgcn_raw_buffer_store_b128(x1[i], rsWS, so, WS_X1 + sso + i * 4096, SC1);
      }
    }
    ST(8);
    const unsigned ring = lds0 + w * SLOT;
    const unsigned kr0 = ring + (r32 >> 2) * 128 + (hh ^ (r32 >> 4)) * 64 + (r32 & 3) * 16, kr1 = kr0 ^ 128;
    const unsigned vr0 = ring + (g & 1) * 1024 + (hh ^ (g & 1)) * 128 + (tp >> 1) * 64 + tq * 16 + 8 * (tp & 1), vr1 = (vr0 ^ 64) + 512;
    const unsigned lo[3] = {l * 16, l * 16 + 4096, l * 16 + 8192};
    const u64 hbu = (u64)(ws + WS_H2 + (size_t)t * FRAME_B);
    const u32x4 rs = {(unsigned)hbu, (unsigned)(hbu >> 32) & 0xffffu, (unsigned)FRAME_B, 0x00020000u};
    unsigned toff[8];
#pragma unroll
    for (int i = 0; i < 8; ++i) toff[i] = (8 * w + ((i + qb) & 7)) * TILE_B;
    u32x4 sel;
    { const unsigned one = (tp == (g & 1)) ? 0x3f803f80u : 0u; sel = u32x4{one, one, one, one}; }
    auto piece = [&](auto Tc, auto Pc) {
      constexpr int T = decltype(Tc)::value, p = decltype(Pc)::value;
      if constexpr (T < 8) {
        if constexpr (p % 4 == 0) setm0<(T % 3) * TILE_B + p * 1024>(ring);
        bdma<(p % 4) * 1024>(lo[p / 4], rs, toff[T]);
      }
    };
#define P6(T, a) piece(IC(T), IC(a)); piece(IC(T), IC(a + 1)); piece(IC(T), IC(a + 2)); piece(IC(T), IC(a + 3)); piece(IC(T), IC(a + 4)); piece(IC(T), IC(a + 5));
    auto validate = [&](unsigned dst, unsigned off, const int n) {
      for (int sp = 0; sp < SPIN_CAP; ++sp) {
        unsigned m = 0;
#pragma unroll
        for (int k = 0; k < n; ++k) { const u32x4 v = lds_r128(dst + k * 1024 + lo[0]); m = umax3(m, v[0], v[1]); m = umax3(m, v[2], v[3]); }
        if (__builtin_expect(!__any(m == POISON), 1)) break;
#pragma unroll 1
        for (int k = 0; k < n; ++k) { setm0<0>(dst + k * 1024); bdma<0>(lo[0], rs, off + k * 1024); }
        waitvm<0>();
      }
    };
    BARV(3);
    ST(9);
    P6(0, 0) P6(0, 6)
    BARV(12);
    if (w == 0 && l == 0) __builtin_amdgcn_raw_buffer_store_b64(tag, rsWS, 0, WS_FLAG2 + b * 8, SC1);
    P6(1, 0) P6(1, 6) P6(2, 0)
    waitvm<18>();
    validate(ring, toff[0], 12);
    waitvm<6>();
    validate(ring + TILE_B, toff[1], 12);
    waitvm<0>();
    validate(ring + 2 * TILE_B, toff[2], 6);
    ST(10);
    BAR();
    u32x4 Q[12];
#pragma unroll
    for (int s = 0; s < 12; ++s) Q[s] = lds_r128(lds0 + L_Q + s * 1024 + lo[0]);
    float mref;
    {
      const f32x4 s = lds_r128f(lds0 + L_N + r32 * 16) + lds_r128f(lds0 + L_N + 512 + r32 * 16);
      mref = __builtin_amdgcn_sqrtf(((s[0] + s[1]) + (s[2] + s[3])) * 192.0f) * 1.01f;
    }
    constexpr int KW = 4, VW = 4;
#pragma unroll 1
    for (int attempt = 0;; ++attempt) {
      f32x16 S[2], O[6], negm;
      u32x4 K[12], V[12], P[2][2];
      f32x4 lsum = {0, 0, 0, 0};
      float pe[2][2];
#pragma unroll
      for (int i = 0; i < 16; ++i) negm[i] = -mref;
#define RK(k, T) K[k] = lds_r128((((k) & 1) ? kr1 : kr0) + ((T) % 3) * TILE_B + (k) * 1024);
#define RV(u, T) { const unsigned a_ = (((u) & 1) ? vr1 : vr0) + ((T) % 3) * TILE_B + ((u) >> 1) * 2048; V[u] = lds_tr2(a_, a_ + 256); }
      auto sm = [&](auto Tc, auto Hc, auto Gc) {
        constexpr int T = decltype(Tc)::value, H = decltype(Hc)::value, G = decltype(Gc)::value;
        if constexpr (G < 8) pe[(G >> 1) & 1][G & 1] = __builtin_amdgcn_exp2f(S[T & 1][8 * H + G]);
        if constexpr (G >= 2 && G <= 8 && G % 2 == 0) { constexpr int q = G / 2 - 1; P[T & 1][H][q] = cvtpk(pe[q & 1][0], pe[q & 1][1]); }
        if constexpr (G == 10) lsum = mfma16(sel, P[T & 1][H], lsum);
      };
      auto qkphase = [&](auto Ic) {
        constexpr int I = decltype(Ic)::value;
        auto step = [&](auto Kc) {
          constexpr int k = decltype(Kc)::value;
          if constexpr (I + 1 < 8) { S[(I + 1) & 1] = mfma32(K[k], Q[k], k == 0 ? negm : S[(I + 1) & 1]); if constexpr (k + KW < 12) RK(k + KW, I + 1) }
          if constexpr (I >= 0) { if constexpr (k & 1) piece(IC(I + 2), IC(6 + k / 2)); if constexpr (k < 11) sm(IC(I), IC(1), IC(k)); }
          if constexpr (I >= 0 && I < 7 && k >= 12 - VW) RV(k - (12 - VW), I)
          if constexpr (I == 7 && k < VW) RV(k, I)
          SBAR();
        };
        step(IC(0)); step(IC(1)); step(IC(2)); step(IC(3)); step(IC(4)); step(IC(5)); step(IC(6)); step(IC(7)); step(IC(8)); step(IC(9)); step(IC(10)); step(IC(11));
      };
      auto pvphase = [&](auto Ic) {
        constexpr int I = decltype(Ic)::value;
        auto step = [&](auto Uc) {
          constexpr int u = decltype(Uc)::value;
          if constexpr (I >= 0) {
            f32x16 z;
#pragma unroll
            for (int e = 0; e < 16; ++e) z[e] = 0.f;
            O[u >> 1] = mfma32(V[u], P[I & 1][u & 1], (I == 0 && !(u & 1)) ? z : O[u >> 1]);
            if constexpr (u & 1) piece(IC(I + 3), IC(u / 2));
            if constexpr (u + VW < 12) RV(u + VW, I)
          }
          if constexpr (I + 1 < 8 && u >= 1) sm(IC(I + 1), IC(0), IC(u - 1));
          if constexpr (I + 2 < 8 && I >= 0) {
            if constexpr (u == 12 - KW) waitvm<(I + 3 < 8 ? 6 + (12 - KW) / 2 : 6)>();
            if constexpr (u >= 12 - KW) RK(u - (12 - KW), I + 2)
            if constexpr (u == 11) waitvm<(I + 3 < 8 ? 6 : 0)>();
          }
          if constexpr (I < 0 && u < KW) RK(u, 1)
          if constexpr (I == 7 && u >= 3) {
            constexpr int un = u - 3;
#pragma unroll
            for (int hf = 0; hf < 2; ++hf) {
              const u32x2 pk = {cvtpk(O[un >> 1][8 * (un & 1) + 4 * hf], O[un >> 1][8 * (un & 1) + 4 * hf + 1]), cvtpk(O[un >> 1][8 * (un & 1) + 4 * hf + 2], O[un >> 1][8 * (un & 1) + 4 * hf + 3])};
              lds_w64(ring + (2 * un + hf) * 512 + l * 8, pk);
            }
          }
          SBAR();
        };
        step(IC(0)); step(IC(1)); step(IC(2)); step(IC(3)); step(IC(4)); step(IC(5)); step(IC(6)); step(IC(7)); step(IC(8)); step(IC(9)); step(IC(10)); step(IC(11));
      };
      setm0<2 * TILE_B + 4096>(ring);
      RK(0, 0) RK(1, 0) RK(2, 0) RK(3, 0)
      qkphase(IC(-1)); pvphase(IC(-1));
      qkphase(IC(0)); pvphase(IC(0)); qkphase(IC(1)); pvphase(IC(1)); qkphase(IC(2)); pvphase(IC(2)); qkphase(IC(3)); pvphase(IC(3));
      qkphase(IC(4)); pvphase(IC(4)); qkphase(IC(5)); pvphase(IC(5)); qkphase(IC(6)); pvphase(IC(6)); qkphase(IC(7)); pvphase(IC(7));
      if (l < 16) { const u32x2 ls = {asu(lsum[0]), asu(lsum[1])}; lds_w64(lds0 + L_L + (l * 4 + w) * 8, ls); }
#pragma unroll
      for (int un = 9; un < 12; ++un)
#pragma unroll
        for (int hf = 0; hf < 2; ++hf) {
          const u32x2 pk = {cvtpk(O[un >> 1][8 * (un & 1) + 4 * hf], O[un >> 1][8 * (un & 1) + 4 * hf + 1]), cvtpk(O[un >> 1][8 * (un & 1) + 4 * hf + 2], O[un >> 1][8 * (un & 1) + 4 * hf + 3])};
          lds_w64(ring + (2 * un + hf) * 512 + l * 8, pk);
        }
      ST(11);
      BAR();
      float lt[2];
      const bool redo = row_sums(lt);
      if (__builtin_expect(attempt > 0 || !redo, 1)) break;
      float mx = -3.0e38f;
#pragma unroll 1
      for (unsigned i = 0; i < 8; ++i) {
        const unsigned off = (8 * w + i) * TILE_B;
#pragma unroll 1
        for (int k = 0; k < 12; ++k) { setm0<0>(ring + k * 1024); bdma<0>(lo[0], rs, off + k * 1024); }
        waitvm<0>();
        validate(ring, off, 12);
        f32x16 s;
#pragma unroll
        for (int e = 0; e < 16; ++e) s[e] = 0.f;
#pragma unroll
        for (int k = 0; k < 12; ++k) s = mfma32(lds_r128(((k & 1) ? kr1 : kr0) + k * 1024), Q[k], s);
#pragma unroll
        for (int e = 0; e < 16; ++e) mx = fmaxf(mx, s[e]);
      }
      lds_w32(lds0 + L_N + (r32 * 8 + w * 2 + hh) * 4, mx);
      P6(0, 0) P6(0, 6) P6(1, 0) P6(1, 6) P6(2, 0)
      BARV(0);
      { const f32x4 c = lds_r128f(lds0 + L_N + r32 * 32), d = lds_r128f(lds0 + L_N + r32 * 32 + 16);
        mref = fmaxf(fmaxf(fmaxf(c[0], c[1]), fmaxf(c[2], c[3])), fmaxf(fmaxf(d[0], d[1]), fmaxf(d[2], d[3]))); }
    }
    reduce_o();
    BAR();
    ST_OUT(ws, 0, 12);
  } else {
    ST(12);
    const unsigned ldsw = lds0 + 9216 * w;
    unsigned qo[9];
    {
      const unsigned qa = (9216 * w + l * 16) ^ ((w & 1) << 6), qc = qa ^ 32;
      qo[0] = qa; qo[1] = qa; qo[2] = l >= 16 ? qc : qa; qo[3] = qc; qo[5] = qa + 4096; qo[7] = qc + 4096;
      qo[4] = l < 32 ? qo[7] : qo[5]; qo[6] = l >= 48 ? qo[7] : qo[5]; qo[8] = qc + 8192;
    }
#define DMAQ(c) DMA9(qo, wqkv_t + (c) * SLOT, ldsw + ((c) & 3) * SLOT)
    DMAQ(0);
    const bool merger = b < 144;
    const unsigned mm = b >= 72, mf = mm ? b - 72 : b, mtau = mf / 6, mks = mf % 6;
    if (merger) {
#pragma unroll
      for (int i = 0; i < 3; ++i) {
        const unsigned pos = (3 * w + i) * 64 + l, row = (pos * 2731) >> 16, ch = pos - row * 24;
        DMA1((32 * mks + row) * 1152 + mm * 768 + ((ch ^ (row & 7)) * 16), wqkv_s, lds0 + L_MA + (3 * w + i) * 1024);
      }
      if (w < 3) {
#pragma unroll
        for (int i = 0; i < 2; ++i) {
          const unsigned pos = (2 * w + i) * 64 + l, row = (pos * 2731) >> 16, ch = pos - row * 24;
          const unsigned o0 = (16 * mtau + row) * 1152 + 384 + ((ch ^ (row & 7)) * 16), o1 = (pos >> 1) * 384 + mtau * 32 + (pos & 1) * 16;
          DMA1(o0 + mm * (o1 - o0), mm ? wo_s : wqkv_s, lds0 + L_MB + (2 * w + i) * 1024);
        }
      }
    }
    DMAQ(1); DMAQ(2);
    OPAQUE(l);
    LANE_IDS;
    unsigned oo[9];
#pragma unroll
    for (int i = 0; i < 9; ++i) {
      const unsigned row = 24 * w + (((8 * i + (l >> 3)) * 171) >> 9);
      oo[i] = ((9216 * w + l * 16) ^ (((row & 2) | ((row & 8) >> 1)) << 4)) + (i >> 2) * 4096;
    }
#define DMAO(c) DMA9(oo, wo_t + ((c) - 6) * SLOT, ldsw + ((c) & 3) * SLOT)
    ST(13);
    BARV(18);
    if (merger && w == 0) {
      f32x4 d0 = {0, 0, 0, 0}, d1 = {0, 0, 0, 0};
      const unsigned ra = 8 * tq + tp;
      u32x4 bb[6];
      if (mm) {
#pragma unroll
        for (int kk = 0; kk < 6; ++kk) { const unsigned a = lds0 + L_MB + (32 * kk + 8 * g + tq) * 32 + tp * 8; bb[kk] = lds_tr2(a, a + 128); }
      } else {
#pragma unroll
        for (int kk = 0; kk < 6; ++kk) bb[kk] = lds_r128(lds0 + L_MB + i16 * 384 + (((4 * kk + g) ^ (i16 & 7)) * 16));
      }
#pragma unroll
      for (int kk = 0; kk < 6; ++kk) {
        const u32x4 a0 = lds_r128(lds0 + L_MA + ra * 384 + (((4 * kk + g) ^ (ra & 7)) * 16));
        const u32x4 a1 = lds_r128(lds0 + L_MA + (ra + 4) * 384 + (((4 * kk + g) ^ ((ra + 4) & 7)) * 16));
        d0 = mfma16(a0, bb[kk], d0); d1 = mfma16(a1, bb[kk], d1);
      }
      const float sc = mm ? 1.0f : QSCALE;
      const u32x2 o0 = pack4(d0 * sc), o1 = pack4(d1 * sc);
      const u32x4 o = {o0[0], o0[1], o1[0], o1[1]};
      __builtin_amdgcn_raw_buffer_store_b128(o, rsWS, l * 16, WS_WM + b * 1024, SC1);
    }
    const u32x2 ep = __builtin_amdgcn_raw_buffer_load_b64(rsWS, 0, WS_EPOCH, SC1);
    BARV(10);
    DMAQ(3); DMAQ(4);
    BARV(18);
    DMAQ(5);
    BARV(18);
    DMAO(6);
    BARV(18);
    DMAO(7);
    BARV(18);
    FRAG_POS;
    BARV(9);
    u32x4 fl[2];
    fl[0] = __builtin_amdgcn_raw_buffer_load_b128(rsWS, l * 16, WS_FLAG, SC1);
    fl[1] = __builtin_amdgcn_raw_buffer_load_b128(rsWS, l * 16 + 1024, WS_FLAG, SC1);
    if (w < 2) {
      f32x4 s = lds_r128f(lds0 + L_SP + (w * 64 + l) * 16);
#pragma unroll
      for (int ww = 1; ww < 4; ++ww) s += lds_r128f(lds0 + L_SP + ((ww * 2 + w) * 64 + l) * 16);
      const float mx = xor16_max(fmaxf(fmaxf(s[0], s[1]), fmaxf(s[2], s[3])));
      f32x4 e;
#pragma unroll
      for (int k = 0; k < 4; ++k) e[k] = __builtin_amdgcn_exp2f((s[k] - mx) * QSCALE);
      const float sum = xor16_add((e[0] + e[1]) + (e[2] + e[3]));
      const float inv = ((g >> 1) == (i16 >> 3)) ? __builtin_amdgcn_rcpf(sum) : 0.f;
      lds_w64(lds0 + L_PT + w * 512 + l * 8, pack4(e * inv));
    }
    BARV(2);
    const u64 tag64 = (((u64)ep[1] << 32) | ep[0]) + 1;
    const u32x2 tag = {(unsigned)tag64, (unsigned)(tag64 >> 32)};
    const bool early = __all(tags_ok(fl[0], tag) && tags_ok(fl[1], tag));
    u32x4 wq[18], wv[18];
    auto ldw = [&](u32x4 (&W)[18], const int m, const int i0, const int i1) {
#pragma unroll
      for (int i = i0; i < i1; ++i) W[i] = __builtin_amdgcn_raw_buffer_load_b128(rsWS, l * 16 + (i & 3) * 1024, WS_WM + m * 73728 + w * 18432 + (i >> 2) * 4096, SC1);
    };
    ldw(wq, 0, 0, 10);
    BAR();
    if (__builtin_expect(!early, 0)) {
      for (int sp = 0; sp < SPIN_CAP; ++sp) {
        asm volatile("" ::: "memory");
        fl[0] = __builtin_amdgcn_raw_buffer_load_b128(rsWS, l * 16, WS_FLAG, SC1);
        fl[1] = __builtin_amdgcn_raw_buffer_load_b128(rsWS, l * 16 + 1024, WS_FLAG, SC1);
        if (__all(tags_ok(fl[0], tag) && tags_ok(fl[1], tag))) break;
      }
      asm volatile("" ::: "memory");
      ldw(wq, 0, 0, 10);
    }
    if (b == 0 && w == 0 && l == 0) __builtin_amdgcn_raw_buffer_store_b64(tag, rsWS, 0, WS_EPOCH, SC1);
    ldw(wq, 0, 10, 18);
    ldw(wv, 1, 0, 18);
    ST(14);
    BAR();
    {
      u32x4 hq[3];
      for (int sp = 0; sp < SPIN_CAP; ++sp) {
        asm volatile("" ::: "memory");
#pragma unroll
        for (int i = 0; i < 3; ++i) hq[i] = __builtin_amdgcn_raw_buffer_load_b128(rsWS, l * 16 + i * 1024, WS_H2 + t * FRAME_B + qb * TILE_B + w * 3072, SC1);
        if (!poisoned(hq)) break;
      }
#pragma unroll
      for (int i = 0; i < 3; ++i) lds_w128(lds0 + w * SLOT + L_HQ + i * 1024 + l * 16, hq[i]);
    }
    ST(15);
    BAR();
    auto gemm = [&](const u32x4 (&wf)[18], const u32x4 (&bf)[6][2], f32x4 (&d)[3][2]) {
#pragma unroll
      for (int i = 0; i < 3; ++i) { d[i][0] = f32x4{0, 0, 0, 0}; d[i][1] = f32x4{0, 0, 0, 0}; }
#pragma unroll
      for (int ks = 0; ks < 6; ++ks)
#pragma unroll
        for (int i = 0; i < 3; ++i) { d[i][0] = mfma16(wf[i * 6 + ks], bf[ks][0], d[i][0]); d[i][1] = mfma16(wf[i * 6 + ks], bf[ks][1], d[i][1]); }
    };
    {
      u32x4 bf[6][2];
#pragma unroll
      for (int ks = 0; ks < 6; ++ks)
#pragma unroll
        for (int hf = 0; hf < 2; ++hf) {
          const unsigned pe_ = ((2 * ks) / 3) * SLOT + ((2 * ks) % 3) * 1024, po_ = ((2 * ks + 1) / 3) * SLOT + ((2 * ks + 1) % 3) * 1024;
          bf[ks][hf] = lds_r128(lds0 + L_HQ + (hh ? po_ : pe_) + ((4 * hf + tq) ^ hh) * 128 + ((g & 1) ^ hf) * 64 + tp * 16);
        }
      SBAR();
      f32x4 d[3][2];
      gemm(wq, bf, d);
      float n2[2];
#pragma unroll
      for (int hf = 0; hf < 2; ++hf) {
        n2[hf] = 0.f;
#pragma unroll
        for (int i = 0; i < 3; ++i) {
          lds_w64(lds0 + L_Q + ((3 * w + i) * 64 + (g >> 1) * 32 + 16 * hf + i16) * 16 + 8 * (g & 1), pack4(d[i][hf]));
#pragma unroll
          for (int e = 0; e < 4; ++e) n2[hf] = fmaf(d[i][hf][e], d[i][hf][e], n2[hf]);
        }
      }
      auto rr = __builtin_amdgcn_permlane16_swap(asu(n2[0]), asu(n2[1]), false, false);
      lds_w32(lds0 + L_N + l * 16 + w * 4, asf(rr[0]) + asf(rr[1]));
    }
    ST(16);
    BAR();
    __builtin_amdgcn_s_sleep(32);
    for (int sp = 0; sp < SPIN_CAP; ++sp) {
      asm volatile("" ::: "memory");
      const u32x4 f = __builtin_amdgcn_raw_buffer_load_b128(rsWS, (l & 3) * 16, WS_FLAG2 + qb * 64, SC1);
      if (__all(tags_ok(f, tag))) break;
      __builtin_amdgcn_s_sleep(8);
    }
    asm volatile("" ::: "memory");
    const unsigned tok = 16 * (g & 1) + i16;
    u32x4 x1[3];
#pragma unroll
    for (int k = 0; k < 3; ++k)
      x1[k] = __builtin_amdgcn_raw_buffer_load_b128(rsWS, ((tok >> 2) ^ ((w + k) & 1)) * 128 + (hh ^ (g & 1)) * 64 + tp * 16 + k * 1024, WS_X1 + t * FRAME_B + qb * TILE_B + w * 3072, SC1);
    float lt[2];
#pragma unroll 1
    for (int attempt = 0;; ++attempt) {
      BAR();
      const bool redo = row_sums(lt);
      if (__builtin_expect(attempt > 0 || !redo, 1)) break;
      BAR();
    }
    ST(17);
    reduce_o();
    const float linv[2] = {__builtin_amdgcn_rcpf(lt[0]), __builtin_amdgcn_rcpf(lt[1])};
    BAR();
    ST(18);
    u32x4 bf[6][2];
#pragma unroll
    for (int ks = 0; ks < 6; ++ks) { bf[ks][0] = lds_r128(fr[0] + L_FI + ks * 1024); bf[ks][1] = lds_r128(fr[1] + L_FI + ks * 1024); }
    SBAR();
    f32x4 d[3][2];
    gemm(wv, bf, d);
#pragma unroll
    for (int i = 0; i < 3; ++i) {
      const u32x2 y0 = pack4(d[i][0] * linv[0]), y1 = pack4(d[i][1] * linv[1]);
      auto s0 = __builtin_amdgcn_permlane16_swap(y0[0], y1[0], false, false);
      auto s1 = __builtin_amdgcn_permlane16_swap(y0[1], y1[1], false, false);
      const u32x4 y = {s0[0], s1[0], s0[1], s1[1]};
      u32x4 o;
#pragma unroll
      for (int e = 0; e < 4; ++e) o[e] = cvtpk(bflo(x1[i][e]) + bflo(y[e]), bfhi(x1[i][e]) + bfhi(y[e]));
      *(u32x4*)(out + (size_t)(t * 1024 + qb * 32 + tok) * 384 + (2 * (3 * w + i) + hh) * 16) = o;
    }
    ST(19);
    ST_OUT(ws, 12, 20);
  }
}

extern "C" void kernel_launch(void* const* d_in, const int* in_sizes, int n_in, void* d_out, int out_size, void* d_ws, size_t ws_size, hipStream_t stream) {
  (void)hipFuncSetAttribute((const void*)k_timesformer, hipFuncAttributeMaxDynamicSharedMemorySize, LDS_BYTES);
  k_timesformer<<<256, 512, LDS_BYTES, stream>>>((const char*)d_in[0], (const char*)d_in[1], (const char*)d_in[2], (const char*)d_in[3], (const char*)d_in[4],
                                                 (char*)d_ws, (char*)d_out);
}
```

```cpp
#include <hip/hip_runtime.h>
#include <hip/hip_bf16.h>
#include <type_traits>

typedef short s16x4 __attribute__((ext_vector_type(4)));
typedef __bf16 b16x8 __attribute__((ext_vector_type(8)));
typedef __bf16 b16x2 __attribute__((ext_vector_type(2)));
typedef float f32x2 __attribute__((ext_vector_type(2)));
typedef float f32x4 __attribute__((ext_vector_type(4)));
typedef float f32x16 __attribute__((ext_vector_type(16)));
typedef unsigned u32x2 __attribute__((ext_vector_type(2)));
typedef unsigned u32x4 __attribute__((ext_vector_type(4)));
typedef unsigned long long u64;
#define LAS __attribute__((address_space(3)))
#define DI __device__ __forceinline__
#define SBAR() __builtin_amdgcn_sched_barrier(0)

constexpr int TILE_B = 12288;
constexpr int FRAME_B = 393216;
constexpr unsigned WS_H2 = 0, WS_X1 = 8 * FRAME_B, WS_WM = 16 * FRAME_B, WS_FLAG = WS_WM + 2 * 73728, WS_FLAG2 = WS_FLAG + 2048, WS_EPOCH = WS_FLAG2 + 2048, WS_END = WS_EPOCH + 256;
constexpr float QSCALE = 0.07216878364870322f * 1.4426950408889634f;
constexpr int SC1 = 16;
constexpr unsigned POISON = 0xffffffffu;
constexpr int SPIN_CAP = 1 << 16;


constexpr int PROBE = 99; constexpr size_t WS_T0 = 32u << 20;
#define ST_DECL u64 st_ = 0
#define ST(k) do { if ((k) == PROBE || (k) == 0) { SBAR(); u64 t_; asm volatile("s_memrealtime %0\n\ts_waitcnt lgkmcnt(0)" : "=s"(t_) :: "memory"); SBAR(); \
    if ((k) == PROBE) st_ = t_; \
    if ((k) == 0 && threadIdx.x == 0) __hip_atomic_store((u64*)(ws + WS_T0) + blockIdx.x, t_, __ATOMIC_RELAXED, __HIP_MEMORY_SCOPE_AGENT); } } while (0)
#define ST_OUT(ws, k0, k1) do { if (PROBE >= (k0) && PROBE <= (k1)) { u64 m_ = ~0ull; \
    for (int q_ = 0; q_ < 4; ++q_) { u64 v_ = __hip_atomic_load((u64*)(ws + WS_T0) + (threadIdx.x & 63) * 4 + q_, __ATOMIC_RELAXED, __HIP_MEMORY_SCOPE_AGENT); m_ = v_ < m_ ? v_ : m_; } \
    for (int o_ = 32; o_; o_ >>= 1) { u64 v_ = __shfl_xor(m_, o_); m_ = v_ < m_ ? v_ : m_; } \
    long long d_ = (long long)(st_ - m_); if (d_ < 0) d_ = 0; if (d_ > 5000) d_ = 5000; \
    const u64 e_ = m_ + 4000 + 4 * d_; while (__builtin_amdgcn_s_memrealtime() < e_) __builtin_amdgcn_s_sleep(1); } } while (0)


DI float asf(unsigned u) { return __builtin_bit_cast(float, u); }
DI unsigned asu(float f) { return __builtin_bit_cast(unsigned, f); }
DI unsigned cvtpk(float lo, float hi) { f32x2 v = {lo, hi}; return __builtin_bit_cast(unsigned, __builtin_convertvector(v, b16x2)); }
DI float bflo(unsigned u) { return asf(u << 16); }
DI float bfhi(unsigned u) { return asf(u & 0xffff0000u); }
DI f32x16 mfma32(u32x4 a, u32x4 b, f32x16 c) { return __builtin_amdgcn_mfma_f32_32x32x16_bf16(__builtin_bit_cast(b16x8, a), __builtin_bit_cast(b16x8, b), c, 0, 0, 0); }
DI f32x4 mfma16(u32x4 a, u32x4 b, f32x4 c) { return __builtin_amdgcn_mfma_f32_16x16x32_bf16(__builtin_bit_cast(b16x8, a), __builtin_bit_cast(b16x8, b), c, 0, 0, 0); }
DI f32x4 mfma16k16(u32x2 a, u32x2 b, f32x4 c) { return __builtin_amdgcn_mfma_f32_16x16x16bf16_1k(__builtin_bit_cast(s16x4, a), __builtin_bit_cast(s16x4, b), c, 0, 0, 0); }
DI u32x2 pack4(f32x4 v) { u32x2 r = {cvtpk(v[0], v[1]), cvtpk(v[2], v[3])}; return r; }
DI u32x4 lds_r128(unsigned a) { return *(LAS u32x4*)(size_t)a; }
DI f32x4 lds_r128f(unsigned a) { return *(LAS f32x4*)(size_t)a; }
DI u32x2 lds_r64(unsigned a) { return *(LAS u32x2*)(size_t)a; }
DI void lds_w128(unsigned a, u32x4 v) { *(LAS u32x4*)(size_t)a = v; }
DI void lds_w128f(unsigned a, f32x4 v) { *(LAS f32x4*)(size_t)a = v; }
DI void lds_w64(unsigned a, u32x2 v) { *(LAS u32x2*)(size_t)a = v; }
DI void lds_w32(unsigned a, float v) { *(LAS float*)(size_t)a = v; }
DI u32x2 lds_tr(unsigned a) { return __builtin_bit_cast(u32x2, __builtin_amdgcn_ds_read_tr16_b64_v4i16((LAS s16x4*)(size_t)a)); }
DI u32x4 lds_tr2(unsigned a0, unsigned a1) { u32x2 x = lds_tr(a0), y = lds_tr(a1); u32x4 r = {x[0], x[1], y[0], y[1]}; return r; }
#define BAR() asm volatile("s_waitcnt lgkmcnt(0)\n\ts_barrier" ::: "memory")
#define BARV(n) asm volatile("s_waitcnt vmcnt(" #n ") lgkmcnt(0)\n\ts_barrier" ::: "memory")
template <int CTRL> DI float dpp_add(float v) { return v + asf(__builtin_amdgcn_update_dpp(0u, asu(v), CTRL, 0xf, 0xf, true)); }
DI float xor16_add(float v) { auto r = __builtin_amdgcn_permlane16_swap(asu(v), asu(v), false, false); return asf(r[0]) + asf(r[1]); }
DI float xor16_max(float v) { auto r = __builtin_amdgcn_permlane16_swap(asu(v), asu(v), false, false); return fmaxf(asf(r[0]), asf(r[1])); }
DI unsigned umax3(unsigned a, unsigned b, unsigned c) { return max(max(a, b), c); }
template <int N> DI bool poisoned(const u32x4 (&v)[N]) {
  unsigned m = 0;
#pragma unroll
  for (int i = 0; i < N; ++i) { m = umax3(m, v[i][0], v[i][1]); m = umax3(m, v[i][2], v[i][3]); }
  return __any(m == POISON);
}
DI u32x4 unpoison(u32x4 v) { u32x4 r = {min(v[0], POISON - 1), min(v[1], POISON - 1), min(v[2], POISON - 1), min(v[3], POISON - 1)}; return r; }
DI unsigned fi_pos(unsigned r, unsigned c8) {
  return (((r >> 4) * 6 + (c8 >> 2)) * 64 + (c8 & 3) * 16 + ((r & 15) ^ ((c8 & 1) | (((r >> 3) & 1) << 1) | ((r >> 4) << 2)))) * 16;
}
DI void layernorm24(const float (&v)[24], u32x4 (&o)[3]) {
  float s0 = 0.f, s1 = 0.f;
#pragma unroll
  for (int i = 0; i < 24; i += 2) { s0 += v[i]; s1 += v[i + 1]; }
  float s = s0 + s1;
  s = dpp_add<0x124>(s); s = dpp_add<0x128>(s); s = xor16_add(s);
  const float mu = s * (1.0f / 192.0f);
  float d[24], q0 = 0.f, q1 = 0.f;
#pragma unroll
  for (int i = 0; i < 24; i += 2) { d[i] = v[i] - mu; d[i + 1] = v[i + 1] - mu; q0 = fmaf(d[i], d[i], q0); q1 = fmaf(d[i + 1], d[i + 1], q1); }
  float q = q0 + q1;
  q = dpp_add<0x124>(q); q = dpp_add<0x128>(q); q = xor16_add(q);
  const float rs = __builtin_amdgcn_rsqf(q * (1.0f / 192.0f) + 1e-5f);
#pragma unroll
  for (int i = 0; i < 3; ++i)
#pragma unroll
    for (int e = 0; e < 4; ++e) o[i][e] = cvtpk(d[8 * i + 2 * e] * rs, d[8 * i + 2 * e + 1] * rs);
}

constexpr unsigned SLOT = 36864, L_H = 4 * SLOT, L_PT = L_H + TILE_B, LDS_BYTES = 163840;
constexpr unsigned L_VI = 0, L_SP = TILE_B, L_YI = SLOT, L_MA = 3 * SLOT, L_MB = L_MA + TILE_B;
constexpr unsigned L_HQ = 30720, L_Q = L_H, L_FI = L_H, L_N = L_PT + 1024, L_L = L_N + 1024;

#define DMA4(v0, v1, v2, v3, sbase, ldsa)                                                                  \
  asm volatile("s_mov_b32 m0, %5\n\ts_nop 0\n\t"                                                            \
               "global_load_lds_dwordx4 %0, %4\n\tglobal_load_lds_dwordx4 %1, %4 offset:1024\n\t"           \
               "global_load_lds_dwordx4 %2, %4 offset:2048\n\tglobal_load_lds_dwordx4 %3, %4 offset:3072"   \
               :: "v"(v0), "v"(v1), "v"(v2), "v"(v3), "s"(sbase), "s"(ldsa) : "memory")
#define DMA1(v0, sbase, ldsa) \
  asm volatile("s_mov_b32 m0, %2\n\ts_nop 0\n\tglobal_load_lds_dwordx4 %0, %1" :: "v"(v0), "s"(sbase), "s"(ldsa) : "memory")
#define DMA9(o, sbase, ldsa) do { DMA4(o[0], o[1], o[2], o[3], sbase, ldsa); DMA4(o[4], o[5], o[6], o[7], sbase, (ldsa) + 4096); DMA1(o[8], sbase, (ldsa) + 8192); } while (0)

template <int OFF> DI void bdma(unsigned voff, u32x4 rs, unsigned soff) { asm volatile("buffer_load_dwordx4 %0, %1, %2 offen offset:%3 sc1 lds" :: "v"(voff), "s"(rs), "s"(soff), "i"(OFF) : "memory"); }
template <int OFF> DI void setm0(unsigned base) { asm volatile("s_add_u32 m0, %0, %1\n\ts_nop 0" :: "s"(base), "i"(OFF) : "memory", "scc"); }
template <int N> DI void waitvm() { asm volatile("s_waitcnt vmcnt(%0)" :: "i"(N) : "memory"); }
#define IC(x) std::integral_constant<int, (x)>{}
#define OPAQUE(v) asm volatile("" : "+v"(v))
DI bool tags_ok(u32x4 f, u32x2 tag) { return f[0] == tag[0] && f[1] == tag[1] && f[2] == tag[0] && f[3] == tag[1]; }

__global__ __launch_bounds__(512, 2) void k_timesformer(const char* __restrict__ x, const char* __restrict__ wqkv_t, const char* __restrict__ wo_t,
                                                        const char* __restrict__ wqkv_s, const char* __restrict__ wo_s, char* __restrict__ ws, char* __restrict__ out) {
  extern __shared__ __attribute__((aligned(16))) char smem[];
  const unsigned lds0 = (unsigned)(size_t)(LAS char*)smem;
  const unsigned b = blockIdx.x, w8 = __builtin_amdgcn_readfirstlane(threadIdx.x >> 6), w = w8 & 3;
  unsigned l = threadIdx.x & 63;
  const unsigned t = b & 7, qb = b >> 3;
  const auto rsWS = __builtin_amdgcn_make_buffer_rsrc((void*)ws, 0, WS_END, 0x00020000);
  ST_DECL;
  ST(0);
#define LANE_IDS const unsigned i16 = l & 15, g = l >> 4, r32 = l & 31, hh = l >> 5, tq = i16 >> 2, tp = l & 3
#define FRAG_POS unsigned fr[2], dpos[3][2];                                                                        \
  _Pragma("unroll") for (int hf = 0; hf < 2; ++hf) { fr[hf] = lds0 + fi_pos(16 * hf + i16, g);                     \
    _Pragma("unroll") for (int i = 0; i < 3; ++i) dpos[i][hf] = lds0 + fi_pos(16 * hf + i16, 2 * (3 * w + i) + (g >> 1)) + 8 * (g & 1); }
  auto reduce_o = [&]() {
    const unsigned r32 = l & 31, hh = l >> 5;
#pragma unroll
    for (int i = 0; i < 3; ++i) {
      const unsigned hu = 3 * w8 + i;
      u32x2 p[4];
#pragma unroll
      for (int ww = 0; ww < 4; ++ww) p[ww] = lds_r64(lds0 + ww * SLOT + hu * 512 + l * 8);
      f32x4 a;
      a[0] = (bflo(p[0][0]) + bflo(p[1][0])) + (bflo(p[2][0]) + bflo(p[3][0]));
      a[1] = (bfhi(p[0][0]) + bfhi(p[1][0])) + (bfhi(p[2][0]) + bfhi(p[3][0]));
      a[2] = (bflo(p[0][1]) + bflo(p[1][1])) + (bflo(p[2][1]) + bflo(p[3][1]));
      a[3] = (bfhi(p[0][1]) + bfhi(p[1][1])) + (bfhi(p[2][1]) + bfhi(p[3][1]));
      lds_w64(lds0 + L_FI + fi_pos(r32, hu) + 8 * hh, pack4(a));
    }
  };
  auto row_sums = [&](float (&lt)[2]) {
    const f32x4 a = lds_r128f(lds0 + L_L + (l & 15) * 32), c = lds_r128f(lds0 + L_L + (l & 15) * 32 + 16);
    lt[0] = (a[0] + a[2]) + (c[0] + c[2]);
    lt[1] = (a[1] + a[3]) + (c[1] + c[3]);
    return (bool)__any(!(lt[0] >= 8.7e-19f && lt[1] >= 8.7e-19f));
  };

  if (w8 < 4) {
    const unsigned nl = l & 3, j = (l >> 2) & 7, tt = 2 * w + (l >> 5), r = nl * 8 + tt;
    const auto rsX = __builtin_amdgcn_make_buffer_rsrc((void*)x, 0, 8 * FRAME_B, 0x00020000);
    u32x4 xr[3];
#pragma unroll
    for (int i = 0; i < 3; ++i) xr[i] = __builtin_amdgcn_raw_buffer_load_b128(rsX, (tt * 1024 + nl) * 384 + j * 16 + i * 128, b * 1536, 0);
    const u32x2 ep = __builtin_amdgcn_raw_buffer_load_b64(rsWS, 0, WS_EPOCH, SC1);
    SBAR();
    const unsigned so = tt * FRAME_B + (j >> 1) * 1024 + ((t ^ ((j >> 1) & 1)) * 128) + (((j & 1) ^ (t >> 2)) * 64) + nl * 16, sso = qb * TILE_B;
    const u32x4 pz = {POISON, POISON, POISON, POISON};
    __builtin_amdgcn_raw_buffer_store_b128(pz, rsWS, so, WS_H2 + sso, SC1);
    OPAQUE(l);
    LANE_IDS;
    FRAG_POS;
    const unsigned phi = ((tq >> 1) & 1) | ((g & 1) << 1);
    unsigned tcol[3], rpos[3];
#pragma unroll
    for (int i = 0; i < 3; ++i) { tcol[i] = (((3 * w + i) ^ phi) * 2 + (tp >> 1)) * 16 + 8 * (tp & 1); rpos[i] = lds0 + fi_pos(r, j + 8 * i); }
    SBAR();
    float xv[24];
    {
      u32x4 h[3];
#pragma unroll
      for (int i = 0; i < 3; ++i)
#pragma unroll
        for (int e = 0; e < 4; ++e) { xv[8 * i + 2 * e] = bflo(xr[i][e]); xv[8 * i + 2 * e + 1] = bfhi(xr[i][e]); }
      ST(1);
      layernorm24(xv, h);
#pragma unroll
      for (int i = 0; i < 3; ++i) lds_w128(rpos[i] + L_H, h[i]);
    }
    __builtin_amdgcn_raw_buffer_store_b128(pz, rsWS, so, WS_H2 + sso + 4096, SC1);
    ST(2);
    const u64 tag64 = (((u64)ep[1] << 32) | ep[0]) + 1;
    const u32x2 tag = {(unsigned)tag64, (unsigned)(tag64 >> 32)};
    f32x4 acc[3][3][2];
#pragma unroll
    for (int m = 0; m < 3; ++m)
#pragma unroll
      for (int i = 0; i < 3; ++i) { acc[m][i][0] = f32x4{0, 0, 0, 0}; acc[m][i][1] = f32x4{0, 0, 0, 0}; }
#pragma unroll
    for (int ks = 0; ks < 6; ++ks) {
      if (ks == 3) BARV(0); else BAR();
      if (ks == 0) ST(3);
      if (ks == 5) ST(4);
      if (ks == 0) __builtin_amdgcn_raw_buffer_store_b128(pz, rsWS, so, WS_H2 + sso + 8192, SC1);
      if (ks == 3 && w == 0 && l == 0) __builtin_amdgcn_raw_buffer_store_b64(tag, rsWS, 0, WS_FLAG + b * 8, SC1);
      const u32x4 h0 = lds_r128(fr[0] + L_H + ks * 1024), h1 = lds_r128(fr[1] + L_H + ks * 1024);
      const unsigned base = lds0 + (ks & 3) * SLOT + (8 * g + tq) * 1152;
      u32x4 a[9];
#pragma unroll
      for (int m = 0; m < 3; ++m)
#pragma unroll
        for (int i = 0; i < 3; ++i) a[m * 3 + i] = lds_tr2(base + tcol[i] + m * 384, base + tcol[i] + m * 384 + 4608);
      SBAR();
#pragma unroll
      for (int m = 0; m < 3; ++m)
#pragma unroll
        for (int i = 0; i < 3; ++i) {
          acc[m][i][0] = mfma16(a[m * 3 + i], h0, acc[m][i][0]);
          acc[m][i][1] = mfma16(a[m * 3 + i], h1, acc[m][i][1]);
        }
      if (ks < 5) SBAR();
    }
#pragma unroll
    for (int hf = 0; hf < 2; ++hf) {
      f32x4 s = {0, 0, 0, 0};
#pragma unroll
      for (int i = 0; i < 3; ++i) s = mfma16k16(pack4(acc[1][i][hf]), pack4(acc[0][i][hf]), s);
#pragma unroll
      for (int i = 0; i < 3; ++i) lds_w64(dpos[i][hf] + L_VI, pack4(acc[2][i][hf]));
      lds_w128f(lds0 + L_SP + ((w * 2 + hf) * 64 + l) * 16, s);
    }
    ST(5);
    BAR();
    f32x4 vp[3][2];
#pragma unroll
    for (int i = 0; i < 3; ++i) { vp[i][0] = f32x4{0, 0, 0, 0}; vp[i][1] = f32x4{0, 0, 0, 0}; }
    u32x4 vf[6][2];
    u32x2 pt[2];
#pragma unroll
    for (int c = 0; c < 2; ++c) {
      if (c == 1) { BAR(); ST(6); }
      u32x4 bw[9];
#pragma unroll
      for (int k = 0; k < 3; ++k) {
        if (c == 0) { vf[k][0] = lds_r128(fr[0] + L_VI + k * 1024); vf[k][1] = lds_r128(fr[1] + L_VI + k * 1024); }
#pragma unroll
        for (int i = 0; i < 3; ++i) {
          const unsigned base = lds0 + (2 + c) * SLOT + (32 * k + 8 * g + tq) * 384 + tcol[i];
          bw[k * 3 + i] = lds_tr2(base, base + 1536);
        }
      }
      if (c == 0) {
#pragma unroll
        for (int k = 3; k < 6; ++k) { vf[k][0] = lds_r128(fr[0] + L_VI + k * 1024); vf[k][1] = lds_r128(fr[1] + L_VI + k * 1024); }
      } else { pt[0] = lds_r64(lds0 + L_PT + l * 8); pt[1] = lds_r64(lds0 + L_PT + 512 + l * 8); }
      SBAR();
#pragma unroll
      for (int k = 0; k < 3; ++k)
#pragma unroll
        for (int i = 0; i < 3; ++i) {
          vp[i][0] = mfma16(vf[3 * c + k][0], bw[k * 3 + i], vp[i][0]);
          vp[i][1] = mfma16(vf[3 * c + k][1], bw[k * 3 + i], vp[i][1]);
        }
      if (c == 0) SBAR();
    }
#pragma unroll
    for (int i = 0; i < 3; ++i)
#pragma unroll
      for (int hf = 0; hf < 2; ++hf) lds_w64(dpos[i][hf] + L_YI, pack4(mfma16k16(pack4(vp[i][hf]), pt[hf], f32x4{0, 0, 0, 0})));
    BAR();
    ST(7);
    {
      u32x4 x1[3], h[3];
#pragma unroll
      for (int i = 0; i < 3; ++i) {
        const u32x4 y = lds_r128(rpos[i] + L_YI);
#pragma unroll
        for (int e = 0; e < 4; ++e) { xv[8 * i + 2 * e] += bflo(y[e]); xv[8 * i + 2 * e + 1] += bfhi(y[e]); }
      }
      layernorm24(xv, h);
#pragma unroll
      for (int i = 0; i < 3; ++i) __builtin_amdgcn_raw_buffer_store_b128(unpoison(h[i]), rsWS, so, WS_H2 + sso + i * 4096, SC1);
      SBAR();
#pragma unroll
      for (int i = 0; i < 3; ++i) {
#pragma unroll
        for (int e = 0; e < 4; ++e) x1[i][e] = cvtpk(xv[8 * i + 2 * e], xv[8 * i + 2 * e + 1]);
        __builtin_amdgcn_raw_buffer_store_b128(x1[i], rsWS, so, WS_X1 + sso + i * 4096, SC1);
      }
    }
    ST(8);
    const unsigned ring = lds0 + w * SLOT;
    const unsigned kr0 = ring + (r32 >> 2) * 128 + (hh ^ (r32 >> 4)) * 64 + (r32 & 3) * 16, kr1 = kr0 ^ 128;
    const unsigned vr0 = ring + (g & 1) * 1024 + (hh ^ (g & 1)) * 128 + (tp >> 1) * 64 + tq * 16 + 8 * (tp & 1), vr1 = (vr0 ^ 64) + 512;
    const unsigned lo[3] = {l * 16, l * 16 + 4096, l * 16 + 8192};
    const u64 hbu = (u64)(ws + WS_H2 + (size_t)t * FRAME_B);
    const u32x4 rs = {(unsigned)hbu, (unsigned)(hbu >> 32) & 0xffffu, (unsigned)FRAME_B, 0x00020000u};
    unsigned toff[8];
#pragma unroll
    for (int i = 0; i < 8; ++i) toff[i] = (8 * w + ((i + qb) & 7)) * TILE_B;
    u32x4 sel;
    { const unsigned one = (tp == (g & 1)) ? 0x3f803f80u : 0u; sel = u32x4{one, one, one, one}; }
    auto piece = [&](auto Tc, auto Pc) {
      constexpr int T = decltype(Tc)::value, p = decltype(Pc)::value;
      if constexpr (T < 8) {
        if constexpr (p % 4 == 0) setm0<(T % 3) * TILE_B + p * 1024>(ring);
        bdma<(p % 4) * 1024>(lo[p / 4], rs, toff[T]);
      }
    };
#define P6(T, a) piece(IC(T), IC(a)); piece(IC(T), IC(a + 1)); piece(IC(T), IC(a + 2)); piece(IC(T), IC(a + 3)); piece(IC(T), IC(a + 4)); piece(IC(T), IC(a + 5));
    auto validate = [&](unsigned dst, unsigned off, const int n) {
      for (int sp = 0; sp < SPIN_CAP; ++sp) {
        unsigned m = 0;
#pragma unroll
        for (int k = 0; k < n; ++k) { const u32x4 v = lds_r128(dst + k * 1024 + lo[0]); m = umax3(m, v[0], v[1]); m = umax3(m, v[2], v[3]); }
        if (__builtin_expect(!__any(m == POISON), 1)) break;
#pragma unroll 1
        for (int k = 0; k < n; ++k) { setm0<0>(dst + k * 1024); bdma<0>(lo[0], rs, off + k * 1024); }
        waitvm<0>();
      }
    };
    BARV(3);
    ST(9);
    P6(0, 0) P6(0, 6)
    BARV(12);
    if (w == 0 && l == 0) __builtin_amdgcn_raw_buffer_store_b64(tag, rsWS, 0, WS_FLAG2 + b * 8, SC1);
    P6(1, 0) P6(1, 6) P6(2, 0)
    waitvm<18>();
    validate(ring, toff[0], 12);
    waitvm<6>();
    validate(ring + TILE_B, toff[1], 12);
    waitvm<0>();
    validate(ring + 2 * TILE_B, toff[2], 6);
    ST(10);
    BAR();
    u32x4 Q[12];
#pragma unroll
    for (int s = 0; s < 12; ++s) Q[s] = lds_r128(lds0 + L_Q + s * 1024 + lo[0]);
    float mref;
    {
      const f32x4 s = lds_r128f(lds0 + L_N + r32 * 16) + lds_r128f(lds0 + L_N + 512 + r32 * 16);
      mref = __builtin_amdgcn_sqrtf(((s[0] + s[1]) + (s[2] + s[3])) * 192.0f) * 1.01f;
    }
    constexpr int KW = 4, VW = 4;
#pragma unroll 1
    for (int attempt = 0;; ++attempt) {
      f32x16 S[2], O[6], negm;
      u32x4 K[12], V[12], P[2][2];
      f32x4 lsum = {0, 0, 0, 0};
      float pe[2][2];
#pragma unroll
      for (int i = 0; i < 16; ++i) negm[i] = -mref;
#define RK(k, T) K[k] = lds_r128((((k) & 1) ? kr1 : kr0) + ((T) % 3) * TILE_B + (k) * 1024);
#define RV(u, T) { const unsigned a_ = (((u) & 1) ? vr1 : vr0) + ((T) % 3) * TILE_B + ((u) >> 1) * 2048; V[u] = lds_tr2(a_, a_ + 256); }
      auto sm = [&](auto Tc, auto Hc, auto Gc) {
        constexpr int T = decltype(Tc)::value, H = decltype(Hc)::value, G = decltype(Gc)::value;
        if constexpr (G < 8) pe[(G >> 1) & 1][G & 1] = __builtin_amdgcn_exp2f(S[T & 1][8 * H + G]);
        if constexpr (G >= 2 && G <= 8 && G % 2 == 0) { constexpr int q = G / 2 - 1; P[T & 1][H][q] = cvtpk(pe[q & 1][0], pe[q & 1][1]); }
        if constexpr (G == 10) lsum = mfma16(sel, P[T & 1][H], lsum);
      };
      auto qkphase = [&](auto Ic) {
        constexpr int I = decltype(Ic)::value;
        auto step = [&](auto Kc) {
          constexpr int k = decltype(Kc)::value;
          if constexpr (I + 1 < 8) { S[(I + 1) & 1] = mfma32(K[k], Q[k], k == 0 ? negm : S[(I + 1) & 1]); if constexpr (k + KW < 12) RK(k + KW, I + 1) }
          if constexpr (I >= 0) { if constexpr (k & 1) piece(IC(I + 2), IC(6 + k / 2)); if constexpr (k < 11) sm(IC(I), IC(1), IC(k)); }
          if constexpr (I >= 0 && I < 7 && k >= 12 - VW) RV(k - (12 - VW), I)
          if constexpr (I == 7 && k < VW) RV(k, I)
          SBAR();
        };
        step(IC(0)); step(IC(1)); step(IC(2)); step(IC(3)); step(IC(4)); step(IC(5)); step(IC(6)); step(IC(7)); step(IC(8)); step(IC(9)); step(IC(10)); step(IC(11));
      };
      auto pvphase = [&](auto Ic) {
        constexpr int I = decltype(Ic)::value;
        auto step = [&](auto Uc) {
          constexpr int u = decltype(Uc)::value;
          if constexpr (I >= 0) {
            f32x16 z;
#pragma unroll
            for (int e = 0; e < 16; ++e) z[e] = 0.f;
            O[u >> 1] = mfma32(V[u], P[I & 1][u & 1], (I == 0 && !(u & 1)) ? z : O[u >> 1]);
            if constexpr (u & 1) piece(IC(I + 3), IC(u / 2));
            if constexpr (u + VW < 12) RV(u + VW, I)
          }
          if constexpr (I + 1 < 8 && u >= 1) sm(IC(I + 1), IC(0), IC(u - 1));
          if constexpr (I + 2 < 8 && I >= 0) {
            if constexpr (u == 12 - KW) waitvm<(I + 3 < 8 ? 6 + (12 - KW) / 2 : 6)>();
            if constexpr (u >= 12 - KW) RK(u - (12 - KW), I + 2)
            if constexpr (u == 11) waitvm<(I + 3 < 8 ? 6 : 0)>();
          }
          if constexpr (I < 0 && u < KW) RK(u, 1)
          if constexpr (I == 7 && u >= 3) {
            constexpr int un = u - 3;
#pragma unroll
            for (int hf = 0; hf < 2; ++hf) {
              const u32x2 pk = {cvtpk(O[un >> 1][8 * (un & 1) + 4 * hf], O[un >> 1][8 * (un & 1) + 4 * hf + 1]), cvtpk(O[un >> 1][8 * (un & 1) + 4 * hf + 2], O[un >> 1][8 * (un & 1) + 4 * hf + 3])};
              lds_w64(ring + (2 * un + hf) * 512 + l * 8, pk);
            }
          }
          SBAR();
        };
        step(IC(0)); step(IC(1)); step(IC(2)); step(IC(3)); step(IC(4)); step(IC(5)); step(IC(6)); step(IC(7)); step(IC(8)); step(IC(9)); step(IC(10)); step(IC(11));
      };
      setm0<2 * TILE_B + 4096>(ring);
      RK(0, 0) RK(1, 0) RK(2, 0) RK(3, 0)
      qkphase(IC(-1)); pvphase(IC(-1));
      qkphase(IC(0)); pvphase(IC(0)); qkphase(IC(1)); pvphase(IC(1)); qkphase(IC(2)); pvphase(IC(2)); qkphase(IC(3)); pvphase(IC(3));
      qkphase(IC(4)); pvphase(IC(4)); qkphase(IC(5)); pvphase(IC(5)); qkphase(IC(6)); pvphase(IC(6)); qkphase(IC(7)); pvphase(IC(7));
      if (l < 16) { const u32x2 ls = {asu(lsum[0]), asu(lsum[1])}; lds_w64(lds0 + L_L + (l * 4 + w) * 8, ls); }
#pragma unroll
      for (int un = 9; un < 12; ++un)
#pragma unroll
        for (int hf = 0; hf < 2; ++hf) {
          const u32x2 pk = {cvtpk(O[un >> 1][8 * (un & 1) + 4 * hf], O[un >> 1][8 * (un & 1) + 4 * hf + 1]), cvtpk(O[un >> 1][8 * (un & 1) + 4 * hf + 2], O[un >> 1][8 * (un & 1) + 4 * hf + 3])};
          lds_w64(ring + (2 * un + hf) * 512 + l * 8, pk);
        }
      ST(11);
      BAR();
      float lt[2];
      const bool redo = row_sums(lt);
      if (__builtin_expect(attempt > 0 || !redo, 1)) break;
      float mx = -3.0e38f;
#pragma unroll 1
      for (unsigned i = 0; i < 8; ++i) {
        const unsigned off = (8 * w + i) * TILE_B;
#pragma unroll 1
        for (int k = 0; k < 12; ++k) { setm0<0>(ring + k * 1024); bdma<0>(lo[0], rs, off + k * 1024); }
        waitvm<0>();
        validate(ring, off, 12);
        f32x16 s;
#pragma unroll
        for (int e = 0; e < 16; ++e) s[e] = 0.f;
#pragma unroll
        for (int k = 0; k < 12; ++k) s = mfma32(lds_r128(((k & 1) ? kr1 : kr0) + k * 1024), Q[k], s);
#pragma unroll
        for (int e = 0; e < 16; ++e) mx = fmaxf(mx, s[e]);
      }
      lds_w32(lds0 + L_N + (r32 * 8 + w * 2 + hh) * 4, mx);
      P6(0, 0) P6(0, 6) P6(1, 0) P6(1, 6) P6(2, 0)
      BARV(0);
      { const f32x4 c = lds_r128f(lds0 + L_N + r32 * 32), d = lds_r128f(lds0 + L_N + r32 * 32 + 16);
        mref = fmaxf(fmaxf(fmaxf(c[0], c[1]), fmaxf(c[2], c[3])), fmaxf(fmaxf(d[0], d[1]), fmaxf(d[2], d[3]))); }
    }
    reduce_o();
    BAR();
    ST_OUT(ws, 0, 12);
  } else {
    ST(12);
    const unsigned ldsw = lds0 + 9216 * w;
    unsigned qo[9];
    {
      const unsigned qa = (9216 * w + l * 16) ^ ((w & 1) << 6), qc = qa ^ 32;
      qo[0] = qa; qo[1] = qa; qo[2] = l >= 16 ? qc : qa; qo[3] = qc; qo[5] = qa + 4096; qo[7] = qc + 4096;
      qo[4] = l < 32 ? qo[7] : qo[5]; qo[6] = l >= 48 ? qo[7] : qo[5]; qo[8] = qc + 8192;
    }
#define DMAQ(c) DMA9(qo, wqkv_t + (c) * SLOT, ldsw + ((c) & 3) * SLOT)
    DMAQ(0);
    const bool merger = b < 144;
    const unsigned mm = b >= 72, mf = mm ? b - 72 : b, mtau = mf / 6, mks = mf % 6;
    if (merger) {
#pragma unroll
      for (int i = 0; i < 3; ++i) {
        const unsigned pos = (3 * w + i) * 64 + l, row = (pos * 2731) >> 16, ch = pos - row * 24;
        DMA1((32 * mks + row) * 1152 + mm * 768 + ((ch ^ (row & 7)) * 16), wqkv_s, lds0 + L_MA + (3 * w + i) * 1024);
      }
      if (w < 3) {
#pragma unroll
        for (int i = 0; i < 2; ++i) {
          const unsigned pos = (2 * w + i) * 64 + l, row = (pos * 2731) >> 16, ch = pos - row * 24;
          const unsigned o0 = (16 * mtau + row) * 1152 + 384 + ((ch ^ (row & 7)) * 16), o1 = (pos >> 1) * 384 + mtau * 32 + (pos & 1) * 16;
          DMA1(o0 + mm * (o1 - o0), mm ? wo_s : wqkv_s, lds0 + L_MB + (2 * w + i) * 1024);
        }
      }
    }
    DMAQ(1); DMAQ(2);
    OPAQUE(l);
    LANE_IDS;
    unsigned oo[9];
#pragma unroll
    for (int i = 0; i < 9; ++i) {
      const unsigned row = 24 * w + (((8 * i + (l >> 3)) * 171) >> 9);
      oo[i] = ((9216 * w + l * 16) ^ (((row & 2) | ((row & 8) >> 1)) << 4)) + (i >> 2) * 4096;
    }
#define DMAO(c) DMA9(oo, wo_t + ((c) - 6) * SLOT, ldsw + ((c) & 3) * SLOT)
    ST(13);
    BARV(18);
    if (merger && w == 0) {
      f32x4 d0 = {0, 0, 0, 0}, d1 = {0, 0, 0, 0};
      const unsigned ra = 8 * tq + tp;
      u32x4 bb[6];
      if (mm) {
#pragma unroll
        for (int kk = 0; kk < 6; ++kk) { const unsigned a = lds0 + L_MB + (32 * kk + 8 * g + tq) * 32 + tp * 8; bb[kk] = lds_tr2(a, a + 128); }
      } else {
#pragma unroll
        for (int kk = 0; kk < 6; ++kk) bb[kk] = lds_r128(lds0 + L_MB + i16 * 384 + (((4 * kk + g) ^ (i16 & 7)) * 16));
      }
#pragma unroll
      for (int kk = 0; kk < 6; ++kk) {
        const u32x4 a0 = lds_r128(lds0 + L_MA + ra * 384 + (((4 * kk + g) ^ (ra & 7)) * 16));
        const u32x4 a1 = lds_r128(lds0 + L_MA + (ra + 4) * 384 + (((4 * kk + g) ^ ((ra + 4) & 7)) * 16));
        d0 = mfma16(a0, bb[kk], d0); d1 = mfma16(a1, bb[kk], d1);
      }
      const float sc = mm ? 1.0f : QSCALE;
      const u32x2 o0 = pack4(d0 * sc), o1 = pack4(d1 * sc);
      const u32x4 o = {o0[0], o0[1], o1[0], o1[1]};
      __builtin_amdgcn_raw_buffer_store_b128(o, rsWS, l * 16, WS_WM + b * 1024, SC1);
    }
    const u32x2 ep = __builtin_amdgcn_raw_buffer_load_b64(rsWS, 0, WS_EPOCH, SC1);
    BARV(10);
    DMAQ(3); DMAQ(4);
    BARV(18);
    DMAQ(5);
    BARV(18);
    DMAO(6);
    BARV(18);
    DMAO(7);
    BARV(18);
    FRAG_POS;
    BARV(9);
    u32x4 fl[2];
    fl[0] = __builtin_amdgcn_raw_buffer_load_b128(rsWS, l * 16, WS_FLAG, SC1);
    fl[1] = __builtin_amdgcn_raw_buffer_load_b128(rsWS, l * 16 + 1024, WS_FLAG, SC1);
    if (w < 2) {
      f32x4 s = lds_r128f(lds0 + L_SP + (w * 64 + l) * 16);
#pragma unroll
      for (int ww = 1; ww < 4; ++ww) s += lds_r128f(lds0 + L_SP + ((ww * 2 + w) * 64 + l) * 16);
      const float mx = xor16_max(fmaxf(fmaxf(s[0], s[1]), fmaxf(s[2], s[3])));
      f32x4 e;
#pragma unroll
      for (int k = 0; k < 4; ++k) e[k] = __builtin_amdgcn_exp2f((s[k] - mx) * QSCALE);
      const float sum = xor16_add((e[0] + e[1]) + (e[2] + e[3]));
      const float inv = ((g >> 1) == (i16 >> 3)) ? __builtin_amdgcn_rcpf(sum) : 0.f;
      lds_w64(lds0 + L_PT + w * 512 + l * 8, pack4(e * inv));
    }
    BARV(2);
    const u64 tag64 = (((u64)ep[1] << 32) | ep[0]) + 1;
    const u32x2 tag = {(unsigned)tag64, (unsigned)(tag64 >> 32)};
    const bool early = __all(tags_ok(fl[0], tag) && tags_ok(fl[1], tag));
    u32x4 wq[18], wv[18];
    auto ldw = [&](u32x4 (&W)[18], const int m, const int i0, const int i1) {
#pragma unroll
      for (int i = i0; i < i1; ++i) W[i] = __builtin_amdgcn_raw_buffer_load_b128(rsWS, l * 16 + (i & 3) * 1024, WS_WM + m * 73728 + w * 18432 + (i >> 2) * 4096, SC1);
    };
    ldw(wq, 0, 0, 10);
    BAR();
    if (__builtin_expect(!early, 0)) {
      for (int sp = 0; sp < SPIN_CAP; ++sp) {
        asm volatile("" ::: "memory");
        fl[0] = __builtin_amdgcn_raw_buffer_load_b128(rsWS, l * 16, WS_FLAG, SC1);
        fl[1] = __builtin_amdgcn_raw_buffer_load_b128(rsWS, l * 16 + 1024, WS_FLAG, SC1);
        if (__all(tags_ok(fl[0], tag) && tags_ok(fl[1], tag))) break;
      }
      asm volatile("" ::: "memory");
      ldw(wq, 0, 0, 10);
    }
    if (b == 0 && w == 0 && l == 0) __builtin_amdgcn_raw_buffer_store_b64(tag, rsWS, 0, WS_EPOCH, SC1);
    ldw(wq, 0, 10, 18);
    ldw(wv, 1, 0, 18);
    ST(14);
    BAR();
    {
      u32x4 hq[3];
      for (int sp = 0; sp < SPIN_CAP; ++sp) {
        asm volatile("" ::: "memory");
#pragma unroll
        for (int i = 0; i < 3; ++i) hq[i] = __builtin_amdgcn_raw_buffer_load_b128(rsWS, l * 16 + i * 1024, WS_H2 + t * FRAME_B + qb * TILE_B + w * 3072, SC1);
        if (!poisoned(hq)) break;
      }
#pragma unroll
      for (int i = 0; i < 3; ++i) lds_w128(lds0 + w * SLOT + L_HQ + i * 1024 + l * 16, hq[i]);
    }
    ST(15);
    BAR();
    auto gemm = [&](const u32x4 (&wf)[18], const u32x4 (&bf)[6][2], f32x4 (&d)[3][2]) {
#pragma unroll
      for (int i = 0; i < 3; ++i) { d[i][0] = f32x4{0, 0, 0, 0}; d[i][1] = f32x4{0, 0, 0, 0}; }
#pragma unroll
      for (int ks = 0; ks < 6; ++ks)
#pragma unroll
        for (int i = 0; i < 3; ++i) { d[i][0] = mfma16(wf[i * 6 + ks], bf[ks][0], d[i][0]); d[i][1] = mfma16(wf[i * 6 + ks], bf[ks][1], d[i][1]); }
    };
    {
      u32x4 bf[6][2];
#pragma unroll
      for (int ks = 0; ks < 6; ++ks)
#pragma unroll
        for (int hf = 0; hf < 2; ++hf) {
          const unsigned pe_ = ((2 * ks) / 3) * SLOT + ((2 * ks) % 3) * 1024, po_ = ((2 * ks + 1) / 3) * SLOT + ((2 * ks + 1) % 3) * 1024;
          bf[ks][hf] = lds_r128(lds0 + L_HQ + (hh ? po_ : pe_) + ((4 * hf + tq) ^ hh) * 128 + ((g & 1) ^ hf) * 64 + tp * 16);
        }
      SBAR();
      f32x4 d[3][2];
      gemm(wq, bf, d);
      float n2[2];
#pragma unroll
      for (int hf = 0; hf < 2; ++hf) {
        n2[hf] = 0.f;
#pragma unroll
        for (int i = 0; i < 3; ++i) {
          lds_w64(lds0 + L_Q + ((3 * w + i) * 64 + (g >> 1) * 32 + 16 * hf + i16) * 16 + 8 * (g & 1), pack4(d[i][hf]));
#pragma unroll
          for (int e = 0; e < 4; ++e) n2[hf] = fmaf(d[i][hf][e], d[i][hf][e], n2[hf]);
        }
      }
      auto rr = __builtin_amdgcn_permlane16_swap(asu(n2[0]), asu(n2[1]), false, false);
      lds_w32(lds0 + L_N + l * 16 + w * 4, asf(rr[0]) + asf(rr[1]));
    }
    ST(16);
    BAR();
    __builtin_amdgcn_s_sleep(32);
    for (int sp = 0; sp < SPIN_CAP; ++sp) {
      asm volatile("" ::: "memory");
      const u32x4 f = __builtin_amdgcn_raw_buffer_load_b128(rsWS, (l & 3) * 16, WS_FLAG2 + qb * 64, SC1);
      if (__all(tags_ok(f, tag))) break;
      __builtin_amdgcn_s_sleep(8);
    }
    asm volatile("" ::: "memory");
    const unsigned tok = 16 * (g & 1) + i16;
    u32x4 x1[3];
#pragma unroll
    for (int k = 0; k < 3; ++k)
      x1[k] = __builtin_amdgcn_raw_buffer_load_b128(rsWS, ((tok >> 2) ^ ((w + k) & 1)) * 128 + (hh ^ (g & 1)) * 64 + tp * 16 + k * 1024, WS_X1 + t * FRAME_B + qb * TILE_B + w * 3072, SC1);
    float lt[2];
#pragma unroll 1
    for (int attempt = 0;; ++attempt) {
      BAR();
      const bool redo = row_sums(lt);
      if (__builtin_expect(attempt > 0 || !redo, 1)) break;
      BAR();
    }
    ST(17);
    reduce_o();
    const float linv[2] = {__builtin_amdgcn_rcpf(lt[0]), __builtin_amdgcn_rcpf(lt[1])};
    BAR();
    ST(18);
    u32x4 bf[6][2];
#pragma unroll
    for (int ks = 0; ks < 6; ++ks) { bf[ks][0] = lds_r128(fr[0] + L_FI + ks * 1024); bf[ks][1] = lds_r128(fr[1] + L_FI + ks * 1024); }
    SBAR();
    f32x4 d[3][2];
    gemm(wv, bf, d);
#pragma unroll
    for (int i = 0; i < 3; ++i) {
      const u32x2 y0 = pack4(d[i][0] * linv[0]), y1 = pack4(d[i][1] * linv[1]);
      auto s0 = __builtin_amdgcn_permlane16_swap(y0[0], y1[0], false, false);
      auto s1 = __builtin_amdgcn_permlane16_swap(y0[1], y1[1], false, false);
      const u32x4 y = {s0[0], s1[0], s0[1], s1[1]};
      u32x4 o;
#pragma unroll
      for (int e = 0; e < 4; ++e) o[e] = cvtpk(bflo(x1[i][e]) + bflo(y[e]), bfhi(x1[i][e]) + bfhi(y[e]));
      __builtin_amdgcn_raw_buffer_store_b128(o, __builtin_amdgcn_make_buffer_rsrc((void*)out, 0, 8 * FRAME_B, 0x00020000), (t * 1024 + qb * 32 + tok) * 384 + (2 * (3 * w + i) + hh) * 16, 0, SC1);
    }
    ST(19);
    ST_OUT(ws, 12, 20);
  }
}

__global__ void k_tiny(u64* p) { u64 m_ = ~0ull; for (int q = 0; q < 4; ++q) { u64 v = p[threadIdx.x * 4 + q]; m_ = v < m_ ? v : m_; }
  for (int o_ = 32; o_; o_ >>= 1) { u64 v_ = __shfl_xor(m_, o_); m_ = v_ < m_ ? v_ : m_; }
  const u64 n_ = __builtin_amdgcn_s_memrealtime(); u64 e_ = m_ + 4000; if (e_ > n_ + 5000) e_ = n_; while (__builtin_amdgcn_s_memrealtime() < e_) __builtin_amdgcn_s_sleep(1); }
extern "C" void kernel_launch(void* const* d_in, const int* in_sizes, int n_in, void* d_out, int out_size, void* d_ws, size_t ws_size, hipStream_t stream) {
  (void)hipFuncSetAttribute((const void*)k_timesformer, hipFuncAttributeMaxDynamicSharedMemorySize, LDS_BYTES);
  k_timesformer<<<256, 512, LDS_BYTES, stream>>>((const char*)d_in[0], (const char*)d_in[1], (const char*)d_in[2], (const char*)d_in[3], (const char*)d_in[4],
                                                 (char*)d_ws, (char*)d_out);
  k_tiny<<<1, 64, 0, stream>>>((u64*)((char*)d_ws + WS_T0));
}
```

```cpp
#include <hip/hip_runtime.h>
#include <hip/hip_bf16.h>
#include <type_traits>

typedef short s16x4 __attribute__((ext_vector_type(4)));
typedef __bf16 b16x8 __attribute__((ext_vector_type(8)));
typedef __bf16 b16x2 __attribute__((ext_vector_type(2)));
typedef float f32x2 __attribute__((ext_vector_type(2)));
typedef float f32x4 __attribute__((ext_vector_type(4)));
typedef float f32x16 __attribute__((ext_vector_type(16)));
typedef unsigned u32x2 __attribute__((ext_vector_type(2)));
typedef unsigned u32x4 __attribute__((ext_vector_type(4)));
typedef unsigned long long u64;
#define LAS __attribute__((address_space(3)))
#define DI __device__ __forceinline__
#define SBAR() __builtin_amdgcn_sched_barrier(0)

constexpr int TILE_B = 12288;
constexpr int FRAME_B = 393216;
constexpr unsigned WS_H2 = 0, WS_ST = 8 * FRAME_B, WS_WM = 16 * FRAME_B, WS_FLAG = WS_WM + 2 * 73728, WS_FLAG2 = WS_FLAG + 2048, WS_EPOCH = WS_FLAG2 + 2048, WS_END = WS_EPOCH + 256;
constexpr float QSCALE = 0.07216878364870322f * 1.4426950408889634f;
constexpr int SC1 = 16;
constexpr unsigned POISON = 0xffffffffu;
constexpr unsigned CODE_BYTES = 26624;
constexpr int TILE0_DELAY = 2;
constexpr int WV_DELAY = 64;
constexpr int SPIN_CAP = 1 << 16;

#define ST_DECL
#define ST(k)
#define ST_OUT(ws, k0, k1)

DI float asf(unsigned u) { return __builtin_bit_cast(float, u); }
DI unsigned asu(float f) { return __builtin_bit_cast(unsigned, f); }
DI unsigned cvtpk(float lo, float hi) { f32x2 v = {lo, hi}; return __builtin_bit_cast(unsigned, __builtin_convertvector(v, b16x2)); }
DI float bflo(unsigned u) { return asf(u << 16); }
DI float bfhi(unsigned u) { return asf(u & 0xffff0000u); }
DI f32x16 mfma32(u32x4 a, u32x4 b, f32x16 c) { return __builtin_amdgcn_mfma_f32_32x32x16_bf16(__builtin_bit_cast(b16x8, a), __builtin_bit_cast(b16x8, b), c, 0, 0, 0); }
DI f32x4 mfma16(u32x4 a, u32x4 b, f32x4 c) { return __builtin_amdgcn_mfma_f32_16x16x32_bf16(__builtin_bit_cast(b16x8, a), __builtin_bit_cast(b16x8, b), c, 0, 0, 0); }
DI f32x4 mfma16k16(u32x2 a, u32x2 b, f32x4 c) { return __builtin_amdgcn_mfma_f32_16x16x16bf16_1k(__builtin_bit_cast(s16x4, a), __builtin_bit_cast(s16x4, b), c, 0, 0, 0); }
DI u32x2 pack4(f32x4 v) { u32x2 r = {cvtpk(v[0], v[1]), cvtpk(v[2], v[3])}; return r; }
DI u32x4 lds_r128(unsigned a) { return *(LAS u32x4*)(size_t)a; }
DI f32x4 lds_r128f(unsigned a) { return *(LAS f32x4*)(size_t)a; }
DI u32x2 lds_r64(unsigned a) { return *(LAS u32x2*)(size_t)a; }
DI void lds_w128(unsigned a, u32x4 v) { *(LAS u32x4*)(size_t)a = v; }
DI void lds_w128f(unsigned a, f32x4 v) { *(LAS f32x4*)(size_t)a = v; }
DI void lds_w64(unsigned a, u32x2 v) { *(LAS u32x2*)(size_t)a = v; }
DI void lds_w32(unsigned a, float v) { *(LAS float*)(size_t)a = v; }
DI u32x2 lds_tr(unsigned a) { return __builtin_bit_cast(u32x2, __builtin_amdgcn_ds_read_tr16_b64_v4i16((LAS s16x4*)(size_t)a)); }
DI u32x4 lds_tr2(unsigned a0, unsigned a1) { u32x2 x = lds_tr(a0), y = lds_tr(a1); u32x4 r = {x[0], x[1], y[0], y[1]}; return r; }
#define BAR() asm volatile("s_waitcnt lgkmcnt(0)\n\ts_barrier" ::: "memory")
#define BARV(n) asm volatile("s_waitcnt vmcnt(" #n ") lgkmcnt(0)\n\ts_barrier" ::: "memory")
template <int CTRL> DI float dpp_add(float v) { return v + asf(__builtin_amdgcn_update_dpp(0u, asu(v), CTRL, 0xf, 0xf, true)); }
DI float xor16_add(float v) { auto r = __builtin_amdgcn_permlane16_swap(asu(v), asu(v), false, false); return asf(r[0]) + asf(r[1]); }
DI float xor16_max(float v) { auto r = __builtin_amdgcn_permlane16_swap(asu(v), asu(v), false, false); return fmaxf(asf(r[0]), asf(r[1])); }
DI unsigned umax3(unsigned a, unsigned b, unsigned c) { return max(max(a, b), c); }
template <int N> DI bool poisoned(const u32x4 (&v)[N]) {
  unsigned m = 0;
#pragma unroll
  for (int i = 0; i < N; ++i) { m = umax3(m, v[i][0], v[i][1]); m = umax3(m, v[i][2], v[i][3]); }
  return __any(m == POISON);
}
DI u32x4 unpoison(u32x4 v) { u32x4 r = {min(v[0], POISON - 1), min(v[1], POISON - 1), min(v[2], POISON - 1), min(v[3], POISON - 1)}; return r; }
DI unsigned fi_pos(unsigned r, unsigned c8) {
  return (((r >> 4) * 6 + (c8 >> 2)) * 64 + (c8 & 3) * 16 + ((r & 15) ^ ((c8 & 1) | (((r >> 3) & 1) << 1) | ((r >> 4) << 2)))) * 16;
}
DI void layernorm24(const f32x2 (&v)[12], u32x4 (&o)[3], float& mu, float& var) {
  f32x2 s0 = v[0] + v[1], s1 = v[2] + v[3];
#pragma unroll
  for (int i = 4; i < 12; i += 2) { s0 += v[i]; s1 += v[i + 1]; }
  s0 += s1;
  float s = s0[0] + s0[1];
  s = dpp_add<0x124>(s); s = dpp_add<0x128>(s); s = xor16_add(s);
  mu = s * (1.0f / 192.0f);
  f32x2 d[12];
#pragma unroll
  for (int i = 0; i < 12; ++i) d[i] = v[i] - mu;
  f32x2 q0 = d[0] * d[0], q1 = d[1] * d[1];
#pragma unroll
  for (int i = 2; i < 12; i += 2) { q0 = __builtin_elementwise_fma(d[i], d[i], q0); q1 = __builtin_elementwise_fma(d[i + 1], d[i + 1], q1); }
  q0 += q1;
  float q = q0[0] + q0[1];
  q = dpp_add<0x124>(q); q = dpp_add<0x128>(q); q = xor16_add(q);
  var = q * (1.0f / 192.0f) + 1e-5f;
  const float rs = __builtin_amdgcn_rsqf(var);
#pragma unroll
  for (int i = 0; i < 12; ++i) { const f32x2 n = d[i] * rs; o[i >> 2][i & 3] = cvtpk(n[0], n[1]); }
}
DI f32x2 unpack2(unsigned u) { f32x2 r = {bflo(u), bfhi(u)}; return r; }

constexpr unsigned SLOT = 36864, L_X = 3 * SLOT, L_H = 4 * SLOT, L_N = L_H + TILE_B, L_L = L_N + 1024, L_PT = L_L + 1024, LDS_BYTES = 163840;
constexpr unsigned L_VI = L_X, L_SP = L_X + TILE_B, L_YI = L_X + 20480;
constexpr unsigned L_HQ = 24576, L_Q = L_H, L_FI = L_H;

#define DMA4(v0, v1, v2, v3, sbase, ldsa)                                                                  \
  asm volatile("s_mov_b32 m0, %5\n\ts_nop 0\n\t"                                                            \
               "global_load_lds_dwordx4 %0, %4\n\tglobal_load_lds_dwordx4 %1, %4 offset:1024\n\t"           \
               "global_load_lds_dwordx4 %2, %4 offset:2048\n\tglobal_load_lds_dwordx4 %3, %4 offset:3072"   \
               :: "v"(v0), "v"(v1), "v"(v2), "v"(v3), "s"(sbase), "s"(ldsa) : "memory")
#define DMA1(v0, sbase, ldsa) \
  asm volatile("s_mov_b32 m0, %2\n\ts_nop 0\n\tglobal_load_lds_dwordx4 %0, %1" :: "v"(v0), "s"(sbase), "s"(ldsa) : "memory")
#define DMA9(o, sbase, ldsa) do { DMA4(o[0], o[1], o[2], o[3], sbase, ldsa); DMA4(o[4], o[5], o[6], o[7], sbase, (ldsa) + 4096); DMA1(o[8], sbase, (ldsa) + 8192); } while (0)

template <int OFF> DI void bdma(unsigned voff, u32x4 rs, unsigned soff) { asm volatile("buffer_load_dwordx4 %0, %1, %2 offen offset:%3 sc1 lds" :: "v"(voff), "s"(rs), "s"(soff), "i"(OFF) : "memory"); }
template <int OFF> DI void setm0(unsigned base) { asm volatile("s_add_u32 m0, %0, %1\n\ts_nop 0" :: "s"(base), "i"(OFF) : "memory", "scc"); }
template <int N> DI void waitvm() { asm volatile("s_waitcnt vmcnt(%0)" :: "i"(N) : "memory"); }
#define IC(x) std::integral_constant<int, (x)>{}
#define OPAQUE(v) asm volatile("" : "+v"(v))
template <class R> DI void st128(u32x4 v, R rs, unsigned voff, unsigned soff) { __builtin_amdgcn_raw_buffer_store_b128(v, rs, voff, soff, SC1); asm volatile("s_nop 1" :: "v"(v)); }
DI bool tags_ok(u32x4 f, u32x2 tag) { return f[0] == tag[0] && f[1] == tag[1] && f[2] == tag[0] && f[3] == tag[1]; }

__global__ __launch_bounds__(512, 2) void k_timesformer(const char* __restrict__ x, const char* __restrict__ wqkv_t, const char* __restrict__ wo_t,
                                                        const char* __restrict__ wqkv_s, const char* __restrict__ wo_s, char* __restrict__ ws, char* __restrict__ out) {
  extern __shared__ __attribute__((aligned(16))) char smem[];
  u64 pc;
  asm volatile("s_getpc_b64 %0" : "=s"(pc));
  const unsigned lds0 = (unsigned)(size_t)(LAS char*)smem;
  const unsigned b = blockIdx.x;
  unsigned w8 = __builtin_amdgcn_readfirstlane(threadIdx.x >> 6);
  unsigned l = threadIdx.x & 63;
  const unsigned t = b & 7, qb = b >> 3;
  const auto rsWS = __builtin_amdgcn_make_buffer_rsrc((void*)ws, 0, WS_END, 0x00020000);
  ST_DECL;
  ST(0);
  const bool merger = b < 144;
  const unsigned mtau = (b >> 1) / 6, mrow = ((b >> 1) - mtau * 6) * 32 + (b & 1) * 16;
  auto rows16 = [&](const char* src, const unsigned dst) {
#pragma unroll
    for (int i = 0; i < 3; ++i) {
      const unsigned pos = i * 64 + l, row = (pos * 2731) >> 16, o = row * 1152 + (((pos - row * 24) ^ row) * 16);
      DMA1(o, src, dst + i * 1024); DMA1(o, src + 8 * 1152, dst + (3 + i) * 1024);
    }
  };
  unsigned qo[9], d0, d1;
  if (w8 < 4) {
#pragma unroll
    for (int i = 0; i < 3; ++i) { const unsigned p = i * 64 + l; DMA1(p * 16 + (p >= 96 ? FRAME_B - 1536 : 0), x + w8 * 2 * FRAME_B + b * 1536, lds0 + L_YI + w8 * 3072 + i * 1024); }
  } else if (w8 < 6) {
    const unsigned qa = 18432 * (w8 & 1) + l * 16, qc = qa ^ 32;
    qo[0] = qa; qo[1] = qa; qo[2] = l >= 16 ? qc : qa; qo[3] = qc; qo[5] = qa + 4096; qo[7] = qc + 4096;
    qo[4] = l < 32 ? qo[7] : qo[5]; qo[6] = l >= 48 ? qo[7] : qo[5]; qo[8] = qc + 8192;
    DMA9(qo, wqkv_t, lds0 + 18432 * (w8 & 1));
  } else if (w8 == 7) {
    const unsigned line = qb * 72 + l, line2 = qb * 72 + 64 + (l & 7);
    const char* p0 = line < 1728 ? wqkv_t + line * 128 : wo_t + (line - 1728) * 128;
    const char* p1 = line2 < 1728 ? wqkv_t + line2 * 128 : wo_t + (line2 - 1728) * 128;
    if (l >= 8) p1 = (const char*)(pc + min((qb * 8 + (l & 7)) * 128, CODE_BYTES));
    asm volatile("global_load_dword %0, %2, off\n\tglobal_load_dword %1, %3, off" : "=&v"(d0), "=&v"(d1) : "v"(p0), "v"(p1) : "memory");
  }
  asm volatile("" : "+s"(w8));
  const unsigned w = w8 & 3;
  auto get_tag = [&]() {
    u64 e;
    asm volatile("s_load_dwordx2 %0, %1, 0x0\n\ts_waitcnt lgkmcnt(0)" : "=s"(e) : "s"(ws + WS_EPOCH) : "memory");
    e += 1;
    return u32x2{(unsigned)e, (unsigned)(e >> 32)};
  };
#define LANE_IDS const unsigned i16 = l & 15, g = l >> 4, r32 = l & 31, hh = l >> 5, tq = i16 >> 2, tp = l & 3
#define FRAG_POS unsigned fr[2], dpos[3][2];                                                                        \
  _Pragma("unroll") for (int hf = 0; hf < 2; ++hf) { fr[hf] = lds0 + fi_pos(16 * hf + i16, g);                     \
    _Pragma("unroll") for (int i = 0; i < 3; ++i) dpos[i][hf] = lds0 + fi_pos(16 * hf + i16, 2 * (3 * w + i) + (g >> 1)) + 8 * (g & 1); }
  auto low_sum = [&](const f32x4 a, const f32x4 c, float (&lt)[2]) {
    lt[0] = (a[0] + a[2]) + (c[0] + c[2]);
    lt[1] = (a[1] + a[3]) + (c[1] + c[3]);
    return (bool)__any(lt[0] < 8.7e-19f || lt[1] < 8.7e-19f);
  };

  if (w8 < 4) {
    const unsigned nl = l & 3, j = (l >> 2) & 7, tt = 2 * w + (l >> 5), r = nl * 8 + tt;
    const unsigned so = tt * FRAME_B + (j >> 1) * 1024 + ((t ^ ((j >> 1) & 1)) * 128) + (((j & 1) ^ (t >> 2)) * 64) + nl * 16, sso = qb * TILE_B;
    LANE_IDS;
    FRAG_POS;
    const unsigned phi = ((tq >> 1) & 1) | ((g & 1) << 1);
    unsigned tcol[3], rpos[3];
#pragma unroll
    for (int i = 0; i < 3; ++i) { tcol[i] = (((3 * w + i) ^ phi) * 2 + (tp >> 1)) * 16 + 8 * (tp & 1); rpos[i] = lds0 + fi_pos(r, j + 8 * i); }
    const u32x2 tag = get_tag();
    waitvm<0>();
    f32x2 xv[12];
    {
      u32x4 h[3], xr[3];
#pragma unroll
      for (int i = 0; i < 3; ++i) xr[i] = lds_r128(lds0 + L_YI + tt * 1536 + nl * 384 + j * 16 + i * 128);
#pragma unroll
      for (int i = 0; i < 12; ++i) xv[i] = unpack2(xr[i >> 2][i & 3]);
      float mu, var;
      layernorm24(xv, h, mu, var);
#pragma unroll
      for (int i = 0; i < 3; ++i) lds_w128(rpos[i] + L_H, h[i]);
    }
    f32x4 acc[3][3][2];
#pragma unroll
    for (int m = 0; m < 3; ++m)
#pragma unroll
      for (int i = 0; i < 3; ++i) { acc[m][i][0] = f32x4{0, 0, 0, 0}; acc[m][i][1] = f32x4{0, 0, 0, 0}; }
    u32x4 hf0, hf1;
#pragma unroll
    for (int ks = 0; ks < 6; ++ks) {
      BAR();
      if (ks == 0) { ST(1); hf0 = lds_r128(fr[0] + L_H); hf1 = lds_r128(fr[1] + L_H); }
      const unsigned base = lds0 + (ks % 3) * SLOT + (8 * g + tq) * 1152;
      u32x4 a[9];
#pragma unroll
      for (int m = 0; m < 3; ++m)
#pragma unroll
        for (int i = 0; i < 3; ++i) a[m * 3 + i] = lds_tr2(base + tcol[i] + m * 384, base + tcol[i] + m * 384 + 4608);
      const u32x4 h0 = hf0, h1 = hf1;
      if (ks < 5) { hf0 = lds_r128(fr[0] + L_H + (ks + 1) * 1024); hf1 = lds_r128(fr[1] + L_H + (ks + 1) * 1024); }
#pragma unroll
      for (int m = 0; m < 3; ++m)
#pragma unroll
        for (int i = 0; i < 3; ++i) {
          acc[m][i][0] = mfma16(a[m * 3 + i], h0, acc[m][i][0]);
          acc[m][i][1] = mfma16(a[m * 3 + i], h1, acc[m][i][1]);
        }
      if (ks == 0) __builtin_amdgcn_sched_group_barrier(0x100, 8, 0); else __builtin_amdgcn_sched_group_barrier(0x100, 6, 0);
#pragma unroll
      for (int q = 0; q < 7; ++q) { __builtin_amdgcn_sched_group_barrier(0x8, 2, 0); __builtin_amdgcn_sched_group_barrier(0x100, 2, 0); }
      __builtin_amdgcn_sched_group_barrier(0x8, 4, 0);
      SBAR();
    }
    ST(2);
    u32x2 vpk[3][2];
    f32x4 spart[2];
#pragma unroll
    for (int hf = 0; hf < 2; ++hf) {
      spart[hf] = f32x4{0, 0, 0, 0};
#pragma unroll
      for (int i = 0; i < 3; ++i) { spart[hf] = mfma16k16(pack4(acc[1][i][hf]), pack4(acc[0][i][hf]), spart[hf]); vpk[i][hf] = pack4(acc[2][i][hf]); }
    }
    unsigned wa[3], vfa[2], ypos[3][2], yr[3];
#pragma unroll
    for (int i = 0; i < 3; ++i) {
      wa[i] = lds0 + (8 * g + tq) * 384 + tcol[i]; yr[i] = rpos[i] + L_YI; OPAQUE(wa[i]); OPAQUE(yr[i]);
#pragma unroll
      for (int hf = 0; hf < 2; ++hf) { ypos[i][hf] = dpos[i][hf] + L_VI; OPAQUE(ypos[i][hf]); }
    }
    vfa[0] = fr[0] + L_VI; vfa[1] = fr[1] + L_VI; OPAQUE(vfa[0]); OPAQUE(vfa[1]);
    unsigned spa = lds0 + L_SP + (w * 128 + l) * 16, pta = lds0 + L_PT + l * 8;
    OPAQUE(spa); OPAQUE(pta);
    u32x4 bw[6][3];
    auto rdw = [&](const int k) {
#pragma unroll
      for (int i = 0; i < 3; ++i) bw[k][i] = lds_tr2(wa[i] + k * 12288, wa[i] + k * 12288 + 1536);
    };
    lds_w128f(spa, spart[0]); lds_w128f(spa + 1024, spart[1]);
    ST(3);
    BAR();
    rdw(0); rdw(1); rdw(2);
    SBAR();
    ST(4);
#pragma unroll
    for (int hf = 0; hf < 2; ++hf)
#pragma unroll
      for (int i = 0; i < 3; ++i) lds_w64(ypos[i][hf], vpk[i][hf]);
    ST(5);
    BAR();
    ST(6);
    {
      f32x4 vp[3][2];
      u32x4 vf[6][2];
      u32x2 pt[2];
      auto rdv = [&](const int k) { vf[k][0] = lds_r128(vfa[0] + k * 1024); vf[k][1] = lds_r128(vfa[1] + k * 1024); };
      auto mm = [&](const int k) {
#pragma unroll
        for (int i = 0; i < 3; ++i) {
          vp[i][0] = mfma16(vf[k][0], bw[k][i], k ? vp[i][0] : f32x4{0, 0, 0, 0});
          vp[i][1] = mfma16(vf[k][1], bw[k][i], k ? vp[i][1] : f32x4{0, 0, 0, 0});
        }
      };
#define MIX6() _Pragma("unroll") for (int q_ = 0; q_ < 6; ++q_) { __builtin_amdgcn_sched_group_barrier(0x8, 1, 0); __builtin_amdgcn_sched_group_barrier(0x100, 2, 0); } SBAR()
      rdv(0); rdv(1); rdv(2);
      SBAR();
      mm(0); rdw(3); rdv(3); MIX6();
      mm(1); rdw(4); rdv(4); MIX6();
      mm(2); rdw(5); rdv(5); MIX6();
      mm(3);
      SBAR();
      ST(7);
      asm volatile("s_barrier" ::: "memory");
      pt[0] = lds_r64(pta); pt[1] = lds_r64(pta + 512);
      SBAR();
      mm(4); mm(5);
      ST(8);
#pragma unroll
      for (int i = 0; i < 3; ++i)
#pragma unroll
        for (int hf = 0; hf < 2; ++hf) lds_w64(ypos[i][hf] + (L_YI - L_VI), pack4(mfma16k16(pack4(vp[i][hf]), pt[hf], f32x4{0, 0, 0, 0})));
    }
    ST(9);
    BAR();
    {
      u32x4 y[3], h[3];
#pragma unroll
      for (int i = 0; i < 3; ++i) y[i] = lds_r128(yr[i]);
#pragma unroll
      for (int i = 0; i < 12; ++i) xv[i] += unpack2(y[i >> 2][i & 3]);
      float mu, var;
      layernorm24(xv, h, mu, var);
#pragma unroll
      for (int i = 0; i < 3; ++i) st128(h[i], rsWS, so, WS_H2 + sso + i * 4096);
      const u32x2 stat = {asu(mu), asu(var)};
      __builtin_amdgcn_raw_buffer_store_b64(stat, rsWS, j == 0 ? (tt * 1024 + nl) * 8 : WS_END, WS_ST + b * 32, SC1);
    }
    ST(10);
    const unsigned ring = lds0 + w * SLOT;
    const unsigned kr0 = ring + (r32 >> 2) * 128 + (hh ^ (r32 >> 4)) * 64 + (r32 & 3) * 16, kr1 = kr0 ^ 128;
    const unsigned vr0 = ring + (g & 1) * 1024 + (hh ^ (g & 1)) * 128 + (tp >> 1) * 64 + tq * 16 + 8 * (tp & 1), vr1 = (vr0 ^ 64) + 512;
    const unsigned lo[3] = {l * 16, l * 16 + 4096, l * 16 + 8192};
    const u64 hbu = (u64)(ws + WS_H2 + (size_t)t * FRAME_B);
    const u32x4 rs = {(unsigned)hbu, (unsigned)(hbu >> 32) & 0xffffu, (unsigned)FRAME_B, 0x00020000u};
    unsigned toff[8];
#pragma unroll
    for (int i = 0; i < 8; ++i) toff[i] = (8 * w + ((i + qb) & 7)) * TILE_B;
    u32x4 sel;
    { const unsigned one = (tp == (g & 1)) ? 0x3f803f80u : 0u; sel = u32x4{one, one, one, one}; }
    auto piece = [&](auto Tc, auto Pc) {
      constexpr int T = decltype(Tc)::value, p = decltype(Pc)::value;
      if constexpr (T < 8) {
        if constexpr (p % 4 == 0) setm0<(T % 3) * TILE_B + p * 1024>(ring);
        bdma<(p % 4) * 1024>(lo[p / 4], rs, toff[T]);
      }
    };
#define P6(T, a) piece(IC(T), IC(a)); piece(IC(T), IC(a + 1)); piece(IC(T), IC(a + 2)); piece(IC(T), IC(a + 3)); piece(IC(T), IC(a + 4)); piece(IC(T), IC(a + 5));
    auto read_valid = [&](u32x4 (&KK)[12], const unsigned dst, unsigned off) {
      for (int sp = 0; sp < SPIN_CAP; ++sp) {
#pragma unroll
        for (int k = 0; k < 12; ++k) KK[k] = lds_r128(((k & 1) ? kr1 : kr0) + dst + k * 1024);
        if (__builtin_expect(!poisoned(KK), 1)) break;
#pragma unroll 1
        for (int k = 0; k < 12; ++k) { setm0<0>(ring + dst + k * 1024); bdma<0>(lo[0], rs, off + k * 1024); }
        waitvm<0>();
      }
    };
    unsigned qa = lds0 + L_Q + lo[0], na = lds0 + L_N + r32 * 16;
    OPAQUE(qa); OPAQUE(na);
    BARV(0);
    __builtin_amdgcn_s_sleep(TILE0_DELAY);
    P6(0, 0) P6(0, 6)
    if (w == 0 && l == 0) __builtin_amdgcn_raw_buffer_store_b64(tag, rsWS, 0, WS_FLAG2 + b * 8, SC1);
    ST(11);
    BAR();
    u32x4 K0[12];
    if (w == 0) waitvm<1>(); else waitvm<0>();
    ST(12);
    read_valid(K0, 0, toff[0]);
    ST(13);
    BAR();
    piece(IC(1), IC(0)); piece(IC(1), IC(1)); piece(IC(1), IC(2)); piece(IC(1), IC(3));
    BAR();
    ST(14);
    u32x4 Q[12];
    float mref;
    {
      const f32x4 n0 = lds_r128f(na), n1 = lds_r128f(na + 512);
#pragma unroll
      for (int s = 0; s < 12; ++s) Q[s] = lds_r128(qa + s * 1024);
      SBAR();
      const f32x4 s = n0 + n1;
      mref = __builtin_amdgcn_sqrtf(((s[0] + s[1]) + (s[2] + s[3])) * (192.0f * 1.02f));
    }
    constexpr int KW = 4, VW = 4;
    bool safe = false, exact = false;
#pragma unroll 1
    for (;;) {
      f32x16 S[2], O[6], negm;
      u32x4 K[12], V[12], P[2][2];
      f32x4 lsum = {0, 0, 0, 0};
      float pe[2][2];
#pragma unroll
      for (int i = 0; i < 16; ++i) negm[i] = -mref;
#define RK(k, T) K[k] = lds_r128((((k) & 1) ? kr1 : kr0) + ((T) % 3) * TILE_B + (k) * 1024);
#define RV(u, T) { const unsigned a_ = (((u) & 1) ? vr1 : vr0) + ((T) % 3) * TILE_B + ((u) >> 1) * 2048; V[u] = lds_tr2(a_, a_ + 256); }
      auto sm = [&](auto Tc, auto Hc, auto Gc) {
        constexpr int T = decltype(Tc)::value, H = decltype(Hc)::value, G = decltype(Gc)::value;
        if constexpr (G < 8) pe[(G >> 1) & 1][G & 1] = __builtin_amdgcn_exp2f(S[T & 1][8 * H + G]);
        if constexpr (G >= 2 && G <= 8 && G % 2 == 0) { constexpr int q = G / 2 - 1; P[T & 1][H][q] = cvtpk(pe[q & 1][0], pe[q & 1][1]); }
        if constexpr (G == 10) lsum = mfma16(sel, P[T & 1][H], lsum);
      };
      auto put_o = [&](const int un) {
        u32x4 pk;
#pragma unroll
        for (int e = 0; e < 4; ++e) pk[e] = cvtpk(O[un >> 1][8 * (un & 1) + 2 * e], O[un >> 1][8 * (un & 1) + 2 * e + 1]);
        lds_w128(ring + lo[0] + un * 1024, pk);
      };
      auto qkphase = [&](auto Ic) {
        constexpr int I = decltype(Ic)::value;
        auto step = [&](auto Kc) {
          constexpr int k = decltype(Kc)::value;
          if constexpr (I + 1 < 8) {
            S[(I + 1) & 1] = mfma32(I < 0 ? K0[k] : K[k], Q[k], k == 0 ? negm : S[(I + 1) & 1]);
            if constexpr (k + KW < 12 && I >= 0) RK(k + KW, I + 1)
          }
          if constexpr (I < 0 && k < 8) piece(IC(1), IC(4 + k));
          if constexpr (I >= 0) { if constexpr (k & 1) piece(IC(I + 2), IC(6 + k / 2)); if constexpr (k < 11) sm(IC(I), IC(1), IC(k)); }
          if constexpr (I >= 0 && I < 7 && k >= 12 - VW) RV(k - (12 - VW), I)
          if constexpr (I == 7 && k < VW) RV(k, I)
          SBAR();
        };
        step(IC(0)); step(IC(1)); step(IC(2)); step(IC(3)); step(IC(4)); step(IC(5)); step(IC(6)); step(IC(7)); step(IC(8)); step(IC(9)); step(IC(10)); step(IC(11));
      };
      auto pvphase = [&](auto Ic) {
        constexpr int I = decltype(Ic)::value;
        auto step = [&](auto Uc) {
          constexpr int u = decltype(Uc)::value;
          if constexpr (I >= 0) {
            f32x16 z;
#pragma unroll
            for (int e = 0; e < 16; ++e) z[e] = 0.f;
            O[u >> 1] = mfma32(V[u], P[I & 1][u & 1], (I == 0 && !(u & 1)) ? z : O[u >> 1]);
            if constexpr (u & 1) piece(IC(I + 3), IC(u / 2));
            if constexpr (u + VW < 12) RV(u + VW, I)
          }
          if constexpr (I + 1 < 8 && u >= 1) sm(IC(I + 1), IC(0), IC(u - 1));
          if constexpr (I < 0 && (u & 1)) piece(IC(2), IC(u / 2));
          if constexpr (I + 2 < 8) {
            if constexpr (u == 12 - KW) waitvm<(I < 0 ? 12 : I + 3 < 8 ? 6 + (12 - KW) / 2 : 6)>();
            if constexpr (u >= 12 - KW) RK(u - (12 - KW), I + 2)
            if constexpr (u == 11) waitvm<(I + 3 < 8 ? 6 : 0)>();
          }
          if constexpr (I == 7 && u >= 3) {
            put_o(u - 3);
          }
          SBAR();
        };
        step(IC(0)); step(IC(1)); step(IC(2)); step(IC(3)); step(IC(4)); step(IC(5)); step(IC(6)); step(IC(7)); step(IC(8)); step(IC(9)); step(IC(10)); step(IC(11));
      };
#define STALE() (__builtin_expect(!safe && __any(lsum[0] != lsum[0]), 0))
      bool done = false;
      do {
        qkphase(IC(-1));
        if (!safe) asm volatile("s_barrier" ::: "memory");
        ST(15);
        pvphase(IC(-1));
        ST(16);
        qkphase(IC(0)); pvphase(IC(0)); qkphase(IC(1)); pvphase(IC(1));
        if (STALE()) break;
        qkphase(IC(2)); pvphase(IC(2)); qkphase(IC(3)); pvphase(IC(3));
        if (STALE()) break;
        qkphase(IC(4)); pvphase(IC(4)); qkphase(IC(5)); pvphase(IC(5)); qkphase(IC(6)); pvphase(IC(6)); qkphase(IC(7)); pvphase(IC(7));
        if (STALE()) break;
        if (l < 16) { const u32x2 ls = {asu(lsum[0]), asu(lsum[1])}; lds_w64(lds0 + L_L + (l * 4 + w) * 8, ls); }
        put_o(9); put_o(10); put_o(11);
        done = true;
      } while (false);
#pragma unroll
      for (int k = 0; k < 12; ++k) asm volatile("" : "=v"(K0[k]));
      if (__builtin_expect(done, 1)) {
        ST(17);
        BAR();
        ST(18);
        float lt[2];
        if (__builtin_expect(!low_sum(lds_r128f(lds0 + L_L + i16 * 32), lds_r128f(lds0 + L_L + i16 * 32 + 16), lt) || exact, 1)) break;
        BAR();
        float mx = -3.0e38f;
#pragma unroll 1
        for (unsigned i = 0; i < 8; ++i) {
#pragma unroll 1
          for (int k = 0; k < 12; ++k) { setm0<0>(ring + k * 1024); bdma<0>(lo[0], rs, (8 * w + i) * TILE_B + k * 1024); }
          waitvm<0>();
          f32x16 s;
#pragma unroll
          for (int e = 0; e < 16; ++e) s[e] = 0.f;
#pragma unroll
          for (int k = 0; k < 12; ++k) s = mfma32(lds_r128(((k & 1) ? kr1 : kr0) + k * 1024), Q[k], s);
#pragma unroll
          for (int e = 0; e < 16; ++e) mx = fmaxf(mx, s[e]);
        }
        lds_w32(lds0 + L_N + (r32 * 8 + w * 2 + hh) * 4, mx);
        BAR();
        const f32x4 c = lds_r128f(lds0 + L_N + r32 * 32), d = lds_r128f(lds0 + L_N + r32 * 32 + 16);
        mref = fmaxf(fmaxf(fmaxf(c[0], c[1]), fmaxf(c[2], c[3])), fmaxf(fmaxf(d[0], d[1]), fmaxf(d[2], d[3])));
        exact = true;
      } else {
        for (int sp = 0; sp < SPIN_CAP; ++sp) {
          asm volatile("" ::: "memory");
          const u32x2 f = __builtin_amdgcn_raw_buffer_load_b64(rsWS, l * 8, WS_FLAG2 + w * 512, SC1);
          if (__all(f[0] == tag[0] && f[1] == tag[1])) break;
        }
      }
      safe = true;
      asm volatile("s_waitcnt vmcnt(0) lgkmcnt(0)" ::: "memory");
      P6(0, 0) P6(0, 6)
      waitvm<0>();
      piece(IC(1), IC(0)); piece(IC(1), IC(1)); piece(IC(1), IC(2)); piece(IC(1), IC(3));
#pragma unroll
      for (int k = 0; k < 12; ++k) K0[k] = lds_r128(((k & 1) ? kr1 : kr0) + k * 1024);
    }
    ST(19);
    BAR();
    ST_OUT(ws, 0, 12);
  } else {
    auto merge16 = [&](const u32x4 (&a)[6], const u32x4 (&bb)[6], const float sc, const unsigned where) {
      f32x4 d = {0, 0, 0, 0};
#pragma unroll
      for (int kk = 0; kk < 6; ++kk) d = mfma16(a[kk], bb[kk], d);
      __builtin_amdgcn_raw_buffer_store_b64(pack4(d * sc), rsWS, ((l >> 5) * 16 + (l & 15)) * 16 + ((l >> 4) & 1) * 8, where + b * 512, SC1);
    };
    auto rd16 = [&](u32x4 (&a)[6], const unsigned at) {
#pragma unroll
      for (int kk = 0; kk < 6; ++kk) a[kk] = lds_r128(lds0 + at + (l & 15) * 384 + (((4 * kk + (l >> 4)) ^ (l & 7)) * 16));
    };
    u32x2 tag7 = {0, 0};
    if (w < 2) {
      const unsigned ldsw = lds0 + 18432 * w;
      unsigned qp[9];
#pragma unroll
      for (int i = 0; i < 9; ++i) qp[i] = (qo[i] ^ 64) + 9216;
      DMA9(qp, wqkv_t, ldsw + 9216);
      ST(1);
      unsigned slot = SLOT;
      u32x2 tag;
#pragma unroll 1
      for (int c = 1; c < 6; ++c) {
        DMA9(qo, wqkv_t + c * SLOT, ldsw + slot); DMA9(qp, wqkv_t + c * SLOT, ldsw + slot + 9216);
        slot = slot == 2 * SLOT ? 0 : slot + SLOT;
        if (c == 1) tag = get_tag();
        if (c == 5) {
#pragma unroll
          for (int i = 0; i < 9; ++i) {
            const unsigned row = ((8 * i + (l >> 3)) * 171) >> 9;
            qo[i] = ((18432 * w + l * 16) ^ (((row & 2) | ((row & 8) >> 1)) << 4)) + (i >> 2) * 4096;
            qp[i] = (qo[i] ^ 64) + 9216;
          }
        }
        BARV(18);
      }
      DMA9(qo, wo_t, ldsw); DMA9(qp, wo_t, ldsw + 9216);
      BARV(18);
      DMA9(qo, wo_t + SLOT, ldsw + SLOT); DMA9(qp, wo_t + SLOT, ldsw + SLOT + 9216);
      ST(3);
      u32x4 f = __builtin_amdgcn_raw_buffer_load_b128(rsWS, l * 16, WS_FLAG + w * 1024, SC1);
      BARV(19);
      BARV(1);
      for (int sp = 0; sp < SPIN_CAP && !__all(tags_ok(f, tag)); ++sp) {
        asm volatile("" ::: "memory");
        f = __builtin_amdgcn_raw_buffer_load_b128(rsWS, l * 16, WS_FLAG + w * 1024, SC1);
      }
    } else if (w == 2) {
      if (merger) { rows16(wqkv_s + mrow * 1152, lds0 + L_X); rows16(wqkv_s + 16 * mtau * 1152 + 384, lds0 + L_X + 6144); }
      ST(1);
      BAR();
      BAR();
      u32x4 a[6], bb[6];
      waitvm<0>();
      if (merger) { rd16(a, L_X); rd16(bb, L_X + 6144); }
      ST(2);
      BAR();
      BAR();
      if (merger) merge16(a, bb, QSCALE, WS_WM);
      BAR();
      BAR();
    } else {
      const u32x4 pz = {POISON, POISON, POISON, POISON};
#pragma unroll
      for (int i = 0; i < 12; ++i) __builtin_amdgcn_raw_buffer_store_b128(pz, rsWS, l * 16 + (i & 3) * 1024, WS_H2 + t * FRAME_B + qb * TILE_B + (i >> 2) * 4096, SC1);
      __builtin_amdgcn_raw_buffer_store_b128(pz, rsWS, merger ? (l >> 5) * 73728 + (l & 31) * 16 : WS_END, WS_WM + b * 512, SC1);
      __builtin_amdgcn_raw_buffer_store_b32(POISON, rsWS, l * 4, WS_ST + (t * 1024 + qb * 32) * 8, SC1);
      asm volatile("s_nop 1" :: "v"(pz));
      tag7 = get_tag();
      ST(1);
      BAR(); BAR(); BAR();
      waitvm<0>();
      asm volatile("" :: "v"(d0), "v"(d1));
      if (l == 0) __builtin_amdgcn_raw_buffer_store_b64(tag7, rsWS, 0, WS_FLAG + b * 8, SC1);
      ST(2);
      BAR(); BAR(); BAR();
    }
    if (w >= 2) {
      const unsigned i16 = l & 15, g = l >> 4;
      unsigned a = lds0 + L_SP + ((w - 2) * 64 + l) * 16, pa = lds0 + L_PT + (w - 2) * 512 + l * 8;
      OPAQUE(a); OPAQUE(pa);
      __builtin_amdgcn_s_setprio(3);
      BAR();
      ST(4);
      const f32x4 s = (lds_r128f(a) + lds_r128f(a + 2048)) + (lds_r128f(a + 4096) + lds_r128f(a + 6144));
      f32x4 e = (s - xor16_max(fmaxf(fmaxf(s[0], s[1]), fmaxf(s[2], s[3])))) * QSCALE;
      OPAQUE(e);
      BAR();
#pragma unroll
      for (int k = 0; k < 4; ++k) e[k] = __builtin_amdgcn_exp2f(e[k]);
      const float sum = xor16_add((e[0] + e[1]) + (e[2] + e[3]));
      const float inv = ((g >> 1) == (i16 >> 3)) ? __builtin_amdgcn_rcpf(sum) : 0.f;
      lds_w64(pa, pack4(e * inv));
      __builtin_amdgcn_s_setprio(0);
    }
    ST(5);
    BAR();
    if (b == 0 && w == 3 && l == 0) __builtin_amdgcn_raw_buffer_store_b64(tag7, rsWS, 0, WS_EPOCH, SC1);
    OPAQUE(l);
    LANE_IDS;
    FRAG_POS;
    u32x4 wq[18], wv[18];
    auto ldw = [&](u32x4 (&W)[18], const int m, const int i0, const int i1) {
#pragma unroll
      for (int i = i0; i < i1; ++i) W[i] = __builtin_amdgcn_raw_buffer_load_b128(rsWS, l * 16 + (i & 3) * 1024, WS_WM + m * 73728 + w * 18432 + (i >> 2) * 4096, SC1);
    };
    ldw(wq, 0, 0, 8);
    BAR();
    ldw(wq, 0, 8, 18);
    unsigned hqa[2][2], qw[3], own[3];
#pragma unroll
    for (int hf = 0; hf < 2; ++hf) {
      const unsigned in = lds0 + L_HQ + ((4 * hf + tq) ^ hh) * 128 + ((g & 1) ^ hf) * 64 + tp * 16;
      hqa[hf][0] = in + hh * 1024; hqa[hf][1] = in + 2048 + hh * (SLOT - 2048);
      OPAQUE(hqa[hf][0]); OPAQUE(hqa[hf][1]);
    }
    const unsigned tok = 16 * (g & 1) + i16;
    unsigned nw = lds0 + L_N + l * 16 + w * 4;
    OPAQUE(nw);
#pragma unroll
    for (int i = 0; i < 3; ++i) {
      qw[i] = lds0 + L_Q + ((3 * w + i) * 64 + (g >> 1) * 32 + i16) * 16 + 8 * (g & 1);
      own[i] = lds0 + w * SLOT + L_HQ + i * 1024 + ((tok >> 2) ^ ((w + i) & 1)) * 128 + (hh ^ (g & 1)) * 64 + tp * 16;
      OPAQUE(qw[i]); OPAQUE(own[i]);
    }
    ST(6);
    BAR();
    ST(7);
    u32x4 hq[3];
    {
      const u64 hbu = (u64)(ws + WS_H2 + (size_t)t * FRAME_B);
      const u32x4 rs = {(unsigned)hbu, (unsigned)(hbu >> 32) & 0xffffu, (unsigned)FRAME_B, 0x00020000u};
      for (int sp = 0; sp < SPIN_CAP; ++sp) {
        setm0<L_HQ>(lds0 + w * SLOT);
        bdma<0>(l * 16, rs, qb * TILE_B + w * 3072); bdma<1024>(l * 16, rs, qb * TILE_B + w * 3072); bdma<2048>(l * 16, rs, qb * TILE_B + w * 3072);
        waitvm<0>();
#pragma unroll
        for (int i = 0; i < 3; ++i) hq[i] = lds_r128(own[i]);
        if (!poisoned(hq)) break;
      }
    }
    ST(8);
    BAR();
    ST(9);
    auto gemm = [&](const u32x4 (&wf)[18], const u32x4 (&bf)[6][2], f32x4 (&d)[3][2]) {
#pragma unroll
      for (int i = 0; i < 3; ++i) { d[i][0] = f32x4{0, 0, 0, 0}; d[i][1] = f32x4{0, 0, 0, 0}; }
#pragma unroll
      for (int ks = 0; ks < 6; ++ks)
#pragma unroll
        for (int i = 0; i < 3; ++i) { d[i][0] = mfma16(wf[i * 6 + ks], bf[ks][0], d[i][0]); d[i][1] = mfma16(wf[i * 6 + ks], bf[ks][1], d[i][1]); }
    };
    {
      u32x4 bf[6][2];
#pragma unroll
      for (int ks = 0; ks < 6; ++ks)
#pragma unroll
        for (int hf = 0; hf < 2; ++hf) {
          if (ks == 3) { hqa[hf][0] += 2 * SLOT; hqa[hf][1] += 2 * SLOT; }
          bf[ks][hf] = lds_r128(ks % 3 == 1 ? hqa[hf][1] : hqa[hf][0] + (ks % 3 == 2 ? SLOT + 1024 : 0));
        }
      SBAR();
      f32x4 d[3][2];
      gemm(wq, bf, d);
      SBAR();
      ST(10);
      asm volatile("s_barrier" ::: "memory");
#pragma unroll 1
      for (int sp = 0;; ++sp) {
        float n2[2];
#pragma unroll
        for (int hf = 0; hf < 2; ++hf) {
          f32x2 n = {0.f, 0.f};
#pragma unroll
          for (int i = 0; i < 3; ++i) {
            lds_w64(qw[i] + hf * 256, pack4(d[i][hf]));
            const f32x2 a = {d[i][hf][0], d[i][hf][1]}, c = {d[i][hf][2], d[i][hf][3]};
            n = __builtin_elementwise_fma(a, a, n); n = __builtin_elementwise_fma(c, c, n);
          }
          n2[hf] = n[0] + n[1];
        }
        auto rr = __builtin_amdgcn_permlane16_swap(asu(n2[0]), asu(n2[1]), false, false);
        const float nn = asf(rr[0]) + asf(rr[1]);
        lds_w32(nw, nn);
        if (__builtin_expect(!__any(nn != nn), 1) || sp >= SPIN_CAP || !poisoned(wq)) break;
        asm volatile("" ::: "memory");
        ldw(wq, 0, 0, 18);
        gemm(wq, bf, d);
      }
    }
    ST(11);
    BAR();
    BAR();
    if (merger && w == 2) {
      rows16(wqkv_s + mrow * 1152 + 768, lds0 + L_H);
#pragma unroll
      for (int i = 0; i < 6; ++i) DMA1((l >> 1) * 384 + (l & 1) * 16, wo_s + i * 12288 + mtau * 32, lds0 + L_H + 6144 + i * 1024);
      waitvm<0>();
      u32x4 a[6], bb[6];
      rd16(a, L_H);
#pragma unroll
      for (int kk = 0; kk < 6; ++kk) { const unsigned at = lds0 + L_H + 6144 + (32 * kk + 8 * g + tq) * 32 + tp * 8; bb[kk] = lds_tr2(at, at + 128); }
      merge16(a, bb, 1.0f, WS_WM + 73728);
      __builtin_amdgcn_s_sleep(WV_DELAY / 2);
    } else __builtin_amdgcn_s_sleep(WV_DELAY);
    for (int sp = 0; sp < SPIN_CAP; ++sp) {
      asm volatile("" ::: "memory");
      ldw(wv, 1, 0, 18);
      if (!poisoned(wv)) break;
    }
    f32x2 x1[12];
    {
      u32x2 st;
      for (int sp = 0; sp < SPIN_CAP; ++sp) {
        asm volatile("" ::: "memory");
        st = __builtin_amdgcn_raw_buffer_load_b64(rsWS, tok * 8, WS_ST + (t * 1024 + qb * 32) * 8, SC1);
        if (!__any(st[0] == POISON || st[1] == POISON)) break;
      }
      const float mu = asf(st[0]), sd = __builtin_amdgcn_sqrtf(asf(st[1]));
#pragma unroll
      for (int i = 0; i < 12; ++i) { x1[i] = unpack2(hq[i >> 2][i & 3]) * sd + mu; OPAQUE(x1[i]); }
    }
    float lt[2];
    u32x4 pick;
#pragma unroll
    for (int e = 0; e < 4; ++e) {
      const unsigned d = 8 * (e >> 1) + 4 * (g & 1) + 2 * (e & 1);
      pick[e] = (i16 == d ? 0x3f80u : 0u) | (i16 == d + 1 ? 0x3f800000u : 0u);
    }
    unsigned pr[2], la = lds0 + L_L + i16 * 32;
    pr[0] = lds0 + hh * SLOT + 3 * w * 1024 + ((g & 1) * 32 + i16) * 16; pr[1] = pr[0] + 2 * SLOT;
    OPAQUE(pick); OPAQUE(pr[0]); OPAQUE(pr[1]); OPAQUE(la);
#pragma unroll
    for (int hf = 0; hf < 2; ++hf) {
      fr[hf] += L_FI; OPAQUE(fr[hf]);
#pragma unroll
      for (int i = 0; i < 3; ++i) { dpos[i][hf] += L_FI; OPAQUE(dpos[i][hf]); }
    }
    ST(12);
#pragma unroll 1
    for (bool exact = false;; exact = true) {
      BAR();
      ST(13);
      u32x4 p[3][2][2];
#pragma unroll
      for (int i = 0; i < 3; ++i)
#pragma unroll
        for (int hf = 0; hf < 2; ++hf) { p[i][hf][0] = lds_r128(pr[0] + i * 1024 + hf * 256); p[i][hf][1] = lds_r128(pr[1] + i * 1024 + hf * 256); }
      const f32x4 l0 = lds_r128f(la), l1 = lds_r128f(la + 16);
      SBAR();
#pragma unroll
      for (int i = 0; i < 3; ++i)
#pragma unroll
        for (int hf = 0; hf < 2; ++hf) lds_w64(dpos[i][hf], pack4(mfma16(pick, p[i][hf][1], mfma16(pick, p[i][hf][0], f32x4{0, 0, 0, 0}))));
      if (__builtin_expect(!low_sum(l0, l1, lt) || exact, 1)) break;
      BAR();
      BAR();
    }
    ST(14);
    const float linv[2] = {__builtin_amdgcn_rcpf(lt[0]), __builtin_amdgcn_rcpf(lt[1])};
    BAR();
    ST(15);
    u32x4 bf[6][2];
#pragma unroll
    for (int ks = 0; ks < 6; ++ks) { bf[ks][0] = lds_r128(fr[0] + ks * 1024); bf[ks][1] = lds_r128(fr[1] + ks * 1024); }
    SBAR();
    f32x4 d[3][2];
    gemm(wv, bf, d);
#pragma unroll
    for (int i = 0; i < 3; ++i) {
      const u32x2 y0 = pack4(d[i][0] * linv[0]), y1 = pack4(d[i][1] * linv[1]);
      auto s0 = __builtin_amdgcn_permlane16_swap(y0[0], y1[0], false, false);
      auto s1 = __builtin_amdgcn_permlane16_swap(y0[1], y1[1], false, false);
      const u32x4 y = {s0[0], s1[0], s0[1], s1[1]};
      u32x4 o;
#pragma unroll
      for (int e = 0; e < 4; ++e) { const f32x2 v = x1[4 * i + e] + unpack2(y[e]); o[e] = cvtpk(v[0], v[1]); }
      *(u32x4*)(out + (size_t)(t * 1024 + qb * 32 + tok) * 384 + (2 * (3 * w + i) + hh) * 16) = o;
    }
    ST(17);
    ST_OUT(ws, 12, 20);
  }
}

extern "C" void kernel_launch(void* const* d_in, const int* in_sizes, int n_in, void* d_out, int out_size, void* d_ws, size_t ws_size, hipStream_t stream) {
  (void)hipFuncSetAttribute((const void*)k_timesformer, hipFuncAttributeMaxDynamicSharedMemorySize, LDS_BYTES);
  k_timesformer<<<256, 512, LDS_BYTES, stream>>>((const char*)d_in[0], (const char*)d_in[1], (const char*)d_in[2], (const char*)d_in[3], (const char*)d_in[4],
                                                 (char*)d_ws, (char*)d_out);
}
```

```cpp
#include <hip/hip_runtime.h>
#include <hip/hip_bf16.h>
#include <type_traits>

typedef short s16x4 __attribute__((ext_vector_type(4)));
typedef __bf16 b16x8 __attribute__((ext_vector_type(8)));
typedef __bf16 b16x2 __attribute__((ext_vector_type(2)));
typedef float f32x2 __attribute__((ext_vector_type(2)));
typedef float f32x4 __attribute__((ext_vector_type(4)));
typedef float f32x16 __attribute__((ext_vector_type(16)));
typedef unsigned u32x2 __attribute__((ext_vector_type(2)));
typedef unsigned u32x4 __attribute__((ext_vector_type(4)));
typedef unsigned long long u64;
#define LAS __attribute__((address_space(3)))
#define DI __device__ __forceinline__
#define SBAR() __builtin_amdgcn_sched_barrier(0)

constexpr int TILE_B = 12288;
constexpr int FRAME_B = 393216;
constexpr unsigned WS_H2 = 0, WS_ST = 8 * FRAME_B, WS_WM = 16 * FRAME_B, WS_FLAG = WS_WM + 2 * 73728, WS_FLAG2 = WS_FLAG + 2048, WS_EPOCH = WS_FLAG2 + 2048, WS_END = WS_EPOCH + 256;
constexpr float QSCALE = 0.07216878364870322f * 1.4426950408889634f;
constexpr int SC1 = 16;
constexpr unsigned POISON = 0xffffffffu;
constexpr unsigned CODE_BYTES = 26624;
constexpr int TILE0_DELAY = 2;
constexpr int WV_DELAY = 64;
constexpr int SPIN_CAP = 1 << 16;


constexpr int PROBE = 1, ROLE = 0; constexpr size_t WS_T0 = 32u << 20;
#define ST_DECL u64 st_ = 0
#define ST(k) do { if ((k) == PROBE || (k) == 0) { SBAR(); u64 t_; asm volatile("s_memrealtime %0\n\ts_waitcnt lgkmcnt(0)" : "=s"(t_) :: "memory"); SBAR(); \
    if ((k) == PROBE) st_ = t_; \
    if ((k) == 0 && threadIdx.x == 448) __hip_atomic_store((u64*)(ws + WS_T0) + blockIdx.x, t_, __ATOMIC_RELAXED, __HIP_MEMORY_SCOPE_AGENT); } } while (0)
#define ST_OUT(ws, k0, k1) do { if (ROLE == ((k0) != 0)) { u64 m_ = ~0ull; \
    for (int q_ = 0; q_ < 4; ++q_) { u64 v_ = __hip_atomic_load((u64*)(ws + WS_T0) + (threadIdx.x & 63) * 4 + q_, __ATOMIC_RELAXED, __HIP_MEMORY_SCOPE_AGENT); m_ = v_ < m_ ? v_ : m_; } \
    for (int o_ = 32; o_; o_ >>= 1) { u64 v_ = __shfl_xor(m_, o_); m_ = v_ < m_ ? v_ : m_; } \
    long long d_ = (long long)(st_ - m_); if (d_ < 0) d_ = 0; if (d_ > 5000) d_ = 5000; \
    const u64 e_ = m_ + 4000 + 4 * d_; while (__builtin_amdgcn_s_memrealtime() < e_) __builtin_amdgcn_s_sleep(1); } } while (0)


DI float asf(unsigned u) { return __builtin_bit_cast(float, u); }
DI unsigned asu(float f) { return __builtin_bit_cast(unsigned, f); }
DI unsigned cvtpk(float lo, float hi) { f32x2 v = {lo, hi}; return __builtin_bit_cast(unsigned, __builtin_convertvector(v, b16x2)); }
DI float bflo(unsigned u) { return asf(u << 16); }
DI float bfhi(unsigned u) { return asf(u & 0xffff0000u); }
DI f32x16 mfma32(u32x4 a, u32x4 b, f32x16 c) { return __builtin_amdgcn_mfma_f32_32x32x16_bf16(__builtin_bit_cast(b16x8, a), __builtin_bit_cast(b16x8, b), c, 0, 0, 0); }
DI f32x4 mfma16(u32x4 a, u32x4 b, f32x4 c) { return __builtin_amdgcn_mfma_f32_16x16x32_bf16(__builtin_bit_cast(b16x8, a), __builtin_bit_cast(b16x8, b), c, 0, 0, 0); }
DI f32x4 mfma16k16(u32x2 a, u32x2 b, f32x4 c) { return __builtin_amdgcn_mfma_f32_16x16x16bf16_1k(__builtin_bit_cast(s16x4, a), __builtin_bit_cast(s16x4, b), c, 0, 0, 0); }
DI u32x2 pack4(f32x4 v) { u32x2 r = {cvtpk(v[0], v[1]), cvtpk(v[2], v[3])}; return r; }
DI u32x4 lds_r128(unsigned a) { return *(LAS u32x4*)(size_t)a; }
DI f32x4 lds_r128f(unsigned a) { return *(LAS f32x4*)(size_t)a; }
DI u32x2 lds_r64(unsigned a) { return *(LAS u32x2*)(size_t)a; }
DI void lds_w128(unsigned a, u32x4 v) { *(LAS u32x4*)(size_t)a = v; }
DI void lds_w128f(unsigned a, f32x4 v) { *(LAS f32x4*)(size_t)a = v; }
DI void lds_w64(unsigned a, u32x2 v) { *(LAS u32x2*)(size_t)a = v; }
DI void lds_w32(unsigned a, float v) { *(LAS float*)(size_t)a = v; }
DI u32x2 lds_tr(unsigned a) { return __builtin_bit_cast(u32x2, __builtin_amdgcn_ds_read_tr16_b64_v4i16((LAS s16x4*)(size_t)a)); }
DI u32x4 lds_tr2(unsigned a0, unsigned a1) { u32x2 x = lds_tr(a0), y = lds_tr(a1); u32x4 r = {x[0], x[1], y[0], y[1]}; return r; }
#define BAR() asm volatile("s_waitcnt lgkmcnt(0)\n\ts_barrier" ::: "memory")
#define BARV(n) asm volatile("s_waitcnt vmcnt(" #n ") lgkmcnt(0)\n\ts_barrier" ::: "memory")
template <int CTRL> DI float dpp_add(float v) { return v + asf(__builtin_amdgcn_update_dpp(0u, asu(v), CTRL, 0xf, 0xf, true)); }
DI float xor16_add(float v) { auto r = __builtin_amdgcn_permlane16_swap(asu(v), asu(v), false, false); return asf(r[0]) + asf(r[1]); }
DI float xor16_max(float v) { auto r = __builtin_amdgcn_permlane16_swap(asu(v), asu(v), false, false); return fmaxf(asf(r[0]), asf(r[1])); }
DI unsigned umax3(unsigned a, unsigned b, unsigned c) { return max(max(a, b), c); }
template <int N> DI bool poisoned(const u32x4 (&v)[N]) {
  unsigned m = 0;
#pragma unroll
  for (int i = 0; i < N; ++i) { m = umax3(m, v[i][0], v[i][1]); m = umax3(m, v[i][2], v[i][3]); }
  return __any(m == POISON);
}
DI u32x4 unpoison(u32x4 v) { u32x4 r = {min(v[0], POISON - 1), min(v[1], POISON - 1), min(v[2], POISON - 1), min(v[3], POISON - 1)}; return r; }
DI unsigned fi_pos(unsigned r, unsigned c8) {
  return (((r >> 4) * 6 + (c8 >> 2)) * 64 + (c8 & 3) * 16 + ((r & 15) ^ ((c8 & 1) | (((r >> 3) & 1) << 1) | ((r >> 4) << 2)))) * 16;
}
DI void layernorm24(const f32x2 (&v)[12], u32x4 (&o)[3], float& mu, float& var) {
  f32x2 s0 = v[0] + v[1], s1 = v[2] + v[3];
#pragma unroll
  for (int i = 4; i < 12; i += 2) { s0 += v[i]; s1 += v[i + 1]; }
  s0 += s1;
  float s = s0[0] + s0[1];
  s = dpp_add<0x124>(s); s = dpp_add<0x128>(s); s = xor16_add(s);
  mu = s * (1.0f / 192.0f);
  f32x2 d[12];
#pragma unroll
  for (int i = 0; i < 12; ++i) d[i] = v[i] - mu;
  f32x2 q0 = d[0] * d[0], q1 = d[1] * d[1];
#pragma unroll
  for (int i = 2; i < 12; i += 2) { q0 = __builtin_elementwise_fma(d[i], d[i], q0); q1 = __builtin_elementwise_fma(d[i + 1], d[i + 1], q1); }
  q0 += q1;
  float q = q0[0] + q0[1];
  q = dpp_add<0x124>(q); q = dpp_add<0x128>(q); q = xor16_add(q);
  var = q * (1.0f / 192.0f) + 1e-5f;
  const float rs = __builtin_amdgcn_rsqf(var);
#pragma unroll
  for (int i = 0; i < 12; ++i) { const f32x2 n = d[i] * rs; o[i >> 2][i & 3] = cvtpk(n[0], n[1]); }
}
DI f32x2 unpack2(unsigned u) { f32x2 r = {bflo(u), bfhi(u)}; return r; }

constexpr unsigned SLOT = 36864, L_X = 3 * SLOT, L_H = 4 * SLOT, L_N = L_H + TILE_B, L_L = L_N + 1024, L_PT = L_L + 1024, LDS_BYTES = 163840;
constexpr unsigned L_VI = L_X, L_SP = L_X + TILE_B, L_YI = L_X + 20480;
constexpr unsigned L_HQ = 24576, L_Q = L_H, L_FI = L_H;

#define DMA4(v0, v1, v2, v3, sbase, ldsa)                                                                  \
  asm volatile("s_mov_b32 m0, %5\n\ts_nop 0\n\t"                                                            \
               "global_load_lds_dwordx4 %0, %4\n\tglobal_load_lds_dwordx4 %1, %4 offset:1024\n\t"           \
               "global_load_lds_dwordx4 %2, %4 offset:2048\n\tglobal_load_lds_dwordx4 %3, %4 offset:3072"   \
               :: "v"(v0), "v"(v1), "v"(v2), "v"(v3), "s"(sbase), "s"(ldsa) : "memory")
#define DMA1(v0, sbase, ldsa) \
  asm volatile("s_mov_b32 m0, %2\n\ts_nop 0\n\tglobal_load_lds_dwordx4 %0, %1" :: "v"(v0), "s"(sbase), "s"(ldsa) : "memory")
#define DMA9(o, sbase, ldsa) do { DMA4(o[0], o[1], o[2], o[3], sbase, ldsa); DMA4(o[4], o[5], o[6], o[7], sbase, (ldsa) + 4096); DMA1(o[8], sbase, (ldsa) + 8192); } while (0)

template <int OFF> DI void bdma(unsigned voff, u32x4 rs, unsigned soff) { asm volatile("buffer_load_dwordx4 %0, %1, %2 offen offset:%3 sc1 lds" :: "v"(voff), "s"(rs), "s"(soff), "i"(OFF) : "memory"); }
template <int OFF> DI void setm0(unsigned base) { asm volatile("s_add_u32 m0, %0, %1\n\ts_nop 0" :: "s"(base), "i"(OFF) : "memory", "scc"); }
template <int N> DI void waitvm() { asm volatile("s_waitcnt vmcnt(%0)" :: "i"(N) : "memory"); }
#define IC(x) std::integral_constant<int, (x)>{}
#define OPAQUE(v) asm volatile("" : "+v"(v))
template <class R> DI void st128(u32x4 v, R rs, unsigned voff, unsigned soff) { __builtin_amdgcn_raw_buffer_store_b128(v, rs, voff, soff, SC1); asm volatile("s_nop 1" :: "v"(v)); }
DI bool tags_ok(u32x4 f, u32x2 tag) { return f[0] == tag[0] && f[1] == tag[1] && f[2] == tag[0] && f[3] == tag[1]; }

__global__ __launch_bounds__(512, 2) void k_timesformer(const char* __restrict__ x, const char* __restrict__ wqkv_t, const char* __restrict__ wo_t,
                                                        const char* __restrict__ wqkv_s, const char* __restrict__ wo_s, char* __restrict__ ws, char* __restrict__ out) {
  extern __shared__ __attribute__((aligned(16))) char smem[];
  u64 pc;
  asm volatile("s_getpc_b64 %0" : "=s"(pc));
  const unsigned lds0 = (unsigned)(size_t)(LAS char*)smem;
  const unsigned b = blockIdx.x;
  unsigned w8 = __builtin_amdgcn_readfirstlane(threadIdx.x >> 6);
  unsigned l = threadIdx.x & 63;
  const unsigned t = b & 7, qb = b >> 3;
  const auto rsWS = __builtin_amdgcn_make_buffer_rsrc((void*)ws, 0, WS_END, 0x00020000);
  ST_DECL;
  ST(0);
  const bool merger = b < 144;
  const unsigned mtau = (b >> 1) / 6, mrow = ((b >> 1) - mtau * 6) * 32 + (b & 1) * 16;
  auto rows16 = [&](const char* src, const unsigned dst) {
#pragma unroll
    for (int i = 0; i < 3; ++i) {
      const unsigned pos = i * 64 + l, row = (pos * 2731) >> 16, o = row * 1152 + (((pos - row * 24) ^ row) * 16);
      DMA1(o, src, dst + i * 1024); DMA1(o, src + 8 * 1152, dst + (3 + i) * 1024);
    }
  };
  unsigned qo[9], d0, d1;
  if (w8 < 4) {
#pragma unroll
    for (int i = 0; i < 3; ++i) { const unsigned p = i * 64 + l; DMA1(p * 16 + (p >= 96 ? FRAME_B - 1536 : 0), x + w8 * 2 * FRAME_B + b * 1536, lds0 + L_YI + w8 * 3072 + i * 1024); }
  } else if (w8 < 6) {
    const unsigned qa = 18432 * (w8 & 1) + l * 16, qc = qa ^ 32;
    qo[0] = qa; qo[1] = qa; qo[2] = l >= 16 ? qc : qa; qo[3] = qc; qo[5] = qa + 4096; qo[7] = qc + 4096;
    qo[4] = l < 32 ? qo[7] : qo[5]; qo[6] = l >= 48 ? qo[7] : qo[5]; qo[8] = qc + 8192;
    DMA9(qo, wqkv_t, lds0 + 18432 * (w8 & 1));
  } else if (w8 == 7) {
    const unsigned line = qb * 72 + l, line2 = qb * 72 + 64 + (l & 7);
    const char* p0 = line < 1728 ? wqkv_t + line * 128 : wo_t + (line - 1728) * 128;
    const char* p1 = line2 < 1728 ? wqkv_t + line2 * 128 : wo_t + (line2 - 1728) * 128;
    if (l >= 8) p1 = (const char*)(pc + min((qb * 8 + (l & 7)) * 128, CODE_BYTES));
    asm volatile("global_load_dword %0, %2, off\n\tglobal_load_dword %1, %3, off" : "=&v"(d0), "=&v"(d1) : "v"(p0), "v"(p1) : "memory");
  }
  asm volatile("" : "+s"(w8));
  const unsigned w = w8 & 3;
  auto get_tag = [&]() {
    u64 e;
    asm volatile("s_load_dwordx2 %0, %1, 0x0\n\ts_waitcnt lgkmcnt(0)" : "=s"(e) : "s"(ws + WS_EPOCH) : "memory");
    e += 1;
    return u32x2{(unsigned)e, (unsigned)(e >> 32)};
  };
#define LANE_IDS const unsigned i16 = l & 15, g = l >> 4, r32 = l & 31, hh = l >> 5, tq = i16 >> 2, tp = l & 3
#define FRAG_POS unsigned fr[2], dpos[3][2];                                                                        \
  _Pragma("unroll") for (int hf = 0; hf < 2; ++hf) { fr[hf] = lds0 + fi_pos(16 * hf + i16, g);                     \
    _Pragma("unroll") for (int i = 0; i < 3; ++i) dpos[i][hf] = lds0 + fi_pos(16 * hf + i16, 2 * (3 * w + i) + (g >> 1)) + 8 * (g & 1); }
  auto low_sum = [&](const f32x4 a, const f32x4 c, float (&lt)[2]) {
    lt[0] = (a[0] + a[2]) + (c[0] + c[2]);
    lt[1] = (a[1] + a[3]) + (c[1] + c[3]);
    return (bool)__any(lt[0] < 8.7e-19f || lt[1] < 8.7e-19f);
  };

  if (w8 < 4) {
    const unsigned nl = l & 3, j = (l >> 2) & 7, tt = 2 * w + (l >> 5), r = nl * 8 + tt;
    const unsigned so = tt * FRAME_B + (j >> 1) * 1024 + ((t ^ ((j >> 1) & 1)) * 128) + (((j & 1) ^ (t >> 2)) * 64) + nl * 16, sso = qb * TILE_B;
    LANE_IDS;
    FRAG_POS;
    const unsigned phi = ((tq >> 1) & 1) | ((g & 1) << 1);
    unsigned tcol[3], rpos[3];
#pragma unroll
    for (int i = 0; i < 3; ++i) { tcol[i] = (((3 * w + i) ^ phi) * 2 + (tp >> 1)) * 16 + 8 * (tp & 1); rpos[i] = lds0 + fi_pos(r, j + 8 * i); }
    const u32x2 tag = get_tag();
    waitvm<0>();
    f32x2 xv[12];
    {
      u32x4 h[3], xr[3];
#pragma unroll
      for (int i = 0; i < 3; ++i) xr[i] = lds_r128(lds0 + L_YI + tt * 1536 + nl * 384 + j * 16 + i * 128);
#pragma unroll
      for (int i = 0; i < 12; ++i) xv[i] = unpack2(xr[i >> 2][i & 3]);
      float mu, var;
      layernorm24(xv, h, mu, var);
#pragma unroll
      for (int i = 0; i < 3; ++i) lds_w128(rpos[i] + L_H, h[i]);
    }
    f32x4 acc[3][3][2];
#pragma unroll
    for (int m = 0; m < 3; ++m)
#pragma unroll
      for (int i = 0; i < 3; ++i) { acc[m][i][0] = f32x4{0, 0, 0, 0}; acc[m][i][1] = f32x4{0, 0, 0, 0}; }
    u32x4 hf0, hf1;
#pragma unroll
    for (int ks = 0; ks < 6; ++ks) {
      BAR();
      if (ks == 0) { ST(1); hf0 = lds_r128(fr[0] + L_H); hf1 = lds_r128(fr[1] + L_H); }
      const unsigned base = lds0 + (ks % 3) * SLOT + (8 * g + tq) * 1152;
      u32x4 a[9];
#pragma unroll
      for (int m = 0; m < 3; ++m)
#pragma unroll
        for (int i = 0; i < 3; ++i) a[m * 3 + i] = lds_tr2(base + tcol[i] + m * 384, base + tcol[i] + m * 384 + 4608);
      const u32x4 h0 = hf0, h1 = hf1;
      if (ks < 5) { hf0 = lds_r128(fr[0] + L_H + (ks + 1) * 1024); hf1 = lds_r128(fr[1] + L_H + (ks + 1) * 1024); }
#pragma unroll
      for (int m = 0; m < 3; ++m)
#pragma unroll
        for (int i = 0; i < 3; ++i) {
          acc[m][i][0] = mfma16(a[m * 3 + i], h0, acc[m][i][0]);
          acc[m][i][1] = mfma16(a[m * 3 + i], h1, acc[m][i][1]);
        }
      if (ks == 0) __builtin_amdgcn_sched_group_barrier(0x100, 8, 0); else __builtin_amdgcn_sched_group_barrier(0x100, 6, 0);
#pragma unroll
      for (int q = 0; q < 7; ++q) { __builtin_amdgcn_sched_group_barrier(0x8, 2, 0); __builtin_amdgcn_sched_group_barrier(0x100, 2, 0); }
      __builtin_amdgcn_sched_group_barrier(0x8, 4, 0);
      SBAR();
    }
    ST(2);
    u32x2 vpk[3][2];
    f32x4 spart[2];
#pragma unroll
    for (int hf = 0; hf < 2; ++hf) {
      spart[hf] = f32x4{0, 0, 0, 0};
#pragma unroll
      for (int i = 0; i < 3; ++i) { spart[hf] = mfma16k16(pack4(acc[1][i][hf]), pack4(acc[0][i][hf]), spart[hf]); vpk[i][hf] = pack4(acc[2][i][hf]); }
    }
    unsigned wa[3], vfa[2], ypos[3][2], yr[3];
#pragma unroll
    for (int i = 0; i < 3; ++i) {
      wa[i] = lds0 + (8 * g + tq) * 384 + tcol[i]; yr[i] = rpos[i] + L_YI; OPAQUE(wa[i]); OPAQUE(yr[i]);
#pragma unroll
      for (int hf = 0; hf < 2; ++hf) { ypos[i][hf] = dpos[i][hf] + L_VI; OPAQUE(ypos[i][hf]); }
    }
    vfa[0] = fr[0] + L_VI; vfa[1] = fr[1] + L_VI; OPAQUE(vfa[0]); OPAQUE(vfa[1]);
    unsigned spa = lds0 + L_SP + (w * 128 + l) * 16, pta = lds0 + L_PT + l * 8;
    OPAQUE(spa); OPAQUE(pta);
    u32x4 bw[6][3];
    auto rdw = [&](const int k) {
#pragma unroll
      for (int i = 0; i < 3; ++i) bw[k][i] = lds_tr2(wa[i] + k * 12288, wa[i] + k * 12288 + 1536);
    };
    lds_w128f(spa, spart[0]); lds_w128f(spa + 1024, spart[1]);
    ST(3);
    BAR();
    rdw(0); rdw(1); rdw(2);
    SBAR();
    ST(4);
#pragma unroll
    for (int hf = 0; hf < 2; ++hf)
#pragma unroll
      for (int i = 0; i < 3; ++i) lds_w64(ypos[i][hf], vpk[i][hf]);
    ST(5);
    BAR();
    ST(6);
    {
      f32x4 vp[3][2];
      u32x4 vf[6][2];
      u32x2 pt[2];
      auto rdv = [&](const int k) { vf[k][0] = lds_r128(vfa[0] + k * 1024); vf[k][1] = lds_r128(vfa[1] + k * 1024); };
      auto mm = [&](const int k) {
#pragma unroll
        for (int i = 0; i < 3; ++i) {
          vp[i][0] = mfma16(vf[k][0], bw[k][i], k ? vp[i][0] : f32x4{0, 0, 0, 0});
          vp[i][1] = mfma16(vf[k][1], bw[k][i], k ? vp[i][1] : f32x4{0, 0, 0, 0});
        }
      };
#define MIX6() _Pragma("unroll") for (int q_ = 0; q_ < 6; ++q_) { __builtin_amdgcn_sched_group_barrier(0x8, 1, 0); __builtin_amdgcn_sched_group_barrier(0x100, 2, 0); } SBAR()
      rdv(0); rdv(1); rdv(2);
      SBAR();
      mm(0); rdw(3); rdv(3); MIX6();
      mm(1); rdw(4); rdv(4); MIX6();
      mm(2); rdw(5); rdv(5); MIX6();
      mm(3);
      SBAR();
      ST(7);
      asm volatile("s_barrier" ::: "memory");
      pt[0] = lds_r64(pta); pt[1] = lds_r64(pta + 512);
      SBAR();
      mm(4); mm(5);
      ST(8);
#pragma unroll
      for (int i = 0; i < 3; ++i)
#pragma unroll
        for (int hf = 0; hf < 2; ++hf) lds_w64(ypos[i][hf] + (L_YI - L_VI), pack4(mfma16k16(pack4(vp[i][hf]), pt[hf], f32x4{0, 0, 0, 0})));
    }
    ST(9);
    BAR();
    {
      u32x4 y[3], h[3];
#pragma unroll
      for (int i = 0; i < 3; ++i) y[i] = lds_r128(yr[i]);
#pragma unroll
      for (int i = 0; i < 12; ++i) xv[i] += unpack2(y[i >> 2][i & 3]);
      float mu, var;
      layernorm24(xv, h, mu, var);
#pragma unroll
      for (int i = 0; i < 3; ++i) st128(h[i], rsWS, so, WS_H2 + sso + i * 4096);
      const u32x2 stat = {asu(mu), asu(var)};
      __builtin_amdgcn_raw_buffer_store_b64(stat, rsWS, j == 0 ? (tt * 1024 + nl) * 8 : WS_END, WS_ST + b * 32, SC1);
    }
    ST(10);
    const unsigned ring = lds0 + w * SLOT;
    const unsigned kr0 = ring + (r32 >> 2) * 128 + (hh ^ (r32 >> 4)) * 64 + (r32 & 3) * 16, kr1 = kr0 ^ 128;
    const unsigned vr0 = ring + (g & 1) * 1024 + (hh ^ (g & 1)) * 128 + (tp >> 1) * 64 + tq * 16 + 8 * (tp & 1), vr1 = (vr0 ^ 64) + 512;
    const unsigned lo[3] = {l * 16, l * 16 + 4096, l * 16 + 8192};
    const u64 hbu = (u64)(ws + WS_H2 + (size_t)t * FRAME_B);
    const u32x4 rs = {(unsigned)hbu, (unsigned)(hbu >> 32) & 0xffffu, (unsigned)FRAME_B, 0x00020000u};
    unsigned toff[8];
#pragma unroll
    for (int i = 0; i < 8; ++i) toff[i] = (8 * w + ((i + qb) & 7)) * TILE_B;
    u32x4 sel;
    { const unsigned one = (tp == (g & 1)) ? 0x3f803f80u : 0u; sel = u32x4{one, one, one, one}; }
    auto piece = [&](auto Tc, auto Pc) {
      constexpr int T = decltype(Tc)::value, p = decltype(Pc)::value;
      if constexpr (T < 8) {
        if constexpr (p % 4 == 0) setm0<(T % 3) * TILE_B + p * 1024>(ring);
        bdma<(p % 4) * 1024>(lo[p / 4], rs, toff[T]);
      }
    };
#define P6(T, a) piece(IC(T), IC(a)); piece(IC(T), IC(a + 1)); piece(IC(T), IC(a + 2)); piece(IC(T), IC(a + 3)); piece(IC(T), IC(a + 4)); piece(IC(T), IC(a + 5));
    auto read_valid = [&](u32x4 (&KK)[12], const unsigned dst, unsigned off) {
      for (int sp = 0; sp < SPIN_CAP; ++sp) {
#pragma unroll
        for (int k = 0; k < 12; ++k) KK[k] = lds_r128(((k & 1) ? kr1 : kr0) + dst + k * 1024);
        if (__builtin_expect(!poisoned(KK), 1)) break;
#pragma unroll 1
        for (int k = 0; k < 12; ++k) { setm0<0>(ring + dst + k * 1024); bdma<0>(lo[0], rs, off + k * 1024); }
        waitvm<0>();
      }
    };
    unsigned qa = lds0 + L_Q + lo[0], na = lds0 + L_N + r32 * 16;
    OPAQUE(qa); OPAQUE(na);
    BARV(0);
    __builtin_amdgcn_s_sleep(TILE0_DELAY);
    P6(0, 0) P6(0, 6)
    if (w == 0 && l == 0) __builtin_amdgcn_raw_buffer_store_b64(tag, rsWS, 0, WS_FLAG2 + b * 8, SC1);
    ST(11);
    BAR();
    u32x4 K0[12];
    if (w == 0) waitvm<1>(); else waitvm<0>();
    ST(12);
    read_valid(K0, 0, toff[0]);
    ST(13);
    BAR();
    piece(IC(1), IC(0)); piece(IC(1), IC(1)); piece(IC(1), IC(2)); piece(IC(1), IC(3));
    BAR();
    ST(14);
    u32x4 Q[12];
    float mref;
    {
      const f32x4 n0 = lds_r128f(na), n1 = lds_r128f(na + 512);
#pragma unroll
      for (int s = 0; s < 12; ++s) Q[s] = lds_r128(qa + s * 1024);
      SBAR();
      const f32x4 s = n0 + n1;
      mref = __builtin_amdgcn_sqrtf(((s[0] + s[1]) + (s[2] + s[3])) * (192.0f * 1.02f));
    }
    constexpr int KW = 4, VW = 4;
    bool safe = false, exact = false;
#pragma unroll 1
    for (;;) {
      f32x16 S[2], O[6], negm;
      u32x4 K[12], V[12], P[2][2];
      f32x4 lsum = {0, 0, 0, 0};
      float pe[2][2];
#pragma unroll
      for (int i = 0; i < 16; ++i) negm[i] = -mref;
#define RK(k, T) K[k] = lds_r128((((k) & 1) ? kr1 : kr0) + ((T) % 3) * TILE_B + (k) * 1024);
#define RV(u, T) { const unsigned a_ = (((u) & 1) ? vr1 : vr0) + ((T) % 3) * TILE_B + ((u) >> 1) * 2048; V[u] = lds_tr2(a_, a_ + 256); }
      auto sm = [&](auto Tc, auto Hc, auto Gc) {
        constexpr int T = decltype(Tc)::value, H = decltype(Hc)::value, G = decltype(Gc)::value;
        if constexpr (G < 8) pe[(G >> 1) & 1][G & 1] = __builtin_amdgcn_exp2f(S[T & 1][8 * H + G]);
        if constexpr (G >= 2 && G <= 8 && G % 2 == 0) { constexpr int q = G / 2 - 1; P[T & 1][H][q] = cvtpk(pe[q & 1][0], pe[q & 1][1]); }
        if constexpr (G == 10) lsum = mfma16(sel, P[T & 1][H], lsum);
      };
      auto put_o = [&](const int un) {
        u32x4 pk;
#pragma unroll
        for (int e = 0; e < 4; ++e) pk[e] = cvtpk(O[un >> 1][8 * (un & 1) + 2 * e], O[un >> 1][8 * (un & 1) + 2 * e + 1]);
        lds_w128(ring + lo[0] + un * 1024, pk);
      };
      auto qkphase = [&](auto Ic) {
        constexpr int I = decltype(Ic)::value;
        auto step = [&](auto Kc) {
          constexpr int k = decltype(Kc)::value;
          if constexpr (I + 1 < 8) {
            S[(I + 1) & 1] = mfma32(I < 0 ? K0[k] : K[k], Q[k], k == 0 ? negm : S[(I + 1) & 1]);
            if constexpr (k + KW < 12 && I >= 0) RK(k + KW, I + 1)
          }
          if constexpr (I < 0 && k < 8) piece(IC(1), IC(4 + k));
          if constexpr (I >= 0) { if constexpr (k & 1) piece(IC(I + 2), IC(6 + k / 2)); if constexpr (k < 11) sm(IC(I), IC(1), IC(k)); }
          if constexpr (I >= 0 && I < 7 && k >= 12 - VW) RV(k - (12 - VW), I)
          if constexpr (I == 7 && k < VW) RV(k, I)
          SBAR();
        };
        step(IC(0)); step(IC(1)); step(IC(2)); step(IC(3)); step(IC(4)); step(IC(5)); step(IC(6)); step(IC(7)); step(IC(8)); step(IC(9)); step(IC(10)); step(IC(11));
      };
      auto pvphase = [&](auto Ic) {
        constexpr int I = decltype(Ic)::value;
        auto step = [&](auto Uc) {
          constexpr int u = decltype(Uc)::value;
          if constexpr (I >= 0) {
            f32x16 z;
#pragma unroll
            for (int e = 0; e < 16; ++e) z[e] = 0.f;
            O[u >> 1] = mfma32(V[u], P[I & 1][u & 1], (I == 0 && !(u & 1)) ? z : O[u >> 1]);
            if constexpr (u & 1) piece(IC(I + 3), IC(u / 2));
            if constexpr (u + VW < 12) RV(u + VW, I)
          }
          if constexpr (I + 1 < 8 && u >= 1) sm(IC(I + 1), IC(0), IC(u - 1));
          if constexpr (I < 0 && (u & 1)) piece(IC(2), IC(u / 2));
          if constexpr (I + 2 < 8) {
            if constexpr (u == 12 - KW) waitvm<(I < 0 ? 12 : I + 3 < 8 ? 6 + (12 - KW) / 2 : 6)>();
            if constexpr (u >= 12 - KW) RK(u - (12 - KW), I + 2)
            if constexpr (u == 11) waitvm<(I + 3 < 8 ? 6 : 0)>();
          }
          if constexpr (I == 7 && u >= 3) {
            put_o(u - 3);
          }
          SBAR();
        };
        step(IC(0)); step(IC(1)); step(IC(2)); step(IC(3)); step(IC(4)); step(IC(5)); step(IC(6)); step(IC(7)); step(IC(8)); step(IC(9)); step(IC(10)); step(IC(11));
      };
#define STALE() (__builtin_expect(!safe && __any(lsum[0] != lsum[0]), 0))
      bool done = false;
      do {
        qkphase(IC(-1));
        if (!safe) asm volatile("s_barrier" ::: "memory");
        ST(15);
        pvphase(IC(-1));
        ST(16);
        qkphase(IC(0)); pvphase(IC(0)); qkphase(IC(1)); pvphase(IC(1));
        if (STALE()) break;
        qkphase(IC(2)); pvphase(IC(2)); qkphase(IC(3)); pvphase(IC(3));
        if (STALE()) break;
        qkphase(IC(4)); pvphase(IC(4)); qkphase(IC(5)); pvphase(IC(5)); qkphase(IC(6)); pvphase(IC(6)); qkphase(IC(7)); pvphase(IC(7));
        if (STALE()) break;
        if (l < 16) { const u32x2 ls = {asu(lsum[0]), asu(lsum[1])}; lds_w64(lds0 + L_L + (l * 4 + w) * 8, ls); }
        put_o(9); put_o(10); put_o(11);
        done = true;
      } while (false);
#pragma unroll
      for (int k = 0; k < 12; ++k) asm volatile("" : "=v"(K0[k]));
      if (__builtin_expect(done, 1)) {
        ST(17);
        BAR();
        ST(18);
        float lt[2];
        if (__builtin_expect(!low_sum(lds_r128f(lds0 + L_L + i16 * 32), lds_r128f(lds0 + L_L + i16 * 32 + 16), lt) || exact, 1)) break;
        BAR();
        float mx = -3.0e38f;
#pragma unroll 1
        for (unsigned i = 0; i < 8; ++i) {
#pragma unroll 1
          for (int k = 0; k < 12; ++k) { setm0<0>(ring + k * 1024); bdma<0>(lo[0], rs, (8 * w + i) * TILE_B + k * 1024); }
          waitvm<0>();
          f32x16 s;
#pragma unroll
          for (int e = 0; e < 16; ++e) s[e] = 0.f;
#pragma unroll
          for (int k = 0; k < 12; ++k) s = mfma32(lds_r128(((k & 1) ? kr1 : kr0) + k * 1024), Q[k], s);
#pragma unroll
          for (int e = 0; e < 16; ++e) mx = fmaxf(mx, s[e]);
        }
        lds_w32(lds0 + L_N + (r32 * 8 + w * 2 + hh) * 4, mx);
        BAR();
        const f32x4 c = lds_r128f(lds0 + L_N + r32 * 32), d = lds_r128f(lds0 + L_N + r32 * 32 + 16);
        mref = fmaxf(fmaxf(fmaxf(c[0], c[1]), fmaxf(c[2], c[3])), fmaxf(fmaxf(d[0], d[1]), fmaxf(d[2], d[3])));
        exact = true;
      } else {
        for (int sp = 0; sp < SPIN_CAP; ++sp) {
          asm volatile("" ::: "memory");
          const u32x2 f = __builtin_amdgcn_raw_buffer_load_b64(rsWS, l * 8, WS_FLAG2 + w * 512, SC1);
          if (__all(f[0] == tag[0] && f[1] == tag[1])) break;
        }
      }
      safe = true;
      asm volatile("s_waitcnt vmcnt(0) lgkmcnt(0)" ::: "memory");
      P6(0, 0) P6(0, 6)
      waitvm<0>();
      piece(IC(1), IC(0)); piece(IC(1), IC(1)); piece(IC(1), IC(2)); piece(IC(1), IC(3));
#pragma unroll
      for (int k = 0; k < 12; ++k) K0[k] = lds_r128(((k & 1) ? kr1 : kr0) + k * 1024);
    }
    ST(19);
    BAR();
    ST_OUT(ws, 0, 12);
  } else {
    auto merge16 = [&](const u32x4 (&a)[6], const u32x4 (&bb)[6], const float sc, const unsigned where) {
      f32x4 d = {0, 0, 0, 0};
#pragma unroll
      for (int kk = 0; kk < 6; ++kk) d = mfma16(a[kk], bb[kk], d);
      __builtin_amdgcn_raw_buffer_store_b64(pack4(d * sc), rsWS, ((l >> 5) * 16 + (l & 15)) * 16 + ((l >> 4) & 1) * 8, where + b * 512, SC1);
    };
    auto rd16 = [&](u32x4 (&a)[6], const unsigned at) {
#pragma unroll
      for (int kk = 0; kk < 6; ++kk) a[kk] = lds_r128(lds0 + at + (l & 15) * 384 + (((4 * kk + (l >> 4)) ^ (l & 7)) * 16));
    };
    u32x2 tag7 = {0, 0};
    if (w < 2) {
      const unsigned ldsw = lds0 + 18432 * w;
      unsigned qp[9];
#pragma unroll
      for (int i = 0; i < 9; ++i) qp[i] = (qo[i] ^ 64) + 9216;
      DMA9(qp, wqkv_t, ldsw + 9216);
      ST(1);
      unsigned slot = SLOT;
      u32x2 tag;
#pragma unroll 1
      for (int c = 1; c < 6; ++c) {
        DMA9(qo, wqkv_t + c * SLOT, ldsw + slot); DMA9(qp, wqkv_t + c * SLOT, ldsw + slot + 9216);
        slot = slot == 2 * SLOT ? 0 : slot + SLOT;
        if (c == 1) tag = get_tag();
        if (c == 5) {
#pragma unroll
          for (int i = 0; i < 9; ++i) {
            const unsigned row = ((8 * i + (l >> 3)) * 171) >> 9;
            qo[i] = ((18432 * w + l * 16) ^ (((row & 2) | ((row & 8) >> 1)) << 4)) + (i >> 2) * 4096;
            qp[i] = (qo[i] ^ 64) + 9216;
          }
        }
        BARV(18);
      }
      DMA9(qo, wo_t, ldsw); DMA9(qp, wo_t, ldsw + 9216);
      BARV(18);
      DMA9(qo, wo_t + SLOT, ldsw + SLOT); DMA9(qp, wo_t + SLOT, ldsw + SLOT + 9216);
      ST(3);
      u32x4 f = __builtin_amdgcn_raw_buffer_load_b128(rsWS, l * 16, WS_FLAG + w * 1024, SC1);
      BARV(19);
      BARV(1);
      for (int sp = 0; sp < SPIN_CAP && !__all(tags_ok(f, tag)); ++sp) {
        asm volatile("" ::: "memory");
        f = __builtin_amdgcn_raw_buffer_load_b128(rsWS, l * 16, WS_FLAG + w * 1024, SC1);
      }
    } else if (w == 2) {
      if (merger) { rows16(wqkv_s + mrow * 1152, lds0 + L_X); rows16(wqkv_s + 16 * mtau * 1152 + 384, lds0 + L_X + 6144); }
      ST(1);
      BAR();
      BAR();
      u32x4 a[6], bb[6];
      waitvm<0>();
      if (merger) { rd16(a, L_X); rd16(bb, L_X + 6144); }
      ST(2);
      BAR();
      BAR();
      if (merger) merge16(a, bb, QSCALE, WS_WM);
      BAR();
      BAR();
    } else {
      const u32x4 pz = {POISON, POISON, POISON, POISON};
#pragma unroll
      for (int i = 0; i < 12; ++i) __builtin_amdgcn_raw_buffer_store_b128(pz, rsWS, l * 16 + (i & 3) * 1024, WS_H2 + t * FRAME_B + qb * TILE_B + (i >> 2) * 4096, SC1);
      __builtin_amdgcn_raw_buffer_store_b128(pz, rsWS, merger ? (l >> 5) * 73728 + (l & 31) * 16 : WS_END, WS_WM + b * 512, SC1);
      __builtin_amdgcn_raw_buffer_store_b32(POISON, rsWS, l * 4, WS_ST + (t * 1024 + qb * 32) * 8, SC1);
      asm volatile("s_nop 1" :: "v"(pz));
      tag7 = get_tag();
      ST(1);
      BAR(); BAR(); BAR();
      waitvm<0>();
      asm volatile("" :: "v"(d0), "v"(d1));
      if (l == 0) __builtin_amdgcn_raw_buffer_store_b64(tag7, rsWS, 0, WS_FLAG + b * 8, SC1);
      ST(2);
      BAR(); BAR(); BAR();
    }
    if (w >= 2) {
      const unsigned i16 = l & 15, g = l >> 4;
      unsigned a = lds0 + L_SP + ((w - 2) * 64 + l) * 16, pa = lds0 + L_PT + (w - 2) * 512 + l * 8;
      OPAQUE(a); OPAQUE(pa);
      __builtin_amdgcn_s_setprio(3);
      BAR();
      ST(4);
      const f32x4 s = (lds_r128f(a) + lds_r128f(a + 2048)) + (lds_r128f(a + 4096) + lds_r128f(a + 6144));
      f32x4 e = (s - xor16_max(fmaxf(fmaxf(s[0], s[1]), fmaxf(s[2], s[3])))) * QSCALE;
      OPAQUE(e);
      BAR();
#pragma unroll
      for (int k = 0; k < 4; ++k) e[k] = __builtin_amdgcn_exp2f(e[k]);
      const float sum = xor16_add((e[0] + e[1]) + (e[2] + e[3]));
      const float inv = ((g >> 1) == (i16 >> 3)) ? __builtin_amdgcn_rcpf(sum) : 0.f;
      lds_w64(pa, pack4(e * inv));
      __builtin_amdgcn_s_setprio(0);
    }
    ST(5);
    BAR();
    if (b == 0 && w == 3 && l == 0) __builtin_amdgcn_raw_buffer_store_b64(tag7, rsWS, 0, WS_EPOCH, SC1);
    OPAQUE(l);
    LANE_IDS;
    FRAG_POS;
    u32x4 wq[18], wv[18];
    auto ldw = [&](u32x4 (&W)[18], const int m, const int i0, const int i1) {
#pragma unroll
      for (int i = i0; i < i1; ++i) W[i] = __builtin_amdgcn_raw_buffer_load_b128(rsWS, l * 16 + (i & 3) * 1024, WS_WM + m * 73728 + w * 18432 + (i >> 2) * 4096, SC1);
    };
    ldw(wq, 0, 0, 8);
    BAR();
    ldw(wq, 0, 8, 18);
    unsigned hqa[2][2], qw[3], own[3];
#pragma unroll
    for (int hf = 0; hf < 2; ++hf) {
      const unsigned in = lds0 + L_HQ + ((4 * hf + tq) ^ hh) * 128 + ((g & 1) ^ hf) * 64 + tp * 16;
      hqa[hf][0] = in + hh * 1024; hqa[hf][1] = in + 2048 + hh * (SLOT - 2048);
      OPAQUE(hqa[hf][0]); OPAQUE(hqa[hf][1]);
    }
    const unsigned tok = 16 * (g & 1) + i16;
    unsigned nw = lds0 + L_N + l * 16 + w * 4;
    OPAQUE(nw);
#pragma unroll
    for (int i = 0; i < 3; ++i) {
      qw[i] = lds0 + L_Q + ((3 * w + i) * 64 + (g >> 1) * 32 + i16) * 16 + 8 * (g & 1);
      own[i] = lds0 + w * SLOT + L_HQ + i * 1024 + ((tok >> 2) ^ ((w + i) & 1)) * 128 + (hh ^ (g & 1)) * 64 + tp * 16;
      OPAQUE(qw[i]); OPAQUE(own[i]);
    }
    ST(6);
    BAR();
    ST(7);
    u32x4 hq[3];
    {
      const u64 hbu = (u64)(ws + WS_H2 + (size_t)t * FRAME_B);
      const u32x4 rs = {(unsigned)hbu, (unsigned)(hbu >> 32) & 0xffffu, (unsigned)FRAME_B, 0x00020000u};
      for (int sp = 0; sp < SPIN_CAP; ++sp) {
        setm0<L_HQ>(lds0 + w * SLOT);
        bdma<0>(l * 16, rs, qb * TILE_B + w * 3072); bdma<1024>(l * 16, rs, qb * TILE_B + w * 3072); bdma<2048>(l * 16, rs, qb * TILE_B + w * 3072);
        waitvm<0>();
#pragma unroll
        for (int i = 0; i < 3; ++i) hq[i] = lds_r128(own[i]);
        if (!poisoned(hq)) break;
      }
    }
    ST(8);
    BAR();
    ST(9);
    auto gemm = [&](const u32x4 (&wf)[18], const u32x4 (&bf)[6][2], f32x4 (&d)[3][2]) {
#pragma unroll
      for (int i = 0; i < 3; ++i) { d[i][0] = f32x4{0, 0, 0, 0}; d[i][1] = f32x4{0, 0, 0, 0}; }
#pragma unroll
      for (int ks = 0; ks < 6; ++ks)
#pragma unroll
        for (int i = 0; i < 3; ++i) { d[i][0] = mfma16(wf[i * 6 + ks], bf[ks][0], d[i][0]); d[i][1] = mfma16(wf[i * 6 + ks], bf[ks][1], d[i][1]); }
    };
    {
      u32x4 bf[6][2];
#pragma unroll
      for (int ks = 0; ks < 6; ++ks)
#pragma unroll
        for (int hf = 0; hf < 2; ++hf) {
          if (ks == 3) { hqa[hf][0] += 2 * SLOT; hqa[hf][1] += 2 * SLOT; }
          bf[ks][hf] = lds_r128(ks % 3 == 1 ? hqa[hf][1] : hqa[hf][0] + (ks % 3 == 2 ? SLOT + 1024 : 0));
        }
      SBAR();
      f32x4 d[3][2];
      gemm(wq, bf, d);
      SBAR();
      ST(10);
      asm volatile("s_barrier" ::: "memory");
#pragma unroll 1
      for (int sp = 0;; ++sp) {
        float n2[2];
#pragma unroll
        for (int hf = 0; hf < 2; ++hf) {
          f32x2 n = {0.f, 0.f};
#pragma unroll
          for (int i = 0; i < 3; ++i) {
            lds_w64(qw[i] + hf * 256, pack4(d[i][hf]));
            const f32x2 a = {d[i][hf][0], d[i][hf][1]}, c = {d[i][hf][2], d[i][hf][3]};
            n = __builtin_elementwise_fma(a, a, n); n = __builtin_elementwise_fma(c, c, n);
          }
          n2[hf] = n[0] + n[1];
        }
        auto rr = __builtin_amdgcn_permlane16_swap(asu(n2[0]), asu(n2[1]), false, false);
        const float nn = asf(rr[0]) + asf(rr[1]);
        lds_w32(nw, nn);
        if (__builtin_expect(!__any(nn != nn), 1) || sp >= SPIN_CAP || !poisoned(wq)) break;
        asm volatile("" ::: "memory");
        ldw(wq, 0, 0, 18);
        gemm(wq, bf, d);
      }
    }
    ST(11);
    BAR();
    BAR();
    if (merger && w == 2) {
      rows16(wqkv_s + mrow * 1152 + 768, lds0 + L_H);
#pragma unroll
      for (int i = 0; i < 6; ++i) DMA1((l >> 1) * 384 + (l & 1) * 16, wo_s + i * 12288 + mtau * 32, lds0 + L_H + 6144 + i * 1024);
      waitvm<0>();
      u32x4 a[6], bb[6];
      rd16(a, L_H);
#pragma unroll
      for (int kk = 0; kk < 6; ++kk) { const unsigned at = lds0 + L_H + 6144 + (32 * kk + 8 * g + tq) * 32 + tp * 8; bb[kk] = lds_tr2(at, at + 128); }
      merge16(a, bb, 1.0f, WS_WM + 73728);
      __builtin_amdgcn_s_sleep(WV_DELAY / 2);
    } else __builtin_amdgcn_s_sleep(WV_DELAY);
    for (int sp = 0; sp < SPIN_CAP; ++sp) {
      asm volatile("" ::: "memory");
      ldw(wv, 1, 0, 18);
      if (!poisoned(wv)) break;
    }
    f32x2 x1[12];
    {
      u32x2 st;
      for (int sp = 0; sp < SPIN_CAP; ++sp) {
        asm volatile("" ::: "memory");
        st = __builtin_amdgcn_raw_buffer_load_b64(rsWS, tok * 8, WS_ST + (t * 1024 + qb * 32) * 8, SC1);
        if (!__any(st[0] == POISON || st[1] == POISON)) break;
      }
      const float mu = asf(st[0]), sd = __builtin_amdgcn_sqrtf(asf(st[1]));
#pragma unroll
      for (int i = 0; i < 12; ++i) { x1[i] = unpack2(hq[i >> 2][i & 3]) * sd + mu; OPAQUE(x1[i]); }
    }
    float lt[2];
    u32x4 pick;
#pragma unroll
    for (int e = 0; e < 4; ++e) {
      const unsigned d = 8 * (e >> 1) + 4 * (g & 1) + 2 * (e & 1);
      pick[e] = (i16 == d ? 0x3f80u : 0u) | (i16 == d + 1 ? 0x3f800000u : 0u);
    }
    unsigned pr[2], la = lds0 + L_L + i16 * 32;
    pr[0] = lds0 + hh * SLOT + 3 * w * 1024 + ((g & 1) * 32 + i16) * 16; pr[1] = pr[0] + 2 * SLOT;
    OPAQUE(pick); OPAQUE(pr[0]); OPAQUE(pr[1]); OPAQUE(la);
#pragma unroll
    for (int hf = 0; hf < 2; ++hf) {
      fr[hf] += L_FI; OPAQUE(fr[hf]);
#pragma unroll
      for (int i = 0; i < 3; ++i) { dpos[i][hf] += L_FI; OPAQUE(dpos[i][hf]); }
    }
    ST(12);
#pragma unroll 1
    for (bool exact = false;; exact = true) {
      BAR();
      ST(13);
      u32x4 p[3][2][2];
#pragma unroll
      for (int i = 0; i < 3; ++i)
#pragma unroll
        for (int hf = 0; hf < 2; ++hf) { p[i][hf][0] = lds_r128(pr[0] + i * 1024 + hf * 256); p[i][hf][1] = lds_r128(pr[1] + i * 1024 + hf * 256); }
      const f32x4 l0 = lds_r128f(la), l1 = lds_r128f(la + 16);
      SBAR();
#pragma unroll
      for (int i = 0; i < 3; ++i)
#pragma unroll
        for (int hf = 0; hf < 2; ++hf) lds_w64(dpos[i][hf], pack4(mfma16(pick, p[i][hf][1], mfma16(pick, p[i][hf][0], f32x4{0, 0, 0, 0}))));
      if (__builtin_expect(!low_sum(l0, l1, lt) || exact, 1)) break;
      BAR();
      BAR();
    }
    ST(14);
    const float linv[2] = {__builtin_amdgcn_rcpf(lt[0]), __builtin_amdgcn_rcpf(lt[1])};
    BAR();
    ST(15);
    u32x4 bf[6][2];
#pragma unroll
    for (int ks = 0; ks < 6; ++ks) { bf[ks][0] = lds_r128(fr[0] + ks * 1024); bf[ks][1] = lds_r128(fr[1] + ks * 1024); }
    SBAR();
    f32x4 d[3][2];
    gemm(wv, bf, d);
#pragma unroll
    for (int i = 0; i < 3; ++i) {
      const u32x2 y0 = pack4(d[i][0] * linv[0]), y1 = pack4(d[i][1] * linv[1]);
      auto s0 = __builtin_amdgcn_permlane16_swap(y0[0], y1[0], false, false);
      auto s1 = __builtin_amdgcn_permlane16_swap(y0[1], y1[1], false, false);
      const u32x4 y = {s0[0], s1[0], s0[1], s1[1]};
      u32x4 o;
#pragma unroll
      for (int e = 0; e < 4; ++e) { const f32x2 v = x1[4 * i + e] + unpack2(y[e]); o[e] = cvtpk(v[0], v[1]); }
      *(u32x4*)(out + (size_t)(t * 1024 + qb * 32 + tok) * 384 + (2 * (3 * w + i) + hh) * 16) = o;
    }
    ST(17);
    ST_OUT(ws, 12, 20);
  }
}

extern "C" void kernel_launch(void* const* d_in, const int* in_sizes, int n_in, void* d_out, int out_size, void* d_ws, size_t ws_size, hipStream_t stream) {
  (void)hipFuncSetAttribute((const void*)k_timesformer, hipFuncAttributeMaxDynamicSharedMemorySize, LDS_BYTES);
  k_timesformer<<<256, 512, LDS_BYTES, stream>>>((const char*)d_in[0], (const char*)d_in[1], (const char*)d_in[2], (const char*)d_in[3], (const char*)d_in[4],
                                                 (char*)d_ws, (char*)d_out);
}
```

```cpp
#include <hip/hip_runtime.h>
#include <hip/hip_bf16.h>
#include <type_traits>

typedef short s16x4 __attribute__((ext_vector_type(4)));
typedef __bf16 b16x8 __attribute__((ext_vector_type(8)));
typedef __bf16 b16x2 __attribute__((ext_vector_type(2)));
typedef float f32x2 __attribute__((ext_vector_type(2)));
typedef float f32x4 __attribute__((ext_vector_type(4)));
typedef float f32x16 __attribute__((ext_vector_type(16)));
typedef unsigned u32x2 __attribute__((ext_vector_type(2)));
typedef unsigned u32x4 __attribute__((ext_vector_type(4)));
typedef unsigned long long u64;
#define LAS __attribute__((address_space(3)))
#define DI __device__ __forceinline__
#define SBAR() __builtin_amdgcn_sched_barrier(0)

constexpr int TILE_B = 12288;
constexpr int FRAME_B = 393216;
constexpr unsigned WS_H2 = 0, WS_ST = 8 * FRAME_B, WS_WM = 16 * FRAME_B, WS_FLAG = WS_WM + 2 * 73728, WS_FLAG2 = WS_FLAG + 2048, WS_EPOCH = WS_FLAG2 + 2048, WS_END = WS_EPOCH + 256;
constexpr float QSCALE = 0.07216878364870322f * 1.4426950408889634f;
constexpr int SC1 = 16;
constexpr unsigned POISON = 0xffffffffu;
constexpr unsigned CODE_BYTES = 26624;
constexpr int TILE0_DELAY = 2;
constexpr int SPIN_CAP = 1 << 16;

#define ST_DECL
#define ST(k)
#define ST_OUT(ws, k0, k1)

DI float asf(unsigned u) { return __builtin_bit_cast(float, u); }
DI unsigned asu(float f) { return __builtin_bit_cast(unsigned, f); }
DI unsigned cvtpk(float lo, float hi) { f32x2 v = {lo, hi}; return __builtin_bit_cast(unsigned, __builtin_convertvector(v, b16x2)); }
DI float bflo(unsigned u) { return asf(u << 16); }
DI float bfhi(unsigned u) { return asf(u & 0xffff0000u); }
DI f32x16 mfma32(u32x4 a, u32x4 b, f32x16 c) { return __builtin_amdgcn_mfma_f32_32x32x16_bf16(__builtin_bit_cast(b16x8, a), __builtin_bit_cast(b16x8, b), c, 0, 0, 0); }
DI f32x4 mfma16(u32x4 a, u32x4 b, f32x4 c) { return __builtin_amdgcn_mfma_f32_16x16x32_bf16(__builtin_bit_cast(b16x8, a), __builtin_bit_cast(b16x8, b), c, 0, 0, 0); }
DI f32x4 mfma16k16(u32x2 a, u32x2 b, f32x4 c) { return __builtin_amdgcn_mfma_f32_16x16x16bf16_1k(__builtin_bit_cast(s16x4, a), __builtin_bit_cast(s16x4, b), c, 0, 0, 0); }
DI u32x2 pack4(f32x4 v) { u32x2 r = {cvtpk(v[0], v[1]), cvtpk(v[2], v[3])}; return r; }
DI u32x4 lds_r128(unsigned a) { return *(LAS u32x4*)(size_t)a; }
DI f32x4 lds_r128f(unsigned a) { return *(LAS f32x4*)(size_t)a; }
DI u32x2 lds_r64(unsigned a) { return *(LAS u32x2*)(size_t)a; }
DI void lds_w128(unsigned a, u32x4 v) { *(LAS u32x4*)(size_t)a = v; }
DI void lds_w128f(unsigned a, f32x4 v) { *(LAS f32x4*)(size_t)a = v; }
DI void lds_w64(unsigned a, u32x2 v) { *(LAS u32x2*)(size_t)a = v; }
DI void lds_w32(unsigned a, float v) { *(LAS float*)(size_t)a = v; }
DI u32x2 lds_tr(unsigned a) { return __builtin_bit_cast(u32x2, __builtin_amdgcn_ds_read_tr16_b64_v4i16((LAS s16x4*)(size_t)a)); }
DI u32x4 lds_tr2(unsigned a0, unsigned a1) { u32x2 x = lds_tr(a0), y = lds_tr(a1); u32x4 r = {x[0], x[1], y[0], y[1]}; return r; }
#define BAR() asm volatile("s_waitcnt lgkmcnt(0)\n\ts_barrier" ::: "memory")
#define BARV(n) asm volatile("s_waitcnt vmcnt(" #n ") lgkmcnt(0)\n\ts_barrier" ::: "memory")
template <int CTRL> DI float dpp_add(float v) { return v + asf(__builtin_amdgcn_update_dpp(0u, asu(v), CTRL, 0xf, 0xf, true)); }
DI float xor16_add(float v) { auto r = __builtin_amdgcn_permlane16_swap(asu(v), asu(v), false, false); return asf(r[0]) + asf(r[1]); }
DI float xor16_max(float v) { auto r = __builtin_amdgcn_permlane16_swap(asu(v), asu(v), false, false); return fmaxf(asf(r[0]), asf(r[1])); }
DI unsigned umax3(unsigned a, unsigned b, unsigned c) { return max(max(a, b), c); }
template <int N> DI bool poisoned(const u32x4 (&v)[N]) {
  unsigned m = 0;
#pragma unroll
  for (int i = 0; i < N; ++i) { m = umax3(m, v[i][0], v[i][1]); m = umax3(m, v[i][2], v[i][3]); }
  return __any(m == POISON);
}
DI u32x4 unpoison(u32x4 v) { u32x4 r = {min(v[0], POISON - 1), min(v[1], POISON - 1), min(v[2], POISON - 1), min(v[3], POISON - 1)}; return r; }
DI unsigned fi_pos(unsigned r, unsigned c8) {
  return (((r >> 4) * 6 + (c8 >> 2)) * 64 + (c8 & 3) * 16 + ((r & 15) ^ ((c8 & 1) | (((r >> 3) & 1) << 1) | ((r >> 4) << 2)))) * 16;
}
DI float row8_sum(float s) { s = dpp_add<0x124>(s); s = dpp_add<0x128>(s); return xor16_add(s); }
DI float sum24(const f32x2 (&v)[12]) {
  f32x2 s0 = v[0] + v[1], s1 = v[2] + v[3];
#pragma unroll
  for (int i = 4; i < 12; i += 2) { s0 += v[i]; s1 += v[i + 1]; }
  s0 += s1;
  return s0[0] + s0[1];
}
DI float sumsq24(const f32x2 (&v)[12]) {
  f32x2 q0 = v[0] * v[0], q1 = v[1] * v[1];
#pragma unroll
  for (int i = 2; i < 12; i += 2) { q0 = __builtin_elementwise_fma(v[i], v[i], q0); q1 = __builtin_elementwise_fma(v[i + 1], v[i + 1], q1); }
  q0 += q1;
  return q0[0] + q0[1];
}
DI f32x2 unpack2(unsigned u) { f32x2 r = {bflo(u), bfhi(u)}; return r; }

constexpr unsigned SLOT = 36864, L_X = 3 * SLOT, L_H = 4 * SLOT, L_N = L_H + TILE_B, L_L = L_N + 1024, L_PT = L_L + 1024, LDS_BYTES = 163840;
constexpr unsigned L_VI = L_X, L_SP = L_X + TILE_B, L_YI = L_X + 20480;
constexpr unsigned L_HQ = 24576, L_Q = L_H, L_FI = L_H;

#define DMA4(v0, v1, v2, v3, sbase, ldsa)                                                                  \
  asm volatile("s_mov_b32 m0, %5\n\ts_nop 0\n\t"                                                            \
               "global_load_lds_dwordx4 %0, %4\n\tglobal_load_lds_dwordx4 %1, %4 offset:1024\n\t"           \
               "global_load_lds_dwordx4 %2, %4 offset:2048\n\tglobal_load_lds_dwordx4 %3, %4 offset:3072"   \
               :: "v"(v0), "v"(v1), "v"(v2), "v"(v3), "s"(sbase), "s"(ldsa) : "memory")
#define DMA1(v0, sbase, ldsa) \
  asm volatile("s_mov_b32 m0, %2\n\ts_nop 0\n\tglobal_load_lds_dwordx4 %0, %1" :: "v"(v0), "s"(sbase), "s"(ldsa) : "memory")
#define DMA9(o, sbase, ldsa) do { DMA4(o[0], o[1], o[2], o[3], sbase, ldsa); DMA4(o[4], o[5], o[6], o[7], sbase, (ldsa) + 4096); DMA1(o[8], sbase, (ldsa) + 8192); } while (0)

template <int OFF> DI void bdma(unsigned voff, u32x4 rs, unsigned soff) { asm volatile("buffer_load_dwordx4 %0, %1, %2 offen offset:%3 sc1 lds" :: "v"(voff), "s"(rs), "s"(soff), "i"(OFF) : "memory"); }
template <int OFF> DI void setm0(unsigned base) { asm volatile("s_add_u32 m0, %0, %1\n\ts_nop 0" :: "s"(base), "i"(OFF) : "memory", "scc"); }
template <int N> DI void waitvm() { asm volatile("s_waitcnt vmcnt(%0)" :: "i"(N) : "memory"); }
#define IC(x) std::integral_constant<int, (x)>{}
#define OPAQUE(v) asm volatile("" : "+v"(v))
template <class R> DI void st128(u32x4 v, R rs, unsigned voff, unsigned soff) { __builtin_amdgcn_raw_buffer_store_b128(v, rs, voff, soff, SC1); asm volatile("s_nop 1" :: "v"(v)); }
DI bool tags_ok(u32x4 f, u32x2 tag) { return f[0] == tag[0] && f[1] == tag[1] && f[2] == tag[0] && f[3] == tag[1]; }

__global__ __launch_bounds__(512, 2) void k_timesformer(const char* __restrict__ x, const char* __restrict__ wqkv_t, const char* __restrict__ wo_t,
                                                        const char* __restrict__ wqkv_s, const char* __restrict__ wo_s, char* __restrict__ ws, char* __restrict__ out) {
  u64 pc;
  asm volatile("s_getpc_b64 %0" : "=s"(pc));
  constexpr unsigned lds0 = 0;
  const unsigned b = blockIdx.x;
  unsigned w8 = __builtin_amdgcn_readfirstlane(threadIdx.x >> 6);
  unsigned l = threadIdx.x & 63;
  const unsigned t = b & 7, qb = b >> 3;
  const auto rsWS = __builtin_amdgcn_make_buffer_rsrc((void*)ws, 0, WS_END, 0x00020000);
  ST_DECL;
  ST(0);
  const bool merger = b < 144;
  const unsigned mtau = (b >> 1) / 6, mrow = ((b >> 1) - mtau * 6) * 32 + (b & 1) * 16;
  auto rows16 = [&](const char* src, const unsigned dst) {
#pragma unroll
    for (int i = 0; i < 3; ++i) {
      const unsigned pos = i * 64 + l, row = (pos * 2731) >> 16, o = row * 1152 + (((pos - row * 24) ^ row) * 16);
      DMA1(o, src, dst + i * 1024); DMA1(o, src + 8 * 1152, dst + (3 + i) * 1024);
    }
  };
  unsigned qo[9], d0, d1;
  if (w8 < 4) {
#pragma unroll
    for (int i = 0; i < 3; ++i) { const unsigned p = i * 64 + l; DMA1(p * 16 + (p >= 96 ? FRAME_B - 1536 : 0), x + w8 * 2 * FRAME_B + b * 1536, lds0 + L_YI + w8 * 3072 + i * 1024); }
  } else if (w8 < 6) {
    const unsigned qa = 18432 * (w8 & 1) + l * 16, qc = qa ^ 32;
    qo[0] = qa; qo[1] = qa; qo[2] = l >= 16 ? qc : qa; qo[3] = qc; qo[5] = qa + 4096; qo[7] = qc + 4096;
    qo[4] = l < 32 ? qo[7] : qo[5]; qo[6] = l >= 48 ? qo[7] : qo[5]; qo[8] = qc + 8192;
    DMA9(qo, wqkv_t, lds0 + 18432 * (w8 & 1));
  } else if (w8 == 7) {
    const unsigned line = qb * 72 + l, line2 = qb * 72 + 64 + (l & 7);
    const char* p0 = line < 1728 ? wqkv_t + line * 128 : wo_t + (line - 1728) * 128;
    const char* p1 = line2 < 1728 ? wqkv_t + line2 * 128 : wo_t + (line2 - 1728) * 128;
    if (l >= 8) p1 = (const char*)(pc + min((qb * 8 + (l & 7)) * 128, CODE_BYTES));
    asm volatile("global_load_dword %0, %2, off\n\tglobal_load_dword %1, %3, off" : "=&v"(d0), "=&v"(d1) : "v"(p0), "v"(p1) : "memory");
  }
  asm volatile("" : "+s"(w8));
  const unsigned w = w8 & 3;
  auto get_tag = [&]() {
    u64 e;
    asm volatile("s_load_dwordx2 %0, %1, 0x0\n\ts_waitcnt lgkmcnt(0)" : "=s"(e) : "s"(ws + WS_EPOCH) : "memory");
    e += 1;
    return u32x2{(unsigned)e, (unsigned)(e >> 32)};
  };
#define LANE_IDS const unsigned i16 = l & 15, g = l >> 4, r32 = l & 31, hh = l >> 5, tq = i16 >> 2, tp = l & 3
#define FRAG_POS unsigned fr[2], dpos[3][2];                                                                        \
  _Pragma("unroll") for (int hf = 0; hf < 2; ++hf) { fr[hf] = lds0 + fi_pos(16 * hf + i16, g);                     \
    _Pragma("unroll") for (int i = 0; i < 3; ++i) dpos[i][hf] = lds0 + fi_pos(16 * hf + i16, 2 * (3 * w + i) + (g >> 1)) + 8 * (g & 1); }
  auto low_sum = [&](const f32x4 a, const f32x4 c, float (&lt)[2]) {
    lt[0] = (a[0] + a[2]) + (c[0] + c[2]);
    lt[1] = (a[1] + a[3]) + (c[1] + c[3]);
    return (bool)__any(lt[0] < 8.7e-19f || lt[1] < 8.7e-19f);
  };

  if (w8 < 4) {
    const unsigned nl = l & 3, j = (l >> 2) & 7, tt = 2 * w + (l >> 5), r = nl * 8 + tt;
    const unsigned so = tt * FRAME_B + (j >> 1) * 1024 + ((t ^ ((j >> 1) & 1)) * 128) + (((j & 1) ^ (t >> 2)) * 64) + nl * 16, sso = qb * TILE_B;
    LANE_IDS;
    FRAG_POS;
    const unsigned phi = ((tq >> 1) & 1) | ((g & 1) << 1);
    unsigned tcol[3], rpos[3];
#pragma unroll
    for (int i = 0; i < 3; ++i) { tcol[i] = (((3 * w + i) ^ phi) * 2 + (tp >> 1)) * 16 + 8 * (tp & 1); rpos[i] = lds0 + fi_pos(r, j + 8 * i); }
    const u32x2 tag = get_tag();
    waitvm<0>();
    f32x2 xv[12];
    float mu1;
    {
      u32x4 h[3], xr[3];
#pragma unroll
      for (int i = 0; i < 3; ++i) xr[i] = lds_r128(lds0 + L_YI + tt * 1536 + nl * 384 + j * 16 + i * 128);
#pragma unroll
      for (int i = 0; i < 12; ++i) xv[i] = unpack2(xr[i >> 2][i & 3]);
      mu1 = row8_sum(sum24(xv)) * (1.0f / 192.0f);
#pragma unroll
      for (int i = 0; i < 12; ++i) xv[i] -= mu1;
      const float rs = __builtin_amdgcn_rsqf(row8_sum(sumsq24(xv)) * (1.0f / 192.0f) + 1e-5f);
#pragma unroll
      for (int i = 0; i < 12; ++i) { const f32x2 n = xv[i] * rs; h[i >> 2][i & 3] = cvtpk(n[0], n[1]); }
#pragma unroll
      for (int i = 0; i < 3; ++i) lds_w128(rpos[i] + L_H, h[i]);
    }
    f32x4 acc[3][3][2];
#pragma unroll
    for (int m = 0; m < 3; ++m)
#pragma unroll
      for (int i = 0; i < 3; ++i) { acc[m][i][0] = f32x4{0, 0, 0, 0}; acc[m][i][1] = f32x4{0, 0, 0, 0}; }
    unsigned wa[3], vfa[2], ypos[3][2], yr[3];
#pragma unroll
    for (int i = 0; i < 3; ++i) {
      wa[i] = lds0 + (8 * g + tq) * 384 + tcol[i]; yr[i] = rpos[i] + L_YI; OPAQUE(wa[i]); OPAQUE(yr[i]);
#pragma unroll
      for (int hf = 0; hf < 2; ++hf) { ypos[i][hf] = dpos[i][hf] + L_VI; OPAQUE(ypos[i][hf]); }
    }
    vfa[0] = fr[0] + L_VI; vfa[1] = fr[1] + L_VI; OPAQUE(vfa[0]); OPAQUE(vfa[1]);
    unsigned spa = lds0 + L_SP + (w * 128 + l) * 16, pta = lds0 + L_PT + l * 8;
    unsigned sto = j == 0 ? (tt * 1024 + nl) * 8 : WS_END;
    OPAQUE(spa); OPAQUE(pta); OPAQUE(sto);
    u32x4 hf0, hf1;
#pragma unroll
    for (int ks = 0; ks < 6; ++ks) {
      BAR();
      if (ks == 0) { ST(1); hf0 = lds_r128(fr[0] + L_H); hf1 = lds_r128(fr[1] + L_H); }
      const unsigned base = lds0 + (ks % 3) * SLOT + (8 * g + tq) * 1152;
      u32x4 a[9];
#pragma unroll
      for (int m = 0; m < 3; ++m)
#pragma unroll
        for (int i = 0; i < 3; ++i) a[m * 3 + i] = lds_tr2(base + tcol[i] + m * 384, base + tcol[i] + m * 384 + 4608);
      const u32x4 h0 = hf0, h1 = hf1;
      if (ks < 5) { hf0 = lds_r128(fr[0] + L_H + (ks + 1) * 1024); hf1 = lds_r128(fr[1] + L_H + (ks + 1) * 1024); }
#pragma unroll
      for (int m = 0; m < 3; ++m)
#pragma unroll
        for (int i = 0; i < 3; ++i) {
          acc[m][i][0] = mfma16(a[m * 3 + i], h0, acc[m][i][0]);
          acc[m][i][1] = mfma16(a[m * 3 + i], h1, acc[m][i][1]);
        }
      if (ks == 0) __builtin_amdgcn_sched_group_barrier(0x100, 8, 0); else __builtin_amdgcn_sched_group_barrier(0x100, 6, 0);
#pragma unroll
      for (int q = 0; q < 7; ++q) { __builtin_amdgcn_sched_group_barrier(0x8, 2, 0); __builtin_amdgcn_sched_group_barrier(0x100, 2, 0); }
      __builtin_amdgcn_sched_group_barrier(0x8, 4, 0);
      SBAR();
    }
    ST(2);
    u32x2 vpk[3][2];
    f32x4 spart[2];
#pragma unroll
    for (int hf = 0; hf < 2; ++hf) {
      spart[hf] = f32x4{0, 0, 0, 0};
#pragma unroll
      for (int i = 0; i < 3; ++i) { spart[hf] = mfma16k16(pack4(acc[1][i][hf]), pack4(acc[0][i][hf]), spart[hf]); vpk[i][hf] = pack4(acc[2][i][hf]); }
    }
    u32x4 bw[6][3];
    auto rdw = [&](const int k) {
#pragma unroll
      for (int i = 0; i < 3; ++i) bw[k][i] = lds_tr2(wa[i] + k * 12288, wa[i] + k * 12288 + 1536);
    };
    ST(3);
#pragma unroll
    for (int hf = 0; hf < 2; ++hf)
#pragma unroll
      for (int i = 0; i < 3; ++i) lds_w64(ypos[i][hf], vpk[i][hf]);
    lds_w128f(spa, spart[0]); lds_w128f(spa + 1024, spart[1]);
    ST(5);
    BAR();
    ST(6);
    {
      f32x4 vp[3][2];
      u32x4 vf[6][2];
      u32x2 pt[2];
      auto rd = [&](const int k) { vf[k][0] = lds_r128(vfa[0] + k * 1024); vf[k][1] = lds_r128(vfa[1] + k * 1024); rdw(k); };
      auto mm = [&](const int k) {
#pragma unroll
        for (int i = 0; i < 3; ++i) {
          vp[i][0] = mfma16(vf[k][0], bw[k][i], k ? vp[i][0] : f32x4{0, 0, 0, 0});
          vp[i][1] = mfma16(vf[k][1], bw[k][i], k ? vp[i][1] : f32x4{0, 0, 0, 0});
        }
      };
#define MIX6() _Pragma("unroll") for (int q_ = 0; q_ < 6; ++q_) { __builtin_amdgcn_sched_group_barrier(0x8, 1, 0); __builtin_amdgcn_sched_group_barrier(0x100, 2, 0); } SBAR()
      rd(0); SBAR(); rd(1);
      SBAR();
      mm(0); rd(2); MIX6();
      mm(1); rd(3); MIX6();
      mm(2); rd(4); MIX6();
      mm(3); rd(5); MIX6();
      mm(4);
      SBAR();
      ST(7);
      asm volatile("s_barrier" ::: "memory");
      pt[0] = lds_r64(pta); pt[1] = lds_r64(pta + 512);
      SBAR();
      mm(5);
      ST(8);
#pragma unroll
      for (int i = 0; i < 3; ++i)
#pragma unroll
        for (int hf = 0; hf < 2; ++hf) lds_w64(ypos[i][hf] + (L_YI - L_VI), pack4(mfma16k16(pack4(vp[i][hf]), pt[hf], f32x4{0, 0, 0, 0})));
    }
    ST(9);
    BAR();
    {
      u32x4 y[3], h[3];
#pragma unroll
      for (int i = 0; i < 3; ++i) y[i] = lds_r128(yr[i]);
#pragma unroll
      for (int i = 0; i < 12; ++i) xv[i] += unpack2(y[i >> 2][i & 3]);
      const float mu = row8_sum(sum24(xv)) * (1.0f / 192.0f), sq = row8_sum(sumsq24(xv));
      const float var = fmaxf(fmaf(sq, 1.0f / 192.0f, -mu * mu), 0.f) + 1e-5f;
      const float rs = __builtin_amdgcn_rsqf(var), nb = -mu * rs;
#pragma unroll
      for (int i = 0; i < 3; ++i) {
#pragma unroll
        for (int e = 0; e < 4; ++e) { const f32x2 n = xv[4 * i + e] * rs + nb; h[i][e] = cvtpk(n[0], n[1]); }
        st128(h[i], rsWS, so, WS_H2 + sso + i * 4096);
        SBAR();
      }
      const u32x2 stat = {asu(mu + mu1), asu(var)};
      __builtin_amdgcn_raw_buffer_store_b64(stat, rsWS, sto, WS_ST + b * 32, SC1);
    }
    ST(10);
    asm volatile("s_barrier" ::: "memory");
    const unsigned ring = lds0 + w * SLOT;
    unsigned kr0 = ring + (r32 >> 2) * 128 + (hh ^ (r32 >> 4)) * 64 + (r32 & 3) * 16, kr1 = kr0 ^ 128;
    unsigned vr0 = ring + (g & 1) * 1024 + (hh ^ (g & 1)) * 128 + (tp >> 1) * 64 + tq * 16 + 8 * (tp & 1), vr1 = (vr0 ^ 64) + 512;
    unsigned lo[3] = {l * 16, l * 16 + 4096, l * 16 + 8192}, pw = ring + l * 16, lw = l < 16 ? L_L + (l * 4 + w) * 8 : L_L + 512 + l * 8;
    OPAQUE(kr0); OPAQUE(kr1); OPAQUE(vr0); OPAQUE(vr1); OPAQUE(lo[0]); OPAQUE(lo[1]); OPAQUE(lo[2]); OPAQUE(pw); OPAQUE(lw);
    const u64 hbu = (u64)(ws + WS_H2 + (size_t)t * FRAME_B);
    const u32x4 rs = {(unsigned)hbu, (unsigned)(hbu >> 32) & 0xffffu, (unsigned)FRAME_B, 0x00020000u};
    unsigned toff[8];
#pragma unroll
    for (int i = 0; i < 8; ++i) { toff[i] = (8 * w + ((i + qb) & 7)) * TILE_B; asm volatile("" : "+s"(toff[i])); }
    u32x4 sel;
    { const unsigned one = (tp == (g & 1)) ? 0x3f803f80u : 0u; sel = u32x4{one, one, one, one}; OPAQUE(sel); }
    auto piece = [&](auto Tc, auto Pc) {
      constexpr int T = decltype(Tc)::value, p = decltype(Pc)::value;
      if constexpr (T < 8) {
        if constexpr (p % 4 == 0) setm0<(T % 3) * TILE_B + p * 1024>(ring);
        bdma<(p % 4) * 1024>(lo[p / 4], rs, toff[T]);
      }
    };
#define P6(T, a) piece(IC(T), IC(a)); piece(IC(T), IC(a + 1)); piece(IC(T), IC(a + 2)); piece(IC(T), IC(a + 3)); piece(IC(T), IC(a + 4)); piece(IC(T), IC(a + 5));
    auto read_valid = [&](u32x4 (&KK)[12], const unsigned dst, unsigned off) {
      for (int sp = 0; sp < SPIN_CAP; ++sp) {
#pragma unroll
        for (int k = 0; k < 12; ++k) KK[k] = lds_r128(((k & 1) ? kr1 : kr0) + dst + k * 1024);
        if (__builtin_expect(!poisoned(KK), 1)) break;
#pragma unroll 1
        for (int k = 0; k < 12; ++k) { setm0<0>(ring + dst + k * 1024); bdma<0>(lo[0], rs, off + k * 1024); }
        waitvm<0>();
      }
    };
    unsigned qa = lds0 + L_Q + lo[0], na = lds0 + L_N + r32 * 16;
    OPAQUE(qa); OPAQUE(na);
    BARV(0);
    __builtin_amdgcn_s_sleep(TILE0_DELAY);
    P6(0, 0) P6(0, 6)
    if (w == 0 && l == 0) __builtin_amdgcn_raw_buffer_store_b64(tag, rsWS, 0, WS_FLAG2 + b * 8, SC1);
    ST(11);
    BAR();
    u32x4 K0[12];
    if (w == 0) waitvm<1>(); else waitvm<0>();
    ST(12);
    read_valid(K0, 0, toff[0]);
    ST(13);
    piece(IC(1), IC(0)); piece(IC(1), IC(1)); piece(IC(1), IC(2)); piece(IC(1), IC(3));
    BAR();
    ST(14);
    u32x4 Q[12];
    float mref;
    {
      const f32x4 n0 = lds_r128f(na), n1 = lds_r128f(na + 512);
#pragma unroll
      for (int s = 0; s < 6; ++s) Q[s] = lds_r128(qa + s * 1024);
      SBAR();
      const f32x4 s = n0 + n1;
      mref = __builtin_amdgcn_sqrtf(((s[0] + s[1]) + (s[2] + s[3])) * (192.0f * 1.02f));
      OPAQUE(mref);
      SBAR();
#pragma unroll
      for (int s = 6; s < 12; ++s) Q[s] = lds_r128(qa + s * 1024);
      SBAR();
    }
    constexpr int KW = 4, VW = 4;
    bool safe = false, exact = false, late = false;
#pragma unroll 1
    for (;;) {
      f32x16 S[2], O[6], negm;
      u32x4 K[12], V[12], P[2][2];
      f32x4 lsum = {0, 0, 0, 0};
      float pe[2][2];
#pragma unroll
      for (int i = 0; i < 16; ++i) negm[i] = -mref;
#define RK(k, T) K[k] = lds_r128((((k) & 1) ? kr1 : kr0) + ((T) % 3) * TILE_B + (k) * 1024);
#define RV(u, T) { const unsigned a_ = (((u) & 1) ? vr1 : vr0) + ((T) % 3) * TILE_B + ((u) >> 1) * 2048; V[u] = lds_tr2(a_, a_ + 256); }
      auto sm = [&](auto Tc, auto Hc, auto Gc) {
        constexpr int T = decltype(Tc)::value, H = decltype(Hc)::value, G = decltype(Gc)::value;
        if constexpr (G < 8) pe[(G >> 1) & 1][G & 1] = __builtin_amdgcn_exp2f(S[T & 1][8 * H + G]);
        if constexpr (G >= 2 && G <= 8 && G % 2 == 0) { constexpr int q = G / 2 - 1; P[T & 1][H][q] = cvtpk(pe[q & 1][0], pe[q & 1][1]); }
        if constexpr (G == 10) lsum = mfma16(sel, P[T & 1][H], lsum);
      };
      auto put_o = [&](const int un) {
        u32x4 pk;
#pragma unroll
        for (int e = 0; e < 4; ++e) pk[e] = cvtpk(O[un >> 1][8 * (un & 1) + 2 * e], O[un >> 1][8 * (un & 1) + 2 * e + 1]);
        lds_w128(pw + un * 1024, pk);
      };
      auto qkphase = [&](auto Ic) {
        constexpr int I = decltype(Ic)::value;
        auto step = [&](auto Kc) {
          constexpr int k = decltype(Kc)::value;
          if constexpr (I + 1 < 8) {
            S[(I + 1) & 1] = mfma32(I < 0 ? K0[k] : K[k], Q[k], k == 0 ? negm : S[(I + 1) & 1]);
            if constexpr (k + KW < 12 && I >= 0) RK(k + KW, I + 1)
          }
          if constexpr (I < 0 && k < 8) piece(IC(1), IC(4 + k));
          if constexpr (I >= 0) { if constexpr (k & 1) piece(IC(I + 2), IC(6 + k / 2)); if constexpr (k < 11) sm(IC(I), IC(1), IC(k)); }
          if constexpr (I >= 0 && I < 7 && k >= 12 - VW) RV(k - (12 - VW), I)
          if constexpr (I == 7 && k < VW) RV(k, I)
          SBAR();
        };
        step(IC(0)); step(IC(1)); step(IC(2)); step(IC(3)); step(IC(4)); step(IC(5)); step(IC(6)); step(IC(7)); step(IC(8)); step(IC(9)); step(IC(10)); step(IC(11));
      };
      auto pvphase = [&](auto Ic) {
        constexpr int I = decltype(Ic)::value;
        auto step = [&](auto Uc) {
          constexpr int u = decltype(Uc)::value;
          if constexpr (I >= 0) {
            f32x16 z;
#pragma unroll
            for (int e = 0; e < 16; ++e) z[e] = 0.f;
            O[u >> 1] = mfma32(V[u], P[I & 1][u & 1], (I == 0 && !(u & 1)) ? z : O[u >> 1]);
            if constexpr (u & 1) piece(IC(I + 3), IC(u / 2));
            if constexpr (u + VW < 12) RV(u + VW, I)
          }
          if constexpr (I + 1 < 8 && u >= 1) sm(IC(I + 1), IC(0), IC(u - 1));
          if constexpr (I < 0 && (u & 1)) piece(IC(2), IC(u / 2));
          if constexpr (I + 2 < 8) {
            if constexpr (u == 12 - KW) waitvm<(I < 0 ? 12 : I + 3 < 8 ? 6 + (12 - KW) / 2 : 6)>();
            if constexpr (u >= 12 - KW) RK(u - (12 - KW), I + 2)
            if constexpr (u == 11) waitvm<(I + 3 < 8 ? 6 : 0)>();
          }
          if constexpr (I == 7 && u >= 3) {
            put_o(u - 3);
          }
          SBAR();
        };
        step(IC(0)); step(IC(1)); step(IC(2)); step(IC(3)); step(IC(4)); step(IC(5)); step(IC(6)); step(IC(7)); step(IC(8)); step(IC(9)); step(IC(10)); step(IC(11));
      };
#define STALE(v) (__builtin_expect(!safe && __any((v) != (v)), 0))
      bool done = false;
      do {
        qkphase(IC(-1));
        ST(15);
        pvphase(IC(-1));
        ST(16);
        qkphase(IC(0)); pvphase(IC(0)); qkphase(IC(1)); pvphase(IC(1)); qkphase(IC(2));
        const float seen = lsum[0];
        pvphase(IC(2));
        if (STALE(seen)) break;
        qkphase(IC(3)); pvphase(IC(3)); qkphase(IC(4)); pvphase(IC(4)); qkphase(IC(5));
        if (!late) asm volatile("s_barrier" ::: "memory");
        late = true;
        pvphase(IC(5)); qkphase(IC(6)); pvphase(IC(6)); qkphase(IC(7)); pvphase(IC(7));
        if (STALE(lsum[0])) break;
        { const u32x2 ls = {asu(lsum[0]), asu(lsum[1])}; lds_w64(lw, ls); }
        put_o(9); put_o(10); put_o(11);
        done = true;
      } while (false);
#pragma unroll
      for (int k = 0; k < 12; ++k) asm volatile("" : "=v"(K0[k]));
      if (__builtin_expect(done, 1)) {
        ST(17);
        BAR();
        ST(18);
        float lt[2];
        unsigned ll = lo[0];
        OPAQUE(ll);
        ll >>= 4;
        if (__builtin_expect(!low_sum(lds_r128f(L_L + (ll & 15) * 32), lds_r128f(L_L + (ll & 15) * 32 + 16), lt) || exact, 1)) break;
        BAR();
        float mx = -3.0e38f;
#pragma unroll 1
        for (unsigned i = 0; i < 8; ++i) {
#pragma unroll 1
          for (int k = 0; k < 12; ++k) { setm0<0>(ring + k * 1024); bdma<0>(lo[0], rs, (8 * w + i) * TILE_B + k * 1024); }
          waitvm<0>();
          f32x16 s;
#pragma unroll
          for (int e = 0; e < 16; ++e) s[e] = 0.f;
#pragma unroll
          for (int k = 0; k < 12; ++k) s = mfma32(lds_r128(((k & 1) ? kr1 : kr0) + k * 1024), Q[k], s);
#pragma unroll
          for (int e = 0; e < 16; ++e) mx = fmaxf(mx, s[e]);
        }
        lds_w32(L_N + ((ll & 31) * 8 + w * 2 + (ll >> 5)) * 4, mx);
        BAR();
        const f32x4 c = lds_r128f(L_N + (ll & 31) * 32), d = lds_r128f(L_N + (ll & 31) * 32 + 16);
        mref = fmaxf(fmaxf(fmaxf(c[0], c[1]), fmaxf(c[2], c[3])), fmaxf(fmaxf(d[0], d[1]), fmaxf(d[2], d[3])));
        exact = true;
      } else {
        unsigned ll = lo[0];
        OPAQUE(ll);
        for (int sp = 0; sp < SPIN_CAP; ++sp) {
          asm volatile("" ::: "memory");
          const u32x2 f = __builtin_amdgcn_raw_buffer_load_b64(rsWS, ll >> 1, WS_FLAG2 + w * 512, SC1);
          if (__all(f[0] == tag[0] && f[1] == tag[1])) break;
        }
      }
      safe = true;
      asm volatile("s_waitcnt vmcnt(0) lgkmcnt(0)" ::: "memory");
      P6(0, 0) P6(0, 6)
      waitvm<0>();
      piece(IC(1), IC(0)); piece(IC(1), IC(1)); piece(IC(1), IC(2)); piece(IC(1), IC(3));
#pragma unroll
      for (int k = 0; k < 12; ++k) K0[k] = lds_r128(((k & 1) ? kr1 : kr0) + k * 1024);
    }
    ST(19);
    BAR();
    ST_OUT(ws, 0, 12);
  } else {
    auto merge16 = [&](const u32x4 (&a)[6], const u32x4 (&bb)[6], const float sc, const unsigned where) {
      f32x4 d = {0, 0, 0, 0};
#pragma unroll
      for (int kk = 0; kk < 6; ++kk) d = mfma16(a[kk], bb[kk], d);
      __builtin_amdgcn_raw_buffer_store_b64(pack4(d * sc), rsWS, ((l >> 5) * 16 + (l & 15)) * 16 + ((l >> 4) & 1) * 8, where + b * 512, SC1);
    };
    auto rd16 = [&](u32x4 (&a)[6], const unsigned at) {
#pragma unroll
      for (int kk = 0; kk < 6; ++kk) a[kk] = lds_r128(lds0 + at + (l & 15) * 384 + (((4 * kk + (l >> 4)) ^ (l & 7)) * 16));
    };
    u32x2 tag7 = {0, 0};
    if (w < 2) {
      const unsigned ldsw = lds0 + 18432 * w;
      unsigned qp[9];
#pragma unroll
      for (int i = 0; i < 9; ++i) qp[i] = (qo[i] ^ 64) + 9216;
      DMA9(qp, wqkv_t, ldsw + 9216);
      ST(1);
      unsigned slot = SLOT;
      u32x2 tag;
#pragma unroll 1
      for (int c = 1; c < 6; ++c) {
        DMA9(qo, wqkv_t + c * SLOT, ldsw + slot); DMA9(qp, wqkv_t + c * SLOT, ldsw + slot + 9216);
        slot = slot == 2 * SLOT ? 0 : slot + SLOT;
        if (c == 1) tag = get_tag();
        if (c == 5) {
#pragma unroll
          for (int i = 0; i < 9; ++i) {
            const unsigned row = ((8 * i + (l >> 3)) * 171) >> 9;
            qo[i] = ((18432 * w + l * 16) ^ (((row & 2) | ((row & 8) >> 1)) << 4)) + (i >> 2) * 4096;
            qp[i] = (qo[i] ^ 64) + 9216;
          }
        }
        BARV(18);
      }
      DMA9(qo, wo_t, ldsw); DMA9(qp, wo_t, ldsw + 9216);
      BARV(18);
      DMA9(qo, wo_t + SLOT, ldsw + SLOT); DMA9(qp, wo_t + SLOT, ldsw + SLOT + 9216);
      ST(3);
      u32x4 f = __builtin_amdgcn_raw_buffer_load_b128(rsWS, l * 16, WS_FLAG + w * 1024, SC1);
      BARV(1);
      for (int sp = 0; sp < SPIN_CAP && !__all(tags_ok(f, tag)); ++sp) {
        asm volatile("" ::: "memory");
        f = __builtin_amdgcn_raw_buffer_load_b128(rsWS, l * 16, WS_FLAG + w * 1024, SC1);
      }
    } else if (w == 2) {
      if (merger) { rows16(wqkv_s + mrow * 1152, lds0 + L_X); rows16(wqkv_s + 16 * mtau * 1152 + 384, lds0 + L_X + 6144); }
      ST(1);
      BAR();
      BAR();
      u32x4 a[6], bb[6];
      waitvm<0>();
      if (merger) { rd16(a, L_X); rd16(bb, L_X + 6144); }
      ST(2);
      BAR();
      BAR();
      if (merger) merge16(a, bb, QSCALE, WS_WM);
      BAR();
      BAR();
    } else {
      const u32x4 pz = {POISON, POISON, POISON, POISON};
#pragma unroll
      for (int i = 0; i < 12; ++i) __builtin_amdgcn_raw_buffer_store_b128(pz, rsWS, l * 16 + (i & 3) * 1024, WS_H2 + t * FRAME_B + qb * TILE_B + (i >> 2) * 4096, SC1);
      __builtin_amdgcn_raw_buffer_store_b128(pz, rsWS, merger ? (l >> 5) * 73728 + (l & 31) * 16 : WS_END, WS_WM + b * 512, SC1);
      __builtin_amdgcn_raw_buffer_store_b32(POISON, rsWS, l * 4, WS_ST + (t * 1024 + qb * 32) * 8, SC1);
      asm volatile("s_nop 1" :: "v"(pz));
      tag7 = get_tag();
      ST(1);
      BAR(); BAR(); BAR();
      waitvm<0>();
      asm volatile("" :: "v"(d0), "v"(d1));
      if (l == 0) __builtin_amdgcn_raw_buffer_store_b64(tag7, rsWS, 0, WS_FLAG + b * 8, SC1);
      ST(2);
      BAR(); BAR(); BAR();
    }
    if (w >= 2) {
      const unsigned i16 = l & 15, g = l >> 4;
      unsigned a = lds0 + L_SP + ((w - 2) * 64 + l) * 16, pa = lds0 + L_PT + (w - 2) * 512 + l * 8;
      OPAQUE(a); OPAQUE(pa);
      __builtin_amdgcn_s_setprio(3);
      BAR();
      ST(4);
      const f32x4 s = (lds_r128f(a) + lds_r128f(a + 2048)) + (lds_r128f(a + 4096) + lds_r128f(a + 6144));
      f32x4 e = (s - xor16_max(fmaxf(fmaxf(s[0], s[1]), fmaxf(s[2], s[3])))) * QSCALE;
#pragma unroll
      for (int k = 0; k < 4; ++k) e[k] = __builtin_amdgcn_exp2f(e[k]);
      const float sum = xor16_add((e[0] + e[1]) + (e[2] + e[3]));
      const float inv = ((g >> 1) == (i16 >> 3)) ? __builtin_amdgcn_rcpf(sum) : 0.f;
      lds_w64(pa, pack4(e * inv));
      __builtin_amdgcn_s_setprio(0);
    }
    ST(5);
    BAR();
    if (b == 0 && w == 3 && l == 0) __builtin_amdgcn_raw_buffer_store_b64(tag7, rsWS, 0, WS_EPOCH, SC1);
    OPAQUE(l);
    LANE_IDS;
    FRAG_POS;
    u32x4 wq[18], wv[18];
    auto ldw = [&](u32x4 (&W)[18], const int m, const int i0, const int i1) {
#pragma unroll
      for (int i = i0; i < i1; ++i) W[i] = __builtin_amdgcn_raw_buffer_load_b128(rsWS, l * 16 + (i & 3) * 1024, WS_WM + m * 73728 + w * 18432 + (i >> 2) * 4096, SC1);
    };
    ldw(wq, 0, 0, 8);
    BAR();
    ldw(wq, 0, 8, 18);
    unsigned hqa[2][2], qw[3], own[3][2];
#pragma unroll
    for (int hf = 0; hf < 2; ++hf) {
      const unsigned in = lds0 + L_HQ + ((4 * hf + tq) ^ hh) * 128 + ((g & 1) ^ hf) * 64 + tp * 16;
      hqa[hf][0] = in + hh * 1024; hqa[hf][1] = in + 2048 + hh * (SLOT - 2048);
      OPAQUE(hqa[hf][0]); OPAQUE(hqa[hf][1]);
    }
    const unsigned tok = 16 * (g & 1) + i16;
    unsigned nw = lds0 + L_N + l * 16 + w * 4;
    OPAQUE(nw);
#pragma unroll
    for (int i = 0; i < 3; ++i) {
      qw[i] = lds0 + L_Q + ((3 * w + i) * 64 + (g >> 1) * 32 + i16) * 16 + 8 * (g & 1);
      own[i][0] = lds0 + w * SLOT + L_HQ + i * 1024 + (tq ^ ((w + i) & 1)) * 128 + hh * 64 + tp * 16 + 8 * (g & 1);
      own[i][1] = (own[i][0] ^ 64) + 512;
      OPAQUE(qw[i]); OPAQUE(own[i][0]); OPAQUE(own[i][1]);
    }
    BAR();
    constexpr unsigned L_M2 = 2 * SLOT + TILE_B;
    const bool merge2 = merger && w == 2;
    if (merge2) {
      rows16(wqkv_s + mrow * 1152 + 768, lds0 + L_M2);
#pragma unroll
      for (int i = 0; i < 6; ++i) DMA1((l >> 1) * 384 + (l & 1) * 16, wo_s + i * 12288 + mtau * 32, lds0 + L_M2 + 6144 + i * 1024);
    }
    ST(6);
    BAR();
    ST(7);
    u32x4 ma[6], mb[6];
    u32x4 hq[3];
    {
      const u64 hbu = (u64)(ws + WS_H2 + (size_t)t * FRAME_B);
      const u32x4 rs = {(unsigned)hbu, (unsigned)(hbu >> 32) & 0xffffu, (unsigned)FRAME_B, 0x00020000u};
      for (int sp = 0; sp < SPIN_CAP; ++sp) {
        setm0<L_HQ>(lds0 + w * SLOT);
        bdma<0>(l * 16, rs, qb * TILE_B + w * 3072); bdma<1024>(l * 16, rs, qb * TILE_B + w * 3072); bdma<2048>(l * 16, rs, qb * TILE_B + w * 3072);
        if (merge2 && sp == 0) {
          waitvm<3>();
          rd16(ma, L_M2);
#pragma unroll
          for (int kk = 0; kk < 6; ++kk) { const unsigned at = lds0 + L_M2 + 6144 + (32 * kk + 8 * g + tq) * 32 + tp * 8; mb[kk] = lds_tr2(at, at + 128); }
        }
        waitvm<0>();
#pragma unroll
        for (int i = 0; i < 3; ++i) { const u32x2 u0 = lds_r64(own[i][0]), u1 = lds_r64(own[i][1]); hq[i] = u32x4{u0[0], u0[1], u1[0], u1[1]}; }
        if (!poisoned(hq)) break;
      }
    }
    ST(8);
    BAR();
    ST(9);
    auto gemm = [&](const u32x4 (&wf)[18], const u32x4 (&bf)[6][2], auto&& fin) {
      f32x4 d[3][2];
#pragma unroll
      for (int i = 0; i < 3; ++i) {
#pragma unroll
        for (int ks = 0; ks < 6; ++ks) {
          d[i][0] = mfma16(wf[i * 6 + ks], bf[ks][0], ks ? d[i][0] : f32x4{0, 0, 0, 0});
          d[i][1] = mfma16(wf[i * 6 + ks], bf[ks][1], ks ? d[i][1] : f32x4{0, 0, 0, 0});
          if (ks == 0) SBAR();
        }
        if (i > 0) fin(i - 1, d[i - 1]);
        SBAR();
      }
      fin(2, d[2]);
    };
    {
      u32x4 bf[6][2];
#pragma unroll
      for (int ks = 0; ks < 6; ++ks)
#pragma unroll
        for (int hf = 0; hf < 2; ++hf) {
          if (ks == 3) { hqa[hf][0] += 2 * SLOT; hqa[hf][1] += 2 * SLOT; }
          bf[ks][hf] = lds_r128(ks % 3 == 1 ? hqa[hf][1] : hqa[hf][0] + (ks % 3 == 2 ? SLOT + 1024 : 0));
        }
      SBAR();
#pragma unroll 1
      for (int sp = 0;; ++sp) {
        f32x2 n[2] = {{0.f, 0.f}, {0.f, 0.f}};
        gemm(wq, bf, [&](const int i, const f32x4 (&d)[2]) {
#pragma unroll
          for (int hf = 0; hf < 2; ++hf) {
            lds_w64(qw[i] + hf * 256, pack4(d[hf]));
            const f32x2 a = {d[hf][0], d[hf][1]}, c = {d[hf][2], d[hf][3]};
            n[hf] = __builtin_elementwise_fma(a, a, n[hf]); n[hf] = __builtin_elementwise_fma(c, c, n[hf]);
          }
        });
        auto rr = __builtin_amdgcn_permlane16_swap(asu(n[0][0] + n[0][1]), asu(n[1][0] + n[1][1]), false, false);
        const float nn = asf(rr[0]) + asf(rr[1]);
        lds_w32(nw, nn);
        if (__builtin_expect(!__any(nn != nn), 1) || sp >= SPIN_CAP || !poisoned(wq)) break;
        asm volatile("" ::: "memory");
        ldw(wq, 0, 0, 18);
      }
    }
    ST(11);
    BAR();
    if (merge2) merge16(ma, mb, 1.0f, WS_WM + 73728);
    unsigned oo = (t * 1024 + qb * 32 + tok) * 384 + (6 * w + hh) * 16;
    OPAQUE(oo);
    float lt[2];
    u32x4 pick;
#pragma unroll
    for (int e = 0; e < 4; ++e) {
      const unsigned d = 8 * (e >> 1) + 4 * (g & 1) + 2 * (e & 1);
      pick[e] = (i16 == d ? 0x3f80u : 0u) | (i16 == d + 1 ? 0x3f800000u : 0u);
    }
    unsigned pr[2], la = lds0 + L_L + i16 * 32;
    pr[0] = lds0 + hh * SLOT + 3 * w * 1024 + ((g & 1) * 32 + i16) * 16; pr[1] = pr[0] + 2 * SLOT;
    OPAQUE(pick); OPAQUE(pr[0]); OPAQUE(pr[1]); OPAQUE(la);
#pragma unroll
    for (int hf = 0; hf < 2; ++hf) {
      fr[hf] += L_FI; OPAQUE(fr[hf]);
#pragma unroll
      for (int i = 0; i < 3; ++i) { dpos[i][hf] += L_FI; OPAQUE(dpos[i][hf]); }
    }
    BAR();
    ST(16);
    f32x4 x1[3][2];
    {
      u32x2 st[2];
      for (int sp = 0; sp < SPIN_CAP; ++sp) {
        asm volatile("" ::: "memory");
        st[0] = __builtin_amdgcn_raw_buffer_load_b64(rsWS, i16 * 8, WS_ST + (t * 1024 + qb * 32) * 8, SC1);
        st[1] = __builtin_amdgcn_raw_buffer_load_b64(rsWS, i16 * 8 + 128, WS_ST + (t * 1024 + qb * 32) * 8, SC1);
        ldw(wv, 1, 0, 18);
        if (!__any(max(max(st[0][0], st[0][1]), max(st[1][0], st[1][1])) == POISON) && !poisoned(wv)) break;
      }
#pragma unroll
      for (int hf = 0; hf < 2; ++hf) {
        const float mu = asf(st[hf][0]), sd = __builtin_amdgcn_sqrtf(asf(st[hf][1]));
#pragma unroll
        for (int i = 0; i < 3; ++i) {
          const f32x4 h = {bflo(hq[i][2 * hf]), bfhi(hq[i][2 * hf]), bflo(hq[i][2 * hf + 1]), bfhi(hq[i][2 * hf + 1])};
          x1[i][hf] = h * sd + mu;
          OPAQUE(x1[i][hf]);
        }
      }
    }
    ST(12);
#pragma unroll 1
    for (bool exact = false;; exact = true) {
      BAR();
      ST(13);
      u32x4 p[3][2][2];
#pragma unroll
      for (int i = 0; i < 3; ++i)
#pragma unroll
        for (int hf = 0; hf < 2; ++hf) { p[i][hf][0] = lds_r128(pr[0] + i * 1024 + hf * 256); p[i][hf][1] = lds_r128(pr[1] + i * 1024 + hf * 256); }
      const f32x4 l0 = lds_r128f(la), l1 = lds_r128f(la + 16);
      SBAR();
#pragma unroll
      for (int i = 0; i < 3; ++i)
#pragma unroll
        for (int hf = 0; hf < 2; ++hf) lds_w64(dpos[i][hf], pack4(mfma16(pick, p[i][hf][1], mfma16(pick, p[i][hf][0], f32x4{0, 0, 0, 0}))));
      if (__builtin_expect(!low_sum(l0, l1, lt) || exact, 1)) break;
      BAR();
      BAR();
    }
    ST(14);
    const float linv[2] = {__builtin_amdgcn_rcpf(lt[0]), __builtin_amdgcn_rcpf(lt[1])};
    BAR();
    ST(15);
    u32x4 bf[6][2];
#pragma unroll
    for (int ks = 0; ks < 6; ++ks) { bf[ks][0] = lds_r128(fr[0] + ks * 1024); bf[ks][1] = lds_r128(fr[1] + ks * 1024); }
    SBAR();
    gemm(wv, bf, [&](const int i, const f32x4 (&d)[2]) {
      const u32x2 y0 = pack4(d[0] * linv[0] + x1[i][0]), y1 = pack4(d[1] * linv[1] + x1[i][1]);
      auto s0 = __builtin_amdgcn_permlane16_swap(y0[0], y1[0], false, false);
      auto s1 = __builtin_amdgcn_permlane16_swap(y0[1], y1[1], false, false);
      const u32x4 o = {s0[0], s1[0], s0[1], s1[1]};
      *(u32x4*)(out + oo + i * 32) = o;
    });
    ST(17);
    ST_OUT(ws, 12, 20);
  }
}

extern "C" void kernel_launch(void* const* d_in, const int* in_sizes, int n_in, void* d_out, int out_size, void* d_ws, size_t ws_size, hipStream_t stream) {
  (void)hipFuncSetAttribute((const void*)k_timesformer, hipFuncAttributeMaxDynamicSharedMemorySize, LDS_BYTES);
  k_timesformer<<<256, 512, LDS_BYTES, stream>>>((const char*)d_in[0], (const char*)d_in[1], (const char*)d_in[2], (const char*)d_in[3], (const char*)d_in[4],
                                                 (char*)d_ws, (char*)d_out);
}
```

```cpp
#include <hip/hip_runtime.h>
#include <hip/hip_bf16.h>
#include <type_traits>

typedef short s16x4 __attribute__((ext_vector_type(4)));
typedef __bf16 b16x8 __attribute__((ext_vector_type(8)));
typedef __bf16 b16x2 __attribute__((ext_vector_type(2)));
typedef float f32x2 __attribute__((ext_vector_type(2)));
typedef float f32x4 __attribute__((ext_vector_type(4)));
typedef float f32x16 __attribute__((ext_vector_type(16)));
typedef unsigned u32x2 __attribute__((ext_vector_type(2)));
typedef unsigned u32x4 __attribute__((ext_vector_type(4)));
typedef unsigned long long u64;
#define LAS __attribute__((address_space(3)))
#define DI __device__ __forceinline__
#define SBAR() __builtin_amdgcn_sched_barrier(0)

constexpr int TILE_B = 12288;
constexpr int FRAME_B = 393216;
constexpr unsigned WS_H2 = 0, WS_ST = 8 * FRAME_B, WS_WM = 16 * FRAME_B, WS_FLAG = WS_WM + 2 * 73728, WS_FLAG2 = WS_FLAG + 2048, WS_EPOCH = WS_FLAG2 + 2048, WS_END = WS_EPOCH + 256;
constexpr float QSCALE = 0.07216878364870322f * 1.4426950408889634f;
constexpr int SC1 = 16;
constexpr unsigned POISON = 0xffffffffu;
constexpr unsigned CODE_BYTES = 26624;
constexpr int TILE0_DELAY = 2;
constexpr int SPIN_CAP = 1 << 16;


constexpr int PA = 6, PB = 9, ROLE = 0; constexpr size_t WS_T0 = 32u << 20;
#define ST_DECL u64 sa_ = 0, sb_ = 0
#define ST(k) do { if ((k) == 0) { SBAR(); u64 t_; asm volatile("s_memrealtime %0\n\ts_waitcnt lgkmcnt(0)" : "=s"(t_) :: "memory"); SBAR(); \
    if (threadIdx.x == 448) __hip_atomic_store((u64*)(ws + WS_T0) + blockIdx.x, t_, __ATOMIC_RELAXED, __HIP_MEMORY_SCOPE_AGENT); } \
    if ((k) == PA || (k) == PB) { SBAR(); u64 t_; asm volatile("s_memtime %0\n\ts_waitcnt lgkmcnt(0)" : "=s"(t_) :: "memory"); SBAR(); if ((k) == PA) sa_ = t_; else sb_ = t_; } } while (0)
#define ST_OUT(ws, k0, k1) do { if (ROLE == ((k0) != 0)) { u64 m_ = ~0ull; \
    for (int q_ = 0; q_ < 4; ++q_) { u64 v_ = __hip_atomic_load((u64*)(ws + WS_T0) + (threadIdx.x & 63) * 4 + q_, __ATOMIC_RELAXED, __HIP_MEMORY_SCOPE_AGENT); m_ = v_ < m_ ? v_ : m_; } \
    for (int o_ = 32; o_; o_ >>= 1) { u64 v_ = __shfl_xor(m_, o_); m_ = v_ < m_ ? v_ : m_; } \
    int d_ = (int)(sb_ - sa_) / 20; if (d_ < 0) d_ = 0; if (d_ > 5000) d_ = 5000; \
    const u64 e_ = m_ + 4000 + 4 * d_; while (__builtin_amdgcn_s_memrealtime() < e_) __builtin_amdgcn_s_sleep(1); } } while (0)


DI float asf(unsigned u) { return __builtin_bit_cast(float, u); }
DI unsigned asu(float f) { return __builtin_bit_cast(unsigned, f); }
DI unsigned cvtpk(float lo, float hi) { f32x2 v = {lo, hi}; return __builtin_bit_cast(unsigned, __builtin_convertvector(v, b16x2)); }
DI float bflo(unsigned u) { return asf(u << 16); }
DI float bfhi(unsigned u) { return asf(u & 0xffff0000u); }
DI f32x16 mfma32(u32x4 a, u32x4 b, f32x16 c) { return __builtin_amdgcn_mfma_f32_32x32x16_bf16(__builtin_bit_cast(b16x8, a), __builtin_bit_cast(b16x8, b), c, 0, 0, 0); }
DI f32x4 mfma16(u32x4 a, u32x4 b, f32x4 c) { return __builtin_amdgcn_mfma_f32_16x16x32_bf16(__builtin_bit_cast(b16x8, a), __builtin_bit_cast(b16x8, b), c, 0, 0, 0); }
DI f32x4 mfma16k16(u32x2 a, u32x2 b, f32x4 c) { return __builtin_amdgcn_mfma_f32_16x16x16bf16_1k(__builtin_bit_cast(s16x4, a), __builtin_bit_cast(s16x4, b), c, 0, 0, 0); }
DI u32x2 pack4(f32x4 v) { u32x2 r = {cvtpk(v[0], v[1]), cvtpk(v[2], v[3])}; return r; }
DI u32x4 lds_r128(unsigned a) { return *(LAS u32x4*)(size_t)a; }
DI f32x4 lds_r128f(unsigned a) { return *(LAS f32x4*)(size_t)a; }
DI u32x2 lds_r64(unsigned a) { return *(LAS u32x2*)(size_t)a; }
DI void lds_w128(unsigned a, u32x4 v) { *(LAS u32x4*)(size_t)a = v; }
DI void lds_w128f(unsigned a, f32x4 v) { *(LAS f32x4*)(size_t)a = v; }
DI void lds_w64(unsigned a, u32x2 v) { *(LAS u32x2*)(size_t)a = v; }
DI void lds_w32(unsigned a, float v) { *(LAS float*)(size_t)a = v; }
DI u32x2 lds_tr(unsigned a) { return __builtin_bit_cast(u32x2, __builtin_amdgcn_ds_read_tr16_b64_v4i16((LAS s16x4*)(size_t)a)); }
DI u32x4 lds_tr2(unsigned a0, unsigned a1) { u32x2 x = lds_tr(a0), y = lds_tr(a1); u32x4 r = {x[0], x[1], y[0], y[1]}; return r; }
#define BAR() asm volatile("s_waitcnt lgkmcnt(0)\n\ts_barrier" ::: "memory")
#define BARV(n) asm volatile("s_waitcnt vmcnt(" #n ") lgkmcnt(0)\n\ts_barrier" ::: "memory")
template <int CTRL> DI float dpp_add(float v) { return v + asf(__builtin_amdgcn_update_dpp(0u, asu(v), CTRL, 0xf, 0xf, true)); }
DI float xor16_add(float v) { auto r = __builtin_amdgcn_permlane16_swap(asu(v), asu(v), false, false); return asf(r[0]) + asf(r[1]); }
DI float xor16_max(float v) { auto r = __builtin_amdgcn_permlane16_swap(asu(v), asu(v), false, false); return fmaxf(asf(r[0]), asf(r[1])); }
DI unsigned umax3(unsigned a, unsigned b, unsigned c) { return max(max(a, b), c); }
template <int N> DI bool poisoned(const u32x4 (&v)[N]) {
  unsigned m = 0;
#pragma unroll
  for (int i = 0; i < N; ++i) { m = umax3(m, v[i][0], v[i][1]); m = umax3(m, v[i][2], v[i][3]); }
  return __any(m == POISON);
}
DI u32x4 unpoison(u32x4 v) { u32x4 r = {min(v[0], POISON - 1), min(v[1], POISON - 1), min(v[2], POISON - 1), min(v[3], POISON - 1)}; return r; }
DI unsigned fi_pos(unsigned r, unsigned c8) {
  return (((r >> 4) * 6 + (c8 >> 2)) * 64 + (c8 & 3) * 16 + ((r & 15) ^ ((c8 & 1) | (((r >> 3) & 1) << 1) | ((r >> 4) << 2)))) * 16;
}
DI float row8_sum(float s) { s = dpp_add<0x124>(s); s = dpp_add<0x128>(s); return xor16_add(s); }
DI float sum24(const f32x2 (&v)[12]) {
  f32x2 s0 = v[0] + v[1], s1 = v[2] + v[3];
#pragma unroll
  for (int i = 4; i < 12; i += 2) { s0 += v[i]; s1 += v[i + 1]; }
  s0 += s1;
  return s0[0] + s0[1];
}
DI float sumsq24(const f32x2 (&v)[12]) {
  f32x2 q0 = v[0] * v[0], q1 = v[1] * v[1];
#pragma unroll
  for (int i = 2; i < 12; i += 2) { q0 = __builtin_elementwise_fma(v[i], v[i], q0); q1 = __builtin_elementwise_fma(v[i + 1], v[i + 1], q1); }
  q0 += q1;
  return q0[0] + q0[1];
}
DI f32x2 unpack2(unsigned u) { f32x2 r = {bflo(u), bfhi(u)}; return r; }

constexpr unsigned SLOT = 36864, L_X = 3 * SLOT, L_H = 4 * SLOT, L_N = L_H + TILE_B, L_L = L_N + 1024, L_PT = L_L + 1024, LDS_BYTES = 163840;
constexpr unsigned L_VI = L_X, L_SP = L_X + TILE_B, L_YI = L_X + 20480;
constexpr unsigned L_HQ = 24576, L_Q = L_H, L_FI = L_H;

#define DMA4(v0, v1, v2, v3, sbase, ldsa)                                                                  \
  asm volatile("s_mov_b32 m0, %5\n\ts_nop 0\n\t"                                                            \
               "global_load_lds_dwordx4 %0, %4\n\tglobal_load_lds_dwordx4 %1, %4 offset:1024\n\t"           \
               "global_load_lds_dwordx4 %2, %4 offset:2048\n\tglobal_load_lds_dwordx4 %3, %4 offset:3072"   \
               :: "v"(v0), "v"(v1), "v"(v2), "v"(v3), "s"(sbase), "s"(ldsa) : "memory")
#define DMA1(v0, sbase, ldsa) \
  asm volatile("s_mov_b32 m0, %2\n\ts_nop 0\n\tglobal_load_lds_dwordx4 %0, %1" :: "v"(v0), "s"(sbase), "s"(ldsa) : "memory")
#define DMA9(o, sbase, ldsa) do { DMA4(o[0], o[1], o[2], o[3], sbase, ldsa); DMA4(o[4], o[5], o[6], o[7], sbase, (ldsa) + 4096); DMA1(o[8], sbase, (ldsa) + 8192); } while (0)

template <int OFF> DI void bdma(unsigned voff, u32x4 rs, unsigned soff) { asm volatile("buffer_load_dwordx4 %0, %1, %2 offen offset:%3 sc1 lds" :: "v"(voff), "s"(rs), "s"(soff), "i"(OFF) : "memory"); }
template <int OFF> DI void setm0(unsigned base) { asm volatile("s_add_u32 m0, %0, %1\n\ts_nop 0" :: "s"(base), "i"(OFF) : "memory", "scc"); }
template <int N> DI void waitvm() { asm volatile("s_waitcnt vmcnt(%0)" :: "i"(N) : "memory"); }
#define IC(x) std::integral_constant<int, (x)>{}
#define OPAQUE(v) asm volatile("" : "+v"(v))
template <class R> DI void st128(u32x4 v, R rs, unsigned voff, unsigned soff) { __builtin_amdgcn_raw_buffer_store_b128(v, rs, voff, soff, SC1); asm volatile("s_nop 1" :: "v"(v)); }
DI bool tags_ok(u32x4 f, u32x2 tag) { return f[0] == tag[0] && f[1] == tag[1] && f[2] == tag[0] && f[3] == tag[1]; }

__global__ __launch_bounds__(512, 2) void k_timesformer(const char* __restrict__ x, const char* __restrict__ wqkv_t, const char* __restrict__ wo_t,
                                                        const char* __restrict__ wqkv_s, const char* __restrict__ wo_s, char* __restrict__ ws, char* __restrict__ out) {
  u64 pc;
  asm volatile("s_getpc_b64 %0" : "=s"(pc));
  constexpr unsigned lds0 = 0;
  const unsigned b = blockIdx.x;
  unsigned w8 = __builtin_amdgcn_readfirstlane(threadIdx.x >> 6);
  unsigned l = threadIdx.x & 63;
  const unsigned t = b & 7, qb = b >> 3;
  const auto rsWS = __builtin_amdgcn_make_buffer_rsrc((void*)ws, 0, WS_END, 0x00020000);
  ST_DECL;
  ST(0);
  const bool merger = b < 144;
  const unsigned mtau = (b >> 1) / 6, mrow = ((b >> 1) - mtau * 6) * 32 + (b & 1) * 16;
  auto rows16 = [&](const char* src, const unsigned dst) {
#pragma unroll
    for (int i = 0; i < 3; ++i) {
      const unsigned pos = i * 64 + l, row = (pos * 2731) >> 16, o = row * 1152 + (((pos - row * 24) ^ row) * 16);
      DMA1(o, src, dst + i * 1024); DMA1(o, src + 8 * 1152, dst + (3 + i) * 1024);
    }
  };
  unsigned qo[9], d0, d1;
  if (w8 < 4) {
#pragma unroll
    for (int i = 0; i < 3; ++i) { const unsigned p = i * 64 + l; DMA1(p * 16 + (p >= 96 ? FRAME_B - 1536 : 0), x + w8 * 2 * FRAME_B + b * 1536, lds0 + L_YI + w8 * 3072 + i * 1024); }
  } else if (w8 < 6) {
    const unsigned qa = 18432 * (w8 & 1) + l * 16, qc = qa ^ 32;
    qo[0] = qa; qo[1] = qa; qo[2] = l >= 16 ? qc : qa; qo[3] = qc; qo[5] = qa + 4096; qo[7] = qc + 4096;
    qo[4] = l < 32 ? qo[7] : qo[5]; qo[6] = l >= 48 ? qo[7] : qo[5]; qo[8] = qc + 8192;
    DMA9(qo, wqkv_t, lds0 + 18432 * (w8 & 1));
  } else if (w8 == 7) {
    const unsigned line = qb * 72 + l, line2 = qb * 72 + 64 + (l & 7);
    const char* p0 = line < 1728 ? wqkv_t + line * 128 : wo_t + (line - 1728) * 128;
    const char* p1 = line2 < 1728 ? wqkv_t + line2 * 128 : wo_t + (line2 - 1728) * 128;
    if (l >= 8) p1 = (const char*)(pc + min((qb * 8 + (l & 7)) * 128, CODE_BYTES));
    asm volatile("global_load_dword %0, %2, off\n\tglobal_load_dword %1, %3, off" : "=&v"(d0), "=&v"(d1) : "v"(p0), "v"(p1) : "memory");
  }
  asm volatile("" : "+s"(w8));
  const unsigned w = w8 & 3;
  auto get_tag = [&]() {
    u64 e;
    asm volatile("s_load_dwordx2 %0, %1, 0x0\n\ts_waitcnt lgkmcnt(0)" : "=s"(e) : "s"(ws + WS_EPOCH) : "memory");
    e += 1;
    return u32x2{(unsigned)e, (unsigned)(e >> 32)};
  };
#define LANE_IDS const unsigned i16 = l & 15, g = l >> 4, r32 = l & 31, hh = l >> 5, tq = i16 >> 2, tp = l & 3
#define FRAG_POS unsigned fr[2], dpos[3][2];                                                                        \
  _Pragma("unroll") for (int hf = 0; hf < 2; ++hf) { fr[hf] = lds0 + fi_pos(16 * hf + i16, g);                     \
    _Pragma("unroll") for (int i = 0; i < 3; ++i) dpos[i][hf] = lds0 + fi_pos(16 * hf + i16, 2 * (3 * w + i) + (g >> 1)) + 8 * (g & 1); }
  auto low_sum = [&](const f32x4 a, const f32x4 c, float (&lt)[2]) {
    lt[0] = (a[0] + a[2]) + (c[0] + c[2]);
    lt[1] = (a[1] + a[3]) + (c[1] + c[3]);
    return (bool)__any(lt[0] < 8.7e-19f || lt[1] < 8.7e-19f);
  };

  if (w8 < 4) {
    const unsigned nl = l & 3, j = (l >> 2) & 7, tt = 2 * w + (l >> 5), r = nl * 8 + tt;
    const unsigned so = tt * FRAME_B + (j >> 1) * 1024 + ((t ^ ((j >> 1) & 1)) * 128) + (((j & 1) ^ (t >> 2)) * 64) + nl * 16, sso = qb * TILE_B;
    LANE_IDS;
    FRAG_POS;
    const unsigned phi = ((tq >> 1) & 1) | ((g & 1) << 1);
    unsigned tcol[3], rpos[3];
#pragma unroll
    for (int i = 0; i < 3; ++i) { tcol[i] = (((3 * w + i) ^ phi) * 2 + (tp >> 1)) * 16 + 8 * (tp & 1); rpos[i] = lds0 + fi_pos(r, j + 8 * i); }
    const u32x2 tag = get_tag();
    waitvm<0>();
    f32x2 xv[12];
    float mu1;
    {
      u32x4 h[3], xr[3];
#pragma unroll
      for (int i = 0; i < 3; ++i) xr[i] = lds_r128(lds0 + L_YI + tt * 1536 + nl * 384 + j * 16 + i * 128);
#pragma unroll
      for (int i = 0; i < 12; ++i) xv[i] = unpack2(xr[i >> 2][i & 3]);
      mu1 = row8_sum(sum24(xv)) * (1.0f / 192.0f);
#pragma unroll
      for (int i = 0; i < 12; ++i) xv[i] -= mu1;
      const float rs = __builtin_amdgcn_rsqf(row8_sum(sumsq24(xv)) * (1.0f / 192.0f) + 1e-5f);
#pragma unroll
      for (int i = 0; i < 12; ++i) { const f32x2 n = xv[i] * rs; h[i >> 2][i & 3] = cvtpk(n[0], n[1]); }
#pragma unroll
      for (int i = 0; i < 3; ++i) lds_w128(rpos[i] + L_H, h[i]);
    }
    f32x4 acc[3][3][2];
#pragma unroll
    for (int m = 0; m < 3; ++m)
#pragma unroll
      for (int i = 0; i < 3; ++i) { acc[m][i][0] = f32x4{0, 0, 0, 0}; acc[m][i][1] = f32x4{0, 0, 0, 0}; }
    unsigned wa[3], vfa[2], ypos[3][2], yr[3];
#pragma unroll
    for (int i = 0; i < 3; ++i) {
      wa[i] = lds0 + (8 * g + tq) * 384 + tcol[i]; yr[i] = rpos[i] + L_YI; OPAQUE(wa[i]); OPAQUE(yr[i]);
#pragma unroll
      for (int hf = 0; hf < 2; ++hf) { ypos[i][hf] = dpos[i][hf] + L_VI; OPAQUE(ypos[i][hf]); }
    }
    vfa[0] = fr[0] + L_VI; vfa[1] = fr[1] + L_VI; OPAQUE(vfa[0]); OPAQUE(vfa[1]);
    unsigned spa = lds0 + L_SP + (w * 128 + l) * 16, pta = lds0 + L_PT + l * 8;
    unsigned sto = j == 0 ? (tt * 1024 + nl) * 8 : WS_END;
    OPAQUE(spa); OPAQUE(pta); OPAQUE(sto);
    u32x4 hf0, hf1;
#pragma unroll
    for (int ks = 0; ks < 6; ++ks) {
      BAR();
      if (ks == 0) { ST(1); hf0 = lds_r128(fr[0] + L_H); hf1 = lds_r128(fr[1] + L_H); }
      const unsigned base = lds0 + (ks % 3) * SLOT + (8 * g + tq) * 1152;
      u32x4 a[9];
#pragma unroll
      for (int m = 0; m < 3; ++m)
#pragma unroll
        for (int i = 0; i < 3; ++i) a[m * 3 + i] = lds_tr2(base + tcol[i] + m * 384, base + tcol[i] + m * 384 + 4608);
      const u32x4 h0 = hf0, h1 = hf1;
      if (ks < 5) { hf0 = lds_r128(fr[0] + L_H + (ks + 1) * 1024); hf1 = lds_r128(fr[1] + L_H + (ks + 1) * 1024); }
#pragma unroll
      for (int m = 0; m < 3; ++m)
#pragma unroll
        for (int i = 0; i < 3; ++i) {
          acc[m][i][0] = mfma16(a[m * 3 + i], h0, acc[m][i][0]);
          acc[m][i][1] = mfma16(a[m * 3 + i], h1, acc[m][i][1]);
        }
      if (ks == 0) __builtin_amdgcn_sched_group_barrier(0x100, 8, 0); else __builtin_amdgcn_sched_group_barrier(0x100, 6, 0);
#pragma unroll
      for (int q = 0; q < 7; ++q) { __builtin_amdgcn_sched_group_barrier(0x8, 2, 0); __builtin_amdgcn_sched_group_barrier(0x100, 2, 0); }
      __builtin_amdgcn_sched_group_barrier(0x8, 4, 0);
      SBAR();
    }
    ST(2);
    u32x2 vpk[3][2];
    f32x4 spart[2];
#pragma unroll
    for (int hf = 0; hf < 2; ++hf) {
      spart[hf] = f32x4{0, 0, 0, 0};
#pragma unroll
      for (int i = 0; i < 3; ++i) { spart[hf] = mfma16k16(pack4(acc[1][i][hf]), pack4(acc[0][i][hf]), spart[hf]); vpk[i][hf] = pack4(acc[2][i][hf]); }
    }
    u32x4 bw[6][3];
    auto rdw = [&](const int k) {
#pragma unroll
      for (int i = 0; i < 3; ++i) bw[k][i] = lds_tr2(wa[i] + k * 12288, wa[i] + k * 12288 + 1536);
    };
    ST(3);
#pragma unroll
    for (int hf = 0; hf < 2; ++hf)
#pragma unroll
      for (int i = 0; i < 3; ++i) lds_w64(ypos[i][hf], vpk[i][hf]);
    lds_w128f(spa, spart[0]); lds_w128f(spa + 1024, spart[1]);
    ST(5);
    BAR();
    ST(6);
    {
      f32x4 vp[3][2];
      u32x4 vf[6][2];
      u32x2 pt[2];
      auto rd = [&](const int k) { vf[k][0] = lds_r128(vfa[0] + k * 1024); vf[k][1] = lds_r128(vfa[1] + k * 1024); rdw(k); };
      auto mm = [&](const int k) {
#pragma unroll
        for (int i = 0; i < 3; ++i) {
          vp[i][0] = mfma16(vf[k][0], bw[k][i], k ? vp[i][0] : f32x4{0, 0, 0, 0});
          vp[i][1] = mfma16(vf[k][1], bw[k][i], k ? vp[i][1] : f32x4{0, 0, 0, 0});
        }
      };
#define MIX6() _Pragma("unroll") for (int q_ = 0; q_ < 6; ++q_) { __builtin_amdgcn_sched_group_barrier(0x8, 1, 0); __builtin_amdgcn_sched_group_barrier(0x100, 2, 0); } SBAR()
      rd(0); SBAR(); rd(1);
      SBAR();
      mm(0); rd(2); MIX6();
      mm(1); rd(3); MIX6();
      mm(2); rd(4); MIX6();
      mm(3); rd(5); MIX6();
      mm(4);
      SBAR();
      ST(7);
      asm volatile("s_barrier" ::: "memory");
      pt[0] = lds_r64(pta); pt[1] = lds_r64(pta + 512);
      SBAR();
      mm(5);
      ST(8);
#pragma unroll
      for (int i = 0; i < 3; ++i)
#pragma unroll
        for (int hf = 0; hf < 2; ++hf) lds_w64(ypos[i][hf] + (L_YI - L_VI), pack4(mfma16k16(pack4(vp[i][hf]), pt[hf], f32x4{0, 0, 0, 0})));
    }
    ST(9);
    BAR();
    {
      u32x4 y[3], h[3];
#pragma unroll
      for (int i = 0; i < 3; ++i) y[i] = lds_r128(yr[i]);
#pragma unroll
      for (int i = 0; i < 12; ++i) xv[i] += unpack2(y[i >> 2][i & 3]);
      const float mu = row8_sum(sum24(xv)) * (1.0f / 192.0f), sq = row8_sum(sumsq24(xv));
      const float var = fmaxf(fmaf(sq, 1.0f / 192.0f, -mu * mu), 0.f) + 1e-5f;
      const float rs = __builtin_amdgcn_rsqf(var), nb = -mu * rs;
#pragma unroll
      for (int i = 0; i < 3; ++i) {
#pragma unroll
        for (int e = 0; e < 4; ++e) { const f32x2 n = xv[4 * i + e] * rs + nb; h[i][e] = cvtpk(n[0], n[1]); }
        st128(h[i], rsWS, so, WS_H2 + sso + i * 4096);
        SBAR();
      }
      const u32x2 stat = {asu(mu + mu1), asu(var)};
      __builtin_amdgcn_raw_buffer_store_b64(stat, rsWS, sto, WS_ST + b * 32, SC1);
    }
    ST(10);
    asm volatile("s_barrier" ::: "memory");
    const unsigned ring = lds0 + w * SLOT;
    unsigned kr0 = ring + (r32 >> 2) * 128 + (hh ^ (r32 >> 4)) * 64 + (r32 & 3) * 16, kr1 = kr0 ^ 128;
    unsigned vr0 = ring + (g & 1) * 1024 + (hh ^ (g & 1)) * 128 + (tp >> 1) * 64 + tq * 16 + 8 * (tp & 1), vr1 = (vr0 ^ 64) + 512;
    unsigned lo[3] = {l * 16, l * 16 + 4096, l * 16 + 8192}, pw = ring + l * 16, lw = l < 16 ? L_L + (l * 4 + w) * 8 : L_L + 512 + l * 8;
    OPAQUE(kr0); OPAQUE(kr1); OPAQUE(vr0); OPAQUE(vr1); OPAQUE(lo[0]); OPAQUE(lo[1]); OPAQUE(lo[2]); OPAQUE(pw); OPAQUE(lw);
    const u64 hbu = (u64)(ws + WS_H2 + (size_t)t * FRAME_B);
    const u32x4 rs = {(unsigned)hbu, (unsigned)(hbu >> 32) & 0xffffu, (unsigned)FRAME_B, 0x00020000u};
    unsigned toff[8];
#pragma unroll
    for (int i = 0; i < 8; ++i) { toff[i] = (8 * w + ((i + qb) & 7)) * TILE_B; asm volatile("" : "+s"(toff[i])); }
    u32x4 sel;
    { const unsigned one = (tp == (g & 1)) ? 0x3f803f80u : 0u; sel = u32x4{one, one, one, one}; OPAQUE(sel); }
    auto piece = [&](auto Tc, auto Pc) {
      constexpr int T = decltype(Tc)::value, p = decltype(Pc)::value;
      if constexpr (T < 8) {
        if constexpr (p % 4 == 0) setm0<(T % 3) * TILE_B + p * 1024>(ring);
        bdma<(p % 4) * 1024>(lo[p / 4], rs, toff[T]);
      }
    };
#define P6(T, a) piece(IC(T), IC(a)); piece(IC(T), IC(a + 1)); piece(IC(T), IC(a + 2)); piece(IC(T), IC(a + 3)); piece(IC(T), IC(a + 4)); piece(IC(T), IC(a + 5));
    auto read_valid = [&](u32x4 (&KK)[12], const unsigned dst, unsigned off) {
      for (int sp = 0; sp < SPIN_CAP; ++sp) {
#pragma unroll
        for (int k = 0; k < 12; ++k) KK[k] = lds_r128(((k & 1) ? kr1 : kr0) + dst + k * 1024);
        if (__builtin_expect(!poisoned(KK), 1)) break;
#pragma unroll 1
        for (int k = 0; k < 12; ++k) { setm0<0>(ring + dst + k * 1024); bdma<0>(lo[0], rs, off + k * 1024); }
        waitvm<0>();
      }
    };
    unsigned qa = lds0 + L_Q + lo[0], na = lds0 + L_N + r32 * 16;
    OPAQUE(qa); OPAQUE(na);
    BARV(0);
    __builtin_amdgcn_s_sleep(TILE0_DELAY);
    P6(0, 0) P6(0, 6)
    if (w == 0 && l == 0) __builtin_amdgcn_raw_buffer_store_b64(tag, rsWS, 0, WS_FLAG2 + b * 8, SC1);
    ST(11);
    BAR();
    u32x4 K0[12];
    if (w == 0) waitvm<1>(); else waitvm<0>();
    ST(12);
    read_valid(K0, 0, toff[0]);
    ST(13);
    piece(IC(1), IC(0)); piece(IC(1), IC(1)); piece(IC(1), IC(2)); piece(IC(1), IC(3));
    BAR();
    ST(14);
    u32x4 Q[12];
    float mref;
    {
      const f32x4 n0 = lds_r128f(na), n1 = lds_r128f(na + 512);
#pragma unroll
      for (int s = 0; s < 6; ++s) Q[s] = lds_r128(qa + s * 1024);
      SBAR();
      const f32x4 s = n0 + n1;
      mref = __builtin_amdgcn_sqrtf(((s[0] + s[1]) + (s[2] + s[3])) * (192.0f * 1.02f));
      OPAQUE(mref);
      SBAR();
#pragma unroll
      for (int s = 6; s < 12; ++s) Q[s] = lds_r128(qa + s * 1024);
      SBAR();
    }
    constexpr int KW = 4, VW = 4;
    bool safe = false, exact = false, late = false;
#pragma unroll 1
    for (;;) {
      f32x16 S[2], O[6], negm;
      u32x4 K[12], V[12], P[2][2];
      f32x4 lsum = {0, 0, 0, 0};
      float pe[2][2];
#pragma unroll
      for (int i = 0; i < 16; ++i) negm[i] = -mref;
#define RK(k, T) K[k] = lds_r128((((k) & 1) ? kr1 : kr0) + ((T) % 3) * TILE_B + (k) * 1024);
#define RV(u, T) { const unsigned a_ = (((u) & 1) ? vr1 : vr0) + ((T) % 3) * TILE_B + ((u) >> 1) * 2048; V[u] = lds_tr2(a_, a_ + 256); }
      auto sm = [&](auto Tc, auto Hc, auto Gc) {
        constexpr int T = decltype(Tc)::value, H = decltype(Hc)::value, G = decltype(Gc)::value;
        if constexpr (G < 8) pe[(G >> 1) & 1][G & 1] = __builtin_amdgcn_exp2f(S[T & 1][8 * H + G]);
        if constexpr (G >= 2 && G <= 8 && G % 2 == 0) { constexpr int q = G / 2 - 1; P[T & 1][H][q] = cvtpk(pe[q & 1][0], pe[q & 1][1]); }
        if constexpr (G == 10) lsum = mfma16(sel, P[T & 1][H], lsum);
      };
      auto put_o = [&](const int un) {
        u32x4 pk;
#pragma unroll
        for (int e = 0; e < 4; ++e) pk[e] = cvtpk(O[un >> 1][8 * (un & 1) + 2 * e], O[un >> 1][8 * (un & 1) + 2 * e + 1]);
        lds_w128(pw + un * 1024, pk);
      };
      auto qkphase = [&](auto Ic) {
        constexpr int I = decltype(Ic)::value;
        auto step = [&](auto Kc) {
          constexpr int k = decltype(Kc)::value;
          if constexpr (I + 1 < 8) {
            S[(I + 1) & 1] = mfma32(I < 0 ? K0[k] : K[k], Q[k], k == 0 ? negm : S[(I + 1) & 1]);
            if constexpr (k + KW < 12 && I >= 0) RK(k + KW, I + 1)
          }
          if constexpr (I < 0 && k < 8) piece(IC(1), IC(4 + k));
          if constexpr (I >= 0) { if constexpr (k & 1) piece(IC(I + 2), IC(6 + k / 2)); if constexpr (k < 11) sm(IC(I), IC(1), IC(k)); }
          if constexpr (I >= 0 && I < 7 && k >= 12 - VW) RV(k - (12 - VW), I)
          if constexpr (I == 7 && k < VW) RV(k, I)
          SBAR();
        };
        step(IC(0)); step(IC(1)); step(IC(2)); step(IC(3)); step(IC(4)); step(IC(5)); step(IC(6)); step(IC(7)); step(IC(8)); step(IC(9)); step(IC(10)); step(IC(11));
      };
      auto pvphase = [&](auto Ic) {
        constexpr int I = decltype(Ic)::value;
        auto step = [&](auto Uc) {
          constexpr int u = decltype(Uc)::value;
          if constexpr (I >= 0) {
            f32x16 z;
#pragma unroll
            for (int e = 0; e < 16; ++e) z[e] = 0.f;
            O[u >> 1] = mfma32(V[u], P[I & 1][u & 1], (I == 0 && !(u & 1)) ? z : O[u >> 1]);
            if constexpr (u & 1) piece(IC(I + 3), IC(u / 2));
            if constexpr (u + VW < 12) RV(u + VW, I)
          }
          if constexpr (I + 1 < 8 && u >= 1) sm(IC(I + 1), IC(0), IC(u - 1));
          if constexpr (I < 0 && (u & 1)) piece(IC(2), IC(u / 2));
          if constexpr (I + 2 < 8) {
            if constexpr (u == 12 - KW) waitvm<(I < 0 ? 12 : I + 3 < 8 ? 6 + (12 - KW) / 2 : 6)>();
            if constexpr (u >= 12 - KW) RK(u - (12 - KW), I + 2)
            if constexpr (u == 11) waitvm<(I + 3 < 8 ? 6 : 0)>();
          }
          if constexpr (I == 7 && u >= 3) {
            put_o(u - 3);
          }
          SBAR();
        };
        step(IC(0)); step(IC(1)); step(IC(2)); step(IC(3)); step(IC(4)); step(IC(5)); step(IC(6)); step(IC(7)); step(IC(8)); step(IC(9)); step(IC(10)); step(IC(11));
      };
#define STALE(v) (__builtin_expect(!safe && __any((v) != (v)), 0))
      bool done = false;
      do {
        qkphase(IC(-1));
        ST(15);
        pvphase(IC(-1));
        ST(16);
        qkphase(IC(0)); pvphase(IC(0)); qkphase(IC(1)); pvphase(IC(1)); qkphase(IC(2));
        const float seen = lsum[0];
        pvphase(IC(2));
        if (STALE(seen)) break;
        qkphase(IC(3)); pvphase(IC(3)); qkphase(IC(4)); pvphase(IC(4)); qkphase(IC(5));
        if (!late) asm volatile("s_barrier" ::: "memory");
        late = true;
        pvphase(IC(5)); qkphase(IC(6)); pvphase(IC(6)); qkphase(IC(7)); pvphase(IC(7));
        if (STALE(lsum[0])) break;
        { const u32x2 ls = {asu(lsum[0]), asu(lsum[1])}; lds_w64(lw, ls); }
        put_o(9); put_o(10); put_o(11);
        done = true;
      } while (false);
#pragma unroll
      for (int k = 0; k < 12; ++k) asm volatile("" : "=v"(K0[k]));
      if (__builtin_expect(done, 1)) {
        ST(17);
        BAR();
        ST(18);
        float lt[2];
        unsigned ll = lo[0];
        OPAQUE(ll);
        ll >>= 4;
        if (__builtin_expect(!low_sum(lds_r128f(L_L + (ll & 15) * 32), lds_r128f(L_L + (ll & 15) * 32 + 16), lt) || exact, 1)) break;
        BAR();
        float mx = -3.0e38f;
#pragma unroll 1
        for (unsigned i = 0; i < 8; ++i) {
#pragma unroll 1
          for (int k = 0; k < 12; ++k) { setm0<0>(ring + k * 1024); bdma<0>(lo[0], rs, (8 * w + i) * TILE_B + k * 1024); }
          waitvm<0>();
          f32x16 s;
#pragma unroll
          for (int e = 0; e < 16; ++e) s[e] = 0.f;
#pragma unroll
          for (int k = 0; k < 12; ++k) s = mfma32(lds_r128(((k & 1) ? kr1 : kr0) + k * 1024), Q[k], s);
#pragma unroll
          for (int e = 0; e < 16; ++e) mx = fmaxf(mx, s[e]);
        }
        lds_w32(L_N + ((ll & 31) * 8 + w * 2 + (ll >> 5)) * 4, mx);
        BAR();
        const f32x4 c = lds_r128f(L_N + (ll & 31) * 32), d = lds_r128f(L_N + (ll & 31) * 32 + 16);
        mref = fmaxf(fmaxf(fmaxf(c[0], c[1]), fmaxf(c[2], c[3])), fmaxf(fmaxf(d[0], d[1]), fmaxf(d[2], d[3])));
        exact = true;
      } else {
        unsigned ll = lo[0];
        OPAQUE(ll);
        for (int sp = 0; sp < SPIN_CAP; ++sp) {
          asm volatile("" ::: "memory");
          const u32x2 f = __builtin_amdgcn_raw_buffer_load_b64(rsWS, ll >> 1, WS_FLAG2 + w * 512, SC1);
          if (__all(f[0] == tag[0] && f[1] == tag[1])) break;
        }
      }
      safe = true;
      asm volatile("s_waitcnt vmcnt(0) lgkmcnt(0)" ::: "memory");
      P6(0, 0) P6(0, 6)
      waitvm<0>();
      piece(IC(1), IC(0)); piece(IC(1), IC(1)); piece(IC(1), IC(2)); piece(IC(1), IC(3));
#pragma unroll
      for (int k = 0; k < 12; ++k) K0[k] = lds_r128(((k & 1) ? kr1 : kr0) + k * 1024);
    }
    ST(19);
    BAR();
    ST_OUT(ws, 0, 12);
  } else {
    auto merge16 = [&](const u32x4 (&a)[6], const u32x4 (&bb)[6], const float sc, const unsigned where) {
      f32x4 d = {0, 0, 0, 0};
#pragma unroll
      for (int kk = 0; kk < 6; ++kk) d = mfma16(a[kk], bb[kk], d);
      __builtin_amdgcn_raw_buffer_store_b64(pack4(d * sc), rsWS, ((l >> 5) * 16 + (l & 15)) * 16 + ((l >> 4) & 1) * 8, where + b * 512, SC1);
    };
    auto rd16 = [&](u32x4 (&a)[6], const unsigned at) {
#pragma unroll
      for (int kk = 0; kk < 6; ++kk) a[kk] = lds_r128(lds0 + at + (l & 15) * 384 + (((4 * kk + (l >> 4)) ^ (l & 7)) * 16));
    };
    u32x2 tag7 = {0, 0};
    if (w < 2) {
      const unsigned ldsw = lds0 + 18432 * w;
      unsigned qp[9];
#pragma unroll
      for (int i = 0; i < 9; ++i) qp[i] = (qo[i] ^ 64) + 9216;
      DMA9(qp, wqkv_t, ldsw + 9216);
      ST(1);
      unsigned slot = SLOT;
      u32x2 tag;
#pragma unroll 1
      for (int c = 1; c < 6; ++c) {
        DMA9(qo, wqkv_t + c * SLOT, ldsw + slot); DMA9(qp, wqkv_t + c * SLOT, ldsw + slot + 9216);
        slot = slot == 2 * SLOT ? 0 : slot + SLOT;
        if (c == 1) tag = get_tag();
        if (c == 5) {
#pragma unroll
          for (int i = 0; i < 9; ++i) {
            const unsigned row = ((8 * i + (l >> 3)) * 171) >> 9;
            qo[i] = ((18432 * w + l * 16) ^ (((row & 2) | ((row & 8) >> 1)) << 4)) + (i >> 2) * 4096;
            qp[i] = (qo[i] ^ 64) + 9216;
          }
        }
        BARV(18);
      }
      DMA9(qo, wo_t, ldsw); DMA9(qp, wo_t, ldsw + 9216);
      BARV(18);
      DMA9(qo, wo_t + SLOT, ldsw + SLOT); DMA9(qp, wo_t + SLOT, ldsw + SLOT + 9216);
      ST(3);
      u32x4 f = __builtin_amdgcn_raw_buffer_load_b128(rsWS, l * 16, WS_FLAG + w * 1024, SC1);
      BARV(1);
      for (int sp = 0; sp < SPIN_CAP && !__all(tags_ok(f, tag)); ++sp) {
        asm volatile("" ::: "memory");
        f = __builtin_amdgcn_raw_buffer_load_b128(rsWS, l * 16, WS_FLAG + w * 1024, SC1);
      }
    } else if (w == 2) {
      if (merger) { rows16(wqkv_s + mrow * 1152, lds0 + L_X); rows16(wqkv_s + 16 * mtau * 1152 + 384, lds0 + L_X + 6144); }
      ST(1);
      BAR();
      BAR();
      u32x4 a[6], bb[6];
      waitvm<0>();
      if (merger) { rd16(a, L_X); rd16(bb, L_X + 6144); }
      ST(2);
      BAR();
      BAR();
      if (merger) merge16(a, bb, QSCALE, WS_WM);
      BAR();
      BAR();
    } else {
      const u32x4 pz = {POISON, POISON, POISON, POISON};
#pragma unroll
      for (int i = 0; i < 12; ++i) __builtin_amdgcn_raw_buffer_store_b128(pz, rsWS, l * 16 + (i & 3) * 1024, WS_H2 + t * FRAME_B + qb * TILE_B + (i >> 2) * 4096, SC1);
      __builtin_amdgcn_raw_buffer_store_b128(pz, rsWS, merger ? (l >> 5) * 73728 + (l & 31) * 16 : WS_END, WS_WM + b * 512, SC1);
      __builtin_amdgcn_raw_buffer_store_b32(POISON, rsWS, l * 4, WS_ST + (t * 1024 + qb * 32) * 8, SC1);
      asm volatile("s_nop 1" :: "v"(pz));
      tag7 = get_tag();
      ST(1);
      BAR(); BAR(); BAR();
      waitvm<0>();
      asm volatile("" :: "v"(d0), "v"(d1));
      if (l == 0) __builtin_amdgcn_raw_buffer_store_b64(tag7, rsWS, 0, WS_FLAG + b * 8, SC1);
      ST(2);
      BAR(); BAR(); BAR();
    }
    if (w >= 2) {
      const unsigned i16 = l & 15, g = l >> 4;
      unsigned a = lds0 + L_SP + ((w - 2) * 64 + l) * 16, pa = lds0 + L_PT + (w - 2) * 512 + l * 8;
      OPAQUE(a); OPAQUE(pa);
      __builtin_amdgcn_s_setprio(3);
      BAR();
      ST(4);
      const f32x4 s = (lds_r128f(a) + lds_r128f(a + 2048)) + (lds_r128f(a + 4096) + lds_r128f(a + 6144));
      f32x4 e = (s - xor16_max(fmaxf(fmaxf(s[0], s[1]), fmaxf(s[2], s[3])))) * QSCALE;
#pragma unroll
      for (int k = 0; k < 4; ++k) e[k] = __builtin_amdgcn_exp2f(e[k]);
      const float sum = xor16_add((e[0] + e[1]) + (e[2] + e[3]));
      const float inv = ((g >> 1) == (i16 >> 3)) ? __builtin_amdgcn_rcpf(sum) : 0.f;
      lds_w64(pa, pack4(e * inv));
      __builtin_amdgcn_s_setprio(0);
    }
    ST(5);
    BAR();
    if (b == 0 && w == 3 && l == 0) __builtin_amdgcn_raw_buffer_store_b64(tag7, rsWS, 0, WS_EPOCH, SC1);
    OPAQUE(l);
    LANE_IDS;
    FRAG_POS;
    u32x4 wq[18], wv[18];
    auto ldw = [&](u32x4 (&W)[18], const int m, const int i0, const int i1) {
#pragma unroll
      for (int i = i0; i < i1; ++i) W[i] = __builtin_amdgcn_raw_buffer_load_b128(rsWS, l * 16 + (i & 3) * 1024, WS_WM + m * 73728 + w * 18432 + (i >> 2) * 4096, SC1);
    };
    ldw(wq, 0, 0, 8);
    BAR();
    ldw(wq, 0, 8, 18);
    unsigned hqa[2][2], qw[3], own[3][2];
#pragma unroll
    for (int hf = 0; hf < 2; ++hf) {
      const unsigned in = lds0 + L_HQ + ((4 * hf + tq) ^ hh) * 128 + ((g & 1) ^ hf) * 64 + tp * 16;
      hqa[hf][0] = in + hh * 1024; hqa[hf][1] = in + 2048 + hh * (SLOT - 2048);
      OPAQUE(hqa[hf][0]); OPAQUE(hqa[hf][1]);
    }
    const unsigned tok = 16 * (g & 1) + i16;
    unsigned nw = lds0 + L_N + l * 16 + w * 4;
    OPAQUE(nw);
#pragma unroll
    for (int i = 0; i < 3; ++i) {
      qw[i] = lds0 + L_Q + ((3 * w + i) * 64 + (g >> 1) * 32 + i16) * 16 + 8 * (g & 1);
      own[i][0] = lds0 + w * SLOT + L_HQ + i * 1024 + (tq ^ ((w + i) & 1)) * 128 + hh * 64 + tp * 16 + 8 * (g & 1);
      own[i][1] = (own[i][0] ^ 64) + 512;
      OPAQUE(qw[i]); OPAQUE(own[i][0]); OPAQUE(own[i][1]);
    }
    BAR();
    constexpr unsigned L_M2 = 2 * SLOT + TILE_B;
    const bool merge2 = merger && w == 2;
    if (merge2) {
      rows16(wqkv_s + mrow * 1152 + 768, lds0 + L_M2);
#pragma unroll
      for (int i = 0; i < 6; ++i) DMA1((l >> 1) * 384 + (l & 1) * 16, wo_s + i * 12288 + mtau * 32, lds0 + L_M2 + 6144 + i * 1024);
    }
    ST(6);
    BAR();
    ST(7);
    u32x4 ma[6], mb[6];
    u32x4 hq[3];
    {
      const u64 hbu = (u64)(ws + WS_H2 + (size_t)t * FRAME_B);
      const u32x4 rs = {(unsigned)hbu, (unsigned)(hbu >> 32) & 0xffffu, (unsigned)FRAME_B, 0x00020000u};
      for (int sp = 0; sp < SPIN_CAP; ++sp) {
        setm0<L_HQ>(lds0 + w * SLOT);
        bdma<0>(l * 16, rs, qb * TILE_B + w * 3072); bdma<1024>(l * 16, rs, qb * TILE_B + w * 3072); bdma<2048>(l * 16, rs, qb * TILE_B + w * 3072);
        if (merge2 && sp == 0) {
          waitvm<3>();
          rd16(ma, L_M2);
#pragma unroll
          for (int kk = 0; kk < 6; ++kk) { const unsigned at = lds0 + L_M2 + 6144 + (32 * kk + 8 * g + tq) * 32 + tp * 8; mb[kk] = lds_tr2(at, at + 128); }
        }
        waitvm<0>();
#pragma unroll
        for (int i = 0; i < 3; ++i) { const u32x2 u0 = lds_r64(own[i][0]), u1 = lds_r64(own[i][1]); hq[i] = u32x4{u0[0], u0[1], u1[0], u1[1]}; }
        if (!poisoned(hq)) break;
      }
    }
    ST(8);
    BAR();
    ST(9);
    auto gemm = [&](const u32x4 (&wf)[18], const u32x4 (&bf)[6][2], auto&& fin) {
      f32x4 d[3][2];
#pragma unroll
      for (int i = 0; i < 3; ++i) {
#pragma unroll
        for (int ks = 0; ks < 6; ++ks) {
          d[i][0] = mfma16(wf[i * 6 + ks], bf[ks][0], ks ? d[i][0] : f32x4{0, 0, 0, 0});
          d[i][1] = mfma16(wf[i * 6 + ks], bf[ks][1], ks ? d[i][1] : f32x4{0, 0, 0, 0});
          if (ks == 0) SBAR();
        }
        if (i > 0) fin(i - 1, d[i - 1]);
        SBAR();
      }
      fin(2, d[2]);
    };
    {
      u32x4 bf[6][2];
#pragma unroll
      for (int ks = 0; ks < 6; ++ks)
#pragma unroll
        for (int hf = 0; hf < 2; ++hf) {
          if (ks == 3) { hqa[hf][0] += 2 * SLOT; hqa[hf][1] += 2 * SLOT; }
          bf[ks][hf] = lds_r128(ks % 3 == 1 ? hqa[hf][1] : hqa[hf][0] + (ks % 3 == 2 ? SLOT + 1024 : 0));
        }
      SBAR();
#pragma unroll 1
      for (int sp = 0;; ++sp) {
        f32x2 n[2] = {{0.f, 0.f}, {0.f, 0.f}};
        gemm(wq, bf, [&](const int i, const f32x4 (&d)[2]) {
#pragma unroll
          for (int hf = 0; hf < 2; ++hf) {
            lds_w64(qw[i] + hf * 256, pack4(d[hf]));
            const f32x2 a = {d[hf][0], d[hf][1]}, c = {d[hf][2], d[hf][3]};
            n[hf] = __builtin_elementwise_fma(a, a, n[hf]); n[hf] = __builtin_elementwise_fma(c, c, n[hf]);
          }
        });
        auto rr = __builtin_amdgcn_permlane16_swap(asu(n[0][0] + n[0][1]), asu(n[1][0] + n[1][1]), false, false);
        const float nn = asf(rr[0]) + asf(rr[1]);
        lds_w32(nw, nn);
        if (__builtin_expect(!__any(nn != nn), 1) || sp >= SPIN_CAP || !poisoned(wq)) break;
        asm volatile("" ::: "memory");
        ldw(wq, 0, 0, 18);
      }
    }
    ST(11);
    BAR();
    if (merge2) merge16(ma, mb, 1.0f, WS_WM + 73728);
    unsigned oo = (t * 1024 + qb * 32 + tok) * 384 + (6 * w + hh) * 16;
    OPAQUE(oo);
    float lt[2];
    u32x4 pick;
#pragma unroll
    for (int e = 0; e < 4; ++e) {
      const unsigned d = 8 * (e >> 1) + 4 * (g & 1) + 2 * (e & 1);
      pick[e] = (i16 == d ? 0x3f80u : 0u) | (i16 == d + 1 ? 0x3f800000u : 0u);
    }
    unsigned pr[2], la = lds0 + L_L + i16 * 32;
    pr[0] = lds0 + hh * SLOT + 3 * w * 1024 + ((g & 1) * 32 + i16) * 16; pr[1] = pr[0] + 2 * SLOT;
    OPAQUE(pick); OPAQUE(pr[0]); OPAQUE(pr[1]); OPAQUE(la);
#pragma unroll
    for (int hf = 0; hf < 2; ++hf) {
      fr[hf] += L_FI; OPAQUE(fr[hf]);
#pragma unroll
      for (int i = 0; i < 3; ++i) { dpos[i][hf] += L_FI; OPAQUE(dpos[i][hf]); }
    }
    BAR();
    ST(16);
    f32x4 x1[3][2];
    {
      u32x2 st[2];
      for (int sp = 0; sp < SPIN_CAP; ++sp) {
        asm volatile("" ::: "memory");
        st[0] = __builtin_amdgcn_raw_buffer_load_b64(rsWS, i16 * 8, WS_ST + (t * 1024 + qb * 32) * 8, SC1);
        st[1] = __builtin_amdgcn_raw_buffer_load_b64(rsWS, i16 * 8 + 128, WS_ST + (t * 1024 + qb * 32) * 8, SC1);
        ldw(wv, 1, 0, 18);
        if (!__any(max(max(st[0][0], st[0][1]), max(st[1][0], st[1][1])) == POISON) && !poisoned(wv)) break;
      }
#pragma unroll
      for (int hf = 0; hf < 2; ++hf) {
        const float mu = asf(st[hf][0]), sd = __builtin_amdgcn_sqrtf(asf(st[hf][1]));
#pragma unroll
        for (int i = 0; i < 3; ++i) {
          const f32x4 h = {bflo(hq[i][2 * hf]), bfhi(hq[i][2 * hf]), bflo(hq[i][2 * hf + 1]), bfhi(hq[i][2 * hf + 1])};
          x1[i][hf] = h * sd + mu;
          OPAQUE(x1[i][hf]);
        }
      }
    }
    ST(12);
#pragma unroll 1
    for (bool exact = false;; exact = true) {
      BAR();
      ST(13);
      u32x4 p[3][2][2];
#pragma unroll
      for (int i = 0; i < 3; ++i)
#pragma unroll
        for (int hf = 0; hf < 2; ++hf) { p[i][hf][0] = lds_r128(pr[0] + i * 1024 + hf * 256); p[i][hf][1] = lds_r128(pr[1] + i * 1024 + hf * 256); }
      const f32x4 l0 = lds_r128f(la), l1 = lds_r128f(la + 16);
      SBAR();
#pragma unroll
      for (int i = 0; i < 3; ++i)
#pragma unroll
        for (int hf = 0; hf < 2; ++hf) lds_w64(dpos[i][hf], pack4(mfma16(pick, p[i][hf][1], mfma16(pick, p[i][hf][0], f32x4{0, 0, 0, 0}))));
      if (__builtin_expect(!low_sum(l0, l1, lt) || exact, 1)) break;
      BAR();
      BAR();
    }
    ST(14);
    const float linv[2] = {__builtin_amdgcn_rcpf(lt[0]), __builtin_amdgcn_rcpf(lt[1])};
    BAR();
    ST(15);
    u32x4 bf[6][2];
#pragma unroll
    for (int ks = 0; ks < 6; ++ks) { bf[ks][0] = lds_r128(fr[0] + ks * 1024); bf[ks][1] = lds_r128(fr[1] + ks * 1024); }
    SBAR();
    gemm(wv, bf, [&](const int i, const f32x4 (&d)[2]) {
      const u32x2 y0 = pack4(d[0] * linv[0] + x1[i][0]), y1 = pack4(d[1] * linv[1] + x1[i][1]);
      auto s0 = __builtin_amdgcn_permlane16_swap(y0[0], y1[0], false, false);
      auto s1 = __builtin_amdgcn_permlane16_swap(y0[1], y1[1], false, false);
      const u32x4 o = {s0[0], s1[0], s0[1], s1[1]};
      *(u32x4*)(out + oo + i * 32) = o;
    });
    ST(17);
    ST_OUT(ws, 12, 20);
  }
}

extern "C" void kernel_launch(void* const* d_in, const int* in_sizes, int n_in, void* d_out, int out_size, void* d_ws, size_t ws_size, hipStream_t stream) {
  (void)hipFuncSetAttribute((const void*)k_timesformer, hipFuncAttributeMaxDynamicSharedMemorySize, LDS_BYTES);
  k_timesformer<<<256, 512, LDS_BYTES, stream>>>((const char*)d_in[0], (const char*)d_in[1], (const char*)d_in[2], (const char*)d_in[3], (const char*)d_in[4],
                                                 (char*)d_ws, (char*)d_out);
}
```

```cpp
#include <hip/hip_runtime.h>
#include <hip/hip_bf16.h>
#include <type_traits>

typedef short s16x4 __attribute__((ext_vector_type(4)));
typedef __bf16 b16x8 __attribute__((ext_vector_type(8)));
typedef __bf16 b16x2 __attribute__((ext_vector_type(2)));
typedef float f32x2 __attribute__((ext_vector_type(2)));
typedef float f32x4 __attribute__((ext_vector_type(4)));
typedef float f32x16 __attribute__((ext_vector_type(16)));
typedef unsigned u32x2 __attribute__((ext_vector_type(2)));
typedef unsigned u32x4 __attribute__((ext_vector_type(4)));
typedef unsigned long long u64;
#define LAS __attribute__((address_space(3)))
#define DI __device__ __forceinline__
#define SBAR() __builtin_amdgcn_sched_barrier(0)

constexpr int TILE_B = 12288;
constexpr int FRAME_B = 393216;
constexpr unsigned WS_H2 = 0, WS_ST = 8 * FRAME_B, WS_WM = 16 * FRAME_B, WS_FLAG = WS_WM + 2 * 73728, WS_FLAG2 = WS_FLAG + 2048, WS_EPOCH = WS_FLAG2 + 2048, WS_END = WS_EPOCH + 256;
constexpr float QSCALE = 0.07216878364870322f * 1.4426950408889634f;
constexpr int SC1 = 16;
constexpr unsigned POISON = 0xffffffffu;
constexpr unsigned CODE_BYTES = 25600;
constexpr int TILE0_DELAY = 2;
constexpr int SPIN_CAP = 1 << 16;

#define ST_DECL
#define ST(k)
#define ST_OUT(ws, k0, k1)

DI float asf(unsigned u) { return __builtin_bit_cast(float, u); }
DI unsigned asu(float f) { return __builtin_bit_cast(unsigned, f); }
DI unsigned cvtpk(float lo, float hi) { f32x2 v = {lo, hi}; return __builtin_bit_cast(unsigned, __builtin_convertvector(v, b16x2)); }
DI float bflo(unsigned u) { return asf(u << 16); }
DI float bfhi(unsigned u) { return asf(u & 0xffff0000u); }
DI f32x16 mfma32(u32x4 a, u32x4 b, f32x16 c) { return __builtin_amdgcn_mfma_f32_32x32x16_bf16(__builtin_bit_cast(b16x8, a), __builtin_bit_cast(b16x8, b), c, 0, 0, 0); }
DI f32x4 mfma16(u32x4 a, u32x4 b, f32x4 c) { return __builtin_amdgcn_mfma_f32_16x16x32_bf16(__builtin_bit_cast(b16x8, a), __builtin_bit_cast(b16x8, b), c, 0, 0, 0); }
DI f32x4 mfma16k16(u32x2 a, u32x2 b, f32x4 c) { return __builtin_amdgcn_mfma_f32_16x16x16bf16_1k(__builtin_bit_cast(s16x4, a), __builtin_bit_cast(s16x4, b), c, 0, 0, 0); }
DI u32x2 pack4(f32x4 v) { u32x2 r = {cvtpk(v[0], v[1]), cvtpk(v[2], v[3])}; return r; }
DI u32x4 lds_r128(unsigned a) { return *(LAS u32x4*)(size_t)a; }
DI f32x4 lds_r128f(unsigned a) { return *(LAS f32x4*)(size_t)a; }
DI u32x2 lds_r64(unsigned a) { return *(LAS u32x2*)(size_t)a; }
DI void lds_w128(unsigned a, u32x4 v) { *(LAS u32x4*)(size_t)a = v; }
DI void lds_w128f(unsigned a, f32x4 v) { *(LAS f32x4*)(size_t)a = v; }
DI void lds_w64(unsigned a, u32x2 v) { *(LAS u32x2*)(size_t)a = v; }
DI void lds_w32(unsigned a, float v) { *(LAS float*)(size_t)a = v; }
DI u32x2 lds_tr(unsigned a) { return __builtin_bit_cast(u32x2, __builtin_amdgcn_ds_read_tr16_b64_v4i16((LAS s16x4*)(size_t)a)); }
DI u32x4 lds_tr2(unsigned a0, unsigned a1) { u32x2 x = lds_tr(a0), y = lds_tr(a1); u32x4 r = {x[0], x[1], y[0], y[1]}; return r; }
#define BAR() asm volatile("s_waitcnt lgkmcnt(0)\n\ts_barrier" ::: "memory")
#define BARV(n) asm volatile("s_waitcnt vmcnt(" #n ") lgkmcnt(0)\n\ts_barrier" ::: "memory")
template <int CTRL> DI float dpp_add(float v) { return v + asf(__builtin_amdgcn_update_dpp(0u, asu(v), CTRL, 0xf, 0xf, true)); }
DI float xor16_add(float v) { auto r = __builtin_amdgcn_permlane16_swap(asu(v), asu(v), false, false); return asf(r[0]) + asf(r[1]); }
DI float xor16_max(float v) { auto r = __builtin_amdgcn_permlane16_swap(asu(v), asu(v), false, false); return __builtin_amdgcn_fmed3f(asf(r[0]), asf(r[1]), __builtin_inff()); }
DI unsigned umax3(unsigned a, unsigned b, unsigned c) { return max(max(a, b), c); }
template <int N> DI bool poisoned(const u32x4 (&v)[N]) {
  unsigned m = 0;
#pragma unroll
  for (int i = 0; i < N; ++i) { m = umax3(m, v[i][0], v[i][1]); m = umax3(m, v[i][2], v[i][3]); }
  return __any(m == POISON);
}
DI u32x4 unpoison(u32x4 v) { u32x4 r = {min(v[0], POISON - 1), min(v[1], POISON - 1), min(v[2], POISON - 1), min(v[3], POISON - 1)}; return r; }
DI unsigned fi_pos(unsigned r, unsigned c8) {
  return (((r >> 4) * 6 + (c8 >> 2)) * 64 + (c8 & 3) * 16 + ((r & 15) ^ ((c8 & 1) | (((r >> 3) & 1) << 1) | ((r >> 4) << 2)))) * 16;
}
DI float row8_sum(float s) { s = dpp_add<0x124>(s); s = dpp_add<0x128>(s); return xor16_add(s); }
DI float sum24(const f32x2 (&v)[12]) {
  f32x2 s0 = v[0] + v[1], s1 = v[2] + v[3];
#pragma unroll
  for (int i = 4; i < 12; i += 2) { s0 += v[i]; s1 += v[i + 1]; }
  s0 += s1;
  return s0[0] + s0[1];
}
DI float sumsq24(const f32x2 (&v)[12]) {
  f32x2 q0 = v[0] * v[0], q1 = v[1] * v[1];
#pragma unroll
  for (int i = 2; i < 12; i += 2) { q0 = __builtin_elementwise_fma(v[i], v[i], q0); q1 = __builtin_elementwise_fma(v[i + 1], v[i + 1], q1); }
  q0 += q1;
  return q0[0] + q0[1];
}
DI f32x2 unpack2(unsigned u) { f32x2 r = {bflo(u), bfhi(u)}; return r; }

constexpr unsigned SLOT = 36864, L_X = 3 * SLOT, L_H = 4 * SLOT, L_N = L_H + TILE_B, L_L = L_N + 1024, L_PT = L_L + 1024, LDS_BYTES = 163840;
constexpr unsigned L_VI = L_X, L_SP = L_X + TILE_B, L_YI = L_X + 20480;
constexpr unsigned L_HQ = 24576, L_Q = L_H, L_FI = L_H;

#define DMA4(v0, v1, v2, v3, sbase, ldsa)                                                                  \
  asm volatile("s_mov_b32 m0, %5\n\ts_nop 0\n\t"                                                            \
               "global_load_lds_dwordx4 %0, %4\n\tglobal_load_lds_dwordx4 %1, %4 offset:1024\n\t"           \
               "global_load_lds_dwordx4 %2, %4 offset:2048\n\tglobal_load_lds_dwordx4 %3, %4 offset:3072"   \
               :: "v"(v0), "v"(v1), "v"(v2), "v"(v3), "s"(sbase), "s"(ldsa) : "memory")
#define DMA1(v0, sbase, ldsa) \
  asm volatile("s_mov_b32 m0, %2\n\ts_nop 0\n\tglobal_load_lds_dwordx4 %0, %1" :: "v"(v0), "s"(sbase), "s"(ldsa) : "memory")
#define DMA9(o, sbase, ldsa) do { DMA4(o[0], o[1], o[2], o[3], sbase, ldsa); DMA4(o[4], o[5], o[6], o[7], sbase, (ldsa) + 4096); DMA1(o[8], sbase, (ldsa) + 8192); } while (0)

template <int OFF> DI void bdma(unsigned voff, u32x4 rs, unsigned soff) { asm volatile("buffer_load_dwordx4 %0, %1, %2 offen offset:%3 sc1 lds" :: "v"(voff), "s"(rs), "s"(soff), "i"(OFF) : "memory"); }
template <int OFF> DI void setm0(unsigned base) { asm volatile("s_add_u32 m0, %0, %1\n\ts_nop 0" :: "s"(base), "i"(OFF) : "memory", "scc"); }
template <int N> DI void waitvm() { asm volatile("s_waitcnt vmcnt(%0)" :: "i"(N) : "memory"); }
#define IC(x) std::integral_constant<int, (x)>{}
#define OPAQUE(v) asm volatile("" : "+v"(v))
template <class R> DI void st128(u32x4 v, R rs, unsigned voff, unsigned soff) { __builtin_amdgcn_raw_buffer_store_b128(v, rs, voff, soff, SC1); asm volatile("s_nop 1" :: "v"(v)); }
DI bool tags_ok(u32x4 f, u32x2 tag) { return f[0] == tag[0] && f[1] == tag[1] && f[2] == tag[0] && f[3] == tag[1]; }

__global__ __launch_bounds__(512, 2) void k_timesformer(const char* __restrict__ x, const char* __restrict__ wqkv_t, const char* __restrict__ wo_t,
                                                        const char* __restrict__ wqkv_s, const char* __restrict__ wo_s, char* __restrict__ ws, char* __restrict__ out) {
  unsigned dc;
  {
    u64 pc;
    asm volatile("s_getpc_b64 %0" : "=s"(pc));
    asm volatile("global_load_dword %0, %1, %2" : "=v"(dc) : "v"(min(threadIdx.x * 128, CODE_BYTES)), "s"(pc) : "memory");
  }
  constexpr unsigned lds0 = 0;
  const unsigned b = blockIdx.x;
  unsigned w8 = __builtin_amdgcn_readfirstlane(threadIdx.x >> 6);
  unsigned l = threadIdx.x & 63;
  const unsigned t = b & 7, qb = b >> 3;
  const auto rsWS = __builtin_amdgcn_make_buffer_rsrc((void*)ws, 0, WS_END, 0x00020000);
  ST_DECL;
  ST(0);
  const bool merger = b < 144;
  const unsigned mtau = (b >> 1) / 6, mrow = ((b >> 1) - mtau * 6) * 32 + (b & 1) * 16;
  auto rows16 = [&](const char* src, const unsigned dst) {
#pragma unroll
    for (int i = 0; i < 3; ++i) {
      const unsigned pos = i * 64 + l, row = (pos * 2731) >> 16, o = row * 1152 + (((pos - row * 24) ^ row) * 16);
      DMA1(o, src, dst + i * 1024); DMA1(o, src + 8 * 1152, dst + (3 + i) * 1024);
    }
  };
  unsigned qo[9], d0, d1;
  if (w8 < 4) {
#pragma unroll
    for (int i = 0; i < 3; ++i) { const unsigned p = i * 64 + l; DMA1(p * 16 + (p >= 96 ? FRAME_B - 1536 : 0), x + w8 * 2 * FRAME_B + b * 1536, lds0 + L_YI + w8 * 3072 + i * 1024); }
  } else if (w8 < 6) {
    const unsigned qa = 18432 * (w8 & 1) + l * 16, qc = qa ^ 32;
    qo[0] = qa; qo[1] = qa; qo[2] = l >= 16 ? qc : qa; qo[3] = qc; qo[5] = qa + 4096; qo[7] = qc + 4096;
    qo[4] = l < 32 ? qo[7] : qo[5]; qo[6] = l >= 48 ? qo[7] : qo[5]; qo[8] = qc + 8192;
    DMA9(qo, wqkv_t, lds0 + 18432 * (w8 & 1));
  } else if (w8 == 7) {
    const unsigned line = qb * 72 + l, line2 = qb * 72 + 64 + (l & 7);
    const char* p0 = line < 1728 ? wqkv_t + line * 128 : wo_t + (line - 1728) * 128;
    const char* p1 = line2 < 1728 ? wqkv_t + line2 * 128 : wo_t + (line2 - 1728) * 128;
    asm volatile("global_load_dword %0, %2, off\n\tglobal_load_dword %1, %3, off" : "=&v"(d0), "=&v"(d1) : "v"(p0), "v"(p1) : "memory");
  }
  asm volatile("" : "+s"(w8));
  const unsigned w = w8 & 3;
  auto get_tag = [&]() {
    u64 e;
    asm volatile("s_load_dwordx2 %0, %1, 0x0\n\ts_waitcnt lgkmcnt(0)" : "=s"(e) : "s"(ws + WS_EPOCH) : "memory");
    e += 1;
    return u32x2{(unsigned)e, (unsigned)(e >> 32)};
  };
#define LANE_IDS const unsigned i16 = l & 15, g = l >> 4, r32 = l & 31, hh = l >> 5, tq = i16 >> 2, tp = l & 3
#define FRAG_POS unsigned fr[2], dp[2];                                                                       \
  fr[0] = L_X + fi_pos(i16, g); fr[1] = (fr[0] ^ 64) + 6144; dp[0] = L_X + fi_pos(i16, hh) + 8 * (g & 1) + w * 1536; dp[1] = (dp[0] ^ 64) + 6144; \
  OPAQUE(fr[0]); OPAQUE(fr[1]); OPAQUE(dp[0]); OPAQUE(dp[1])
  auto low_sum = [&](const f32x4 a, const f32x4 c, float (&lt)[2]) {
    lt[0] = (a[0] + a[2]) + (c[0] + c[2]);
    lt[1] = (a[1] + a[3]) + (c[1] + c[3]);
    return (bool)__any(lt[0] < 8.7e-19f || lt[1] < 8.7e-19f);
  };

  if (w8 < 4) {
    const unsigned nl = l & 3, j = (l >> 2) & 7, tt = 2 * w + (l >> 5), r = nl * 8 + tt;
    unsigned rp = L_X + fi_pos(r, j);
    OPAQUE(rp);
    waitvm<0>();
    asm volatile("" :: "v"(dc));
    f32x2 xv[12];
    float mu1;
    {
      u32x4 h[3], xr[3];
#pragma unroll
      for (int i = 0; i < 3; ++i) xr[i] = lds_r128(lds0 + L_YI + tt * 1536 + nl * 384 + j * 16 + i * 128);
#pragma unroll
      for (int i = 0; i < 12; ++i) xv[i] = unpack2(xr[i >> 2][i & 3]);
      mu1 = row8_sum(sum24(xv)) * (1.0f / 192.0f);
#pragma unroll
      for (int i = 0; i < 12; ++i) xv[i] -= mu1;
      const float rs = __builtin_amdgcn_rsqf(row8_sum(sumsq24(xv)) * (1.0f / 192.0f) + 1e-5f);
#pragma unroll
      for (int i = 0; i < 12; ++i) { const f32x2 n = xv[i] * rs; h[i >> 2][i & 3] = cvtpk(n[0], n[1]); }
#pragma unroll
      for (int i = 0; i < 3; ++i) lds_w128(rp + (L_H - L_X) + i * 2048, h[i]);
    }
    SBAR();
    OPAQUE(l);
    unsigned so = tt * FRAME_B + (j >> 1) * 1024 + ((t ^ ((j >> 1) & 1)) * 128) + (((j & 1) ^ (t >> 2)) * 64) + nl * 16;
    const unsigned sso = qb * TILE_B;
    LANE_IDS;
    FRAG_POS;
    const unsigned phi = ((tq >> 1) & 1) | ((g & 1) << 1);
    unsigned tcol[3];
#pragma unroll
    for (int i = 0; i < 3; ++i) { tcol[i] = (((3 * w + i) ^ phi) * 2 + (tp >> 1)) * 16 + 8 * (tp & 1); }
    const u32x2 tag = get_tag();
    f32x4 acc[3][3][2];
#pragma unroll
    for (int m = 0; m < 3; ++m)
#pragma unroll
      for (int i = 0; i < 3; ++i) { acc[m][i][0] = f32x4{0, 0, 0, 0}; acc[m][i][1] = f32x4{0, 0, 0, 0}; }
    unsigned wb[3], wc[3], wa[3], spa, sto;
#pragma unroll
    for (int i = 0; i < 3; ++i) { wb[i] = (8 * g + tq) * 1152 + tcol[i]; OPAQUE(wb[i]); }
    u32x4 hf0, hf1;
#pragma unroll
    for (int ks = 0; ks < 6; ++ks) {
      BAR();
      if (ks == 0) { ST(1); hf0 = lds_r128(fr[0] + (L_H - L_X)); hf1 = lds_r128(fr[1] + (L_H - L_X)); }
      u32x4 a[9];
#pragma unroll
      for (int m = 0; m < 3; ++m)
#pragma unroll
        for (int i = 0; i < 3; ++i) { const unsigned at = (ks % 3 == 2 ? wc[i] : wb[i] + (ks % 3) * SLOT) + m * 384; a[m * 3 + i] = lds_tr2(at, at + 4608); }
      const u32x4 h0 = hf0, h1 = hf1;
      if (ks < 5) { hf0 = lds_r128(fr[0] + (L_H - L_X) + (ks + 1) * 1024); hf1 = lds_r128(fr[1] + (L_H - L_X) + (ks + 1) * 1024); }
#pragma unroll
      for (int m = 0; m < 3; ++m)
#pragma unroll
        for (int i = 0; i < 3; ++i) {
          acc[m][i][0] = mfma16(a[m * 3 + i], h0, acc[m][i][0]);
          acc[m][i][1] = mfma16(a[m * 3 + i], h1, acc[m][i][1]);
        }
      if (ks == 0) __builtin_amdgcn_sched_group_barrier(0x100, 8, 0); else __builtin_amdgcn_sched_group_barrier(0x100, 6, 0);
#pragma unroll
      for (int q = 0; q < 7; ++q) { __builtin_amdgcn_sched_group_barrier(0x8, 2, 0); __builtin_amdgcn_sched_group_barrier(0x100, 2, 0); }
      __builtin_amdgcn_sched_group_barrier(0x8, 4, 0);
      SBAR();
      if (ks == 1) {
#pragma unroll
        for (int i = 0; i < 3; ++i) { wc[i] = wb[i] + 2 * SLOT; wa[i] = wb[i] - (8 * g + tq) * 768; OPAQUE(wc[i]); OPAQUE(wa[i]); }
      }
      if (ks == 2) {
        spa = L_X + w * 2048 + l * 16; sto = j == 0 ? (tt * 1024 + nl) * 8 : WS_END;
        OPAQUE(spa); OPAQUE(sto); OPAQUE(so);
      }
    }
    ST(2);
    u32x2 vpk[3][2];
    f32x4 spart[2];
#pragma unroll
    for (int hf = 0; hf < 2; ++hf) {
      spart[hf] = f32x4{0, 0, 0, 0};
#pragma unroll
      for (int i = 0; i < 3; ++i) { spart[hf] = mfma16k16(pack4(acc[1][i][hf]), pack4(acc[0][i][hf]), spart[hf]); vpk[i][hf] = pack4(acc[2][i][hf]); }
    }
    u32x4 bw[6][3];
    auto rdw = [&](const int k) {
#pragma unroll
      for (int i = 0; i < 3; ++i) bw[k][i] = lds_tr2(wa[i] + k * 12288, wa[i] + k * 12288 + 1536);
    };
    ST(3);
#pragma unroll
    for (int hf = 0; hf < 2; ++hf)
#pragma unroll
      for (int i = 0; i < 3; ++i) lds_w64(dp[hf] + i * 512, vpk[i][hf]);
    lds_w128f(spa + (L_SP - L_X), spart[0]); lds_w128f(spa + (L_SP - L_X) + 1024, spart[1]);
    unsigned pta = L_PT + l * 8;
    OPAQUE(pta);
    ST(5);
    BAR();
    ST(6);
    {
      f32x4 vp[3][2];
      u32x4 vf[6][2];
      u32x2 pt[2];
      auto rd = [&](const int k) { vf[k][0] = lds_r128(fr[0] + k * 1024); vf[k][1] = lds_r128(fr[1] + k * 1024); rdw(k); };
      auto mm = [&](const int k) {
#pragma unroll
        for (int i = 0; i < 3; ++i) {
          vp[i][0] = mfma16(vf[k][0], bw[k][i], k ? vp[i][0] : f32x4{0, 0, 0, 0});
          vp[i][1] = mfma16(vf[k][1], bw[k][i], k ? vp[i][1] : f32x4{0, 0, 0, 0});
        }
      };
#define MIX6() _Pragma("unroll") for (int q_ = 0; q_ < 6; ++q_) { __builtin_amdgcn_sched_group_barrier(0x8, 1, 0); __builtin_amdgcn_sched_group_barrier(0x100, 2, 0); } SBAR()
      rd(0); SBAR(); rd(1);
      SBAR();
      mm(0); rd(2); MIX6();
      mm(1); rd(3); MIX6();
      mm(2); rd(4); MIX6();
      mm(3); rd(5); MIX6();
      mm(4); mm(5);
      u32x2 vq[3][2];
#pragma unroll
      for (int i = 0; i < 3; ++i) { vq[i][0] = pack4(vp[i][0]); vq[i][1] = pack4(vp[i][1]); OPAQUE(vq[i][0]); OPAQUE(vq[i][1]); }
      ST(7);
      asm volatile("s_barrier" ::: "memory");
      pt[0] = lds_r64(pta); pt[1] = lds_r64(pta + 512);
      ST(8);
      f32x4 y[3][2];
#pragma unroll
      for (int hf = 0; hf < 2; ++hf)
#pragma unroll
        for (int i = 0; i < 3; ++i) y[i][hf] = mfma16k16(vq[i][hf], pt[hf], f32x4{0, 0, 0, 0});
      SBAR();
#pragma unroll
      for (int hf = 0; hf < 2; ++hf)
#pragma unroll
        for (int i = 0; i < 3; ++i) lds_w64(dp[hf] + (L_YI - L_X) + i * 512, pack4(y[i][hf]));
    }
    ST(9);
    BAR();
    {
      u32x4 y[3], h[3];
#pragma unroll
      for (int i = 0; i < 3; ++i) y[i] = lds_r128(rp + (L_YI - L_X) + i * 2048);
#pragma unroll
      for (int i = 0; i < 12; ++i) xv[i] += unpack2(y[i >> 2][i & 3]);
      const float mu = row8_sum(sum24(xv)) * (1.0f / 192.0f), sq = row8_sum(sumsq24(xv));
      const float var = fmaxf(fmaf(sq, 1.0f / 192.0f, -mu * mu), 0.f) + 1e-5f;
      const float rs = __builtin_amdgcn_rsqf(var), nb = -mu * rs;
#pragma unroll
      for (int i = 0; i < 3; ++i) {
#pragma unroll
        for (int e = 0; e < 4; ++e) { const f32x2 n = xv[4 * i + e] * rs + nb; h[i][e] = cvtpk(n[0], n[1]); }
        st128(h[i], rsWS, so, WS_H2 + sso + i * 4096);
        SBAR();
      }
      const u32x2 stat = {asu(mu + mu1), asu(var)};
      __builtin_amdgcn_raw_buffer_store_b64(stat, rsWS, sto, WS_ST + b * 32, SC1);
    }
    ST(10);
    asm volatile("s_barrier" ::: "memory");
    const unsigned ring = lds0 + w * SLOT;
    unsigned kr0 = ring + (r32 >> 2) * 128 + (hh ^ (r32 >> 4)) * 64 + (r32 & 3) * 16, kr1 = kr0 ^ 128;
    unsigned vr0 = ring + (g & 1) * 1024 + (hh ^ (g & 1)) * 128 + (tp >> 1) * 64 + tq * 16 + 8 * (tp & 1), vr1 = (vr0 ^ 64) + 512;
    unsigned lo[3] = {l * 16, l * 16 + 4096, l * 16 + 8192}, pw = ring + l * 16, lw = l < 16 ? L_L + (l * 4 + w) * 8 : L_L + 512 + l * 8;
    OPAQUE(kr0); OPAQUE(kr1); OPAQUE(vr0); OPAQUE(vr1); OPAQUE(lo[0]); OPAQUE(lo[1]); OPAQUE(lo[2]); OPAQUE(pw); OPAQUE(lw);
    const u64 hbu = (u64)(ws + WS_H2 + (size_t)t * FRAME_B);
    const u32x4 rs = {(unsigned)hbu, (unsigned)(hbu >> 32) & 0xffffu, (unsigned)FRAME_B, 0x00020000u};
    unsigned toff[8];
#pragma unroll
    for (int i = 0; i < 8; ++i) { toff[i] = (8 * w + ((i + qb) & 7)) * TILE_B; asm volatile("" : "+s"(toff[i])); }
    u32x4 sel;
    { const unsigned one = (tp == (g & 1)) ? 0x3f803f80u : 0u; sel = u32x4{one, one, one, one}; OPAQUE(sel); }
    auto piece = [&](auto Tc, auto Pc) {
      constexpr int T = decltype(Tc)::value, p = decltype(Pc)::value;
      if constexpr (T < 8) {
        if constexpr (p % 4 == 0) setm0<(T % 3) * TILE_B + p * 1024>(ring);
        bdma<(p % 4) * 1024>(lo[p / 4], rs, toff[T]);
      }
    };
#define P6(T, a) piece(IC(T), IC(a)); piece(IC(T), IC(a + 1)); piece(IC(T), IC(a + 2)); piece(IC(T), IC(a + 3)); piece(IC(T), IC(a + 4)); piece(IC(T), IC(a + 5));
    auto read_valid = [&](u32x4 (&KK)[12], const unsigned dst, unsigned off) {
      for (int sp = 0; sp < SPIN_CAP; ++sp) {
#pragma unroll
        for (int k = 0; k < 12; ++k) KK[k] = lds_r128(((k & 1) ? kr1 : kr0) + dst + k * 1024);
        if (__builtin_expect(!poisoned(KK), 1)) break;
#pragma unroll 1
        for (int k = 0; k < 12; ++k) { setm0<0>(ring + dst + k * 1024); bdma<0>(lo[0], rs, off + k * 1024); }
        waitvm<0>();
      }
    };
    unsigned qa = lds0 + L_Q + lo[0], na = lds0 + L_N + r32 * 16;
    OPAQUE(qa); OPAQUE(na);
    BARV(0);
    __builtin_amdgcn_s_sleep(TILE0_DELAY);
    P6(0, 0) P6(0, 6)
    if (w == 0 && l == 0) __builtin_amdgcn_raw_buffer_store_b64(tag, rsWS, 0, WS_FLAG2 + b * 8, SC1);
    ST(11);
    BAR();
    u32x4 K0[12];
    if (w == 0) waitvm<1>(); else waitvm<0>();
    ST(12);
    read_valid(K0, 0, toff[0]);
    ST(13);
    piece(IC(1), IC(0)); piece(IC(1), IC(1)); piece(IC(1), IC(2)); piece(IC(1), IC(3));
    BAR();
    ST(14);
    u32x4 Q[12];
    float mref;
    {
      const f32x4 n0 = lds_r128f(na), n1 = lds_r128f(na + 512);
#pragma unroll
      for (int s = 0; s < 6; ++s) Q[s] = lds_r128(qa + s * 1024);
      SBAR();
      const f32x4 s = n0 + n1;
      mref = __builtin_amdgcn_sqrtf(((s[0] + s[1]) + (s[2] + s[3])) * (192.0f * 1.02f));
      OPAQUE(mref);
      SBAR();
#pragma unroll
      for (int s = 6; s < 12; ++s) Q[s] = lds_r128(qa + s * 1024);
      SBAR();
    }
    constexpr int KW = 4, VW = 4;
    bool safe = false, exact = false, late = false;
#pragma unroll 1
    for (;;) {
      f32x16 S[2], O[6], negm;
      u32x4 K[12], V[12], P[2][2];
      f32x4 lsum = {0, 0, 0, 0};
      float pe[2][2];
#pragma unroll
      for (int i = 0; i < 16; ++i) negm[i] = -mref;
#define RK(k, T) K[k] = lds_r128((((k) & 1) ? kr1 : kr0) + ((T) % 3) * TILE_B + (k) * 1024);
#define RV(u, T) { const unsigned a_ = (((u) & 1) ? vr1 : vr0) + ((T) % 3) * TILE_B + ((u) >> 1) * 2048; V[u] = lds_tr2(a_, a_ + 256); }
      auto sm = [&](auto Tc, auto Hc, auto Gc) {
        constexpr int T = decltype(Tc)::value, H = decltype(Hc)::value, G = decltype(Gc)::value;
        if constexpr (G < 8) pe[(G >> 1) & 1][G & 1] = __builtin_amdgcn_exp2f(S[T & 1][8 * H + G]);
        if constexpr (G >= 2 && G <= 8 && G % 2 == 0) { constexpr int q = G / 2 - 1; P[T & 1][H][q] = cvtpk(pe[q & 1][0], pe[q & 1][1]); }
        if constexpr (G == 10) lsum = mfma16(sel, P[T & 1][H], lsum);
      };
      auto put_o = [&](const int un) {
        u32x4 pk;
#pragma unroll
        for (int e = 0; e < 4; ++e) pk[e] = cvtpk(O[un >> 1][8 * (un & 1) + 2 * e], O[un >> 1][8 * (un & 1) + 2 * e + 1]);
        lds_w128(pw + un * 1024, pk);
      };
      auto qkphase = [&](auto Ic) {
        constexpr int I = decltype(Ic)::value;
        auto step = [&](auto Kc) {
          constexpr int k = decltype(Kc)::value;
          if constexpr (I + 1 < 8) {
            S[(I + 1) & 1] = mfma32(I < 0 ? K0[k] : K[k], Q[k], k == 0 ? negm : S[(I + 1) & 1]);
            if constexpr (k + KW < 12 && I >= 0) RK(k + KW, I + 1)
          }
          if constexpr (I < 0 && k < 8) piece(IC(1), IC(4 + k));
          if constexpr (I >= 0) { if constexpr (k & 1) piece(IC(I + 2), IC(6 + k / 2)); if constexpr (k < 11) sm(IC(I), IC(1), IC(k)); }
          if constexpr (I >= 0 && I < 7 && k >= 12 - VW) RV(k - (12 - VW), I)
          if constexpr (I == 7 && k < VW) RV(k, I)
          SBAR();
        };
        step(IC(0)); step(IC(1)); step(IC(2)); step(IC(3)); step(IC(4)); step(IC(5)); step(IC(6)); step(IC(7)); step(IC(8)); step(IC(9)); step(IC(10)); step(IC(11));
      };
      auto pvphase = [&](auto Ic) {
        constexpr int I = decltype(Ic)::value;
        auto step = [&](auto Uc) {
          constexpr int u = decltype(Uc)::value;
          if constexpr (I >= 0) {
            f32x16 z;
#pragma unroll
            for (int e = 0; e < 16; ++e) z[e] = 0.f;
            O[u >> 1] = mfma32(V[u], P[I & 1][u & 1], (I == 0 && !(u & 1)) ? z : O[u >> 1]);
            if constexpr (u & 1) piece(IC(I + 3), IC(u / 2));
            if constexpr (u + VW < 12) RV(u + VW, I)
          }
          if constexpr (I + 1 < 8 && u >= 1) sm(IC(I + 1), IC(0), IC(u - 1));
          if constexpr (I < 0 && (u & 1)) piece(IC(2), IC(u / 2));
          if constexpr (I + 2 < 8) {
            if constexpr (u == 12 - KW) waitvm<(I < 0 ? 12 : I + 3 < 8 ? 6 + (12 - KW) / 2 : 6)>();
            if constexpr (u >= 12 - KW) RK(u - (12 - KW), I + 2)
            if constexpr (u == 11) waitvm<(I + 3 < 8 ? 6 : 0)>();
          }
          if constexpr (I == 7 && u >= 3) {
            put_o(u - 3);
          }
          SBAR();
        };
        step(IC(0)); step(IC(1)); step(IC(2)); step(IC(3)); step(IC(4)); step(IC(5)); step(IC(6)); step(IC(7)); step(IC(8)); step(IC(9)); step(IC(10)); step(IC(11));
      };
#define STALE(v) (__builtin_expect(!safe && __any((v) != (v)), 0))
      bool done = false;
      do {
        qkphase(IC(-1));
        ST(15);
        pvphase(IC(-1));
        ST(16);
        qkphase(IC(0)); pvphase(IC(0)); qkphase(IC(1)); pvphase(IC(1)); qkphase(IC(2));
        const float seen = lsum[0];
        pvphase(IC(2));
        if (STALE(seen)) break;
        qkphase(IC(3)); pvphase(IC(3)); qkphase(IC(4)); pvphase(IC(4)); qkphase(IC(5));
        if (!late) asm volatile("s_barrier" ::: "memory");
        late = true;
        pvphase(IC(5)); qkphase(IC(6)); pvphase(IC(6)); qkphase(IC(7)); pvphase(IC(7));
        if (STALE(lsum[0])) break;
        { const u32x2 ls = {asu(lsum[0]), asu(lsum[1])}; lds_w64(lw, ls); }
        put_o(9); put_o(10); put_o(11);
        done = true;
      } while (false);
#pragma unroll
      for (int k = 0; k < 12; ++k) asm volatile("" : "=v"(K0[k]));
      if (__builtin_expect(done, 1)) {
        ST(17);
        BAR();
        ST(18);
        float lt[2];
        unsigned ll = lo[0];
        OPAQUE(ll);
        ll >>= 4;
        if (__builtin_expect(!low_sum(lds_r128f(L_L + (ll & 15) * 32), lds_r128f(L_L + (ll & 15) * 32 + 16), lt) || exact, 1)) break;
        BAR();
        float mx = -3.0e38f;
#pragma unroll 1
        for (unsigned i = 0; i < 8; ++i) {
#pragma unroll 1
          for (int k = 0; k < 12; ++k) { setm0<0>(ring + k * 1024); bdma<0>(lo[0], rs, (8 * w + i) * TILE_B + k * 1024); }
          waitvm<0>();
          f32x16 s;
#pragma unroll
          for (int e = 0; e < 16; ++e) s[e] = 0.f;
#pragma unroll
          for (int k = 0; k < 12; ++k) s = mfma32(lds_r128(((k & 1) ? kr1 : kr0) + k * 1024), Q[k], s);
#pragma unroll
          for (int e = 0; e < 16; ++e) mx = fmaxf(mx, s[e]);
        }
        lds_w32(L_N + ((ll & 31) * 8 + w * 2 + (ll >> 5)) * 4, mx);
        BAR();
        const f32x4 c = lds_r128f(L_N + (ll & 31) * 32), d = lds_r128f(L_N + (ll & 31) * 32 + 16);
        mref = fmaxf(fmaxf(fmaxf(c[0], c[1]), fmaxf(c[2], c[3])), fmaxf(fmaxf(d[0], d[1]), fmaxf(d[2], d[3])));
        exact = true;
      } else {
        unsigned ll = lo[0];
        OPAQUE(ll);
        for (int sp = 0; sp < SPIN_CAP; ++sp) {
          asm volatile("" ::: "memory");
          const u32x2 f = __builtin_amdgcn_raw_buffer_load_b64(rsWS, ll >> 1, WS_FLAG2 + w * 512, SC1);
          if (__all(f[0] == tag[0] && f[1] == tag[1])) break;
        }
      }
      safe = true;
      asm volatile("s_waitcnt vmcnt(0) lgkmcnt(0)" ::: "memory");
      P6(0, 0) P6(0, 6)
      waitvm<0>();
      piece(IC(1), IC(0)); piece(IC(1), IC(1)); piece(IC(1), IC(2)); piece(IC(1), IC(3));
#pragma unroll
      for (int k = 0; k < 12; ++k) K0[k] = lds_r128(((k & 1) ? kr1 : kr0) + k * 1024);
    }
    ST(19);
    BAR();
    ST_OUT(ws, 0, 12);
  } else {
    auto merge16 = [&](const u32x4 (&a)[6], const u32x4 (&bb)[6], const float sc, const unsigned where) {
      f32x4 d = {0, 0, 0, 0};
#pragma unroll
      for (int kk = 0; kk < 6; ++kk) d = mfma16(a[kk], bb[kk], d);
      __builtin_amdgcn_raw_buffer_store_b64(pack4(d * sc), rsWS, ((l >> 5) * 16 + (l & 15)) * 16 + ((l >> 4) & 1) * 8, where + b * 512, SC1);
    };
    auto rd16 = [&](u32x4 (&a)[6], const unsigned at) {
#pragma unroll
      for (int kk = 0; kk < 6; ++kk) a[kk] = lds_r128(lds0 + at + (l & 15) * 384 + (((4 * kk + (l >> 4)) ^ (l & 7)) * 16));
    };
    u32x4 wq[18], wv[18];
    auto ldw = [&](u32x4 (&W)[18], const int m, const int i0, const int i1) {
#pragma unroll
      for (int i = i0; i < i1; ++i) W[i] = __builtin_amdgcn_raw_buffer_load_b128(rsWS, l * 16 + (i & 3) * 1024, WS_WM + m * 73728 + w * 18432 + (i >> 2) * 4096, SC1);
    };
    u32x2 tag7 = {0, 0};
    if (w < 2) {
      const unsigned ldsw = lds0 + 18432 * w;
      unsigned qp[9];
#pragma unroll
      for (int i = 0; i < 9; ++i) qp[i] = (qo[i] ^ 64) + 9216;
      DMA9(qp, wqkv_t, ldsw + 9216);
      ST(1);
      unsigned oo[9], op[9];
      auto wo_off = [&](const int i) {
        const unsigned row = ((8 * i + (l >> 3)) * 171) >> 9;
        oo[i] = ((18432 * w + l * 16) ^ (((row & 2) | ((row & 8) >> 1)) << 4)) + (i >> 2) * 4096;
        op[i] = (oo[i] ^ 64) + 9216;
        OPAQUE(oo[i]); OPAQUE(op[i]);
      };
#define CHUNK(A, B, src, c, mid) do { DMA9(A, src, ldsw + ((c) % 3) * SLOT); mid; DMA9(B, src, ldsw + ((c) % 3) * SLOT + 9216); } while (0)
      u32x2 tag;
      CHUNK(qo, qp, wqkv_t + SLOT, 1, tag = get_tag());
      BARV(18);
      asm volatile("" :: "v"(dc));
      OPAQUE(l);
      CHUNK(qo, qp, wqkv_t + 2 * SLOT, 2, (wo_off(0), wo_off(1), wo_off(2)));
      BARV(18);
      CHUNK(qo, qp, wqkv_t + 3 * SLOT, 3, (wo_off(3), wo_off(4), wo_off(5)));
      BARV(18);
      CHUNK(qo, qp, wqkv_t + 4 * SLOT, 4, (wo_off(6), wo_off(7), wo_off(8)));
      BARV(18);
      CHUNK(qo, qp, wqkv_t + 5 * SLOT, 5, (void)0);
      BARV(18);
      CHUNK(oo, op, wo_t, 6, (void)0);
      BARV(18);
      CHUNK(oo, op, wo_t + SLOT, 7, (void)0);
      ST(3);
      u32x4 f = __builtin_amdgcn_raw_buffer_load_b128(rsWS, l * 16, WS_FLAG + w * 1024, SC1);
      for (int sp = 0; sp < SPIN_CAP && !__all(tags_ok(f, tag)); ++sp) {
        asm volatile("" ::: "memory");
        f = __builtin_amdgcn_raw_buffer_load_b128(rsWS, l * 16, WS_FLAG + w * 1024, SC1);
      }
      ST(4);
      BARV(0);
      ldw(wq, 0, 0, 18);
    } else if (w == 2) {
      if (merger) { rows16(wqkv_s + mrow * 1152, lds0 + L_X); rows16(wqkv_s + 16 * mtau * 1152 + 384, lds0 + L_X + 6144); }
      ST(1);
      BAR();
      BAR();
      u32x4 a[6], bb[6];
      waitvm<0>();
      asm volatile("" :: "v"(dc));
      if (merger) { rd16(a, L_X); rd16(bb, L_X + 6144); }
      ST(2);
      BAR();
      BAR();
      if (merger) merge16(a, bb, QSCALE, WS_WM);
      BAR();
      BAR();
    } else {
      const u32x4 pz = {POISON, POISON, POISON, POISON};
      const unsigned p0 = WS_H2 + t * FRAME_B + (merger ? qb * 9 : 160 + (qb - 18) * 16) * 1024;
#pragma unroll
      for (int i = 0; i < 9; ++i) __builtin_amdgcn_raw_buffer_store_b128(pz, rsWS, l * 16 + (i & 3) * 1024, p0 + (i >> 2) * 4096, SC1);
      if (!merger) {
#pragma unroll
        for (int i = 9; i < 16; ++i) __builtin_amdgcn_raw_buffer_store_b128(pz, rsWS, l * 16 + (i & 3) * 1024, p0 + (i >> 2) * 4096, SC1);
      }
      __builtin_amdgcn_raw_buffer_store_b128(pz, rsWS, merger ? (l >> 5) * 73728 + (l & 31) * 16 : WS_END, WS_WM + b * 512, SC1);
      __builtin_amdgcn_raw_buffer_store_b32(POISON, rsWS, l * 4, WS_ST + (t * 1024 + qb * 32) * 8, SC1);
      asm volatile("s_nop 1" :: "v"(pz));
      tag7 = get_tag();
      ST(1);
      BAR(); BAR(); BAR();
      waitvm<0>();
      asm volatile("" :: "v"(d0), "v"(d1), "v"(dc));
      if (l == 0) __builtin_amdgcn_raw_buffer_store_b64(tag7, rsWS, 0, WS_FLAG + b * 8, SC1);
      ST(2);
      BAR(); BAR(); BAR();
    }
    if (w >= 2) {
      const unsigned i16 = l & 15, g = l >> 4;
      unsigned a = L_SP + (w - 2) * 1024 + l * 16, pa = L_PT + (w - 2) * 512 + l * 8;
      float keep = ((g >> 1) == (i16 >> 3)) ? 1.f : 0.f;
      OPAQUE(a); OPAQUE(pa); OPAQUE(keep);
      __builtin_amdgcn_s_setprio(3);
      BAR();
      ST(4);
      f32x4 e = (lds_r128f(a) + lds_r128f(a + 2048)) + (lds_r128f(a + 4096) + lds_r128f(a + 6144));
      const float mq = xor16_max(fmaxf(fmaxf(e[0], e[1]), fmaxf(e[2], e[3]))) * -QSCALE;
#pragma unroll
      for (int k = 0; k < 4; ++k) e[k] = __builtin_amdgcn_exp2f(fmaf(e[k], QSCALE, mq));
      lds_w64(pa, pack4(e * (keep * __builtin_amdgcn_rcpf(xor16_add((e[0] + e[1]) + (e[2] + e[3]))))));
      __builtin_amdgcn_s_setprio(0);
    }
    ST(5);
    BAR();
    if (b == 0 && w == 3 && l == 0) __builtin_amdgcn_raw_buffer_store_b64(tag7, rsWS, 0, WS_EPOCH, SC1);
    OPAQUE(l);
    LANE_IDS;
    if (w >= 2) ldw(wq, 0, 0, 8);
    BAR();
    if (w >= 2) ldw(wq, 0, 8, 18);
    unsigned hqa[2][2], own[2][2];
#pragma unroll
    for (int hf = 0; hf < 2; ++hf) {
      const unsigned in = lds0 + L_HQ + ((4 * hf + tq) ^ hh) * 128 + ((g & 1) ^ hf) * 64 + tp * 16;
      hqa[hf][0] = in + hh * 1024; hqa[hf][1] = in + 2048 + hh * (SLOT - 2048);
      OPAQUE(hqa[hf][0]); OPAQUE(hqa[hf][1]);
    }
    const unsigned tok = 16 * (g & 1) + i16;
    unsigned nw = lds0 + L_N + l * 16 + w * 4;
    OPAQUE(nw);
    unsigned qw = L_Q + (3 * w * 64 + hh * 32 + i16) * 16 + 8 * (g & 1);
    own[0][0] = w * SLOT + L_HQ + (tq ^ (w & 1)) * 128 + hh * 64 + tp * 16 + 8 * (g & 1); own[0][1] = (own[0][0] ^ 64) + 512;
    own[1][0] = own[0][0] ^ 128; own[1][1] = own[0][1] ^ 128;
    OPAQUE(qw); OPAQUE(own[0][0]); OPAQUE(own[0][1]); OPAQUE(own[1][0]); OPAQUE(own[1][1]);
    BAR();
    constexpr unsigned L_M2 = 2 * SLOT + TILE_B;
    const bool merge2 = merger && w == 2;
    if (merge2) {
      rows16(wqkv_s + mrow * 1152 + 768, lds0 + L_M2);
#pragma unroll
      for (int i = 0; i < 6; ++i) DMA1((l >> 1) * 384 + (l & 1) * 16, wo_s + i * 12288 + mtau * 32, lds0 + L_M2 + 6144 + i * 1024);
    }
    ST(6);
    BAR();
    ST(7);
    u32x4 ma[6], mb[6];
    u32x4 hq[3];
    {
      const u64 hbu = (u64)(ws + WS_H2 + (size_t)t * FRAME_B);
      const u32x4 rs = {(unsigned)hbu, (unsigned)(hbu >> 32) & 0xffffu, (unsigned)FRAME_B, 0x00020000u};
      for (int sp = 0; sp < SPIN_CAP; ++sp) {
        setm0<L_HQ>(lds0 + w * SLOT);
        bdma<0>(l * 16, rs, qb * TILE_B + w * 3072); bdma<1024>(l * 16, rs, qb * TILE_B + w * 3072); bdma<2048>(l * 16, rs, qb * TILE_B + w * 3072);
        if (merge2 && sp == 0) {
          waitvm<3>();
          rd16(ma, L_M2);
#pragma unroll
          for (int kk = 0; kk < 6; ++kk) { const unsigned at = lds0 + L_M2 + 6144 + (32 * kk + 8 * g + tq) * 32 + tp * 8; mb[kk] = lds_tr2(at, at + 128); }
        }
        waitvm<0>();
#pragma unroll
        for (int i = 0; i < 3; ++i) { const u32x2 u0 = lds_r64(own[i & 1][0] + i * 1024), u1 = lds_r64(own[i & 1][1] + i * 1024); hq[i] = u32x4{u0[0], u0[1], u1[0], u1[1]}; }
        if (!poisoned(hq)) break;
      }
    }
    ST(8);
    BAR();
    ST(9);
    auto gemm = [&](const u32x4 (&wf)[18], const u32x4 (&bf)[6][2], auto&& fin) {
      f32x4 d[3][2];
#pragma unroll
      for (int i = 0; i < 3; ++i) {
#pragma unroll
        for (int ks = 0; ks < 6; ++ks) {
          d[i][0] = mfma16(wf[i * 6 + ks], bf[ks][0], ks ? d[i][0] : f32x4{0, 0, 0, 0});
          d[i][1] = mfma16(wf[i * 6 + ks], bf[ks][1], ks ? d[i][1] : f32x4{0, 0, 0, 0});
          if (ks == 0) SBAR();
        }
        if (i > 0) fin(i - 1, d[i - 1]);
        SBAR();
      }
      fin(2, d[2]);
    };
    {
      u32x4 bf[6][2];
#pragma unroll
      for (int ks = 0; ks < 6; ++ks)
#pragma unroll
        for (int hf = 0; hf < 2; ++hf) {
          if (ks == 3) { hqa[hf][0] += 2 * SLOT; hqa[hf][1] += 2 * SLOT; }
          bf[ks][hf] = lds_r128(ks % 3 == 1 ? hqa[hf][1] : hqa[hf][0] + (ks % 3 == 2 ? SLOT + 1024 : 0));
        }
      SBAR();
#pragma unroll 1
      for (int sp = 0;; ++sp) {
        f32x2 n[2] = {{0.f, 0.f}, {0.f, 0.f}};
        gemm(wq, bf, [&](const int i, const f32x4 (&d)[2]) {
#pragma unroll
          for (int hf = 0; hf < 2; ++hf) {
            lds_w64(qw + i * 1024 + hf * 256, pack4(d[hf]));
            const f32x2 a = {d[hf][0], d[hf][1]}, c = {d[hf][2], d[hf][3]};
            n[hf] = __builtin_elementwise_fma(a, a, n[hf]); n[hf] = __builtin_elementwise_fma(c, c, n[hf]);
          }
        });
        auto rr = __builtin_amdgcn_permlane16_swap(asu(n[0][0] + n[0][1]), asu(n[1][0] + n[1][1]), false, false);
        const float nn = asf(rr[0]) + asf(rr[1]);
        lds_w32(nw, nn);
        if (__builtin_expect(!__any(nn != nn), 1) || sp >= SPIN_CAP || !poisoned(wq)) break;
        asm volatile("" ::: "memory");
        ldw(wq, 0, 0, 18);
      }
    }
    ST(11);
    BAR();
    if (merge2) { __builtin_amdgcn_s_sleep(24); merge16(ma, mb, 1.0f, WS_WM + 73728); }
    unsigned oo = (t * 1024 + qb * 32 + tok) * 384 + (6 * w + hh) * 16;
    OPAQUE(oo);
    float lt[2];
    u32x4 pick;
#pragma unroll
    for (int e = 0; e < 4; ++e) {
      const unsigned d = 8 * (e >> 1) + 4 * (g & 1) + 2 * (e & 1);
      pick[e] = (i16 == d ? 0x3f80u : 0u) | (i16 == d + 1 ? 0x3f800000u : 0u);
    }
    unsigned pr[2], la = lds0 + L_L + i16 * 32;
    pr[0] = lds0 + hh * SLOT + 3 * w * 1024 + ((g & 1) * 32 + i16) * 16; pr[1] = pr[0] + 2 * SLOT;
    OPAQUE(pick); OPAQUE(pr[0]); OPAQUE(pr[1]); OPAQUE(la);
    FRAG_POS;
    BAR();
    ST(16);
    f32x4 x1[3][2];
    {
      u32x2 st[2];
      for (int sp = 0; sp < SPIN_CAP; ++sp) {
        asm volatile("" ::: "memory");
        st[0] = __builtin_amdgcn_raw_buffer_load_b64(rsWS, i16 * 8, WS_ST + (t * 1024 + qb * 32) * 8, SC1);
        st[1] = __builtin_amdgcn_raw_buffer_load_b64(rsWS, i16 * 8 + 128, WS_ST + (t * 1024 + qb * 32) * 8, SC1);
        ldw(wv, 1, 0, 18);
        if (!__any(max(max(st[0][0], st[0][1]), max(st[1][0], st[1][1])) == POISON) && !poisoned(wv)) break;
      }
#pragma unroll
      for (int hf = 0; hf < 2; ++hf) {
        const float mu = asf(st[hf][0]), sd = __builtin_amdgcn_sqrtf(asf(st[hf][1]));
#pragma unroll
        for (int i = 0; i < 3; ++i) {
          const f32x4 h = {bflo(hq[i][2 * hf]), bfhi(hq[i][2 * hf]), bflo(hq[i][2 * hf + 1]), bfhi(hq[i][2 * hf + 1])};
          x1[i][hf] = h * sd + mu;
          OPAQUE(x1[i][hf]);
        }
      }
    }
    ST(12);
#pragma unroll 1
    for (bool exact = false;; exact = true) {
      BAR();
      ST(13);
      u32x4 p[3][2][2];
#pragma unroll
      for (int i = 0; i < 3; ++i)
#pragma unroll
        for (int hf = 0; hf < 2; ++hf) { p[i][hf][0] = lds_r128(pr[0] + i * 1024 + hf * 256); p[i][hf][1] = lds_r128(pr[1] + i * 1024 + hf * 256); }
      const f32x4 l0 = lds_r128f(la), l1 = lds_r128f(la + 16);
      SBAR();
#pragma unroll
      for (int i = 0; i < 3; ++i)
#pragma unroll
        for (int hf = 0; hf < 2; ++hf) lds_w64(dp[hf] + (L_FI - L_X) + i * 512, pack4(mfma16(pick, p[i][hf][1], mfma16(pick, p[i][hf][0], f32x4{0, 0, 0, 0}))));
      if (__builtin_expect(!low_sum(l0, l1, lt) || exact, 1)) break;
      BAR();
      BAR();
    }
    ST(14);
    const float linv[2] = {__builtin_amdgcn_rcpf(lt[0]), __builtin_amdgcn_rcpf(lt[1])};
    BAR();
    ST(15);
    u32x4 bf[6][2];
#pragma unroll
    for (int ks = 0; ks < 6; ++ks) { bf[ks][0] = lds_r128(fr[0] + (L_FI - L_X) + ks * 1024); bf[ks][1] = lds_r128(fr[1] + (L_FI - L_X) + ks * 1024); }
    SBAR();
    f32x4 d[3][2];
#pragma unroll
    for (int ks = 0; ks < 6; ++ks)
#pragma unroll
      for (int i = 0; i < 3; ++i) {
        d[i][0] = mfma16(wv[i * 6 + ks], bf[ks][0], ks ? d[i][0] : f32x4{0, 0, 0, 0});
        d[i][1] = mfma16(wv[i * 6 + ks], bf[ks][1], ks ? d[i][1] : f32x4{0, 0, 0, 0});
      }
#pragma unroll
    for (int i = 0; i < 3; ++i) {
      const u32x2 y0 = pack4(d[i][0] * linv[0] + x1[i][0]), y1 = pack4(d[i][1] * linv[1] + x1[i][1]);
      auto s0 = __builtin_amdgcn_permlane16_swap(y0[0], y1[0], false, false);
      auto s1 = __builtin_amdgcn_permlane16_swap(y0[1], y1[1], false, false);
      const u32x4 o = {s0[0], s1[0], s0[1], s1[1]};
      *(u32x4*)(out + oo + i * 32) = o;
    }
    ST(17);
    ST_OUT(ws, 12, 20);
  }
}

extern "C" void kernel_launch(void* const* d_in, const int* in_sizes, int n_in, void* d_out, int out_size, void* d_ws, size_t ws_size, hipStream_t stream) {
  (void)hipFuncSetAttribute((const void*)k_timesformer, hipFuncAttributeMaxDynamicSharedMemorySize, LDS_BYTES);
  k_timesformer<<<256, 512, LDS_BYTES, stream>>>((const char*)d_in[0], (const char*)d_in[1], (const char*)d_in[2], (const char*)d_in[3], (const char*)d_in[4],
                                                 (char*)d_ws, (char*)d_out);
}
```

```cpp
#include <hip/hip_runtime.h>
#include <hip/hip_bf16.h>
#include <type_traits>

typedef short s16x4 __attribute__((ext_vector_type(4)));
typedef __bf16 b16x8 __attribute__((ext_vector_type(8)));
typedef __bf16 b16x2 __attribute__((ext_vector_type(2)));
typedef float f32x2 __attribute__((ext_vector_type(2)));
typedef float f32x4 __attribute__((ext_vector_type(4)));
typedef float f32x16 __attribute__((ext_vector_type(16)));
typedef unsigned u32x2 __attribute__((ext_vector_type(2)));
typedef unsigned u32x4 __attribute__((ext_vector_type(4)));
typedef unsigned long long u64;
#define LAS __attribute__((address_space(3)))
#define DI __device__ __forceinline__
#define SBAR() __builtin_amdgcn_sched_barrier(0)

constexpr int TILE_B = 12288;
constexpr int FRAME_B = 393216;
constexpr unsigned WS_H2 = 0, WS_ST = 8 * FRAME_B, WS_WM = 16 * FRAME_B, WS_JUNK = WS_WM + 4 * 73728, WS_SEAL = WS_JUNK + 40 * 512, WS_FLAG = WS_SEAL + 2 * 256 * 128, WS_FLAG2 = WS_FLAG + 256 * 128, WS_EPOCH = WS_FLAG2 + 2048, WS_END = WS_EPOCH + 256;
constexpr float QSCALE = 0.07216878364870322f * 1.4426950408889634f;
constexpr int SC1 = 16;
constexpr unsigned POISON = 0xffffffffu;
constexpr unsigned CODE_BYTES = 22528;
constexpr int TILE0_DELAY = 2, JOB2_DELAY = 5, READ_DELAY = 6, A_BEFORE_SP = 2;
constexpr int SPIN_CAP = 1 << 16;

#define ST_DECL
#define ST(k)
#define ST_OUT(ws, k0, k1)

DI float asf(unsigned u) { return __builtin_bit_cast(float, u); }
DI unsigned asu(float f) { return __builtin_bit_cast(unsigned, f); }
DI unsigned cvtpk(float lo, float hi) { f32x2 v = {lo, hi}; return __builtin_bit_cast(unsigned, __builtin_convertvector(v, b16x2)); }
DI float bflo(unsigned u) { return asf(u << 16); }
DI float bfhi(unsigned u) { return asf(u & 0xffff0000u); }
DI f32x16 mfma32(u32x4 a, u32x4 b, f32x16 c) { return __builtin_amdgcn_mfma_f32_32x32x16_bf16(__builtin_bit_cast(b16x8, a), __builtin_bit_cast(b16x8, b), c, 0, 0, 0); }
DI f32x4 mfma16(u32x4 a, u32x4 b, f32x4 c) { return __builtin_amdgcn_mfma_f32_16x16x32_bf16(__builtin_bit_cast(b16x8, a), __builtin_bit_cast(b16x8, b), c, 0, 0, 0); }
DI f32x4 mfma16k16(u32x2 a, u32x2 b, f32x4 c) { return __builtin_amdgcn_mfma_f32_16x16x16bf16_1k(__builtin_bit_cast(s16x4, a), __builtin_bit_cast(s16x4, b), c, 0, 0, 0); }
DI u32x2 pack4(f32x4 v) { u32x2 r = {cvtpk(v[0], v[1]), cvtpk(v[2], v[3])}; return r; }
DI u32x4 lds_r128(unsigned a) { return *(LAS u32x4*)(size_t)a; }
DI f32x4 lds_r128f(unsigned a) { return *(LAS f32x4*)(size_t)a; }
DI u32x2 lds_r64(unsigned a) { return *(LAS u32x2*)(size_t)a; }
DI void lds_w128(unsigned a, u32x4 v) { *(LAS u32x4*)(size_t)a = v; }
DI void lds_w128f(unsigned a, f32x4 v) { *(LAS f32x4*)(size_t)a = v; }
DI void lds_w64(unsigned a, u32x2 v) { *(LAS u32x2*)(size_t)a = v; }
DI void lds_w32(unsigned a, float v) { *(LAS float*)(size_t)a = v; }
DI u32x2 lds_tr(unsigned a) { return __builtin_bit_cast(u32x2, __builtin_amdgcn_ds_read_tr16_b64_v4i16((LAS s16x4*)(size_t)a)); }
DI u32x4 lds_tr2(unsigned a0, unsigned a1) { u32x2 x = lds_tr(a0), y = lds_tr(a1); u32x4 r = {x[0], x[1], y[0], y[1]}; return r; }
#define BAR() asm volatile("s_waitcnt lgkmcnt(0)\n\ts_barrier" ::: "memory")
#define BARV(n) asm volatile("s_waitcnt vmcnt(" #n ") lgkmcnt(0)\n\ts_barrier" ::: "memory")
template <int CTRL> DI float dpp_add(float v) { return v + asf(__builtin_amdgcn_update_dpp(0u, asu(v), CTRL, 0xf, 0xf, true)); }
DI float xor16_add(float v) { auto r = __builtin_amdgcn_permlane16_swap(asu(v), asu(v), false, false); return asf(r[0]) + asf(r[1]); }
DI float xor16_max(float v) { auto r = __builtin_amdgcn_permlane16_swap(asu(v), asu(v), false, false); return __builtin_amdgcn_fmed3f(asf(r[0]), asf(r[1]), __builtin_inff()); }
DI unsigned umax3(unsigned a, unsigned b, unsigned c) { return max(max(a, b), c); }
template <int N> DI bool poisoned(const u32x4 (&v)[N]) {
  unsigned m = 0;
#pragma unroll
  for (int i = 0; i < N; ++i) { m = umax3(m, v[i][0], v[i][1]); m = umax3(m, v[i][2], v[i][3]); }
  return __any(m == POISON);
}
DI u32x4 unpoison(u32x4 v) { u32x4 r = {min(v[0], POISON - 1), min(v[1], POISON - 1), min(v[2], POISON - 1), min(v[3], POISON - 1)}; return r; }
DI unsigned fi_pos(unsigned r, unsigned c8) {
  return (((r >> 4) * 6 + (c8 >> 2)) * 64 + (c8 & 3) * 16 + ((r & 15) ^ ((c8 & 1) | (((r >> 3) & 1) << 1) | ((r >> 4) << 2)))) * 16;
}
DI float row8_sum(float s) { s = dpp_add<0x124>(s); s = dpp_add<0x128>(s); return xor16_add(s); }
DI float sum24(const f32x2 (&v)[12]) {
  f32x2 s0 = v[0] + v[1], s1 = v[2] + v[3];
#pragma unroll
  for (int i = 4; i < 12; i += 2) { s0 += v[i]; s1 += v[i + 1]; }
  s0 += s1;
  return s0[0] + s0[1];
}
DI float sumsq24(const f32x2 (&v)[12]) {
  f32x2 q0 = v[0] * v[0], q1 = v[1] * v[1];
#pragma unroll
  for (int i = 2; i < 12; i += 2) { q0 = __builtin_elementwise_fma(v[i], v[i], q0); q1 = __builtin_elementwise_fma(v[i + 1], v[i + 1], q1); }
  q0 += q1;
  return q0[0] + q0[1];
}
DI f32x2 unpack2(unsigned u) { f32x2 r = {bflo(u), bfhi(u)}; return r; }

constexpr unsigned SLOT = 36864, L_X = 3 * SLOT, L_H = 4 * SLOT, L_N = L_H + TILE_B, L_L = L_N + 1024, L_PT = L_L + 1024, LDS_BYTES = 163840;
constexpr unsigned L_VI = L_X, L_SP = L_X + TILE_B, L_YI = L_X + 20480;
constexpr unsigned L_HQ = 24576, L_Q = L_H, L_FI = L_H;

#define DMA1(v0, sbase, ldsa) \
  asm volatile("s_mov_b32 m0, %2\n\ts_nop 0\n\tglobal_load_lds_dwordx4 %0, %1" :: "v"(v0), "s"(sbase), "s"(ldsa) : "memory")

template <int OFF> DI void bdma(unsigned voff, u32x4 rs, unsigned soff) { asm volatile("buffer_load_dwordx4 %0, %1, %2 offen offset:%3 sc1 lds" :: "v"(voff), "s"(rs), "s"(soff), "i"(OFF) : "memory"); }
template <int OFF> DI void setm0(unsigned base) { asm volatile("s_add_u32 m0, %0, %1\n\ts_nop 0" :: "s"(base), "i"(OFF) : "memory", "scc"); }
template <int N> DI void waitvm() { asm volatile("s_waitcnt vmcnt(%0)" :: "i"(N) : "memory"); }
#define IC(x) std::integral_constant<int, (x)>{}
#define OPAQUE(v) asm volatile("" : "+v"(v))
template <class R> DI void st128(u32x4 v, R rs, unsigned voff, unsigned soff) { __builtin_amdgcn_raw_buffer_store_b128(v, rs, voff, soff, SC1); asm volatile("s_nop 1" :: "v"(v)); }
template <int CTRL> DI unsigned dpp_xor(unsigned v) { return v ^ __builtin_amdgcn_update_dpp(0u, v, CTRL, 0xf, 0xf, true); }
DI unsigned fold_xor(unsigned e, unsigned o) {
  auto r = __builtin_amdgcn_permlane32_swap(e, o, false, false);
  unsigned u = r[0] ^ r[1];
  auto q = __builtin_amdgcn_permlane16_swap(u, u, false, false);
  u = q[0] ^ q[1];
  u = dpp_xor<0x128>(u); u = dpp_xor<0x124>(u); u = dpp_xor<0x4e>(u); return dpp_xor<0xb1>(u);
}
DI bool seal_ok(unsigned e, unsigned o, u32x4 seal, bool has, u32x2 tag) {
  return __all(fold_xor(e ^ seal[0], o ^ seal[2]) == 0 && (!has || (seal[1] == tag[0] && seal[3] == tag[1])));
}
DI unsigned xor3(unsigned a, unsigned b, unsigned c) { return __builtin_amdgcn_bitop3_b32(a, b, c, 0x96); }
#define XOR_IN(e, o, v) do { e = xor3(e, (v)[0], (v)[2]); o = xor3(o, (v)[1], (v)[3]); } while (0)
#define MIXV6() _Pragma("unroll") for (int q_ = 0; q_ < 6; ++q_) { __builtin_amdgcn_sched_group_barrier(0x8, 1, 0); __builtin_amdgcn_sched_group_barrier(0x2, 1, 0); } SBAR()
template <int N> DI bool sealed(const u32x4 (&v)[N], u32x4 seal, bool has, u32x2 tag) {
  unsigned e = 0, o = 0;
#pragma unroll
  for (int i = 0; i < N; ++i) XOR_IN(e, o, v[i]);
  return seal_ok(e, o, seal, has, tag);
}
DI bool tags_ok(u32x4 f, u32x2 tag) { return f[0] == tag[0] && f[1] == tag[1] && f[2] == tag[0] && f[3] == tag[1]; }

__global__ __launch_bounds__(512, 2) void k_timesformer(const char* __restrict__ x, const char* __restrict__ wqkv_t, const char* __restrict__ wo_t,
                                                        const char* __restrict__ wqkv_s, const char* __restrict__ wo_s, char* __restrict__ ws, char* __restrict__ out) {
  unsigned dc;
  {
    u64 pc;
    asm volatile("s_getpc_b64 %1\n\tglobal_load_dword %0, %2, %1" : "=v"(dc), "=&s"(pc) : "v"(min(threadIdx.x * 128, CODE_BYTES)) : "memory");
    SBAR();
  }
  constexpr unsigned lds0 = 0;
  const unsigned b = blockIdx.x;
  unsigned w8 = __builtin_amdgcn_readfirstlane(threadIdx.x >> 6);
  unsigned l = threadIdx.x & 63;
  const unsigned t = b & 7, qb = b >> 3;
  const auto rsWS = __builtin_amdgcn_make_buffer_rsrc((void*)ws, 0, WS_END, 0x00020000);
  ST_DECL;
  ST(0);
  const bool jqk = b < 144 || b >= 216;
  const unsigned jp = b < 144 ? b >> 1 : jqk ? b - 216 : b - 144, jw = jp / 18, jks = (jp - jw * 18) / 3, jtau = jp - jw * 15 - jks * 3;
  const unsigned jrow = jks * 32 + (jqk ? (b & 1) * 16 : 0), jslot = b >= 216 ? WS_JUNK + jp * 512 : WS_WM + (jqk ? b * 512 : 73728 + jp * 1024), jstep = b < 216 ? 2 * 73728 : 0;
  auto job_load = [&](const char* wqkv, const char* wo, const unsigned dst) {
    const char* ra = wqkv + jrow * 1152 + (jqk ? 0 : 768);
    const char* rb = jqk ? wqkv + jtau * (16 * 1152) + 384 : ra + 16 * 1152;
#pragma unroll
    for (int i = 0; i < 3; ++i) {
      const unsigned pos = i * 64 + l, row = (pos * 2731) >> 16, o = row * 1152 + (((pos - row * 24) ^ row) * 16);
      DMA1(o, ra, dst + i * 1024); DMA1(o, ra + 8 * 1152, dst + (3 + i) * 1024);
      DMA1(o, rb, dst + (6 + i) * 1024); DMA1(o, rb + 8 * 1152, dst + (9 + i) * 1024);
    }
    if (!jqk) {
#pragma unroll
      for (int i = 0; i < 6; ++i) DMA1((l >> 1) * 384 + (l & 1) * 16, wo + i * 12288 + jtau * 32, dst + (12 + i) * 1024);
    }
  };
  const u32x4 pz = {POISON, POISON, POISON, POISON};
  u64 epoch;
  asm volatile("s_load_dwordx2 %0, %1, 0x0" : "=&s"(epoch) : "s"(ws + WS_EPOCH), "s"(x), "s"(wqkv_t), "s"(wo_t), "s"(wqkv_s), "s"(wo_s), "s"(out) : "memory");
  if (w8 < 4) {
#pragma unroll
    for (int i = 0; i < 3; ++i) { const unsigned p = i * 64 + l; DMA1(p * 16 + (p >= 96 ? FRAME_B - 1536 : 0), x + w8 * 2 * FRAME_B + b * 1536, lds0 + L_YI + w8 * 3072 + i * 1024); }
  } else if (w8 < 6) {
    if (w8 == 5) __builtin_amdgcn_s_sleep(JOB2_DELAY);
    job_load(w8 == 4 ? wqkv_t : wqkv_s, w8 == 4 ? wo_t : wo_s, (w8 - 4) * SLOT);
  }
  asm volatile("s_waitcnt lgkmcnt(0)" : "+s"(epoch) :: "memory");
  epoch += 1;
  const u32x2 tag = {(unsigned)epoch, (unsigned)(epoch >> 32)};
  asm volatile("" : "+s"(w8));
  const unsigned w = w8 & 3;
#define LANE_IDS const unsigned i16 = l & 15, g = l >> 4, r32 = l & 31, hh = l >> 5, tq = i16 >> 2, tp = l & 3
#define FRAG_POS unsigned fr[2], dp[2];                                                                       \
  fr[0] = L_X + fi_pos(i16, g); fr[1] = (fr[0] ^ 64) + 6144; dp[0] = L_X + fi_pos(i16, hh) + 8 * (g & 1) + w * 1536; dp[1] = (dp[0] ^ 64) + 6144; \
  OPAQUE(fr[0]); OPAQUE(fr[1]); OPAQUE(dp[0]); OPAQUE(dp[1])
  auto ldw = [&](u32x4 (&W)[18], const int m, const int k0, const int k1, const unsigned rot) {
#pragma unroll
    for (int kk = k0; kk < k1; ++kk) {
      const unsigned ks = kk + rot >= 6 ? kk + rot - 6 : kk + rot;
#pragma unroll
      for (int i = 0; i < 3; ++i) { SBAR(); W[kk * 3 + i] = __builtin_amdgcn_raw_buffer_load_b128(rsWS, l * 16 + i * 1024, WS_WM + m * 73728 + w * 18432 + ks * 3072, SC1); }
    }
  };
  auto ld_seal = [&](const int m) {
    return __builtin_amdgcn_raw_buffer_load_b128(rsWS, l < ((m & 1) ? 18 : 36) ? l * 128 : WS_END, WS_SEAL + (m >> 1) * 32768 + ((m & 1) ? 144 + 18 * w : 36 * w) * 128, SC1);
  };
  const unsigned rot = qb % 6;
  auto h_frags = [&](u32x4 (&H)[6][2], const unsigned (&fr)[2]) {
#pragma unroll
    for (int kk = 0; kk < 6; ++kk) {
      const unsigned ks = kk + rot >= 6 ? kk + rot - 6 : kk + rot;
      H[kk][0] = lds_r128(fr[0] + ks * 1024 + (L_H - L_X)); H[kk][1] = lds_r128(fr[1] + ks * 1024 + (L_H - L_X));
    }
  };
  auto low_sum = [&](const f32x4 a, const f32x4 c, float (&lt)[2]) {
    lt[0] = (a[0] + a[2]) + (c[0] + c[2]);
    lt[1] = (a[1] + a[3]) + (c[1] + c[3]);
    return (bool)__any(lt[0] < 8.7e-19f || lt[1] < 8.7e-19f);
  };

  if (w8 < 4) {
    const unsigned nl = l & 3, j = (l >> 2) & 7, tt = 2 * w + (l >> 5), r = nl * 8 + tt;
    unsigned rp = L_X + fi_pos(r, j);
    OPAQUE(rp);
    waitvm<0>();
    asm volatile("" :: "v"(dc));
    f32x2 xv[12];
    float mu1;
    {
      u32x4 h[3], xr[3];
#pragma unroll
      for (int i = 0; i < 3; ++i) xr[i] = lds_r128(lds0 + L_YI + tt * 1536 + nl * 384 + j * 16 + i * 128);
#pragma unroll
      for (int i = 0; i < 12; ++i) xv[i] = unpack2(xr[i >> 2][i & 3]);
      mu1 = row8_sum(sum24(xv)) * (1.0f / 192.0f);
#pragma unroll
      for (int i = 0; i < 12; ++i) xv[i] -= mu1;
      const float rs = __builtin_amdgcn_rsqf(row8_sum(sumsq24(xv)) * (1.0f / 192.0f) + 1e-5f);
#pragma unroll
      for (int i = 0; i < 12; ++i) { const f32x2 n = xv[i] * rs; h[i >> 2][i & 3] = cvtpk(n[0], n[1]); }
#pragma unroll
      for (int i = 0; i < 3; ++i) lds_w128(rp + (L_H - L_X) + i * 2048, h[i]);
    }
    SBAR();
    OPAQUE(l);
    unsigned so = tt * FRAME_B + (j >> 1) * 1024 + ((t ^ ((j >> 1) & 1)) * 128) + (((j & 1) ^ (t >> 2)) * 64) + nl * 16;
    const unsigned sso = qb * TILE_B;
    LANE_IDS;
    FRAG_POS;
    unsigned sto = j == 0 ? (tt * 1024 + nl) * 8 : WS_END, pta = L_PT + l * 8;
    OPAQUE(sto); OPAQUE(so); OPAQUE(pta);
    ST(1);
    BAR();
    {
      u32x4 H[6][2], W[18];
      u32x2 pt[2], vq[3][2];
      h_frags(H, fr);
      BAR();
      ST(6);
      asm volatile("s_barrier" ::: "memory");
      ST(2);
#pragma unroll 1
      for (int sp = 0;; ++sp) {
        const u32x4 seal = ld_seal(3);
        ldw(W, 3, 0, 6, rot);
        SBAR();
        ST(5);
        f32x4 vp[3][2];
        unsigned he = 0, ho = 0;
#pragma unroll
        for (int kk = 0; kk < 6; ++kk) {
          if (kk == A_BEFORE_SP) {
            ST(3);
            if (sp == 0) asm volatile("s_barrier" ::: "memory");
          }
#pragma unroll
          for (int i = 0; i < 3; ++i) {
            vp[i][0] = mfma16(H[kk][0], W[kk * 3 + i], kk ? vp[i][0] : f32x4{0, 0, 0, 0});
            vp[i][1] = mfma16(H[kk][1], W[kk * 3 + i], kk ? vp[i][1] : f32x4{0, 0, 0, 0});
            XOR_IN(he, ho, W[kk * 3 + i]);
          }
          MIXV6();
        }
        const bool ok = seal_ok(he, ho, seal, l < 18, tag);
#pragma unroll
        for (int i = 0; i < 3; ++i) { vq[i][0] = pack4(vp[i][0]); vq[i][1] = pack4(vp[i][1]); }
        if (__builtin_expect(ok, 1) || sp >= SPIN_CAP) break;
        asm volatile("" ::: "memory");
      }
      ST(7);
      asm volatile("s_barrier" ::: "memory");
      pt[0] = lds_r64(pta); pt[1] = lds_r64(pta + 512);
      ST(8);
      f32x4 y[3][2];
#pragma unroll
      for (int hf = 0; hf < 2; ++hf)
#pragma unroll
        for (int i = 0; i < 3; ++i) y[i][hf] = mfma16k16(vq[i][hf], pt[hf], f32x4{0, 0, 0, 0});
      SBAR();
#pragma unroll
      for (int hf = 0; hf < 2; ++hf)
#pragma unroll
        for (int i = 0; i < 3; ++i) lds_w64(dp[hf] + (L_YI - L_X) + i * 512, pack4(y[i][hf]));
    }
    ST(9);
    BAR();
    {
      u32x4 y[3], h[3];
#pragma unroll
      for (int i = 0; i < 3; ++i) y[i] = lds_r128(rp + (L_YI - L_X) + i * 2048);
#pragma unroll
      for (int i = 0; i < 12; ++i) xv[i] += unpack2(y[i >> 2][i & 3]);
      const float mu = row8_sum(sum24(xv)) * (1.0f / 192.0f), sq = row8_sum(sumsq24(xv));
      const float var = fmaxf(fmaf(sq, 1.0f / 192.0f, -mu * mu), 0.f) + 1e-5f;
      const float rs = __builtin_amdgcn_rsqf(var), nb = -mu * rs;
#pragma unroll
      for (int i = 0; i < 3; ++i) {
#pragma unroll
        for (int e = 0; e < 4; ++e) { const f32x2 n = xv[4 * i + e] * rs + nb; h[i][e] = cvtpk(n[0], n[1]); }
        st128(h[i], rsWS, so, WS_H2 + sso + i * 4096);
        SBAR();
      }
      const u32x2 stat = {asu(mu + mu1), asu(var)};
      __builtin_amdgcn_raw_buffer_store_b64(stat, rsWS, sto, WS_ST + b * 32, SC1);
    }
    ST(10);
    const unsigned ring = lds0 + w * SLOT;
    unsigned kr0 = ring + (r32 >> 2) * 128 + (hh ^ (r32 >> 4)) * 64 + (r32 & 3) * 16, kr1 = kr0 ^ 128;
    unsigned vr0 = ring + (g & 1) * 1024 + (hh ^ (g & 1)) * 128 + (tp >> 1) * 64 + tq * 16 + 8 * (tp & 1), vr1 = (vr0 ^ 64) + 512;
    unsigned lo[3] = {l * 16, l * 16 + 4096, l * 16 + 8192}, pw = ring + l * 16, lw = l < 16 ? L_L + (l * 4 + w) * 8 : L_L + 512 + l * 8;
    OPAQUE(kr0); OPAQUE(kr1); OPAQUE(vr0); OPAQUE(vr1); OPAQUE(lo[0]); OPAQUE(lo[1]); OPAQUE(lo[2]); OPAQUE(pw); OPAQUE(lw);
    const u64 hbu = (u64)(ws + WS_H2 + (size_t)t * FRAME_B);
    const u32x4 rs = {(unsigned)hbu, (unsigned)(hbu >> 32) & 0xffffu, (unsigned)FRAME_B, 0x00020000u};
    unsigned toff[8];
#pragma unroll
    for (int i = 0; i < 8; ++i) { toff[i] = (8 * w + ((i + qb) & 7)) * TILE_B; asm volatile("" : "+s"(toff[i])); }
    u32x4 sel;
    { const unsigned one = (tp == (g & 1)) ? 0x3f803f80u : 0u; sel = u32x4{one, one, one, one}; OPAQUE(sel); }
    auto piece = [&](auto Tc, auto Pc) {
      constexpr int T = decltype(Tc)::value, p = decltype(Pc)::value;
      if constexpr (T < 8) {
        if constexpr (p % 4 == 0) setm0<(T % 3) * TILE_B + p * 1024>(ring);
        bdma<(p % 4) * 1024>(lo[p / 4], rs, toff[T]);
      }
    };
#define P6(T, a) piece(IC(T), IC(a)); piece(IC(T), IC(a + 1)); piece(IC(T), IC(a + 2)); piece(IC(T), IC(a + 3)); piece(IC(T), IC(a + 4)); piece(IC(T), IC(a + 5));
    auto read_valid = [&](u32x4 (&KK)[12], const unsigned dst, unsigned off) {
      for (int sp = 0; sp < SPIN_CAP; ++sp) {
#pragma unroll
        for (int k = 0; k < 12; ++k) KK[k] = lds_r128(((k & 1) ? kr1 : kr0) + dst + k * 1024);
        if (__builtin_expect(!poisoned(KK), 1)) break;
#pragma unroll 1
        for (int k = 0; k < 12; ++k) { setm0<0>(ring + dst + k * 1024); bdma<0>(lo[0], rs, off + k * 1024); }
        waitvm<0>();
      }
    };
    unsigned qa = lds0 + L_Q + lo[0], na = lds0 + L_N + r32 * 16;
    OPAQUE(qa); OPAQUE(na);
    BARV(0);
    __builtin_amdgcn_s_sleep(TILE0_DELAY);
    P6(0, 0) P6(0, 6)
    if (w == 0 && l == 0) __builtin_amdgcn_raw_buffer_store_b64(tag, rsWS, 0, WS_FLAG2 + b * 8, SC1);
    ST(11);
    BAR();
    u32x4 K0[12];
    if (w == 0) waitvm<1>(); else waitvm<0>();
    ST(12);
    read_valid(K0, 0, toff[0]);
    ST(13);
    piece(IC(1), IC(0)); piece(IC(1), IC(1)); piece(IC(1), IC(2)); piece(IC(1), IC(3));
    BAR();
    ST(14);
    u32x4 Q[12];
    float mref;
    {
      const f32x4 n0 = lds_r128f(na), n1 = lds_r128f(na + 512);
#pragma unroll
      for (int s = 0; s < 6; ++s) Q[s] = lds_r128(qa + s * 1024);
      SBAR();
      const f32x4 s = n0 + n1;
      mref = __builtin_amdgcn_sqrtf(((s[0] + s[1]) + (s[2] + s[3])) * (192.0f * 1.02f));
      OPAQUE(mref);
      SBAR();
#pragma unroll
      for (int s = 6; s < 12; ++s) Q[s] = lds_r128(qa + s * 1024);
      SBAR();
    }
    constexpr int KW = 4, VW = 4;
    bool safe = false, exact = false, late = false;
#pragma unroll 1
    for (;;) {
      f32x16 S[2], O[6], negm;
      u32x4 K[12], V[12], P[2][2];
      f32x4 lsum = {0, 0, 0, 0};
      float pe[2][2];
#pragma unroll
      for (int i = 0; i < 16; ++i) negm[i] = -mref;
#define RK(k, T) K[k] = lds_r128((((k) & 1) ? kr1 : kr0) + ((T) % 3) * TILE_B + (k) * 1024);
#define RV(u, T) { const unsigned a_ = (((u) & 1) ? vr1 : vr0) + ((T) % 3) * TILE_B + ((u) >> 1) * 2048; V[u] = lds_tr2(a_, a_ + 256); }
      auto sm = [&](auto Tc, auto Hc, auto Gc) {
        constexpr int T = decltype(Tc)::value, H = decltype(Hc)::value, G = decltype(Gc)::value;
        if constexpr (G < 8) pe[(G >> 1) & 1][G & 1] = __builtin_amdgcn_exp2f(S[T & 1][8 * H + G]);
        if constexpr (G >= 2 && G <= 8 && G % 2 == 0) { constexpr int q = G / 2 - 1; P[T & 1][H][q] = cvtpk(pe[q & 1][0], pe[q & 1][1]); }
        if constexpr (G == 10) lsum = mfma16(sel, P[T & 1][H], lsum);
      };
      auto put_o = [&](const int un) {
        u32x4 pk;
#pragma unroll
        for (int e = 0; e < 4; ++e) pk[e] = cvtpk(O[un >> 1][8 * (un & 1) + 2 * e], O[un >> 1][8 * (un & 1) + 2 * e + 1]);
        lds_w128(pw + un * 1024, pk);
      };
      auto qkphase = [&](auto Ic) {
        constexpr int I = decltype(Ic)::value;
        auto step = [&](auto Kc) {
          constexpr int k = decltype(Kc)::value;
          if constexpr (I + 1 < 8) {
            S[(I + 1) & 1] = mfma32(I < 0 ? K0[k] : K[k], Q[k], k == 0 ? negm : S[(I + 1) & 1]);
            if constexpr (k + KW < 12 && I >= 0) RK(k + KW, I + 1)
          }
          if constexpr (I < 0 && k < 8) piece(IC(1), IC(4 + k));
          if constexpr (I >= 0) { if constexpr (k & 1) piece(IC(I + 2), IC(6 + k / 2)); if constexpr (k < 11) sm(IC(I), IC(1), IC(k)); }
          if constexpr (I >= 0 && I < 7 && k >= 12 - VW) RV(k - (12 - VW), I)
          if constexpr (I == 7 && k < VW) RV(k, I)
          SBAR();
        };
        step(IC(0)); step(IC(1)); step(IC(2)); step(IC(3)); step(IC(4)); step(IC(5)); step(IC(6)); step(IC(7)); step(IC(8)); step(IC(9)); step(IC(10)); step(IC(11));
      };
      auto pvphase = [&](auto Ic) {
        constexpr int I = decltype(Ic)::value;
        auto step = [&](auto Uc) {
          constexpr int u = decltype(Uc)::value;
          if constexpr (I >= 0) {
            f32x16 z;
#pragma unroll
            for (int e = 0; e < 16; ++e) z[e] = 0.f;
            O[u >> 1] = mfma32(V[u], P[I & 1][u & 1], (I == 0 && !(u & 1)) ? z : O[u >> 1]);
            if constexpr (u & 1) piece(IC(I + 3), IC(u / 2));
            if constexpr (u + VW < 12) RV(u + VW, I)
          }
          if constexpr (I + 1 < 8 && u >= 1) sm(IC(I + 1), IC(0), IC(u - 1));
          if constexpr (I < 0 && (u & 1)) piece(IC(2), IC(u / 2));
          if constexpr (I + 2 < 8) {
            if constexpr (u == 12 - KW) waitvm<(I < 0 ? 12 : I + 3 < 8 ? 6 + (12 - KW) / 2 : 6)>();
            if constexpr (u >= 12 - KW) RK(u - (12 - KW), I + 2)
            if constexpr (u == 11) waitvm<(I + 3 < 8 ? 6 : 0)>();
          }
          if constexpr (I == 7 && u >= 3) {
            put_o(u - 3);
          }
          SBAR();
        };
        step(IC(0)); step(IC(1)); step(IC(2)); step(IC(3)); step(IC(4)); step(IC(5)); step(IC(6)); step(IC(7)); step(IC(8)); step(IC(9)); step(IC(10)); step(IC(11));
      };
#define STALE(v) (__builtin_expect(!safe && __any((v) != (v)), 0))
      bool done = false;
      do {
        qkphase(IC(-1));
        ST(15);
        pvphase(IC(-1));
        ST(16);
        qkphase(IC(0)); pvphase(IC(0)); qkphase(IC(1)); pvphase(IC(1)); qkphase(IC(2));
        const float seen = lsum[0];
        pvphase(IC(2));
        if (STALE(seen)) break;
        qkphase(IC(3)); pvphase(IC(3)); qkphase(IC(4)); pvphase(IC(4)); qkphase(IC(5));
        if (!late) asm volatile("s_barrier" ::: "memory");
        late = true;
        pvphase(IC(5)); qkphase(IC(6)); pvphase(IC(6)); qkphase(IC(7)); pvphase(IC(7));
        if (STALE(lsum[0])) break;
        { const u32x2 ls = {asu(lsum[0]), asu(lsum[1])}; lds_w64(lw, ls); }
        put_o(9); put_o(10); put_o(11);
        done = true;
      } while (false);
#pragma unroll
      for (int k = 0; k < 12; ++k) asm volatile("" : "=v"(K0[k]));
      if (__builtin_expect(done, 1)) {
        ST(17);
        BAR();
        ST(18);
        float lt[2];
        unsigned ll = lo[0];
        OPAQUE(ll);
        ll >>= 4;
        if (__builtin_expect(!low_sum(lds_r128f(L_L + (ll & 15) * 32), lds_r128f(L_L + (ll & 15) * 32 + 16), lt) || exact, 1)) break;
        BAR();
        float mx = -3.0e38f;
#pragma unroll 1
        for (unsigned i = 0; i < 8; ++i) {
#pragma unroll 1
          for (int k = 0; k < 12; ++k) { setm0<0>(ring + k * 1024); bdma<0>(lo[0], rs, (8 * w + i) * TILE_B + k * 1024); }
          waitvm<0>();
          f32x16 s;
#pragma unroll
          for (int e = 0; e < 16; ++e) s[e] = 0.f;
#pragma unroll
          for (int k = 0; k < 12; ++k) s = mfma32(lds_r128(((k & 1) ? kr1 : kr0) + k * 1024), Q[k], s);
#pragma unroll
          for (int e = 0; e < 16; ++e) mx = fmaxf(mx, s[e]);
        }
        lds_w32(L_N + ((ll & 31) * 8 + w * 2 + (ll >> 5)) * 4, mx);
        BAR();
        const f32x4 c = lds_r128f(L_N + (ll & 31) * 32), d = lds_r128f(L_N + (ll & 31) * 32 + 16);
        mref = fmaxf(fmaxf(fmaxf(c[0], c[1]), fmaxf(c[2], c[3])), fmaxf(fmaxf(d[0], d[1]), fmaxf(d[2], d[3])));
        exact = true;
      } else {
        unsigned ll = lo[0];
        OPAQUE(ll);
        for (int sp = 0; sp < SPIN_CAP; ++sp) {
          asm volatile("" ::: "memory");
          const u32x2 f = __builtin_amdgcn_raw_buffer_load_b64(rsWS, ll >> 1, WS_FLAG2 + w * 512, SC1);
          if (__all(f[0] == tag[0] && f[1] == tag[1])) break;
        }
      }
      safe = true;
      asm volatile("s_waitcnt vmcnt(0) lgkmcnt(0)" ::: "memory");
      P6(0, 0) P6(0, 6)
      waitvm<0>();
      piece(IC(1), IC(0)); piece(IC(1), IC(1)); piece(IC(1), IC(2)); piece(IC(1), IC(3));
#pragma unroll
      for (int k = 0; k < 12; ++k) K0[k] = lds_r128(((k & 1) ? kr1 : kr0) + k * 1024);
    }
    ST(19);
    BAR();
    ST_OUT(ws, 0, 12);
  } else {
    LANE_IDS;
    auto job_run = [&](const unsigned at, const unsigned slot, const unsigned sealat) {
      u32x4 a[6], c[6];
#pragma unroll
      for (int kk = 0; kk < 6; ++kk) {
        a[kk] = lds_r128(at + i16 * 384 + (((4 * kk + g) ^ (l & 7)) * 16));
        c[kk] = lds_r128(at + 6144 + i16 * 384 + (((4 * kk + g) ^ (l & 7)) * 16));
      }
      const unsigned o = (hh * 16 + i16) * 16 + (g & 1) * 8;
      f32x4 d = {0, 0, 0, 0};
      u32x2 r;
      if (jqk) {
#pragma unroll
        for (int kk = 0; kk < 6; ++kk) d = mfma16(a[kk], c[kk], d);
        r = pack4(d * QSCALE);
        __builtin_amdgcn_raw_buffer_store_b64(r, rsWS, o, slot, SC1);
      } else {
        f32x4 e = {0, 0, 0, 0};
#pragma unroll
        for (int kk = 0; kk < 6; ++kk) {
          const unsigned p = at + 12288 + (32 * kk + 8 * g + tq) * 32 + tp * 8;
          const u32x4 bb = lds_tr2(p, p + 128);
          d = mfma16(a[kk], bb, d); e = mfma16(c[kk], bb, e);
        }
        const u32x2 r0 = pack4(d), r1 = pack4(e);
        __builtin_amdgcn_raw_buffer_store_b64(r0, rsWS, o, slot, SC1);
        __builtin_amdgcn_raw_buffer_store_b64(r1, rsWS, o + 512, slot, SC1);
        r = r0 ^ r1;
      }
      const u32x2 half = {fold_xor(r[0], r[1]), hh ? tag[1] : tag[0]};
      __builtin_amdgcn_raw_buffer_store_b64(half, rsWS, r32 == 0 ? hh * 8 : WS_END, sealat, SC1);
    };
    auto poison_h2 = [&](const int i0, const int i1) {
#pragma unroll
      for (int i = i0; i < i1; ++i) __builtin_amdgcn_raw_buffer_store_b128(pz, rsWS, l * 16 + (i & 3) * 1024, WS_H2 + t * FRAME_B + qb * TILE_B + (i >> 2) * 4096, SC1);
      asm volatile("s_nop 1" :: "v"(pz));
    };
    FRAG_POS;
    if (w < 2) {
      ST(1);
      if (w == 1) BAR();
      waitvm<0>();
      ST(2);
      job_run(w * SLOT, jslot + (1 - w) * jstep, WS_SEAL + (1 - w) * 32768 + b * 128);
    }
    ST(3);
    if (w != 1) BAR();
    if (w == 0) {
      waitvm<0>();
      __builtin_amdgcn_s_sleep(READ_DELAY);
    } else if (w == 2) {
      poison_h2(0, 12);
      __builtin_amdgcn_raw_buffer_store_b32(POISON, rsWS, l * 4, WS_ST + (t * 1024 + qb * 32) * 8, SC1);
    }
    u32x4 wq[18], wv[18];
    unsigned sa = L_SP + w * 1024 + l * 16, pa = L_PT + w * 512 + l * 8;
    float keep = (hh == (i16 >> 3)) ? 1.f : 0.f;
    OPAQUE(sa); OPAQUE(pa); OPAQUE(keep);
    {
      u32x4 H[6][2];
      u32x2 hs[3][2];
      h_frags(H, fr);
#pragma unroll
      for (int i = 0; i < 3; ++i) { hs[i][0] = lds_r64(dp[0] + (L_H - L_X) + i * 512); hs[i][1] = lds_r64(dp[1] + (L_H - L_X) + i * 512); }
      unsigned spa = L_SP + w * 2048 + l * 16;
      OPAQUE(spa);
      if (w == 2) {
        waitvm<0>();
        if (l == 0) __builtin_amdgcn_raw_buffer_store_b64(tag, rsWS, 0, WS_FLAG + b * 128, SC1);
      }
      ST(4);
      BAR();
      __builtin_amdgcn_s_setprio(3);
#pragma unroll 1
      for (int sp = 0;; ++sp) {
        const u32x4 seal = ld_seal(2);
        ldw(wq, 2, 0, 6, rot);
        if (sp == 0) asm volatile("s_barrier" ::: "memory");
        SBAR();
        f32x4 d[3][2], st[2];
        unsigned he = 0, ho = 0;
#pragma unroll
        for (int kk = 0; kk < 6; ++kk) {
#pragma unroll
          for (int i = 0; i < 3; ++i) {
            d[i][0] = mfma16(wq[kk * 3 + i], H[kk][0], kk ? d[i][0] : f32x4{0, 0, 0, 0});
            d[i][1] = mfma16(wq[kk * 3 + i], H[kk][1], kk ? d[i][1] : f32x4{0, 0, 0, 0});
            XOR_IN(he, ho, wq[kk * 3 + i]);
          }
          MIXV6();
        }
        ST(19);
        const bool ok = seal_ok(he, ho, seal, l < 36, tag);
#pragma unroll
        for (int hf = 0; hf < 2; ++hf) {
          st[hf] = f32x4{0, 0, 0, 0};
#pragma unroll
          for (int i = 0; i < 3; ++i) st[hf] = mfma16k16(hs[i][hf], pack4(d[i][hf]), st[hf]);
          lds_w128f(spa + hf * 1024, st[hf]);
        }
        if (__builtin_expect(ok, 1) || sp >= SPIN_CAP) break;
        asm volatile("" ::: "memory");
      }
      asm volatile("" :: "v"(dc));
    }
    ST(10);
    BAR();
    u32x4 seal;
    if (w < 2) {
      f32x4 e = (lds_r128f(sa) + lds_r128f(sa + 2048)) + (lds_r128f(sa + 4096) + lds_r128f(sa + 6144));
      const float mq = xor16_max(fmaxf(fmaxf(e[0], e[1]), fmaxf(e[2], e[3])));
#pragma unroll
      for (int k = 0; k < 4; ++k) e[k] = __builtin_amdgcn_exp2f(e[k] - mq);
      lds_w64(pa, pack4(e * (keep * __builtin_amdgcn_rcpf(xor16_add((e[0] + e[1]) + (e[2] + e[3]))))));
      ST(5);
      BAR();
      seal = ld_seal(0);
      ldw(wq, 0, 0, 3, 0);
      BAR();
      ldw(wq, 0, 3, 6, 0);
    } else {
      u32x2 f0 = __builtin_amdgcn_raw_buffer_load_b64(rsWS, l * 128, WS_FLAG + (w - 2) * 16384, SC1), f1 = __builtin_amdgcn_raw_buffer_load_b64(rsWS, l * 128, WS_FLAG + (w - 2) * 16384 + 8192, SC1);
      seal = ld_seal(0);
      ldw(wq, 0, 0, 4, 0);
      ST(5);
      BAR();
      for (int sp = 0; sp < SPIN_CAP && !__all(f0[0] == tag[0] && f0[1] == tag[1] && f1[0] == tag[0] && f1[1] == tag[1]); ++sp) {
        asm volatile("" ::: "memory");
        f0 = __builtin_amdgcn_raw_buffer_load_b64(rsWS, l * 128, WS_FLAG + (w - 2) * 16384, SC1); f1 = __builtin_amdgcn_raw_buffer_load_b64(rsWS, l * 128, WS_FLAG + (w - 2) * 16384 + 8192, SC1);
      }
      ST(18);
      BAR();
      if (b == 0 && w == 3 && l == 0) __builtin_amdgcn_raw_buffer_store_b64(tag, rsWS, 0, WS_EPOCH, SC1);
      ldw(wq, 0, 4, 6, 0);
    }
    __builtin_amdgcn_s_setprio(0);
    unsigned hqa[2][2], own[2][2];
#pragma unroll
    for (int hf = 0; hf < 2; ++hf) {
      const unsigned in = lds0 + L_HQ + ((4 * hf + tq) ^ hh) * 128 + ((g & 1) ^ hf) * 64 + tp * 16;
      hqa[hf][0] = in + hh * 1024; hqa[hf][1] = in + 2048 + hh * (SLOT - 2048);
      OPAQUE(hqa[hf][0]); OPAQUE(hqa[hf][1]);
    }
    const unsigned tok = 16 * (g & 1) + i16;
    unsigned nw = lds0 + L_N + l * 16 + w * 4;
    OPAQUE(nw);
    unsigned qw = L_Q + (3 * w * 64 + hh * 32 + i16) * 16 + 8 * (g & 1);
    own[0][0] = w * SLOT + L_HQ + (tq ^ (w & 1)) * 128 + hh * 64 + tp * 16 + 8 * (g & 1); own[0][1] = (own[0][0] ^ 64) + 512;
    own[1][0] = own[0][0] ^ 128; own[1][1] = own[0][1] ^ 128;
    OPAQUE(qw); OPAQUE(own[0][0]); OPAQUE(own[0][1]); OPAQUE(own[1][0]); OPAQUE(own[1][1]);
    for (int sp = 0; sp < SPIN_CAP && !sealed(wq, seal, l < 36, tag); ++sp) {
      asm volatile("" ::: "memory");
      seal = ld_seal(0);
      ldw(wq, 0, 0, 6, 0);
    }
    ST(6);
    BAR();
    ST(7);
    u32x4 hq[3];
    {
      const u64 hbu = (u64)(ws + WS_H2 + (size_t)t * FRAME_B);
      const u32x4 rs = {(unsigned)hbu, (unsigned)(hbu >> 32) & 0xffffu, (unsigned)FRAME_B, 0x00020000u};
      for (int sp = 0; sp < SPIN_CAP; ++sp) {
        setm0<L_HQ>(lds0 + w * SLOT);
        bdma<0>(l * 16, rs, qb * TILE_B + w * 3072); bdma<1024>(l * 16, rs, qb * TILE_B + w * 3072); bdma<2048>(l * 16, rs, qb * TILE_B + w * 3072);
        waitvm<0>();
#pragma unroll
        for (int i = 0; i < 3; ++i) { const u32x2 u0 = lds_r64(own[i & 1][0] + i * 1024), u1 = lds_r64(own[i & 1][1] + i * 1024); hq[i] = u32x4{u0[0], u0[1], u1[0], u1[1]}; }
        if (!poisoned(hq)) break;
      }
    }
    ST(8);
    BAR();
    ST(9);
    auto gemm = [&](const u32x4 (&wf)[18], const u32x4 (&bf)[6][2], auto&& fin) {
      f32x4 d[3][2];
#pragma unroll
      for (int i = 0; i < 3; ++i) {
#pragma unroll
        for (int ks = 0; ks < 6; ++ks) {
          d[i][0] = mfma16(wf[ks * 3 + i], bf[ks][0], ks ? d[i][0] : f32x4{0, 0, 0, 0});
          d[i][1] = mfma16(wf[ks * 3 + i], bf[ks][1], ks ? d[i][1] : f32x4{0, 0, 0, 0});
          if (ks == 0) SBAR();
        }
        if (i > 0) fin(i - 1, d[i - 1]);
        SBAR();
      }
      fin(2, d[2]);
    };
    {
      u32x4 bf[6][2];
#pragma unroll
      for (int ks = 0; ks < 6; ++ks)
#pragma unroll
        for (int hf = 0; hf < 2; ++hf) {
          if (ks == 3) { hqa[hf][0] += 2 * SLOT; hqa[hf][1] += 2 * SLOT; }
          bf[ks][hf] = lds_r128(ks % 3 == 1 ? hqa[hf][1] : hqa[hf][0] + (ks % 3 == 2 ? SLOT + 1024 : 0));
        }
      SBAR();
      f32x2 n[2] = {{0.f, 0.f}, {0.f, 0.f}};
      gemm(wq, bf, [&](const int i, const f32x4 (&d)[2]) {
#pragma unroll
        for (int hf = 0; hf < 2; ++hf) {
          lds_w64(qw + i * 1024 + hf * 256, pack4(d[hf]));
          const f32x2 a = {d[hf][0], d[hf][1]}, c = {d[hf][2], d[hf][3]};
          n[hf] = __builtin_elementwise_fma(a, a, n[hf]); n[hf] = __builtin_elementwise_fma(c, c, n[hf]);
        }
      });
      auto rr = __builtin_amdgcn_permlane16_swap(asu(n[0][0] + n[0][1]), asu(n[1][0] + n[1][1]), false, false);
      lds_w32(nw, asf(rr[0]) + asf(rr[1]));
    }
    ST(11);
    BAR();
    unsigned oo = (t * 1024 + qb * 32 + tok) * 384 + (6 * w + hh) * 16;
    OPAQUE(oo);
    float lt[2];
    u32x4 pick;
#pragma unroll
    for (int e = 0; e < 4; ++e) {
      const unsigned d = 8 * (e >> 1) + 4 * (g & 1) + 2 * (e & 1);
      pick[e] = (i16 == d ? 0x3f80u : 0u) | (i16 == d + 1 ? 0x3f800000u : 0u);
    }
    unsigned pr[2], la = lds0 + L_L + i16 * 32;
    pr[0] = lds0 + hh * SLOT + 3 * w * 1024 + ((g & 1) * 32 + i16) * 16; pr[1] = pr[0] + 2 * SLOT;
    OPAQUE(pick); OPAQUE(pr[0]); OPAQUE(pr[1]); OPAQUE(la);
    f32x4 x1[3][2];
    {
      __builtin_amdgcn_s_sleep(40);
      u32x2 st[2];
      for (int sp = 0; sp < SPIN_CAP; ++sp) {
        asm volatile("" ::: "memory");
        st[0] = __builtin_amdgcn_raw_buffer_load_b64(rsWS, i16 * 8, WS_ST + (t * 1024 + qb * 32) * 8, SC1);
        st[1] = __builtin_amdgcn_raw_buffer_load_b64(rsWS, i16 * 8 + 128, WS_ST + (t * 1024 + qb * 32) * 8, SC1);
        if (!__any(max(max(st[0][0], st[0][1]), max(st[1][0], st[1][1])) == POISON)) break;
      }
#pragma unroll
      for (int hf = 0; hf < 2; ++hf) {
        const float mu = asf(st[hf][0]), sd = __builtin_amdgcn_sqrtf(asf(st[hf][1]));
#pragma unroll
        for (int i = 0; i < 3; ++i) {
          const f32x4 h = {bflo(hq[i][2 * hf]), bfhi(hq[i][2 * hf]), bflo(hq[i][2 * hf + 1]), bfhi(hq[i][2 * hf + 1])};
          x1[i][hf] = h * sd + mu;
          OPAQUE(x1[i][hf]);
        }
      }
    }
    BAR();
    ST(16);
    for (int sp = 0; sp < SPIN_CAP; ++sp) {
      asm volatile("" ::: "memory");
      seal = ld_seal(1);
      ldw(wv, 1, 0, 6, 0);
      if (sealed(wv, seal, l < 18, tag)) break;
    }
    ST(12);
#pragma unroll 1
    for (bool exact = false;; exact = true) {
      BAR();
      ST(13);
      u32x4 p[3][2][2];
#pragma unroll
      for (int i = 0; i < 3; ++i)
#pragma unroll
        for (int hf = 0; hf < 2; ++hf) { p[i][hf][0] = lds_r128(pr[0] + i * 1024 + hf * 256); p[i][hf][1] = lds_r128(pr[1] + i * 1024 + hf * 256); }
      const f32x4 l0 = lds_r128f(la), l1 = lds_r128f(la + 16);
      SBAR();
#pragma unroll
      for (int i = 0; i < 3; ++i)
#pragma unroll
        for (int hf = 0; hf < 2; ++hf) lds_w64(dp[hf] + (L_FI - L_X) + i * 512, pack4(mfma16(pick, p[i][hf][1], mfma16(pick, p[i][hf][0], f32x4{0, 0, 0, 0}))));
      if (__builtin_expect(!low_sum(l0, l1, lt) || exact, 1)) break;
      BAR();
      BAR();
    }
    ST(14);
    const float linv[2] = {__builtin_amdgcn_rcpf(lt[0]), __builtin_amdgcn_rcpf(lt[1])};
    BAR();
    ST(15);
    u32x4 bf[6][2];
#pragma unroll
    for (int ks = 0; ks < 6; ++ks) { bf[ks][0] = lds_r128(fr[0] + (L_FI - L_X) + ks * 1024); bf[ks][1] = lds_r128(fr[1] + (L_FI - L_X) + ks * 1024); }
    SBAR();
    f32x4 d[3][2];
#pragma unroll
    for (int ks = 0; ks < 6; ++ks)
#pragma unroll
      for (int i = 0; i < 3; ++i) {
        d[i][0] = mfma16(wv[ks * 3 + i], bf[ks][0], ks ? d[i][0] : f32x4{0, 0, 0, 0});
        d[i][1] = mfma16(wv[ks * 3 + i], bf[ks][1], ks ? d[i][1] : f32x4{0, 0, 0, 0});
      }
#pragma unroll
    for (int i = 0; i < 3; ++i) {
      const u32x2 y0 = pack4(d[i][0] * linv[0] + x1[i][0]), y1 = pack4(d[i][1] * linv[1] + x1[i][1]);
      auto s0 = __builtin_amdgcn_permlane16_swap(y0[0], y1[0], false, false);
      auto s1 = __builtin_amdgcn_permlane16_swap(y0[1], y1[1], false, false);
      const u32x4 o = {s0[0], s1[0], s0[1], s1[1]};
      *(u32x4*)(out + oo + i * 32) = o;
    }
    ST(17);
    ST_OUT(ws, 12, 20);
  }
}

extern "C" void kernel_launch(void* const* d_in, const int* in_sizes, int n_in, void* d_out, int out_size, void* d_ws, size_t ws_size, hipStream_t stream) {
  (void)hipFuncSetAttribute((const void*)k_timesformer, hipFuncAttributeMaxDynamicSharedMemorySize, LDS_BYTES);
  k_timesformer<<<256, 512, LDS_BYTES, stream>>>((const char*)d_in[0], (const char*)d_in[1], (const char*)d_in[2], (const char*)d_in[3], (const char*)d_in[4],
                                                 (char*)d_ws, (char*)d_out);
}
```

```cpp
#include <hip/hip_runtime.h>
#include <hip/hip_bf16.h>
#include <type_traits>

typedef short s16x4 __attribute__((ext_vector_type(4)));
typedef __bf16 b16x8 __attribute__((ext_vector_type(8)));
typedef __bf16 b16x2 __attribute__((ext_vector_type(2)));
typedef float f32x2 __attribute__((ext_vector_type(2)));
typedef float f32x4 __attribute__((ext_vector_type(4)));
typedef float f32x16 __attribute__((ext_vector_type(16)));
typedef unsigned u32x2 __attribute__((ext_vector_type(2)));
typedef unsigned u32x4 __attribute__((ext_vector_type(4)));
typedef unsigned long long u64;
#define LAS __attribute__((address_space(3)))
#define DI __device__ __forceinline__
#define SBAR() __builtin_amdgcn_sched_barrier(0)

constexpr int TILE_B = 12288;
constexpr int FRAME_B = 393216;
constexpr unsigned WS_H2 = 0, WS_ST = 8 * FRAME_B, WS_WM = 16 * FRAME_B, WS_SEAL = WS_WM + 4 * 73728, WS_FLAG = WS_SEAL + 2 * 256 * 128, WS_FLAG2 = WS_FLAG + 8 * 2048, WS_EPOCH = WS_FLAG2 + 2048, WS_END = WS_EPOCH + 256;
constexpr float QSCALE = 0.07216878364870322f * 1.4426950408889634f;
constexpr int SC1 = 16;
constexpr unsigned POISON = 0xffffffffu;
constexpr unsigned CODE_BYTES = 22528;
constexpr int TILE0_DELAY = 2, JOB2_DELAY = 5, READ_DELAY = 5, VO_DELAY = 4, IDLE_DELAY = 4, WQ_SPLIT = 4;
constexpr int SPIN_CAP = 1 << 16;

#define ST_DECL
#define ST(k)
#define ST_OUT(ws, k0, k1)

DI float asf(unsigned u) { return __builtin_bit_cast(float, u); }
DI unsigned asu(float f) { return __builtin_bit_cast(unsigned, f); }
DI unsigned cvtpk(float lo, float hi) { f32x2 v = {lo, hi}; return __builtin_bit_cast(unsigned, __builtin_convertvector(v, b16x2)); }
DI float bflo(unsigned u) { return asf(u << 16); }
DI float bfhi(unsigned u) { return asf(u & 0xffff0000u); }
DI f32x16 mfma32(u32x4 a, u32x4 b, f32x16 c) { return __builtin_amdgcn_mfma_f32_32x32x16_bf16(__builtin_bit_cast(b16x8, a), __builtin_bit_cast(b16x8, b), c, 0, 0, 0); }
DI f32x4 mfma16(u32x4 a, u32x4 b, f32x4 c) { return __builtin_amdgcn_mfma_f32_16x16x32_bf16(__builtin_bit_cast(b16x8, a), __builtin_bit_cast(b16x8, b), c, 0, 0, 0); }
DI f32x4 mfma16k16(u32x2 a, u32x2 b, f32x4 c) { return __builtin_amdgcn_mfma_f32_16x16x16bf16_1k(__builtin_bit_cast(s16x4, a), __builtin_bit_cast(s16x4, b), c, 0, 0, 0); }
DI u32x2 pack4(f32x4 v) { u32x2 r = {cvtpk(v[0], v[1]), cvtpk(v[2], v[3])}; return r; }
DI u32x4 lds_r128(unsigned a) { return *(LAS u32x4*)(size_t)a; }
DI f32x4 lds_r128f(unsigned a) { return *(LAS f32x4*)(size_t)a; }
DI u32x2 lds_r64(unsigned a) { return *(LAS u32x2*)(size_t)a; }
DI void lds_w128(unsigned a, u32x4 v) { *(LAS u32x4*)(size_t)a = v; }
DI void lds_w128f(unsigned a, f32x4 v) { *(LAS f32x4*)(size_t)a = v; }
DI void lds_w64(unsigned a, u32x2 v) { *(LAS u32x2*)(size_t)a = v; }
DI void lds_w32(unsigned a, float v) { *(LAS float*)(size_t)a = v; }
DI u32x2 lds_tr(unsigned a) { return __builtin_bit_cast(u32x2, __builtin_amdgcn_ds_read_tr16_b64_v4i16((LAS s16x4*)(size_t)a)); }
DI u32x4 lds_tr2(unsigned a0, unsigned a1) { u32x2 x = lds_tr(a0), y = lds_tr(a1); u32x4 r = {x[0], x[1], y[0], y[1]}; return r; }
#define BAR() asm volatile("s_waitcnt lgkmcnt(0)\n\ts_barrier" ::: "memory")
#define BARV(n) asm volatile("s_waitcnt vmcnt(" #n ") lgkmcnt(0)\n\ts_barrier" ::: "memory")
template <int CTRL> DI float dpp_add(float v) { return v + asf(__builtin_amdgcn_update_dpp(0u, asu(v), CTRL, 0xf, 0xf, true)); }
DI float xor16_add(float v) { auto r = __builtin_amdgcn_permlane16_swap(asu(v), asu(v), false, false); return asf(r[0]) + asf(r[1]); }
DI float xor16_max(float v) { auto r = __builtin_amdgcn_permlane16_swap(asu(v), asu(v), false, false); return __builtin_amdgcn_fmed3f(asf(r[0]), asf(r[1]), __builtin_inff()); }
DI unsigned umax3(unsigned a, unsigned b, unsigned c) { return max(max(a, b), c); }
template <int N> DI bool poisoned(const u32x4 (&v)[N]) {
  unsigned m = 0;
#pragma unroll
  for (int i = 0; i < N; ++i) { m = umax3(m, v[i][0], v[i][1]); m = umax3(m, v[i][2], v[i][3]); }
  return __any(m == POISON);
}
DI u32x4 unpoison(u32x4 v) { u32x4 r = {min(v[0], POISON - 1), min(v[1], POISON - 1), min(v[2], POISON - 1), min(v[3], POISON - 1)}; return r; }
DI unsigned fi_pos(unsigned r, unsigned c8) {
  return (((r >> 4) * 6 + (c8 >> 2)) * 64 + (c8 & 3) * 16 + ((r & 15) ^ ((c8 & 1) | (((r >> 3) & 1) << 1) | ((r >> 4) << 2)))) * 16;
}
DI float row8_sum(float s) { s = dpp_add<0x124>(s); s = dpp_add<0x128>(s); return xor16_add(s); }
DI float sum24(const f32x2 (&v)[12]) {
  f32x2 s0 = v[0] + v[1], s1 = v[2] + v[3];
#pragma unroll
  for (int i = 4; i < 12; i += 2) { s0 += v[i]; s1 += v[i + 1]; }
  s0 += s1;
  return s0[0] + s0[1];
}
DI float sumsq24(const f32x2 (&v)[12]) {
  f32x2 q0 = v[0] * v[0], q1 = v[1] * v[1];
#pragma unroll
  for (int i = 2; i < 12; i += 2) { q0 = __builtin_elementwise_fma(v[i], v[i], q0); q1 = __builtin_elementwise_fma(v[i + 1], v[i + 1], q1); }
  q0 += q1;
  return q0[0] + q0[1];
}
DI f32x2 unpack2(unsigned u) { f32x2 r = {bflo(u), bfhi(u)}; return r; }

constexpr unsigned SLOT = 36864, L_X = 3 * SLOT, L_H = 4 * SLOT, L_N = L_H + TILE_B, L_L = L_N + 1024, L_PT = L_L + 1024, LDS_BYTES = 163840;
constexpr unsigned L_VI = L_X, L_SP = L_X + TILE_B, L_YI = L_X + 20480;
constexpr unsigned L_HQ = 24576, L_Q = L_H, L_FI = L_H;

#define DMA1(v0, sbase, ldsa) \
  asm volatile("s_mov_b32 m0, %2\n\ts_nop 0\n\tglobal_load_lds_dwordx4 %0, %1" :: "v"(v0), "s"(sbase), "s"(ldsa) : "memory")

template <int OFF> DI void bdma(unsigned voff, u32x4 rs, unsigned soff) { asm volatile("buffer_load_dwordx4 %0, %1, %2 offen offset:%3 sc1 lds" :: "v"(voff), "s"(rs), "s"(soff), "i"(OFF) : "memory"); }
template <int OFF> DI void setm0(unsigned base) { asm volatile("s_add_u32 m0, %0, %1\n\ts_nop 0" :: "s"(base), "i"(OFF) : "memory", "scc"); }
template <int N> DI void waitvm() { asm volatile("s_waitcnt vmcnt(%0)" :: "i"(N) : "memory"); }
#define IC(x) std::integral_constant<int, (x)>{}
#define OPAQUE(v) asm volatile("" : "+v"(v))
template <class R> DI void st128(u32x4 v, R rs, unsigned voff, unsigned soff) { __builtin_amdgcn_raw_buffer_store_b128(v, rs, voff, soff, SC1); asm volatile("s_nop 1" :: "v"(v)); }
template <int CTRL> DI unsigned dpp_xor(unsigned v) { return v ^ __builtin_amdgcn_update_dpp(0u, v, CTRL, 0xf, 0xf, true); }
DI unsigned fold_xor(unsigned e, unsigned o) {
  auto r = __builtin_amdgcn_permlane32_swap(e, o, false, false);
  unsigned u = r[0] ^ r[1];
  auto q = __builtin_amdgcn_permlane16_swap(u, u, false, false);
  u = q[0] ^ q[1];
  u = dpp_xor<0x128>(u); u = dpp_xor<0x124>(u); u = dpp_xor<0x4e>(u); return dpp_xor<0xb1>(u);
}
DI bool seal_ok(unsigned e, unsigned o, u32x4 seal, bool has, u32x2 tag) {
  const unsigned bad = has ? (seal[1] ^ tag[0]) | (seal[3] ^ tag[1]) : 0u;
  return __builtin_amdgcn_ballot_w64((fold_xor(e ^ seal[0], o ^ seal[2]) | bad) != 0) == 0;
}
DI unsigned xor3(unsigned a, unsigned b, unsigned c) { return __builtin_amdgcn_bitop3_b32(a, b, c, 0x96); }
#define XOR_IN(e, o, v) do { e = xor3(e, (v)[0], (v)[2]); o = xor3(o, (v)[1], (v)[3]); } while (0)
#define MIXV6() _Pragma("unroll") for (int q_ = 0; q_ < 6; ++q_) { __builtin_amdgcn_sched_group_barrier(0x8, 1, 0); __builtin_amdgcn_sched_group_barrier(0x2, 1, 0); } SBAR()
template <int N> DI bool sealed(const u32x4 (&v)[N], u32x4 seal, bool has, u32x2 tag) {
  unsigned e = 0, o = 0;
#pragma unroll
  for (int i = 0; i < N; ++i) XOR_IN(e, o, v[i]);
  return seal_ok(e, o, seal, has, tag);
}
DI bool tags_ok(u32x4 f, u32x2 tag) { return f[0] == tag[0] && f[1] == tag[1] && f[2] == tag[0] && f[3] == tag[1]; }

__global__ __launch_bounds__(512, 2) void k_timesformer(const char* __restrict__ x, const char* __restrict__ wqkv_t, const char* __restrict__ wo_t,
                                                        const char* __restrict__ wqkv_s, const char* __restrict__ wo_s, char* __restrict__ ws, char* __restrict__ out) {
  unsigned dc;
  {
    u64 pc;
    asm volatile("s_getpc_b64 %1\n\tglobal_load_dword %0, %2, %1" : "=v"(dc), "=&s"(pc) : "v"(min(threadIdx.x * 128, CODE_BYTES)) : "memory");
    SBAR();
  }
  constexpr unsigned lds0 = 0;
  const unsigned b = blockIdx.x;
  unsigned w8 = __builtin_amdgcn_readfirstlane(threadIdx.x >> 6);
  unsigned l = threadIdx.x & 63;
  const unsigned t = b & 7, qb = b >> 3;
  const auto rsWS = __builtin_amdgcn_make_buffer_rsrc((void*)ws, 0, WS_END, 0x00020000);
  ST_DECL;
  ST(0);
  const bool jqk = b < 144, job = b < 216;
  const unsigned jp = jqk ? b >> 1 : b - 144, jw = jp / 18, jks = (jp - jw * 18) / 3, jtau = jp - jw * 15 - jks * 3;
  const unsigned jrow = jks * 32 + (jqk ? (b & 1) * 16 : 0), jslot = WS_WM + (jqk ? b * 512 : 73728 + jp * 1024);
  auto job_load = [&](const char* wqkv, const char* wo, const unsigned dst) {
    const char* ra = wqkv + jrow * 1152 + (jqk ? 0 : 768);
    const char* rb = jqk ? wqkv + jtau * (16 * 1152) + 384 : ra + 16 * 1152;
#pragma unroll
    for (int i = 0; i < 3; ++i) {
      const unsigned pos = i * 64 + l, row = (pos * 2731) >> 16, o = row * 1152 + (((pos - row * 24) ^ row) * 16);
      DMA1(o, ra, dst + i * 1024); DMA1(o, ra + 8 * 1152, dst + (3 + i) * 1024);
      DMA1(o, rb, dst + (6 + i) * 1024); DMA1(o, rb + 8 * 1152, dst + (9 + i) * 1024);
    }
    if (!jqk) {
#pragma unroll
      for (int i = 0; i < 6; ++i) DMA1((l >> 1) * 384 + (l & 1) * 16, wo + i * 12288 + jtau * 32, dst + (12 + i) * 1024);
    }
  };
  const u32x4 pz = {POISON, POISON, POISON, POISON};
  u64 epoch;
  asm volatile("s_load_dwordx2 %0, %1, 0x0" : "=&s"(epoch) : "s"(ws + WS_EPOCH), "s"(x), "s"(wqkv_t), "s"(wo_t), "s"(wqkv_s), "s"(wo_s), "s"(out) : "memory");
  if (w8 < 4) {
#pragma unroll
    for (int i = 0; i < 3; ++i) { const unsigned p = i * 64 + l; DMA1(p * 16 + (p >= 96 ? FRAME_B - 1536 : 0), x + w8 * 2 * FRAME_B + b * 1536, lds0 + L_YI + w8 * 3072 + i * 1024); }
  } else if (w8 < 6 && job) {
    if (w8 == 5) { __builtin_amdgcn_s_sleep(JOB2_DELAY); if (!jqk) __builtin_amdgcn_s_sleep(2 * JOB2_DELAY); }
    job_load(w8 == 4 ? wqkv_t : wqkv_s, w8 == 4 ? wo_t : wo_s, (w8 - 4) * SLOT);
  }
  asm volatile("s_waitcnt lgkmcnt(0)" : "+s"(epoch) :: "memory");
  epoch += 1;
  const u32x2 tag = {(unsigned)epoch, (unsigned)(epoch >> 32)};
  asm volatile("" : "+s"(w8));
  const unsigned w = w8 & 3;
#define LANE_IDS const unsigned i16 = l & 15, g = l >> 4, r32 = l & 31, hh = l >> 5, tq = i16 >> 2, tp = l & 3
#define FRAG_POS unsigned fr[2], dp[2];                                                                       \
  fr[0] = L_X + fi_pos(i16, g); fr[1] = (fr[0] ^ 64) + 6144; dp[0] = L_X + fi_pos(i16, hh) + 8 * (g & 1) + w * 1536; dp[1] = (dp[0] ^ 64) + 6144; \
  OPAQUE(fr[0]); OPAQUE(fr[1]); OPAQUE(dp[0]); OPAQUE(dp[1])
  auto ldw = [&](u32x4 (&W)[18], const int m, const int k0, const int k1, const unsigned rot) {
#pragma unroll
    for (int kk = k0; kk < k1; ++kk) {
      const unsigned ks = kk + rot >= 6 ? kk + rot - 6 : kk + rot;
#pragma unroll
      for (int i = 0; i < 3; ++i) { SBAR(); W[kk * 3 + i] = __builtin_amdgcn_raw_buffer_load_b128(rsWS, l * 16 + i * 1024, WS_WM + m * 73728 + w * 18432 + ks * 3072, SC1); }
    }
  };
  auto ld_seal = [&](const int m) {
    return __builtin_amdgcn_raw_buffer_load_b128(rsWS, l < ((m & 1) ? 18 : 36) ? l * 128 : WS_END, WS_SEAL + (m >> 1) * 32768 + ((m & 1) ? 144 + 18 * w : 36 * w) * 128, SC1);
  };
  const unsigned rot = qb % 6;
  auto h_frags = [&](u32x4 (&H)[6][2], const unsigned (&fr)[2]) {
#pragma unroll
    for (int kk = 0; kk < 6; ++kk) {
      const unsigned ks = kk + rot >= 6 ? kk + rot - 6 : kk + rot;
      H[kk][0] = lds_r128(fr[0] + ks * 1024 + (L_H - L_X)); H[kk][1] = lds_r128(fr[1] + ks * 1024 + (L_H - L_X));
    }
  };
  auto low_sum = [&](const f32x4 a, const f32x4 c, float (&lt)[2]) {
    lt[0] = (a[0] + a[2]) + (c[0] + c[2]);
    lt[1] = (a[1] + a[3]) + (c[1] + c[3]);
    return (bool)__any(lt[0] < 8.7e-19f || lt[1] < 8.7e-19f);
  };

  if (w8 < 4) {
    const unsigned nl = l & 3, j = (l >> 2) & 7, tt = 2 * w + (l >> 5), r = nl * 8 + tt;
    unsigned rp = L_X + fi_pos(r, j);
    OPAQUE(rp);
    waitvm<0>();
    asm volatile("" :: "v"(dc));
    f32x2 xv[12];
    float mu1;
    {
      u32x4 h[3], xr[3];
#pragma unroll
      for (int i = 0; i < 3; ++i) xr[i] = lds_r128(lds0 + L_YI + tt * 1536 + nl * 384 + j * 16 + i * 128);
#pragma unroll
      for (int i = 0; i < 12; ++i) xv[i] = unpack2(xr[i >> 2][i & 3]);
      mu1 = row8_sum(sum24(xv)) * (1.0f / 192.0f);
#pragma unroll
      for (int i = 0; i < 12; ++i) xv[i] -= mu1;
      const float rs = __builtin_amdgcn_rsqf(row8_sum(sumsq24(xv)) * (1.0f / 192.0f) + 1e-5f);
#pragma unroll
      for (int i = 0; i < 12; ++i) { const f32x2 n = xv[i] * rs; h[i >> 2][i & 3] = cvtpk(n[0], n[1]); }
#pragma unroll
      for (int i = 0; i < 3; ++i) lds_w128(rp + (L_H - L_X) + i * 2048, h[i]);
    }
    SBAR();
    OPAQUE(l);
    unsigned so = tt * FRAME_B + (j >> 1) * 1024 + ((t ^ ((j >> 1) & 1)) * 128) + (((j & 1) ^ (t >> 2)) * 64) + nl * 16;
    const unsigned sso = qb * TILE_B;
    LANE_IDS;
    FRAG_POS;
    unsigned sto = j == 0 ? (tt * 1024 + nl) * 8 : WS_END;
    unsigned sa = L_SP + l * 16;
    float keep = (hh == (i16 >> 3)) ? 1.f : 0.f;
    OPAQUE(sto); OPAQUE(so); OPAQUE(sa); OPAQUE(keep);
    ST(1);
    BAR();
    {
      u32x4 H[6][2], W[18];
      if (w == 0) {
        __builtin_amdgcn_raw_buffer_store_b32(POISON, rsWS, l * 4, WS_ST + (t * 1024 + qb * 32) * 8, SC1);
        if (jqk) {
#pragma unroll
          for (int i = 0; i < 8; ++i) __builtin_amdgcn_raw_buffer_store_b128(pz, rsWS, l * 16 + (i & 3) * 1024, WS_H2 + t * FRAME_B + qb * 8192 + (i >> 2) * 4096, SC1);
          asm volatile("s_nop 1" :: "v"(pz));
        }
      }
      h_frags(H, fr);
      BAR();
      if (w == 0) {
        waitvm<0>();
        if (l < 8) __builtin_amdgcn_raw_buffer_store_b64(tag, rsWS, l * 2048, WS_FLAG + b * 8, SC1);
      }
      ST(6);
      asm volatile("s_barrier" ::: "memory");
      ST(2);
      u32x2 pt[2];
#pragma unroll 1
      for (int sp = 0;; ++sp) {
        const u32x4 seal = ld_seal(3);
        ldw(W, 3, 0, 6, rot);
        SBAR();
        ST(3);
        if (sp == 0) {
          asm volatile("s_barrier" ::: "memory");
          f32x4 e[2];
#pragma unroll
          for (int hf = 0; hf < 2; ++hf) e[hf] = (lds_r128f(sa + hf * 1024) + lds_r128f(sa + hf * 1024 + 2048)) + (lds_r128f(sa + hf * 1024 + 4096) + lds_r128f(sa + hf * 1024 + 6144));
#pragma unroll
          for (int hf = 0; hf < 2; ++hf) {
            const float mq = xor16_max(fmaxf(fmaxf(e[hf][0], e[hf][1]), fmaxf(e[hf][2], e[hf][3])));
#pragma unroll
            for (int k = 0; k < 4; ++k) e[hf][k] = __builtin_amdgcn_exp2f(e[hf][k] - mq);
            pt[hf] = pack4(e[hf] * (keep * __builtin_amdgcn_rcpf(xor16_add((e[hf][0] + e[hf][1]) + (e[hf][2] + e[hf][3])))));
          }
          ST(4);
        }
        f32x4 vp[3][2];
        unsigned he = 0, ho = 0;
#pragma unroll
        for (int kk = 0; kk < 6; ++kk) {
#pragma unroll
          for (int i = 0; i < 3; ++i) {
            vp[i][0] = mfma16(H[kk][0], W[kk * 3 + i], kk ? vp[i][0] : f32x4{0, 0, 0, 0});
            vp[i][1] = mfma16(H[kk][1], W[kk * 3 + i], kk ? vp[i][1] : f32x4{0, 0, 0, 0});
            XOR_IN(he, ho, W[kk * 3 + i]);
          }
          MIXV6();
        }
        OPAQUE(he); OPAQUE(ho);
        ST(7);
        f32x4 y[3][2];
#pragma unroll
        for (int hf = 0; hf < 2; ++hf)
#pragma unroll
          for (int i = 0; i < 3; ++i) y[i][hf] = mfma16k16(pack4(vp[i][hf]), pt[hf], f32x4{0, 0, 0, 0});
        SBAR();
#pragma unroll
        for (int hf = 0; hf < 2; ++hf)
#pragma unroll
          for (int i = 0; i < 3; ++i) lds_w64(dp[hf] + (L_YI - L_X) + i * 512, pack4(y[i][hf]));
        if (__builtin_expect(seal_ok(he, ho, seal, l < 18, tag), 1) || sp >= SPIN_CAP) break;
        asm volatile("" ::: "memory");
      }
    }
    ST(9);
    BAR();
    {
      u32x4 y[3], h[3];
#pragma unroll
      for (int i = 0; i < 3; ++i) y[i] = lds_r128(rp + (L_YI - L_X) + i * 2048);
#pragma unroll
      for (int i = 0; i < 12; ++i) xv[i] += unpack2(y[i >> 2][i & 3]);
      const float mu = row8_sum(sum24(xv)) * (1.0f / 192.0f), sq = row8_sum(sumsq24(xv));
      const float var = fmaxf(fmaf(sq, 1.0f / 192.0f, -mu * mu), 0.f) + 1e-5f;
      const float rs = __builtin_amdgcn_rsqf(var), nb = -mu * rs;
#pragma unroll
      for (int i = 0; i < 3; ++i) {
#pragma unroll
        for (int e = 0; e < 4; ++e) { const f32x2 n = xv[4 * i + e] * rs + nb; h[i][e] = cvtpk(n[0], n[1]); }
        if (i == 0) { OPAQUE(h[0]); ST(5); asm volatile("s_barrier" ::: "memory"); }
        st128(h[i], rsWS, so, WS_H2 + sso + i * 4096);
        SBAR();
      }
      const u32x2 stat = {asu(mu + mu1), asu(var)};
      __builtin_amdgcn_raw_buffer_store_b64(stat, rsWS, sto, WS_ST + b * 32, SC1);
    }
    ST(10);
    const unsigned ring = lds0 + w * SLOT;
    unsigned kr0 = ring + (r32 >> 2) * 128 + (hh ^ (r32 >> 4)) * 64 + (r32 & 3) * 16, kr1 = kr0 ^ 128;
    unsigned vr0 = ring + (g & 1) * 1024 + (hh ^ (g & 1)) * 128 + (tp >> 1) * 64 + tq * 16 + 8 * (tp & 1), vr1 = (vr0 ^ 64) + 512;
    unsigned lo[3] = {l * 16, l * 16 + 4096, l * 16 + 8192}, pw = ring + l * 16, lw = l < 16 ? L_L + (l * 4 + w) * 8 : L_L + 512 + l * 8;
    OPAQUE(kr0); OPAQUE(kr1); OPAQUE(vr0); OPAQUE(vr1); OPAQUE(lo[0]); OPAQUE(lo[1]); OPAQUE(lo[2]); OPAQUE(pw); OPAQUE(lw);
    const u64 hbu = (u64)(ws + WS_H2 + (size_t)t * FRAME_B);
    const u32x4 rs = {(unsigned)hbu, (unsigned)(hbu >> 32) & 0xffffu, (unsigned)FRAME_B, 0x00020000u};
    unsigned toff[8];
#pragma unroll
    for (int i = 0; i < 8; ++i) { toff[i] = (8 * w + ((i + qb) & 7)) * TILE_B; asm volatile("" : "+s"(toff[i])); }
    u32x4 sel;
    { const unsigned one = (tp == (g & 1)) ? 0x3f803f80u : 0u; sel = u32x4{one, one, one, one}; OPAQUE(sel); }
    auto piece = [&](auto Tc, auto Pc) {
      constexpr int T = decltype(Tc)::value, p = decltype(Pc)::value;
      if constexpr (T < 8) {
        if constexpr (p % 4 == 0) setm0<(T % 3) * TILE_B + p * 1024>(ring);
        bdma<(p % 4) * 1024>(lo[p / 4], rs, toff[T]);
      }
    };
#define P6(T, a) piece(IC(T), IC(a)); piece(IC(T), IC(a + 1)); piece(IC(T), IC(a + 2)); piece(IC(T), IC(a + 3)); piece(IC(T), IC(a + 4)); piece(IC(T), IC(a + 5));
    auto read_valid = [&](u32x4 (&KK)[12], const unsigned dst, unsigned off) {
      for (int sp = 0; sp < SPIN_CAP; ++sp) {
#pragma unroll
        for (int k = 0; k < 12; ++k) KK[k] = lds_r128(((k & 1) ? kr1 : kr0) + dst + k * 1024);
        if (__builtin_expect(!poisoned(KK), 1)) break;
#pragma unroll 1
        for (int k = 0; k < 12; ++k) { setm0<0>(ring + dst + k * 1024); bdma<0>(lo[0], rs, off + k * 1024); }
        waitvm<0>();
      }
    };
    unsigned qa = lds0 + L_Q + lo[0], na = lds0 + L_N + r32 * 16;
    OPAQUE(qa); OPAQUE(na);
    BARV(0);
    __builtin_amdgcn_s_sleep(TILE0_DELAY);
    P6(0, 0) P6(0, 6)
    if (w == 0 && l == 0) __builtin_amdgcn_raw_buffer_store_b64(tag, rsWS, 0, WS_FLAG2 + b * 8, SC1);
    ST(11);
    BAR();
    u32x4 K0[12];
    if (w == 0) waitvm<1>(); else waitvm<0>();
    ST(12);
    read_valid(K0, 0, toff[0]);
    ST(13);
    piece(IC(1), IC(0)); piece(IC(1), IC(1)); piece(IC(1), IC(2)); piece(IC(1), IC(3));
    BAR();
    ST(14);
    u32x4 Q[12];
    float mref;
    {
      const f32x4 n0 = lds_r128f(na), n1 = lds_r128f(na + 512);
#pragma unroll
      for (int s = 0; s < 6; ++s) Q[s] = lds_r128(qa + s * 1024);
      SBAR();
      const f32x4 s = n0 + n1;
      mref = __builtin_amdgcn_sqrtf(((s[0] + s[1]) + (s[2] + s[3])) * (192.0f * 1.02f));
      OPAQUE(mref);
      SBAR();
#pragma unroll
      for (int s = 6; s < 12; ++s) Q[s] = lds_r128(qa + s * 1024);
      SBAR();
    }
    constexpr int KW = 4, VW = 4;
    bool safe = false, exact = false, late = false;
#pragma unroll 1
    for (;;) {
      f32x16 S[2], O[6], negm;
      u32x4 K[12], V[12], P[2][2];
      f32x4 lsum = {0, 0, 0, 0};
      float pe[2][2];
#pragma unroll
      for (int i = 0; i < 16; ++i) negm[i] = -mref;
#define RK(k, T) K[k] = lds_r128((((k) & 1) ? kr1 : kr0) + ((T) % 3) * TILE_B + (k) * 1024);
#define RV(u, T) { const unsigned a_ = (((u) & 1) ? vr1 : vr0) + ((T) % 3) * TILE_B + ((u) >> 1) * 2048; V[u] = lds_tr2(a_, a_ + 256); }
      auto sm = [&](auto Tc, auto Hc, auto Gc) {
        constexpr int T = decltype(Tc)::value, H = decltype(Hc)::value, G = decltype(Gc)::value;
        if constexpr (G < 8) pe[(G >> 1) & 1][G & 1] = __builtin_amdgcn_exp2f(S[T & 1][8 * H + G]);
        if constexpr (G >= 2 && G <= 8 && G % 2 == 0) { constexpr int q = G / 2 - 1; P[T & 1][H][q] = cvtpk(pe[q & 1][0], pe[q & 1][1]); }
        if constexpr (G == 10) lsum = mfma16(sel, P[T & 1][H], lsum);
      };
      auto put_o = [&](const int un) {
        u32x4 pk;
#pragma unroll
        for (int e = 0; e < 4; ++e) pk[e] = cvtpk(O[un >> 1][8 * (un & 1) + 2 * e], O[un >> 1][8 * (un & 1) + 2 * e + 1]);
        lds_w128(pw + un * 1024, pk);
      };
      auto qkphase = [&](auto Ic) {
        constexpr int I = decltype(Ic)::value;
        auto step = [&](auto Kc) {
          constexpr int k = decltype(Kc)::value;
          if constexpr (I + 1 < 8) {
            S[(I + 1) & 1] = mfma32(I < 0 ? K0[k] : K[k], Q[k], k == 0 ? negm : S[(I + 1) & 1]);
            if constexpr (k + KW < 12 && I >= 0) RK(k + KW, I + 1)
          }
          if constexpr (I < 0 && k < 8) piece(IC(1), IC(4 + k));
          if constexpr (I >= 0) { if constexpr (k & 1) piece(IC(I + 2), IC(6 + k / 2)); if constexpr (k < 11) sm(IC(I), IC(1), IC(k)); }
          if constexpr (I >= 0 && I < 7 && k >= 12 - VW) RV(k - (12 - VW), I)
          if constexpr (I == 7 && k < VW) RV(k, I)
          SBAR();
        };
        step(IC(0)); step(IC(1)); step(IC(2)); step(IC(3)); step(IC(4)); step(IC(5)); step(IC(6)); step(IC(7)); step(IC(8)); step(IC(9)); step(IC(10)); step(IC(11));
      };
      auto pvphase = [&](auto Ic) {
        constexpr int I = decltype(Ic)::value;
        auto step = [&](auto Uc) {
          constexpr int u = decltype(Uc)::value;
          if constexpr (I >= 0) {
            f32x16 z;
#pragma unroll
            for (int e = 0; e < 16; ++e) z[e] = 0.f;
            O[u >> 1] = mfma32(V[u], P[I & 1][u & 1], (I == 0 && !(u & 1)) ? z : O[u >> 1]);
            if constexpr (u & 1) piece(IC(I + 3), IC(u / 2));
            if constexpr (u + VW < 12) RV(u + VW, I)
          }
          if constexpr (I + 1 < 8 && u >= 1) sm(IC(I + 1), IC(0), IC(u - 1));
          if constexpr (I < 0 && (u & 1)) piece(IC(2), IC(u / 2));
          if constexpr (I + 2 < 8) {
            if constexpr (u == 12 - KW) waitvm<(I < 0 ? 12 : I + 3 < 8 ? 6 + (12 - KW) / 2 : 6)>();
            if constexpr (u >= 12 - KW) RK(u - (12 - KW), I + 2)
            if constexpr (u == 11) waitvm<(I + 3 < 8 ? 6 : 0)>();
          }
          if constexpr (I == 7 && u >= 3) {
            put_o(u - 3);
          }
          SBAR();
        };
        step(IC(0)); step(IC(1)); step(IC(2)); step(IC(3)); step(IC(4)); step(IC(5)); step(IC(6)); step(IC(7)); step(IC(8)); step(IC(9)); step(IC(10)); step(IC(11));
      };
#define STALE(v) (__builtin_expect(!safe && __any((v) != (v)), 0))
      bool done = false;
      do {
        qkphase(IC(-1));
        ST(15);
        pvphase(IC(-1));
        ST(16);
        qkphase(IC(0)); pvphase(IC(0)); qkphase(IC(1)); pvphase(IC(1)); qkphase(IC(2));
        const float seen = lsum[0];
        pvphase(IC(2));
        if (STALE(seen)) break;
        qkphase(IC(3)); pvphase(IC(3)); qkphase(IC(4)); pvphase(IC(4)); qkphase(IC(5));
        if (!late) asm volatile("s_barrier" ::: "memory");
        late = true;
        pvphase(IC(5)); qkphase(IC(6)); pvphase(IC(6)); qkphase(IC(7)); pvphase(IC(7));
        if (STALE(lsum[0])) break;
        { const u32x2 ls = {asu(lsum[0]), asu(lsum[1])}; lds_w64(lw, ls); }
        put_o(9); put_o(10); put_o(11);
        done = true;
      } while (false);
#pragma unroll
      for (int k = 0; k < 12; ++k) asm volatile("" : "=v"(K0[k]));
      if (__builtin_expect(done, 1)) {
        ST(17);
        BAR();
        ST(18);
        float lt[2];
        unsigned ll = lo[0];
        OPAQUE(ll);
        ll >>= 4;
        if (__builtin_expect(!low_sum(lds_r128f(L_L + (ll & 15) * 32), lds_r128f(L_L + (ll & 15) * 32 + 16), lt) || exact, 1)) break;
        BAR();
        float mx = -3.0e38f;
#pragma unroll 1
        for (unsigned i = 0; i < 8; ++i) {
#pragma unroll 1
          for (int k = 0; k < 12; ++k) { setm0<0>(ring + k * 1024); bdma<0>(lo[0], rs, (8 * w + i) * TILE_B + k * 1024); }
          waitvm<0>();
          f32x16 s;
#pragma unroll
          for (int e = 0; e < 16; ++e) s[e] = 0.f;
#pragma unroll
          for (int k = 0; k < 12; ++k) s = mfma32(lds_r128(((k & 1) ? kr1 : kr0) + k * 1024), Q[k], s);
#pragma unroll
          for (int e = 0; e < 16; ++e) mx = fmaxf(mx, s[e]);
        }
        lds_w32(L_N + ((ll & 31) * 8 + w * 2 + (ll >> 5)) * 4, mx);
        BAR();
        const f32x4 c = lds_r128f(L_N + (ll & 31) * 32), d = lds_r128f(L_N + (ll & 31) * 32 + 16);
        mref = fmaxf(fmaxf(fmaxf(c[0], c[1]), fmaxf(c[2], c[3])), fmaxf(fmaxf(d[0], d[1]), fmaxf(d[2], d[3])));
        exact = true;
      } else {
        unsigned ll = lo[0];
        OPAQUE(ll);
        for (int sp = 0; sp < SPIN_CAP; ++sp) {
          asm volatile("" ::: "memory");
          const u32x2 f = __builtin_amdgcn_raw_buffer_load_b64(rsWS, ll >> 1, WS_FLAG2 + w * 512, SC1);
          if (__all(f[0] == tag[0] && f[1] == tag[1])) break;
        }
      }
      safe = true;
      asm volatile("s_waitcnt vmcnt(0) lgkmcnt(0)" ::: "memory");
      P6(0, 0) P6(0, 6)
      waitvm<0>();
      piece(IC(1), IC(0)); piece(IC(1), IC(1)); piece(IC(1), IC(2)); piece(IC(1), IC(3));
#pragma unroll
      for (int k = 0; k < 12; ++k) K0[k] = lds_r128(((k & 1) ? kr1 : kr0) + k * 1024);
    }
    ST(19);
    BAR();
    ST_OUT(ws, 0, 12);
  } else {
    LANE_IDS;
    auto job_run = [&](const unsigned at, const unsigned slot, const unsigned sealat, auto younger) {
      unsigned ja[2] = {at + i16 * 384 + ((g ^ (l & 7)) * 16), at + i16 * 384 + (((4 + g) ^ (l & 7)) * 16)}, jb = at + 12288 + (8 * g + tq) * 32 + tp * 8;
      OPAQUE(ja[0]); OPAQUE(ja[1]); OPAQUE(jb);
      waitvm<decltype(younger)::value>();
      ST(2);
      u32x4 a[6], c[6];
#pragma unroll
      for (int kk = 0; kk < 6; ++kk) { a[kk] = lds_r128(ja[kk & 1] + (kk >> 1) * 128); c[kk] = lds_r128(ja[kk & 1] + (kk >> 1) * 128 + 6144); }
      const unsigned o = (hh * 16 + i16) * 16 + (g & 1) * 8;
      f32x4 d = {0, 0, 0, 0};
      u32x2 r;
      if (jqk) {
#pragma unroll
        for (int kk = 0; kk < 6; ++kk) d = mfma16(a[kk], c[kk], d);
        r = pack4(d * QSCALE);
        __builtin_amdgcn_raw_buffer_store_b64(r, rsWS, o, slot, SC1);
      } else {
        f32x4 e = {0, 0, 0, 0};
#pragma unroll
        for (int kk = 0; kk < 6; ++kk) {
          const u32x4 bb = lds_tr2(jb + kk * 1024, jb + kk * 1024 + 128);
          d = mfma16(a[kk], bb, d); e = mfma16(c[kk], bb, e);
        }
        const u32x2 r0 = pack4(d), r1 = pack4(e);
        __builtin_amdgcn_raw_buffer_store_b64(r0, rsWS, o, slot, SC1);
        __builtin_amdgcn_raw_buffer_store_b64(r1, rsWS, o + 512, slot, SC1);
        r = r0 ^ r1;
      }
      const u32x2 half = {fold_xor(r[0], r[1]), hh ? tag[1] : tag[0]};
      __builtin_amdgcn_raw_buffer_store_b64(half, rsWS, r32 == 0 ? hh * 8 : WS_END, sealat, SC1);
    };
    FRAG_POS;
    if (w == 0 || (w == 1 && jqk)) {
      ST(1);
      if (job) job_run(w * SLOT, jslot + (1 - w) * 2 * 73728, WS_SEAL + (1 - w) * 32768 + b * 128, IC(0));
      else {
        const unsigned p0 = WS_H2 + t * FRAME_B + (144 + (qb - 27) * 48) * 1024;
#pragma unroll 1
        for (unsigned i = 0; i < 24; ++i) {
          __builtin_amdgcn_raw_buffer_store_b128(pz, rsWS, l * 16, p0 + i * 2048, SC1);
          __builtin_amdgcn_raw_buffer_store_b128(pz, rsWS, l * 16 + 1024, p0 + i * 2048, SC1);
        }
        waitvm<0>();
        asm volatile("" :: "v"(pz));
      }
    }
    ST(3);
    BAR();
    u32x4 wq[18], wv[18];
    {
      u32x4 H[6][2];
      u32x2 hs[3][2];
      h_frags(H, fr);
#pragma unroll
      for (int i = 0; i < 3; ++i) { hs[i][0] = lds_r64(dp[0] + (L_H - L_X) + i * 512); hs[i][1] = lds_r64(dp[1] + (L_H - L_X) + i * 512); }
      unsigned spa = L_SP + w * 2048 + l * 16;
      OPAQUE(spa);
      if (w == 0) {
        if (jqk) { waitvm<0>(); __builtin_amdgcn_s_sleep(READ_DELAY); } else if (job) __builtin_amdgcn_s_sleep(VO_DELAY); else __builtin_amdgcn_s_sleep(IDLE_DELAY);
      }
      ST(4);
      BAR();
      __builtin_amdgcn_s_setprio(3);
#pragma unroll 1
      for (int sp = 0;; ++sp) {
        const u32x4 seal = ld_seal(2);
        ldw(wq, 2, 0, 6, rot);
        if (sp == 0) asm volatile("s_barrier" ::: "memory");
        SBAR();
        f32x4 d[3][2], st[2];
        unsigned he = 0, ho = 0;
#pragma unroll
        for (int kk = 0; kk < 6; ++kk) {
#pragma unroll
          for (int i = 0; i < 3; ++i) {
            d[i][0] = mfma16(wq[kk * 3 + i], H[kk][0], kk ? d[i][0] : f32x4{0, 0, 0, 0});
            d[i][1] = mfma16(wq[kk * 3 + i], H[kk][1], kk ? d[i][1] : f32x4{0, 0, 0, 0});
            XOR_IN(he, ho, wq[kk * 3 + i]);
          }
          MIXV6();
        }
        ST(19);
        const bool ok = seal_ok(he, ho, seal, l < 36, tag);
#pragma unroll
        for (int hf = 0; hf < 2; ++hf) {
          st[hf] = f32x4{0, 0, 0, 0};
#pragma unroll
          for (int i = 0; i < 3; ++i) st[hf] = mfma16k16(hs[i][hf], pack4(d[i][hf]), st[hf]);
          lds_w128f(spa + hf * 1024, st[hf]);
        }
        if (__builtin_expect(ok, 1) || sp >= SPIN_CAP) break;
        asm volatile("" ::: "memory");
      }
      asm volatile("" :: "v"(dc));
    }
    ST(10);
    BAR();
    __builtin_amdgcn_s_setprio(0);
    u32x4 seal = ld_seal(0);
    {
      ldw(wq, 0, 0, WQ_SPLIT, 0);
      u32x2 fl = __builtin_amdgcn_raw_buffer_load_b64(rsWS, l * 8, WS_FLAG + t * 2048 + w * 512, SC1);
      ST(5);
      BAR();
      ldw(wq, 0, WQ_SPLIT, 6, 0);
      if (__builtin_expect(!__all(fl[0] == tag[0] && fl[1] == tag[1]), 0)) {
        for (int sp = 0; sp < SPIN_CAP; ++sp) {
          asm volatile("" ::: "memory");
          const u32x2 f2 = __builtin_amdgcn_raw_buffer_load_b64(rsWS, l * 8, WS_FLAG + t * 2048 + w * 512, SC1);
          if (__all(f2[0] == tag[0] && f2[1] == tag[1])) break;
        }
      }
      ST(18);
      BAR();
    }
    unsigned hqa[2][2], own[2][2];
#pragma unroll
    for (int hf = 0; hf < 2; ++hf) {
      const unsigned in = lds0 + L_HQ + ((4 * hf + tq) ^ hh) * 128 + ((g & 1) ^ hf) * 64 + tp * 16;
      hqa[hf][0] = in + hh * 1024; hqa[hf][1] = in + 2048 + hh * (SLOT - 2048);
      OPAQUE(hqa[hf][0]); OPAQUE(hqa[hf][1]);
    }
    const unsigned tok = 16 * (g & 1) + i16;
    unsigned nw = lds0 + L_N + l * 16 + w * 4;
    OPAQUE(nw);
    unsigned qw = L_Q + (3 * w * 64 + hh * 32 + i16) * 16 + 8 * (g & 1);
    own[0][0] = w * SLOT + L_HQ + (tq ^ (w & 1)) * 128 + hh * 64 + tp * 16 + 8 * (g & 1); own[0][1] = (own[0][0] ^ 64) + 512;
    own[1][0] = own[0][0] ^ 128; own[1][1] = own[0][1] ^ 128;
    OPAQUE(qw); OPAQUE(own[0][0]); OPAQUE(own[0][1]); OPAQUE(own[1][0]); OPAQUE(own[1][1]);
    if (b == 0 && w == 3 && l == 0) __builtin_amdgcn_raw_buffer_store_b64(tag, rsWS, 0, WS_EPOCH, SC1);
    for (int sp = 0; sp < SPIN_CAP && !sealed(wq, seal, l < 36, tag); ++sp) {
      asm volatile("" ::: "memory");
      seal = ld_seal(0);
      ldw(wq, 0, 0, 6, 0);
    }
    if (w == 1 && job && !jqk) job_run(SLOT, jslot, WS_SEAL + b * 128, IC(0));
    ST(6);
    BAR();
    ST(7);
    u32x4 hq[3];
    {
      const u64 hbu = (u64)(ws + WS_H2 + (size_t)t * FRAME_B);
      const u32x4 rs = {(unsigned)hbu, (unsigned)(hbu >> 32) & 0xffffu, (unsigned)FRAME_B, 0x00020000u};
      for (int sp = 0; sp < SPIN_CAP; ++sp) {
        setm0<L_HQ>(lds0 + w * SLOT);
        bdma<0>(l * 16, rs, qb * TILE_B + w * 3072); bdma<1024>(l * 16, rs, qb * TILE_B + w * 3072); bdma<2048>(l * 16, rs, qb * TILE_B + w * 3072);
        waitvm<0>();
#pragma unroll
        for (int i = 0; i < 3; ++i) { const u32x2 u0 = lds_r64(own[i & 1][0] + i * 1024), u1 = lds_r64(own[i & 1][1] + i * 1024); hq[i] = u32x4{u0[0], u0[1], u1[0], u1[1]}; }
        if (!poisoned(hq)) break;
      }
    }
    ST(8);
    BAR();
    ST(9);
    auto gemm = [&](const u32x4 (&wf)[18], const u32x4 (&bf)[6][2], auto&& fin) {
      f32x4 d[3][2];
#pragma unroll
      for (int i = 0; i < 3; ++i) {
#pragma unroll
        for (int ks = 0; ks < 6; ++ks) {
          d[i][0] = mfma16(wf[ks * 3 + i], bf[ks][0], ks ? d[i][0] : f32x4{0, 0, 0, 0});
          d[i][1] = mfma16(wf[ks * 3 + i], bf[ks][1], ks ? d[i][1] : f32x4{0, 0, 0, 0});
          if (ks == 0) SBAR();
        }
        if (i > 0) fin(i - 1, d[i - 1]);
        SBAR();
      }
      fin(2, d[2]);
    };
    {
      u32x4 bf[6][2];
#pragma unroll
      for (int ks = 0; ks < 6; ++ks)
#pragma unroll
        for (int hf = 0; hf < 2; ++hf) {
          if (ks == 3) { hqa[hf][0] += 2 * SLOT; hqa[hf][1] += 2 * SLOT; }
          bf[ks][hf] = lds_r128(ks % 3 == 1 ? hqa[hf][1] : hqa[hf][0] + (ks % 3 == 2 ? SLOT + 1024 : 0));
        }
      SBAR();
      f32x2 n[2] = {{0.f, 0.f}, {0.f, 0.f}};
      gemm(wq, bf, [&](const int i, const f32x4 (&d)[2]) {
#pragma unroll
        for (int hf = 0; hf < 2; ++hf) {
          lds_w64(qw + i * 1024 + hf * 256, pack4(d[hf]));
          const f32x2 a = {d[hf][0], d[hf][1]}, c = {d[hf][2], d[hf][3]};
          n[hf] = __builtin_elementwise_fma(a, a, n[hf]); n[hf] = __builtin_elementwise_fma(c, c, n[hf]);
        }
      });
      auto rr = __builtin_amdgcn_permlane16_swap(asu(n[0][0] + n[0][1]), asu(n[1][0] + n[1][1]), false, false);
      lds_w32(nw, asf(rr[0]) + asf(rr[1]));
    }
    ST(11);
    BAR();
    unsigned oo = (t * 1024 + qb * 32 + tok) * 384 + (6 * w + hh) * 16;
    OPAQUE(oo);
    float lt[2];
    u32x4 pick;
#pragma unroll
    for (int e = 0; e < 4; ++e) {
      const unsigned d = 8 * (e >> 1) + 4 * (g & 1) + 2 * (e & 1);
      pick[e] = (i16 == d ? 0x3f80u : 0u) | (i16 == d + 1 ? 0x3f800000u : 0u);
    }
    unsigned pr[2], la = lds0 + L_L + i16 * 32;
    pr[0] = lds0 + hh * SLOT + 3 * w * 1024 + ((g & 1) * 32 + i16) * 16; pr[1] = pr[0] + 2 * SLOT;
    OPAQUE(pick); OPAQUE(pr[0]); OPAQUE(pr[1]); OPAQUE(la);
    f32x4 x1[3][2];
    {
      __builtin_amdgcn_s_sleep(40);
      u32x2 st[2];
      for (int sp = 0; sp < SPIN_CAP; ++sp) {
        asm volatile("" ::: "memory");
        st[0] = __builtin_amdgcn_raw_buffer_load_b64(rsWS, i16 * 8, WS_ST + (t * 1024 + qb * 32) * 8, SC1);
        st[1] = __builtin_amdgcn_raw_buffer_load_b64(rsWS, i16 * 8 + 128, WS_ST + (t * 1024 + qb * 32) * 8, SC1);
        if (!__any(max(max(st[0][0], st[0][1]), max(st[1][0], st[1][1])) == POISON)) break;
      }
#pragma unroll
      for (int hf = 0; hf < 2; ++hf) {
        const float mu = asf(st[hf][0]), sd = __builtin_amdgcn_sqrtf(asf(st[hf][1]));
#pragma unroll
        for (int i = 0; i < 3; ++i) {
          const f32x4 h = {bflo(hq[i][2 * hf]), bfhi(hq[i][2 * hf]), bflo(hq[i][2 * hf + 1]), bfhi(hq[i][2 * hf + 1])};
          x1[i][hf] = h * sd + mu;
          OPAQUE(x1[i][hf]);
        }
      }
    }
    BAR();
    ST(16);
    for (int sp = 0; sp < SPIN_CAP; ++sp) {
      asm volatile("" ::: "memory");
      seal = ld_seal(1);
      ldw(wv, 1, 0, 6, 0);
      if (sealed(wv, seal, l < 18, tag)) break;
    }
    ST(12);
#pragma unroll 1
    for (bool exact = false;; exact = true) {
      BAR();
      ST(13);
      u32x4 p[3][2][2];
#pragma unroll
      for (int i = 0; i < 3; ++i)
#pragma unroll
        for (int hf = 0; hf < 2; ++hf) { p[i][hf][0] = lds_r128(pr[0] + i * 1024 + hf * 256); p[i][hf][1] = lds_r128(pr[1] + i * 1024 + hf * 256); }
      const f32x4 l0 = lds_r128f(la), l1 = lds_r128f(la + 16);
      SBAR();
#pragma unroll
      for (int i = 0; i < 3; ++i)
#pragma unroll
        for (int hf = 0; hf < 2; ++hf) lds_w64(dp[hf] + (L_FI - L_X) + i * 512, pack4(mfma16(pick, p[i][hf][1], mfma16(pick, p[i][hf][0], f32x4{0, 0, 0, 0}))));
      if (__builtin_expect(!low_sum(l0, l1, lt) || exact, 1)) break;
      BAR();
      BAR();
    }
    ST(14);
    const float linv[2] = {__builtin_amdgcn_rcpf(lt[0]), __builtin_amdgcn_rcpf(lt[1])};
    BAR();
    ST(15);
    u32x4 bf[6][2];
#pragma unroll
    for (int ks = 0; ks < 6; ++ks) { bf[ks][0] = lds_r128(fr[0] + (L_FI - L_X) + ks * 1024); bf[ks][1] = lds_r128(fr[1] + (L_FI - L_X) + ks * 1024); }
    SBAR();
    f32x4 d[3][2];
#pragma unroll
    for (int ks = 0; ks < 6; ++ks)
#pragma unroll
      for (int i = 0; i < 3; ++i) {
        d[i][0] = mfma16(wv[ks * 3 + i], bf[ks][0], ks ? d[i][0] : f32x4{0, 0, 0, 0});
        d[i][1] = mfma16(wv[ks * 3 + i], bf[ks][1], ks ? d[i][1] : f32x4{0, 0, 0, 0});
      }
#pragma unroll
    for (int i = 0; i < 3; ++i) {
      const u32x2 y0 = pack4(d[i][0] * linv[0] + x1[i][0]), y1 = pack4(d[i][1] * linv[1] + x1[i][1]);
      auto s0 = __builtin_amdgcn_permlane16_swap(y0[0], y1[0], false, false);
      auto s1 = __builtin_amdgcn_permlane16_swap(y0[1], y1[1], false, false);
      const u32x4 o = {s0[0], s1[0], s0[1], s1[1]};
      *(u32x4*)(out + oo + i * 32) = o;
    }
    ST(17);
    ST_OUT(ws, 12, 20);
  }
}

extern "C" void kernel_launch(void* const* d_in, const int* in_sizes, int n_in, void* d_out, int out_size, void* d_ws, size_t ws_size, hipStream_t stream) {
  (void)hipFuncSetAttribute((const void*)k_timesformer, hipFuncAttributeMaxDynamicSharedMemorySize, LDS_BYTES);
  k_timesformer<<<256, 512, LDS_BYTES, stream>>>((const char*)d_in[0], (const char*)d_in[1], (const char*)d_in[2], (const char*)d_in[3], (const char*)d_in[4],
                                                 (char*)d_ws, (char*)d_out);
}
```

```cpp
#include <hip/hip_runtime.h>
#include <hip/hip_bf16.h>
#include <type_traits>

typedef short s16x4 __attribute__((ext_vector_type(4)));
typedef __bf16 b16x8 __attribute__((ext_vector_type(8)));
typedef __bf16 b16x2 __attribute__((ext_vector_type(2)));
typedef float f32x2 __attribute__((ext_vector_type(2)));
typedef float f32x4 __attribute__((ext_vector_type(4)));
typedef float f32x16 __attribute__((ext_vector_type(16)));
typedef unsigned u32x2 __attribute__((ext_vector_type(2)));
typedef unsigned u32x4 __attribute__((ext_vector_type(4)));
typedef unsigned long long u64;
typedef u64 u64x4 __attribute__((ext_vector_type(4)));
typedef u64 u64x2 __attribute__((ext_vector_type(2)));
#define LAS __attribute__((address_space(3)))
#define DI __device__ __forceinline__
#define SBAR() __builtin_amdgcn_sched_barrier(0)

constexpr int TILE_B = 12288;
constexpr int FRAME_B = 393216;
constexpr unsigned WS_H2 = 0, WS_ST = 8 * FRAME_B, WS_WM = 16 * FRAME_B, WS_SEAL = WS_WM + 4 * 73728, WS_FLAG = WS_SEAL + 2 * 256 * 128, WS_FLAG2 = WS_FLAG + 8 * 2048, WS_EPOCH = WS_FLAG2 + 2048, WS_END = WS_EPOCH + 256;
constexpr float QSCALE = 0.07216878364870322f * 1.4426950408889634f;
constexpr int SC1 = 16;
constexpr unsigned POISON = 0xffffffffu;
constexpr unsigned CODE_BYTES = 22528;
constexpr int TILE0_DELAY = 2, JOB2_DELAY = 6, X_DELAY = 1, READ_DELAY = 6, VO_DELAY = 6, IDLE_DELAY = 6, WQ_SPLIT = 4;
constexpr int SPIN_CAP = 1 << 16;


constexpr int PROBE = 99, ROLE = 1; constexpr size_t WS_T0 = 32u << 20;
#define ST_DECL u64 st_ = 0, t0_ = 0
#define ST(k) do { if ((k) == PROBE || (k) == 0) { SBAR(); u64 t_; asm volatile("s_memrealtime %0\n\ts_waitcnt lgkmcnt(0)" : "=s"(t_) :: "memory"); SBAR(); \
    if ((k) == PROBE) st_ = t_; \
    if ((k) == 0) t0_ = t_; } STO_##k } while (0)
#define STO_(k)
#define STO_0
#define STO_1
#define STO_2
#define STO_3
#define STO_4
#define STO_5
#define STO_6
#define STO_7
#define STO_8
#define STO_9
#define STO_10
#define STO_11
#define STO_12
#define STO_13
#define STO_14
#define STO_15
#define STO_16 if (threadIdx.x == 0) __hip_atomic_store((__attribute__((address_space(1))) u64*)(ws + WS_T0) + blockIdx.x, t0_, __ATOMIC_RELAXED, __HIP_MEMORY_SCOPE_AGENT);
#define STO_17
#define STO_18
#define STO_19
#define ST_OUT(ws, k0, k1) do { if (ROLE == ((k0) != 0)) { u64 m_ = ~0ull; \
    for (int q_ = 0; q_ < 4; ++q_) { u64 v_ = __hip_atomic_load((__attribute__((address_space(1))) u64*)(ws + WS_T0) + (threadIdx.x & 63) * 4 + q_, __ATOMIC_RELAXED, __HIP_MEMORY_SCOPE_AGENT); m_ = v_ < m_ ? v_ : m_; } \
    for (int o_ = 32; o_; o_ >>= 1) { u64 v_ = __shfl_xor(m_, o_); m_ = v_ < m_ ? v_ : m_; } \
    long long d_ = (long long)(st_ - m_); if (d_ < 0) d_ = 0; if (d_ > 5000) d_ = 5000; \
    const u64 e_ = m_ + 4000 + 4 * d_; while (__builtin_amdgcn_s_memrealtime() < e_) __builtin_amdgcn_s_sleep(1); } } while (0)


DI float asf(unsigned u) { return __builtin_bit_cast(float, u); }
DI unsigned asu(float f) { return __builtin_bit_cast(unsigned, f); }
DI unsigned cvtpk(float lo, float hi) { f32x2 v = {lo, hi}; return __builtin_bit_cast(unsigned, __builtin_convertvector(v, b16x2)); }
DI float bflo(unsigned u) { return asf(u << 16); }
DI float bfhi(unsigned u) { return asf(u & 0xffff0000u); }
DI f32x16 mfma32(u32x4 a, u32x4 b, f32x16 c) { return __builtin_amdgcn_mfma_f32_32x32x16_bf16(__builtin_bit_cast(b16x8, a), __builtin_bit_cast(b16x8, b), c, 0, 0, 0); }
DI f32x4 mfma16(u32x4 a, u32x4 b, f32x4 c) { return __builtin_amdgcn_mfma_f32_16x16x32_bf16(__builtin_bit_cast(b16x8, a), __builtin_bit_cast(b16x8, b), c, 0, 0, 0); }
DI f32x4 mfma16k16(u32x2 a, u32x2 b, f32x4 c) { return __builtin_amdgcn_mfma_f32_16x16x16bf16_1k(__builtin_bit_cast(s16x4, a), __builtin_bit_cast(s16x4, b), c, 0, 0, 0); }
DI u32x2 pack4(f32x4 v) { u32x2 r = {cvtpk(v[0], v[1]), cvtpk(v[2], v[3])}; return r; }
DI u32x4 lds_r128(unsigned a) { return *(LAS u32x4*)(size_t)a; }
DI f32x4 lds_r128f(unsigned a) { return *(LAS f32x4*)(size_t)a; }
DI u32x2 lds_r64(unsigned a) { return *(LAS u32x2*)(size_t)a; }
DI void lds_w128(unsigned a, u32x4 v) { *(LAS u32x4*)(size_t)a = v; }
DI void lds_w128f(unsigned a, f32x4 v) { *(LAS f32x4*)(size_t)a = v; }
DI void lds_w64(unsigned a, u32x2 v) { *(LAS u32x2*)(size_t)a = v; }
DI void lds_w32(unsigned a, float v) { *(LAS float*)(size_t)a = v; }
DI u32x2 lds_tr(unsigned a) { return __builtin_bit_cast(u32x2, __builtin_amdgcn_ds_read_tr16_b64_v4i16((LAS s16x4*)(size_t)a)); }
DI u32x4 lds_tr2(unsigned a0, unsigned a1) { u32x2 x = lds_tr(a0), y = lds_tr(a1); u32x4 r = {x[0], x[1], y[0], y[1]}; return r; }
#define BAR() asm volatile("s_waitcnt lgkmcnt(0)\n\ts_barrier" ::: "memory")
#define BARV(n) asm volatile("s_waitcnt vmcnt(" #n ") lgkmcnt(0)\n\ts_barrier" ::: "memory")
template <int CTRL> DI float dpp_add(float v) { return v + asf(__builtin_amdgcn_update_dpp(0u, asu(v), CTRL, 0xf, 0xf, true)); }
DI float xor16_add(float v) { auto r = __builtin_amdgcn_permlane16_swap(asu(v), asu(v), false, false); return asf(r[0]) + asf(r[1]); }
DI float xor16_max(float v) { auto r = __builtin_amdgcn_permlane16_swap(asu(v), asu(v), false, false); return __builtin_amdgcn_fmed3f(asf(r[0]), asf(r[1]), __builtin_inff()); }
DI unsigned umax3(unsigned a, unsigned b, unsigned c) { return max(max(a, b), c); }
template <int N> DI bool poisoned(const u32x4 (&v)[N]) {
  unsigned m = 0;
#pragma unroll
  for (int i = 0; i < N; ++i) { m = umax3(m, v[i][0], v[i][1]); m = umax3(m, v[i][2], v[i][3]); }
  return __any(m == POISON);
}
DI u32x4 unpoison(u32x4 v) { u32x4 r = {min(v[0], POISON - 1), min(v[1], POISON - 1), min(v[2], POISON - 1), min(v[3], POISON - 1)}; return r; }
DI unsigned fi_pos(unsigned r, unsigned c8) {
  return (((r >> 4) * 6 + (c8 >> 2)) * 64 + (c8 & 3) * 16 + ((r & 15) ^ ((c8 & 1) | (((r >> 3) & 1) << 1) | ((r >> 4) << 2)))) * 16;
}
DI float row8_sum(float s) { s = dpp_add<0x124>(s); s = dpp_add<0x128>(s); return xor16_add(s); }
DI float sum24(const f32x2 (&v)[12]) {
  f32x2 s0 = v[0] + v[1], s1 = v[2] + v[3];
#pragma unroll
  for (int i = 4; i < 12; i += 2) { s0 += v[i]; s1 += v[i + 1]; }
  s0 += s1;
  return s0[0] + s0[1];
}
DI float sumsq24(const f32x2 (&v)[12]) {
  f32x2 q0 = v[0] * v[0], q1 = v[1] * v[1];
#pragma unroll
  for (int i = 2; i < 12; i += 2) { q0 = __builtin_elementwise_fma(v[i], v[i], q0); q1 = __builtin_elementwise_fma(v[i + 1], v[i + 1], q1); }
  q0 += q1;
  return q0[0] + q0[1];
}
DI f32x2 unpack2(unsigned u) { f32x2 r = {bflo(u), bfhi(u)}; return r; }

constexpr unsigned SLOT = 36864, L_X = 3 * SLOT, L_H = 4 * SLOT, L_N = L_H + TILE_B, L_L = L_N + 1024, L_PT = L_L + 1024, LDS_BYTES = 163840;
constexpr unsigned L_VI = L_X, L_SP = L_X + TILE_B, L_YI = L_X + 20480;
constexpr unsigned L_HQ = 24576, L_Q = L_H, L_FI = L_H;

#define DMA3(v0, v1, v2, sbase, ldsa) \
  asm volatile("s_mov_b32 m0, %4\n\ts_nop 0\n\tglobal_load_lds_dwordx4 %0, %3\n\tglobal_load_lds_dwordx4 %1, %3 offset:1024\n\tglobal_load_lds_dwordx4 %2, %3 offset:2048" \
               :: "v"(v0), "v"(v1), "v"(v2), "s"(sbase), "s"(ldsa) : "memory")
#define DMA1(v0, sbase, ldsa) \
  asm volatile("s_mov_b32 m0, %2\n\ts_nop 0\n\tglobal_load_lds_dwordx4 %0, %1" :: "v"(v0), "s"(sbase), "s"(ldsa) : "memory")

template <int OFF> DI void bdma(unsigned voff, u32x4 rs, unsigned soff) { asm volatile("buffer_load_dwordx4 %0, %1, %2 offen offset:%3 sc1 lds" :: "v"(voff), "s"(rs), "s"(soff), "i"(OFF) : "memory"); }
template <int OFF> DI void setm0(unsigned base) { asm volatile("s_add_u32 m0, %0, %1\n\ts_nop 0" :: "s"(base), "i"(OFF) : "memory", "scc"); }
template <int N> DI void waitvm() { asm volatile("s_waitcnt vmcnt(%0)" :: "i"(N) : "memory"); }
#define IC(x) std::integral_constant<int, (x)>{}
#define OPAQUE(v) asm volatile("" : "+v"(v))
template <class R> DI void st128(u32x4 v, R rs, unsigned voff, unsigned soff) { __builtin_amdgcn_raw_buffer_store_b128(v, rs, voff, soff, SC1); asm volatile("s_nop 1" :: "v"(v)); }
template <int CTRL> DI unsigned dpp_xor(unsigned v) { return v ^ __builtin_amdgcn_update_dpp(0u, v, CTRL, 0xf, 0xf, true); }
DI unsigned fold_xor(unsigned e, unsigned o) {
  auto r = __builtin_amdgcn_permlane32_swap(e, o, false, false);
  unsigned u = r[0] ^ r[1];
  auto q = __builtin_amdgcn_permlane16_swap(u, u, false, false);
  u = q[0] ^ q[1];
  u = dpp_xor<0x128>(u); u = dpp_xor<0x124>(u); u = dpp_xor<0x4e>(u); return dpp_xor<0xb1>(u);
}
DI bool seal_ok(unsigned e, unsigned o, u32x4 seal, bool has, u32x2 tag) {
  const unsigned bad = has ? (seal[1] ^ tag[0]) | (seal[3] ^ tag[1]) : 0u;
  return __builtin_amdgcn_ballot_w64((fold_xor(e ^ seal[0], o ^ seal[2]) | bad) != 0) == 0;
}
DI unsigned xor3(unsigned a, unsigned b, unsigned c) { return __builtin_amdgcn_bitop3_b32(a, b, c, 0x96); }
#define XOR_IN(e, o, v) do { e = xor3(e, (v)[0], (v)[2]); o = xor3(o, (v)[1], (v)[3]); } while (0)
#define MIXV6() _Pragma("unroll") for (int q_ = 0; q_ < 6; ++q_) { __builtin_amdgcn_sched_group_barrier(0x8, 1, 0); __builtin_amdgcn_sched_group_barrier(0x2, 1, 0); } SBAR()
template <int N> DI bool sealed(const u32x4 (&v)[N], u32x4 seal, bool has, u32x2 tag) {
  unsigned e = 0, o = 0;
#pragma unroll
  for (int i = 0; i < N; ++i) XOR_IN(e, o, v[i]);
  return seal_ok(e, o, seal, has, tag);
}
DI bool tags_ok(u32x4 f, u32x2 tag) { return f[0] == tag[0] && f[1] == tag[1] && f[2] == tag[0] && f[3] == tag[1]; }

__global__ __launch_bounds__(512, 2) void k_timesformer(const char* x_, const char* wqkv_t_, const char* wo_t_, const char* wqkv_s_, const char* wo_s_, char* ws_, char* out_) {
  unsigned dc;
  u64x4 ka; u64x2 kb; u64 kc;
  {
    u64 pc;
    asm volatile("s_load_dwordx8 %2, %6, 0x0\n\ts_load_dwordx4 %3, %6, 0x20\n\ts_load_dwordx2 %4, %6, 0x30\n\ts_getpc_b64 %1\n\tglobal_load_dword %0, %5, %1"
                 : "=v"(dc), "=&s"(pc), "=&s"(ka), "=&s"(kb), "=&s"(kc) : "v"(min(threadIdx.x * 128, CODE_BYTES)), "s"(__builtin_amdgcn_kernarg_segment_ptr()) : "memory");
  }
  constexpr unsigned lds0 = 0;
  const unsigned b = blockIdx.x;
  unsigned w8 = __builtin_amdgcn_readfirstlane(threadIdx.x >> 6);
  unsigned l = threadIdx.x & 63;
  const unsigned t = b & 7, qb = b >> 3;
  w8 = (w8 & 4) | ((w8 + qb / 6) & 3);
  ST_DECL;
  ST(0);
  const bool jqk = b < 144, job = b < 216;
  const unsigned jp = jqk ? b >> 1 : b - 144, jw = jp / 18, jks = (jp - jw * 18) / 3, jtau = jp - jw * 15 - jks * 3;
  const unsigned jrow = jks * 32 + (jqk ? (b & 1) * 16 : 0), jslot = WS_WM + (jqk ? b * 512 : 73728 + jp * 1024);
  unsigned fo[4][3];
  if (w8 < 4) {
#pragma unroll
    for (int i = 0; i < 3; ++i) { const unsigned p = i * 64 + l; fo[0][i] = p * 16 + (p >= 96 ? FRAME_B - 1536 : 0) + w8 * 2 * FRAME_B + b * 1536; fo[1][i] = fo[2][i] = fo[3][i] = 0; }
  } else {
    const unsigned ra = jrow * 1152 + (jqk ? 0 : 768), rb = jqk ? jtau * (16 * 1152) + 384 : ra + 16 * 1152;
#pragma unroll
    for (int i = 0; i < 3; ++i) {
      const unsigned pos = i * 64 + l, row = (pos * 2731) >> 16, o = row * 1152 + (((pos - row * 24) ^ row) * 16) - i * 1024;
      fo[0][i] = o + ra; fo[1][i] = o + (ra + 8 * 1152); fo[2][i] = o + rb; fo[3][i] = o + (rb + 8 * 1152);
    }
  }
  unsigned so_ = (l >> 1) * 384 + (l & 1) * 16 + jtau * 32;
  asm volatile("s_waitcnt lgkmcnt(0)" : "+s"(ka), "+s"(kb), "+s"(kc), "+v"(fo[0][0]), "+v"(fo[0][1]), "+v"(fo[0][2]), "+v"(fo[1][0]), "+v"(fo[1][1]), "+v"(fo[1][2]),
               "+v"(fo[2][0]), "+v"(fo[2][1]), "+v"(fo[2][2]), "+v"(fo[3][0]), "+v"(fo[3][1]), "+v"(fo[3][2]), "+v"(so_) :: "memory");
  char* const ws = (char*)kb[1];
  if (w8 < 4) {
    __builtin_amdgcn_s_sleep(X_DELAY);
#pragma unroll
    for (int i = 0; i < 3; ++i) DMA1(fo[0][i], ka[0], lds0 + L_YI + w8 * 3072 + i * 1024);
  } else if (w8 < 6 && job) {
    if (w8 == 5) { __builtin_amdgcn_s_sleep(JOB2_DELAY); if (!jqk) __builtin_amdgcn_s_sleep(2 * JOB2_DELAY); }
    const u64 wqkv = w8 == 4 ? ka[1] : ka[3], wo = w8 == 4 ? ka[2] : kb[0];
    const unsigned dst = (w8 - 4) * SLOT;
#pragma unroll
    for (int q = 0; q < 4; ++q) DMA3(fo[q][0], fo[q][1], fo[q][2], wqkv, dst + q * 3072);
    if (!jqk) {
#pragma unroll
      for (int i = 0; i < 6; ++i) DMA1(so_, wo + i * 12288, dst + (12 + i) * 1024);
    }
  }
  const auto rsWS = __builtin_amdgcn_make_buffer_rsrc((void*)ws, 0, WS_END, 0x00020000);
  const u32x4 pz = {POISON, POISON, POISON, POISON};
  u64 epoch;
  asm volatile("s_load_dwordx2 %0, %1, 0x0" : "=&s"(epoch) : "s"(ws + WS_EPOCH) : "memory");
  asm volatile("s_waitcnt lgkmcnt(0)" : "+s"(epoch) :: "memory");
  epoch += 1;
  const u32x2 tag = {(unsigned)epoch, (unsigned)(epoch >> 32)};
  asm volatile("" : "+s"(w8));
  const unsigned w = w8 & 3;
#define LANE_IDS const unsigned i16 = l & 15, g = l >> 4, r32 = l & 31, hh = l >> 5, tq = i16 >> 2, tp = l & 3
#define FRAG_POS unsigned fr[2], dp[2];                                                                       \
  fr[0] = L_X + fi_pos(i16, g); fr[1] = (fr[0] ^ 64) + 6144; dp[0] = L_X + fi_pos(i16, hh) + 8 * (g & 1) + w * 1536; dp[1] = (dp[0] ^ 64) + 6144; \
  OPAQUE(fr[0]); OPAQUE(fr[1]); OPAQUE(dp[0]); OPAQUE(dp[1])
  auto ldw = [&](u32x4 (&W)[18], const int m, const int k0, const int k1, const unsigned rot) {
#pragma unroll
    for (int kk = k0; kk < k1; ++kk) {
      const unsigned ks = kk + rot >= 6 ? kk + rot - 6 : kk + rot;
#pragma unroll
      for (int i = 0; i < 3; ++i) { SBAR(); W[kk * 3 + i] = __builtin_amdgcn_raw_buffer_load_b128(rsWS, l * 16 + i * 1024, WS_WM + m * 73728 + w * 18432 + ks * 3072, SC1); }
    }
  };
  auto ld_seal = [&](const int m) {
    return __builtin_amdgcn_raw_buffer_load_b128(rsWS, l < ((m & 1) ? 18 : 36) ? l * 128 : WS_END, WS_SEAL + (m >> 1) * 32768 + ((m & 1) ? 144 + 18 * w : 36 * w) * 128, SC1);
  };
  const unsigned rot = qb % 6;
  auto h_frags = [&](u32x4 (&H)[6][2], const unsigned (&fr)[2]) {
#pragma unroll
    for (int kk = 0; kk < 6; ++kk) {
      const unsigned ks = kk + rot >= 6 ? kk + rot - 6 : kk + rot;
      H[kk][0] = lds_r128(fr[0] + ks * 1024 + (L_H - L_X)); H[kk][1] = lds_r128(fr[1] + ks * 1024 + (L_H - L_X));
    }
  };
  auto low_sum = [&](const f32x4 a, const f32x4 c, float (&lt)[2]) {
    lt[0] = (a[0] + a[2]) + (c[0] + c[2]);
    lt[1] = (a[1] + a[3]) + (c[1] + c[3]);
    return (bool)__any(lt[0] < 8.7e-19f || lt[1] < 8.7e-19f);
  };

  if (w8 < 4) {
    const unsigned nl = l & 3, j = (l >> 2) & 7, tt = 2 * w + (l >> 5), r = nl * 8 + tt;
    unsigned rp = L_X + fi_pos(r, j);
    OPAQUE(rp);
    waitvm<0>();
    asm volatile("" :: "v"(dc));
    f32x2 xv[12];
    float mu1;
    {
      u32x4 h[3], xr[3];
#pragma unroll
      for (int i = 0; i < 3; ++i) xr[i] = lds_r128(lds0 + L_YI + tt * 1536 + nl * 384 + j * 16 + i * 128);
#pragma unroll
      for (int i = 0; i < 12; ++i) xv[i] = unpack2(xr[i >> 2][i & 3]);
      mu1 = row8_sum(sum24(xv)) * (1.0f / 192.0f);
#pragma unroll
      for (int i = 0; i < 12; ++i) xv[i] -= mu1;
      const float rs = __builtin_amdgcn_rsqf(row8_sum(sumsq24(xv)) * (1.0f / 192.0f) + 1e-5f);
#pragma unroll
      for (int i = 0; i < 12; ++i) { const f32x2 n = xv[i] * rs; h[i >> 2][i & 3] = cvtpk(n[0], n[1]); }
#pragma unroll
      for (int i = 0; i < 3; ++i) lds_w128(rp + (L_H - L_X) + i * 2048, h[i]);
    }
    SBAR();
    OPAQUE(l);
    unsigned so = tt * FRAME_B + (j >> 1) * 1024 + ((t ^ ((j >> 1) & 1)) * 128) + (((j & 1) ^ (t >> 2)) * 64) + nl * 16;
    const unsigned sso = qb * TILE_B;
    LANE_IDS;
    FRAG_POS;
    unsigned sto = j == 0 ? (tt * 1024 + nl) * 8 : WS_END;
    unsigned sa = L_SP + l * 16;
    unsigned keep = (hh == (i16 >> 3)) ? ~0u : 0u;
    OPAQUE(sto); OPAQUE(so); OPAQUE(sa); OPAQUE(keep);
    ST(1);
    BAR();
    ST(6);
    {
      u32x4 H[6][2], W[18];
      h_frags(H, fr);
      asm volatile("s_barrier" ::: "memory");
      ST(2);
#pragma unroll 1
      for (int sp = 0;; ++sp) {
        const u32x4 seal = ld_seal(3);
        ldw(W, 3, 0, 6, rot);
        SBAR();
        ST(3);
        if (sp == 0) asm volatile("s_barrier" ::: "memory");
        f32x4 vp[3][2], sq[4], e;
        float rd;
        u32x2 pt[2];
        unsigned he = 0, ho = 0;
#pragma unroll
        for (int ww = 0; ww < 4; ++ww) sq[ww] = lds_r128f(sa + ww * 1024);
        SBAR();
        ST(4);
#pragma unroll
        for (int kk = 0; kk < 6; ++kk) {
#pragma unroll
          for (int i = 0; i < 3; ++i) {
            vp[i][0] = mfma16(H[kk][0], W[kk * 3 + i], kk ? vp[i][0] : f32x4{0, 0, 0, 0});
            vp[i][1] = mfma16(H[kk][1], W[kk * 3 + i], kk ? vp[i][1] : f32x4{0, 0, 0, 0});
            XOR_IN(he, ho, W[kk * 3 + i]);
          }
          if (kk == 1) { e = (sq[0] + sq[1]) + (sq[2] + sq[3]); rd = fmaxf(fmaxf(e[0], e[1]), fmaxf(e[2], e[3])); OPAQUE(rd); }
          if (kk == 2) { e -= xor16_max(rd); OPAQUE(e); }
          if (kk == 3) {
#pragma unroll
            for (int k = 0; k < 4; ++k) e[k] = __builtin_amdgcn_exp2f(e[k]);
            rd = (e[0] + e[1]) + (e[2] + e[3]);
            OPAQUE(rd);
          }
          if (kk == 4) { pt[0] = pack4(e * __builtin_amdgcn_rcpf(xor16_add(rd))); OPAQUE(pt[0]); }
          if (kk == 5) {
#pragma unroll
            for (int k = 0; k < 2; ++k) {
              auto q = __builtin_amdgcn_permlane32_swap(pt[0][k], pt[0][k], false, false);
              pt[1][k] = (q[0] ^ q[1] ^ pt[0][k]) & keep;
              pt[0][k] &= keep;
            }
            OPAQUE(pt[0]); OPAQUE(pt[1]);
          }
#pragma unroll
          for (int q = 0; q < 6; ++q) { __builtin_amdgcn_sched_group_barrier(0x8, 1, 0); __builtin_amdgcn_sched_group_barrier(0x402, 3, 0); }
          SBAR();
        }
        OPAQUE(he); OPAQUE(ho);
        ST(7);
        f32x4 y[3][2];
#pragma unroll
        for (int hf = 0; hf < 2; ++hf)
#pragma unroll
          for (int i = 0; i < 3; ++i) y[i][hf] = mfma16k16(pack4(vp[i][hf]), pt[hf], f32x4{0, 0, 0, 0});
        SBAR();
#pragma unroll
        for (int hf = 0; hf < 2; ++hf)
#pragma unroll
          for (int i = 0; i < 3; ++i) lds_w64(dp[hf] + (L_YI - L_X) + i * 512, pack4(y[i][hf]));
        if (__builtin_expect(seal_ok(he, ho, seal, l < 18, tag), 1) || sp >= SPIN_CAP) break;
        asm volatile("" ::: "memory");
      }
    }
    ST(9);
    BAR();
    {
      u32x4 y[3], h[3];
#pragma unroll
      for (int i = 0; i < 3; ++i) y[i] = lds_r128(rp + (L_YI - L_X) + i * 2048);
#pragma unroll
      for (int i = 0; i < 12; ++i) xv[i] += unpack2(y[i >> 2][i & 3]);
      const float mu = row8_sum(sum24(xv)) * (1.0f / 192.0f), sq = row8_sum(sumsq24(xv));
      const float var = fmaxf(fmaf(sq, 1.0f / 192.0f, -mu * mu), 0.f) + 1e-5f;
      const float rs = __builtin_amdgcn_rsqf(var), nb = -mu * rs;
#pragma unroll
      for (int i = 0; i < 3; ++i) {
#pragma unroll
        for (int e = 0; e < 4; ++e) { const f32x2 n = xv[4 * i + e] * rs + nb; h[i][e] = cvtpk(n[0], n[1]); }
        if (i == 0) { OPAQUE(h[0]); ST(5); asm volatile("s_barrier" ::: "memory"); }
        st128(h[i], rsWS, so, WS_H2 + sso + i * 4096);
        SBAR();
      }
      const u32x2 stat = {asu(mu + mu1), asu(var)};
      __builtin_amdgcn_raw_buffer_store_b64(stat, rsWS, sto, WS_ST + b * 32, SC1);
    }
    ST(10);
    const unsigned ring = lds0 + w * SLOT;
    unsigned kr0 = ring + (r32 >> 2) * 128 + (hh ^ (r32 >> 4)) * 64 + (r32 & 3) * 16, kr1 = kr0 ^ 128;
    unsigned vr0 = ring + (g & 1) * 1024 + (hh ^ (g & 1)) * 128 + (tp >> 1) * 64 + tq * 16 + 8 * (tp & 1), vr1 = (vr0 ^ 64) + 512;
    unsigned lo[3] = {l * 16, l * 16 + 4096, l * 16 + 8192}, pw = ring + l * 16, lw = l < 16 ? L_L + (l * 4 + w) * 8 : L_L + 512 + l * 8;
    OPAQUE(kr0); OPAQUE(kr1); OPAQUE(vr0); OPAQUE(vr1); OPAQUE(lo[0]); OPAQUE(lo[1]); OPAQUE(lo[2]); OPAQUE(pw); OPAQUE(lw);
    const u64 hbu = (u64)(ws + WS_H2 + (size_t)t * FRAME_B);
    const u32x4 rs = {(unsigned)hbu, (unsigned)(hbu >> 32) & 0xffffu, (unsigned)FRAME_B, 0x00020000u};
    unsigned toff[8];
#pragma unroll
    for (int i = 0; i < 8; ++i) { toff[i] = (8 * w + ((i + qb) & 7)) * TILE_B; asm volatile("" : "+s"(toff[i])); }
    u32x4 sel;
    { const unsigned one = (tp == (g & 1)) ? 0x3f803f80u : 0u; sel = u32x4{one, one, one, one}; OPAQUE(sel); }
    auto piece = [&](auto Tc, auto Pc) {
      constexpr int T = decltype(Tc)::value, p = decltype(Pc)::value;
      if constexpr (T < 8) {
        if constexpr (p % 4 == 0) setm0<(T % 3) * TILE_B + p * 1024>(ring);
        bdma<(p % 4) * 1024>(lo[p / 4], rs, toff[T]);
      }
    };
#define P6(T, a) piece(IC(T), IC(a)); piece(IC(T), IC(a + 1)); piece(IC(T), IC(a + 2)); piece(IC(T), IC(a + 3)); piece(IC(T), IC(a + 4)); piece(IC(T), IC(a + 5));
    auto read_valid = [&](u32x4 (&KK)[12], const unsigned dst, unsigned off) {
      for (int sp = 0; sp < SPIN_CAP; ++sp) {
#pragma unroll
        for (int k = 0; k < 12; ++k) KK[k] = lds_r128(((k & 1) ? kr1 : kr0) + dst + k * 1024);
        if (__builtin_expect(!poisoned(KK), 1)) break;
#pragma unroll 1
        for (int k = 0; k < 12; ++k) { setm0<0>(ring + dst + k * 1024); bdma<0>(lo[0], rs, off + k * 1024); }
        waitvm<0>();
      }
    };
    unsigned qa = lds0 + L_Q + lo[0], na = lds0 + L_N + r32 * 16;
    OPAQUE(qa); OPAQUE(na);
    BARV(0);
    __builtin_amdgcn_s_sleep(TILE0_DELAY);
    P6(0, 0) P6(0, 6)
    if (w == 0 && l == 0) __builtin_amdgcn_raw_buffer_store_b64(tag, rsWS, 0, WS_FLAG2 + b * 8, SC1);
    ST(11);
    BAR();
    u32x4 K0[12];
    if (w == 0) waitvm<1>(); else waitvm<0>();
    ST(12);
    read_valid(K0, 0, toff[0]);
    ST(13);
    piece(IC(1), IC(0)); piece(IC(1), IC(1)); piece(IC(1), IC(2)); piece(IC(1), IC(3));
    BAR();
    ST(14);
    u32x4 Q[12];
    float mref;
    {
      const f32x4 n0 = lds_r128f(na), n1 = lds_r128f(na + 512);
#pragma unroll
      for (int s = 0; s < 6; ++s) Q[s] = lds_r128(qa + s * 1024);
      SBAR();
      const f32x4 s = n0 + n1;
      mref = __builtin_amdgcn_sqrtf(((s[0] + s[1]) + (s[2] + s[3])) * (192.0f * 1.02f));
      OPAQUE(mref);
      SBAR();
#pragma unroll
      for (int s = 6; s < 12; ++s) Q[s] = lds_r128(qa + s * 1024);
      SBAR();
    }
    constexpr int KW = 4, VW = 4;
    bool safe = false, exact = false, late = false;
#pragma unroll 1
    for (;;) {
      f32x16 S[2], O[6], negm;
      u32x4 K[12], V[12], P[2][2];
      f32x4 lsum = {0, 0, 0, 0};
      float pe[2][2];
#pragma unroll
      for (int i = 0; i < 16; ++i) negm[i] = -mref;
#define RK(k, T) K[k] = lds_r128((((k) & 1) ? kr1 : kr0) + ((T) % 3) * TILE_B + (k) * 1024);
#define RV(u, T) { const unsigned a_ = (((u) & 1) ? vr1 : vr0) + ((T) % 3) * TILE_B + ((u) >> 1) * 2048; V[u] = lds_tr2(a_, a_ + 256); }
      auto sm = [&](auto Tc, auto Hc, auto Gc) {
        constexpr int T = decltype(Tc)::value, H = decltype(Hc)::value, G = decltype(Gc)::value;
        if constexpr (G < 8) pe[(G >> 1) & 1][G & 1] = __builtin_amdgcn_exp2f(S[T & 1][8 * H + G]);
        if constexpr (G >= 2 && G <= 8 && G % 2 == 0) { constexpr int q = G / 2 - 1; P[T & 1][H][q] = cvtpk(pe[q & 1][0], pe[q & 1][1]); }
        if constexpr (G == 10) lsum = mfma16(sel, P[T & 1][H], lsum);
      };
      auto put_o = [&](const int un) {
        u32x4 pk;
#pragma unroll
        for (int e = 0; e < 4; ++e) pk[e] = cvtpk(O[un >> 1][8 * (un & 1) + 2 * e], O[un >> 1][8 * (un & 1) + 2 * e + 1]);
        lds_w128(pw + un * 1024, pk);
      };
      auto qkphase = [&](auto Ic) {
        constexpr int I = decltype(Ic)::value;
        auto step = [&](auto Kc) {
          constexpr int k = decltype(Kc)::value;
          if constexpr (I + 1 < 8) {
            S[(I + 1) & 1] = mfma32(I < 0 ? K0[k] : K[k], Q[k], k == 0 ? negm : S[(I + 1) & 1]);
            if constexpr (k + KW < 12 && I >= 0) RK(k + KW, I + 1)
          }
          if constexpr (I < 0 && k < 8) piece(IC(1), IC(4 + k));
          if constexpr (I >= 0) { if constexpr (k & 1) piece(IC(I + 2), IC(6 + k / 2)); if constexpr (k < 11) sm(IC(I), IC(1), IC(k)); }
          if constexpr (I >= 0 && I < 7 && k >= 12 - VW) RV(k - (12 - VW), I)
          if constexpr (I == 7 && k < VW) RV(k, I)
          SBAR();
        };
        step(IC(0)); step(IC(1)); step(IC(2)); step(IC(3)); step(IC(4)); step(IC(5)); step(IC(6)); step(IC(7)); step(IC(8)); step(IC(9)); step(IC(10)); step(IC(11));
      };
      auto pvphase = [&](auto Ic) {
        constexpr int I = decltype(Ic)::value;
        auto step = [&](auto Uc) {
          constexpr int u = decltype(Uc)::value;
          if constexpr (I >= 0) {
            f32x16 z;
#pragma unroll
            for (int e = 0; e < 16; ++e) z[e] = 0.f;
            O[u >> 1] = mfma32(V[u], P[I & 1][u & 1], (I == 0 && !(u & 1)) ? z : O[u >> 1]);
            if constexpr (u & 1) piece(IC(I + 3), IC(u / 2));
            if constexpr (u + VW < 12) RV(u + VW, I)
          }
          if constexpr (I + 1 < 8 && u >= 1) sm(IC(I + 1), IC(0), IC(u - 1));
          if constexpr (I < 0 && (u & 1)) piece(IC(2), IC(u / 2));
          if constexpr (I + 2 < 8) {
            if constexpr (u == 12 - KW) waitvm<(I < 0 ? 12 : I + 3 < 8 ? 6 + (12 - KW) / 2 : 6)>();
            if constexpr (u >= 12 - KW) RK(u - (12 - KW), I + 2)
            if constexpr (u == 11) waitvm<(I + 3 < 8 ? 6 : 0)>();
          }
          if constexpr (I == 7 && u >= 3) {
            put_o(u - 3);
          }
          SBAR();
        };
        step(IC(0)); step(IC(1)); step(IC(2)); step(IC(3)); step(IC(4)); step(IC(5)); step(IC(6)); step(IC(7)); step(IC(8)); step(IC(9)); step(IC(10)); step(IC(11));
      };
#define STALE(v) (__builtin_expect(!safe && __any((v) != (v)), 0))
      bool done = false;
      do {
        qkphase(IC(-1));
        ST(15);
        pvphase(IC(-1));
        ST(16);
        qkphase(IC(0)); pvphase(IC(0)); qkphase(IC(1)); pvphase(IC(1)); qkphase(IC(2));
        const float seen = lsum[0];
        pvphase(IC(2));
        if (STALE(seen)) break;
        qkphase(IC(3)); pvphase(IC(3)); qkphase(IC(4)); pvphase(IC(4)); qkphase(IC(5));
        if (!late) asm volatile("s_barrier" ::: "memory");
        late = true;
        pvphase(IC(5)); qkphase(IC(6)); pvphase(IC(6)); qkphase(IC(7)); pvphase(IC(7));
        if (STALE(lsum[0])) break;
        { const u32x2 ls = {asu(lsum[0]), asu(lsum[1])}; lds_w64(lw, ls); }
        put_o(9); put_o(10); put_o(11);
        done = true;
      } while (false);
#pragma unroll
      for (int k = 0; k < 12; ++k) asm volatile("" : "=v"(K0[k]));
      if (__builtin_expect(done, 1)) {
        ST(17);
        BAR();
        ST(18);
        float lt[2];
        unsigned ll = lo[0];
        OPAQUE(ll);
        ll >>= 4;
        if (__builtin_expect(!low_sum(lds_r128f(L_L + (ll & 15) * 32), lds_r128f(L_L + (ll & 15) * 32 + 16), lt) || exact, 1)) break;
        BAR();
        float mx = -3.0e38f;
#pragma unroll 1
        for (unsigned i = 0; i < 8; ++i) {
#pragma unroll 1
          for (int k = 0; k < 12; ++k) { setm0<0>(ring + k * 1024); bdma<0>(lo[0], rs, (8 * w + i) * TILE_B + k * 1024); }
          waitvm<0>();
          f32x16 s;
#pragma unroll
          for (int e = 0; e < 16; ++e) s[e] = 0.f;
#pragma unroll
          for (int k = 0; k < 12; ++k) s = mfma32(lds_r128(((k & 1) ? kr1 : kr0) + k * 1024), Q[k], s);
#pragma unroll
          for (int e = 0; e < 16; ++e) mx = fmaxf(mx, s[e]);
        }
        lds_w32(L_N + ((ll & 31) * 8 + w * 2 + (ll >> 5)) * 4, mx);
        BAR();
        const f32x4 c = lds_r128f(L_N + (ll & 31) * 32), d = lds_r128f(L_N + (ll & 31) * 32 + 16);
        mref = fmaxf(fmaxf(fmaxf(c[0], c[1]), fmaxf(c[2], c[3])), fmaxf(fmaxf(d[0], d[1]), fmaxf(d[2], d[3])));
        exact = true;
      } else {
        unsigned ll = lo[0];
        OPAQUE(ll);
        for (int sp = 0; sp < SPIN_CAP; ++sp) {
          asm volatile("" ::: "memory");
          const u32x2 f = __builtin_amdgcn_raw_buffer_load_b64(rsWS, ll >> 1, WS_FLAG2 + w * 512, SC1);
          if (__all(f[0] == tag[0] && f[1] == tag[1])) break;
        }
      }
      safe = true;
      asm volatile("s_waitcnt vmcnt(0) lgkmcnt(0)" ::: "memory");
      P6(0, 0) P6(0, 6)
      waitvm<0>();
      piece(IC(1), IC(0)); piece(IC(1), IC(1)); piece(IC(1), IC(2)); piece(IC(1), IC(3));
#pragma unroll
      for (int k = 0; k < 12; ++k) K0[k] = lds_r128(((k & 1) ? kr1 : kr0) + k * 1024);
    }
    ST(19);
    BAR();
    ST_OUT(ws, 0, 12);
  } else {
    LANE_IDS;
    auto job_run = [&](const unsigned at, const unsigned slot, const unsigned sealat, auto younger) {
      unsigned ja[2] = {at + i16 * 384 + ((g ^ (l & 7)) * 16), at + i16 * 384 + (((4 + g) ^ (l & 7)) * 16)}, jb = at + 12288 + (8 * g + tq) * 32 + tp * 8;
      OPAQUE(ja[0]); OPAQUE(ja[1]); OPAQUE(jb);
      waitvm<decltype(younger)::value>();
      ST(2);
      u32x4 a[6], c[6];
#pragma unroll
      for (int kk = 0; kk < 6; ++kk) { a[kk] = lds_r128(ja[kk & 1] + (kk >> 1) * 128); c[kk] = lds_r128(ja[kk & 1] + (kk >> 1) * 128 + 6144); }
      const unsigned o = (hh * 16 + i16) * 16 + (g & 1) * 8;
      f32x4 d = {0, 0, 0, 0};
      u32x2 r;
      if (jqk) {
#pragma unroll
        for (int kk = 0; kk < 6; ++kk) d = mfma16(a[kk], c[kk], d);
        r = pack4(d * QSCALE);
        __builtin_amdgcn_raw_buffer_store_b64(r, rsWS, o, slot, SC1);
      } else {
        f32x4 e = {0, 0, 0, 0};
#pragma unroll
        for (int kk = 0; kk < 6; ++kk) {
          const u32x4 bb = lds_tr2(jb + kk * 1024, jb + kk * 1024 + 128);
          d = mfma16(a[kk], bb, d); e = mfma16(c[kk], bb, e);
        }
        const u32x2 r0 = pack4(d), r1 = pack4(e);
        __builtin_amdgcn_raw_buffer_store_b64(r0, rsWS, o, slot, SC1);
        __builtin_amdgcn_raw_buffer_store_b64(r1, rsWS, o + 512, slot, SC1);
        r = r0 ^ r1;
      }
      const u32x2 half = {fold_xor(r[0], r[1]), hh ? tag[1] : tag[0]};
      __builtin_amdgcn_raw_buffer_store_b64(half, rsWS, r32 == 0 ? hh * 8 : WS_END, sealat, SC1);
    };
    FRAG_POS;
    if (w == 0 || (w == 1 && jqk)) {
      ST(1);
      if (job) job_run(w * SLOT, jslot + (1 - w) * 2 * 73728, WS_SEAL + (1 - w) * 32768 + b * 128, IC(0));
      ST(3);
      if (w == 0) {
        __builtin_amdgcn_raw_buffer_store_b32(POISON, rsWS, l * 4, WS_ST + (t * 1024 + qb * 32) * 8, SC1);
        const unsigned p0 = WS_H2 + t * FRAME_B + (jqk ? qb * 8 : 144 + (qb - 27) * 48) * 1024, np = jqk ? 2 : job ? 0 : 12;
#pragma unroll 1
        for (unsigned i = 0; i < np; ++i) {
#pragma unroll
          for (int q = 0; q < 4; ++q) __builtin_amdgcn_raw_buffer_store_b128(pz, rsWS, l * 16 + q * 1024, p0 + i * 4096, SC1);
        }
        asm volatile("s_nop 1" :: "v"(pz));
        if (jqk) { waitvm<9>(); __builtin_amdgcn_s_sleep(READ_DELAY); } else if (job) __builtin_amdgcn_s_sleep(VO_DELAY); else { waitvm<0>(); __builtin_amdgcn_s_sleep(IDLE_DELAY); }
      }
    }
    u32x4 wq[18], wv[18];
    {
      u32x4 H[6][2];
      u32x2 hs[3][2];
      unsigned spa = hh == (i16 >> 3) ? L_SP + w * 1024 + l * 16 : L_SP + 4096 + l * 16;
      OPAQUE(spa);
      __builtin_amdgcn_s_setprio(3);
      ST(4);
      BAR();
#pragma unroll 1
      for (int sp = 0;; ++sp) {
        const u32x4 seal = ld_seal(2);
        ldw(wq, 2, 0, 3, rot);
        SBAR();
        h_frags(H, fr);
#pragma unroll
        for (int i = 0; i < 3; ++i) { hs[i][0] = lds_r64(dp[0] + (L_H - L_X) + i * 512); hs[i][1] = lds_r64(dp[1] + (L_H - L_X) + i * 512); }
        ldw(wq, 2, 3, 6, rot);
        if (sp == 0) {
          if (w == 0) {
            waitvm<19>();
            if (l < 8) __builtin_amdgcn_raw_buffer_store_b64(tag, rsWS, l * 2048, WS_FLAG + b * 8, SC1);
          }
          asm volatile("s_barrier" ::: "memory");
        }
        SBAR();
        f32x4 d[3][2], st[2];
        unsigned he = 0, ho = 0;
#pragma unroll
        for (int kk = 0; kk < 6; ++kk) {
#pragma unroll
          for (int i = 0; i < 3; ++i) {
            d[i][0] = mfma16(wq[kk * 3 + i], H[kk][0], kk ? d[i][0] : f32x4{0, 0, 0, 0});
            d[i][1] = mfma16(wq[kk * 3 + i], H[kk][1], kk ? d[i][1] : f32x4{0, 0, 0, 0});
            XOR_IN(he, ho, wq[kk * 3 + i]);
          }
          MIXV6();
        }
        ST(19);
        const bool ok = seal_ok(he, ho, seal, l < 36, tag);
#pragma unroll
        for (int hf = 0; hf < 2; ++hf) {
          st[hf] = f32x4{0, 0, 0, 0};
#pragma unroll
          for (int i = 0; i < 3; ++i) st[hf] = mfma16k16(hs[i][hf], pack4(d[i][hf]), st[hf]);
          lds_w128f(spa ^ (hf * 512), st[hf]);
        }
        if (__builtin_expect(ok, 1) || sp >= SPIN_CAP) break;
        asm volatile("" ::: "memory");
      }
      asm volatile("" :: "v"(dc));
    }
    ST(10);
    BAR();
    __builtin_amdgcn_s_setprio(0);
    u32x4 seal = ld_seal(0);
    {
      ldw(wq, 0, 0, WQ_SPLIT, 0);
      u32x2 fl = __builtin_amdgcn_raw_buffer_load_b64(rsWS, l * 8, WS_FLAG + t * 2048 + w * 512, SC1);
      ST(5);
      BAR();
      ldw(wq, 0, WQ_SPLIT, 6, 0);
      if (__builtin_expect(!__all(fl[0] == tag[0] && fl[1] == tag[1]), 0)) {
        for (int sp = 0; sp < SPIN_CAP; ++sp) {
          asm volatile("" ::: "memory");
          const u32x2 f2 = __builtin_amdgcn_raw_buffer_load_b64(rsWS, l * 8, WS_FLAG + t * 2048 + w * 512, SC1);
          if (__all(f2[0] == tag[0] && f2[1] == tag[1])) break;
        }
      }
      ST(18);
      BAR();
    }
    unsigned hqa[2][2], own[2][2];
#pragma unroll
    for (int hf = 0; hf < 2; ++hf) {
      const unsigned in = lds0 + L_HQ + ((4 * hf + tq) ^ hh) * 128 + ((g & 1) ^ hf) * 64 + tp * 16;
      hqa[hf][0] = in + hh * 1024; hqa[hf][1] = in + 2048 + hh * (SLOT - 2048);
      OPAQUE(hqa[hf][0]); OPAQUE(hqa[hf][1]);
    }
    const unsigned tok = 16 * (g & 1) + i16;
    unsigned nw = lds0 + L_N + l * 16 + w * 4;
    OPAQUE(nw);
    unsigned qw = L_Q + (3 * w * 64 + hh * 32 + i16) * 16 + 8 * (g & 1);
    own[0][0] = w * SLOT + L_HQ + (tq ^ (w & 1)) * 128 + hh * 64 + tp * 16 + 8 * (g & 1); own[0][1] = (own[0][0] ^ 64) + 512;
    own[1][0] = own[0][0] ^ 128; own[1][1] = own[0][1] ^ 128;
    OPAQUE(qw); OPAQUE(own[0][0]); OPAQUE(own[0][1]); OPAQUE(own[1][0]); OPAQUE(own[1][1]);
    if (b == 0 && w == 3 && l == 0) __builtin_amdgcn_raw_buffer_store_b64(tag, rsWS, 0, WS_EPOCH, SC1);
    for (int sp = 0; sp < SPIN_CAP && !sealed(wq, seal, l < 36, tag); ++sp) {
      asm volatile("" ::: "memory");
      seal = ld_seal(0);
      ldw(wq, 0, 0, 6, 0);
    }
    if (w == 1 && job && !jqk) job_run(SLOT, jslot, WS_SEAL + b * 128, IC(0));
    ST(6);
    BAR();
    ST(7);
    u32x4 hq[3];
    {
      const u64 hbu = (u64)(ws + WS_H2 + (size_t)t * FRAME_B);
      const u32x4 rs = {(unsigned)hbu, (unsigned)(hbu >> 32) & 0xffffu, (unsigned)FRAME_B, 0x00020000u};
      for (int sp = 0; sp < SPIN_CAP; ++sp) {
        setm0<L_HQ>(lds0 + w * SLOT);
        bdma<0>(l * 16, rs, qb * TILE_B + w * 3072); bdma<1024>(l * 16, rs, qb * TILE_B + w * 3072); bdma<2048>(l * 16, rs, qb * TILE_B + w * 3072);
        waitvm<0>();
#pragma unroll
        for (int i = 0; i < 3; ++i) { const u32x2 u0 = lds_r64(own[i & 1][0] + i * 1024), u1 = lds_r64(own[i & 1][1] + i * 1024); hq[i] = u32x4{u0[0], u0[1], u1[0], u1[1]}; }
        if (!poisoned(hq)) break;
      }
    }
    ST(8);
    BAR();
    ST(9);
    auto gemm = [&](const u32x4 (&wf)[18], const u32x4 (&bf)[6][2], auto&& fin) {
      f32x4 d[3][2];
#pragma unroll
      for (int i = 0; i < 3; ++i) {
#pragma unroll
        for (int ks = 0; ks < 6; ++ks) {
          d[i][0] = mfma16(wf[ks * 3 + i], bf[ks][0], ks ? d[i][0] : f32x4{0, 0, 0, 0});
          d[i][1] = mfma16(wf[ks * 3 + i], bf[ks][1], ks ? d[i][1] : f32x4{0, 0, 0, 0});
          if (ks == 0) SBAR();
        }
        if (i > 0) fin(i - 1, d[i - 1]);
        SBAR();
      }
      fin(2, d[2]);
    };
    {
      u32x4 bf[6][2];
#pragma unroll
      for (int ks = 0; ks < 6; ++ks)
#pragma unroll
        for (int hf = 0; hf < 2; ++hf) {
          if (ks == 3) { hqa[hf][0] += 2 * SLOT; hqa[hf][1] += 2 * SLOT; }
          bf[ks][hf] = lds_r128(ks % 3 == 1 ? hqa[hf][1] : hqa[hf][0] + (ks % 3 == 2 ? SLOT + 1024 : 0));
        }
      SBAR();
      f32x2 n[2] = {{0.f, 0.f}, {0.f, 0.f}};
      gemm(wq, bf, [&](const int i, const f32x4 (&d)[2]) {
#pragma unroll
        for (int hf = 0; hf < 2; ++hf) {
          lds_w64(qw + i * 1024 + hf * 256, pack4(d[hf]));
          const f32x2 a = {d[hf][0], d[hf][1]}, c = {d[hf][2], d[hf][3]};
          n[hf] = __builtin_elementwise_fma(a, a, n[hf]); n[hf] = __builtin_elementwise_fma(c, c, n[hf]);
        }
      });
      auto rr = __builtin_amdgcn_permlane16_swap(asu(n[0][0] + n[0][1]), asu(n[1][0] + n[1][1]), false, false);
      lds_w32(nw, asf(rr[0]) + asf(rr[1]));
    }
    ST(11);
    BAR();
    unsigned oo = (t * 1024 + qb * 32 + tok) * 384 + (6 * w + hh) * 16;
    OPAQUE(oo);
    float lt[2];
    u32x4 pick;
#pragma unroll
    for (int e = 0; e < 4; ++e) {
      const unsigned d = 8 * (e >> 1) + 4 * (g & 1) + 2 * (e & 1);
      pick[e] = (i16 == d ? 0x3f80u : 0u) | (i16 == d + 1 ? 0x3f800000u : 0u);
    }
    unsigned pr[2], la = lds0 + L_L + i16 * 32;
    pr[0] = lds0 + hh * SLOT + 3 * w * 1024 + ((g & 1) * 32 + i16) * 16; pr[1] = pr[0] + 2 * SLOT;
    OPAQUE(pick); OPAQUE(pr[0]); OPAQUE(pr[1]); OPAQUE(la);
    f32x4 x1[3][2];
    {
      __builtin_amdgcn_s_sleep(40);
      u32x2 st[2];
      for (int sp = 0; sp < SPIN_CAP; ++sp) {
        asm volatile("" ::: "memory");
        st[0] = __builtin_amdgcn_raw_buffer_load_b64(rsWS, i16 * 8, WS_ST + (t * 1024 + qb * 32) * 8, SC1);
        st[1] = __builtin_amdgcn_raw_buffer_load_b64(rsWS, i16 * 8 + 128, WS_ST + (t * 1024 + qb * 32) * 8, SC1);
        if (!__any(max(max(st[0][0], st[0][1]), max(st[1][0], st[1][1])) == POISON)) break;
      }
#pragma unroll
      for (int hf = 0; hf < 2; ++hf) {
        const float mu = asf(st[hf][0]), sd = __builtin_amdgcn_sqrtf(asf(st[hf][1]));
#pragma unroll
        for (int i = 0; i < 3; ++i) {
          const f32x4 h = {bflo(hq[i][2 * hf]), bfhi(hq[i][2 * hf]), bflo(hq[i][2 * hf + 1]), bfhi(hq[i][2 * hf + 1])};
          x1[i][hf] = h * sd + mu;
          OPAQUE(x1[i][hf]);
        }
      }
    }
    BAR();
    ST(16);
    for (int sp = 0; sp < SPIN_CAP; ++sp) {
      asm volatile("" ::: "memory");
      seal = ld_seal(1);
      ldw(wv, 1, 0, 6, 0);
      if (sealed(wv, seal, l < 18, tag)) break;
    }
    ST(12);
#pragma unroll 1
    for (bool exact = false;; exact = true) {
      BAR();
      ST(13);
      u32x4 p[3][2][2];
#pragma unroll
      for (int i = 0; i < 3; ++i)
#pragma unroll
        for (int hf = 0; hf < 2; ++hf) { p[i][hf][0] = lds_r128(pr[0] + i * 1024 + hf * 256); p[i][hf][1] = lds_r128(pr[1] + i * 1024 + hf * 256); }
      const f32x4 l0 = lds_r128f(la), l1 = lds_r128f(la + 16);
      SBAR();
#pragma unroll
      for (int i = 0; i < 3; ++i)
#pragma unroll
        for (int hf = 0; hf < 2; ++hf) lds_w64(dp[hf] + (L_FI - L_X) + i * 512, pack4(mfma16(pick, p[i][hf][1], mfma16(pick, p[i][hf][0], f32x4{0, 0, 0, 0}))));
      if (__builtin_expect(!low_sum(l0, l1, lt) || exact, 1)) break;
      BAR();
      BAR();
    }
    ST(14);
    const float linv[2] = {__builtin_amdgcn_rcpf(lt[0]), __builtin_amdgcn_rcpf(lt[1])};
    BAR();
    ST(15);
    u32x4 bf[6][2];
#pragma unroll
    for (int ks = 0; ks < 6; ++ks) { bf[ks][0] = lds_r128(fr[0] + (L_FI - L_X) + ks * 1024); bf[ks][1] = lds_r128(fr[1] + (L_FI - L_X) + ks * 1024); }
    SBAR();
    f32x4 d[3][2];
#pragma unroll
    for (int ks = 0; ks < 6; ++ks)
#pragma unroll
      for (int i = 0; i < 3; ++i) {
        d[i][0] = mfma16(wv[ks * 3 + i], bf[ks][0], ks ? d[i][0] : f32x4{0, 0, 0, 0});
        d[i][1] = mfma16(wv[ks * 3 + i], bf[ks][1], ks ? d[i][1] : f32x4{0, 0, 0, 0});
      }
#pragma unroll
    for (int i = 0; i < 3; ++i) {
      const u32x2 y0 = pack4(d[i][0] * linv[0] + x1[i][0]), y1 = pack4(d[i][1] * linv[1] + x1[i][1]);
      auto s0 = __builtin_amdgcn_permlane16_swap(y0[0], y1[0], false, false);
      auto s1 = __builtin_amdgcn_permlane16_swap(y0[1], y1[1], false, false);
      const u32x4 o = {s0[0], s1[0], s0[1], s1[1]};
      *(__attribute__((address_space(1))) u32x4*)(kc + oo + i * 32) = o;
    }
    ST(17);
    ST_OUT(ws, 12, 20);
  }
}

extern "C" void kernel_launch(void* const* d_in, const int* in_sizes, int n_in, void* d_out, int out_size, void* d_ws, size_t ws_size, hipStream_t stream) {
  (void)hipFuncSetAttribute((const void*)k_timesformer, hipFuncAttributeMaxDynamicSharedMemorySize, LDS_BYTES);
  k_timesformer<<<256, 512, LDS_BYTES, stream>>>((const char*)d_in[0], (const char*)d_in[1], (const char*)d_in[2], (const char*)d_in[3], (const char*)d_in[4],
                                                 (char*)d_ws, (char*)d_out);
}
```

```cpp
#include <hip/hip_runtime.h>
#include <hip/hip_bf16.h>
#include <type_traits>

typedef short s16x4 __attribute__((ext_vector_type(4)));
typedef __bf16 b16x8 __attribute__((ext_vector_type(8)));
typedef __bf16 b16x2 __attribute__((ext_vector_type(2)));
typedef float f32x2 __attribute__((ext_vector_type(2)));
typedef float f32x4 __attribute__((ext_vector_type(4)));
typedef float f32x16 __attribute__((ext_vector_type(16)));
typedef unsigned u32x2 __attribute__((ext_vector_type(2)));
typedef unsigned u32x4 __attribute__((ext_vector_type(4)));
typedef unsigned long long u64;
typedef u64 u64x4 __attribute__((ext_vector_type(4)));
typedef u64 u64x2 __attribute__((ext_vector_type(2)));
#define LAS __attribute__((address_space(3)))
#define DI __device__ __forceinline__
#define SBAR() __builtin_amdgcn_sched_barrier(0)

constexpr int TILE_B = 12288;
constexpr int FRAME_B = 393216;
constexpr unsigned WS_H2 = 0, WS_ST = 8 * FRAME_B, WS_WM = 16 * FRAME_B, WS_SEAL = WS_WM + 4 * 73728, WS_FLAG = WS_SEAL + 2 * 256 * 128, WS_FLAG2 = WS_FLAG + 8 * 2048, WS_EPOCH = WS_FLAG2 + 2048, WS_END = WS_EPOCH + 256;
constexpr float QSCALE = 0.07216878364870322f * 1.4426950408889634f;
constexpr int SC1 = 16;
constexpr unsigned POISON = 0xffffffffu;
constexpr unsigned CODE_BYTES = 22528;
constexpr int TILE0_DELAY = 2, JOB2_DELAY = 6, X_DELAY = 1, READ_DELAY = 6, VO_DELAY = 6, IDLE_DELAY = 6, WQ_SPLIT = 4;
constexpr int SPIN_CAP = 1 << 16;


constexpr int PA = 2, PB = 3, ROLE = 1; constexpr size_t WS_T0 = 32u << 20;
#define ST_DECL u64 sa_ = 0, sb_ = 0, t0_ = 0
#define ST(k) do { if ((k) == 0) { SBAR(); u64 t_; asm volatile("s_memrealtime %0\n\ts_waitcnt lgkmcnt(0)" : "=s"(t_) :: "memory"); SBAR(); \
    t0_ = t_; } STO_##k \
    if ((k) == PA || (k) == PB) { SBAR(); u64 t_; asm volatile("s_memrealtime %0\n\ts_waitcnt lgkmcnt(0)" : "=s"(t_) :: "memory"); SBAR(); if ((k) == PA) sa_ = t_; else if (sb_ == 0) sb_ = t_; } } while (0)
#define STO_(k)
#define STO_0
#define STO_1
#define STO_2
#define STO_3
#define STO_4
#define STO_5
#define STO_6
#define STO_7
#define STO_8
#define STO_9
#define STO_10
#define STO_11
#define STO_12
#define STO_13
#define STO_14
#define STO_15
#define STO_16 if (threadIdx.x == 0) __hip_atomic_store((__attribute__((address_space(1))) u64*)(ws + WS_T0) + blockIdx.x, t0_, __ATOMIC_RELAXED, __HIP_MEMORY_SCOPE_AGENT);
#define STO_17
#define STO_18
#define STO_19
#define ST_OUT(ws, k0, k1) do { if (ROLE == ((k0) != 0)) { u64 m_ = ~0ull; \
    for (int q_ = 0; q_ < 4; ++q_) { u64 v_ = __hip_atomic_load((__attribute__((address_space(1))) u64*)(ws + WS_T0) + (threadIdx.x & 63) * 4 + q_, __ATOMIC_RELAXED, __HIP_MEMORY_SCOPE_AGENT); m_ = v_ < m_ ? v_ : m_; } \
    for (int o_ = 32; o_; o_ >>= 1) { u64 v_ = __shfl_xor(m_, o_); m_ = v_ < m_ ? v_ : m_; } \
    int d_ = sb_ ? (int)(sb_ - sa_) / 1 : 0; if (d_ > 600 || !(w==0&&job)) d_ = 0; if (d_ < 0) d_ = 0; if (d_ > 5000) d_ = 5000; \
    const u64 e_ = m_ + 4000 + 4 * d_; while (__builtin_amdgcn_s_memrealtime() < e_) __builtin_amdgcn_s_sleep(1); } } while (0)


DI float asf(unsigned u) { return __builtin_bit_cast(float, u); }
DI unsigned asu(float f) { return __builtin_bit_cast(unsigned, f); }
DI unsigned cvtpk(float lo, float hi) { f32x2 v = {lo, hi}; return __builtin_bit_cast(unsigned, __builtin_convertvector(v, b16x2)); }
DI float bflo(unsigned u) { return asf(u << 16); }
DI float bfhi(unsigned u) { return asf(u & 0xffff0000u); }
DI f32x16 mfma32(u32x4 a, u32x4 b, f32x16 c) { return __builtin_amdgcn_mfma_f32_32x32x16_bf16(__builtin_bit_cast(b16x8, a), __builtin_bit_cast(b16x8, b), c, 0, 0, 0); }
DI f32x4 mfma16(u32x4 a, u32x4 b, f32x4 c) { return __builtin_amdgcn_mfma_f32_16x16x32_bf16(__builtin_bit_cast(b16x8, a), __builtin_bit_cast(b16x8, b), c, 0, 0, 0); }
DI f32x4 mfma16k16(u32x2 a, u32x2 b, f32x4 c) { return __builtin_amdgcn_mfma_f32_16x16x16bf16_1k(__builtin_bit_cast(s16x4, a), __builtin_bit_cast(s16x4, b), c, 0, 0, 0); }
DI u32x2 pack4(f32x4 v) { u32x2 r = {cvtpk(v[0], v[1]), cvtpk(v[2], v[3])}; return r; }
DI u32x4 lds_r128(unsigned a) { return *(LAS u32x4*)(size_t)a; }
DI f32x4 lds_r128f(unsigned a) { return *(LAS f32x4*)(size_t)a; }
DI u32x2 lds_r64(unsigned a) { return *(LAS u32x2*)(size_t)a; }
DI void lds_w128(unsigned a, u32x4 v) { *(LAS u32x4*)(size_t)a = v; }
DI void lds_w128f(unsigned a, f32x4 v) { *(LAS f32x4*)(size_t)a = v; }
DI void lds_w64(unsigned a, u32x2 v) { *(LAS u32x2*)(size_t)a = v; }
DI void lds_w32(unsigned a, float v) { *(LAS float*)(size_t)a = v; }
DI u32x2 lds_tr(unsigned a) { return __builtin_bit_cast(u32x2, __builtin_amdgcn_ds_read_tr16_b64_v4i16((LAS s16x4*)(size_t)a)); }
DI u32x4 lds_tr2(unsigned a0, unsigned a1) { u32x2 x = lds_tr(a0), y = lds_tr(a1); u32x4 r = {x[0], x[1], y[0], y[1]}; return r; }
#define BAR() asm volatile("s_waitcnt lgkmcnt(0)\n\ts_barrier" ::: "memory")
#define BARV(n) asm volatile("s_waitcnt vmcnt(" #n ") lgkmcnt(0)\n\ts_barrier" ::: "memory")
template <int CTRL> DI float dpp_add(float v) { return v + asf(__builtin_amdgcn_update_dpp(0u, asu(v), CTRL, 0xf, 0xf, true)); }
DI float xor16_add(float v) { auto r = __builtin_amdgcn_permlane16_swap(asu(v), asu(v), false, false); return asf(r[0]) + asf(r[1]); }
DI float xor16_max(float v) { auto r = __builtin_amdgcn_permlane16_swap(asu(v), asu(v), false, false); return __builtin_amdgcn_fmed3f(asf(r[0]), asf(r[1]), __builtin_inff()); }
DI unsigned umax3(unsigned a, unsigned b, unsigned c) { return max(max(a, b), c); }
template <int N> DI bool poisoned(const u32x4 (&v)[N]) {
  unsigned m = 0;
#pragma unroll
  for (int i = 0; i < N; ++i) { m = umax3(m, v[i][0], v[i][1]); m = umax3(m, v[i][2], v[i][3]); }
  return __any(m == POISON);
}
DI u32x4 unpoison(u32x4 v) { u32x4 r = {min(v[0], POISON - 1), min(v[1], POISON - 1), min(v[2], POISON - 1), min(v[3], POISON - 1)}; return r; }
DI unsigned fi_pos(unsigned r, unsigned c8) {
  return (((r >> 4) * 6 + (c8 >> 2)) * 64 + (c8 & 3) * 16 + ((r & 15) ^ ((c8 & 1) | (((r >> 3) & 1) << 1) | ((r >> 4) << 2)))) * 16;
}
DI float row8_sum(float s) { s = dpp_add<0x124>(s); s = dpp_add<0x128>(s); return xor16_add(s); }
DI float sum24(const f32x2 (&v)[12]) {
  f32x2 s0 = v[0] + v[1], s1 = v[2] + v[3];
#pragma unroll
  for (int i = 4; i < 12; i += 2) { s0 += v[i]; s1 += v[i + 1]; }
  s0 += s1;
  return s0[0] + s0[1];
}
DI float sumsq24(const f32x2 (&v)[12]) {
  f32x2 q0 = v[0] * v[0], q1 = v[1] * v[1];
#pragma unroll
  for (int i = 2; i < 12; i += 2) { q0 = __builtin_elementwise_fma(v[i], v[i], q0); q1 = __builtin_elementwise_fma(v[i + 1], v[i + 1], q1); }
  q0 += q1;
  return q0[0] + q0[1];
}
DI f32x2 unpack2(unsigned u) { f32x2 r = {bflo(u), bfhi(u)}; return r; }

constexpr unsigned SLOT = 36864, L_X = 3 * SLOT, L_H = 4 * SLOT, L_N = L_H + TILE_B, L_L = L_N + 1024, L_PT = L_L + 1024, LDS_BYTES = 163840;
constexpr unsigned L_VI = L_X, L_SP = L_X + TILE_B, L_YI = L_X + 20480;
constexpr unsigned L_HQ = 24576, L_Q = L_H, L_FI = L_H;

#define DMA3(v0, v1, v2, sbase, ldsa) \
  asm volatile("s_mov_b32 m0, %4\n\ts_nop 0\n\tglobal_load_lds_dwordx4 %0, %3\n\tglobal_load_lds_dwordx4 %1, %3 offset:1024\n\tglobal_load_lds_dwordx4 %2, %3 offset:2048" \
               :: "v"(v0), "v"(v1), "v"(v2), "s"(sbase), "s"(ldsa) : "memory")
#define DMA1(v0, sbase, ldsa) \
  asm volatile("s_mov_b32 m0, %2\n\ts_nop 0\n\tglobal_load_lds_dwordx4 %0, %1" :: "v"(v0), "s"(sbase), "s"(ldsa) : "memory")

template <int OFF> DI void bdma(unsigned voff, u32x4 rs, unsigned soff) { asm volatile("buffer_load_dwordx4 %0, %1, %2 offen offset:%3 sc1 lds" :: "v"(voff), "s"(rs), "s"(soff), "i"(OFF) : "memory"); }
template <int OFF> DI void setm0(unsigned base) { asm volatile("s_add_u32 m0, %0, %1\n\ts_nop 0" :: "s"(base), "i"(OFF) : "memory", "scc"); }
template <int N> DI void waitvm() { asm volatile("s_waitcnt vmcnt(%0)" :: "i"(N) : "memory"); }
#define IC(x) std::integral_constant<int, (x)>{}
#define OPAQUE(v) asm volatile("" : "+v"(v))
template <class R> DI void st128(u32x4 v, R rs, unsigned voff, unsigned soff) { __builtin_amdgcn_raw_buffer_store_b128(v, rs, voff, soff, SC1); asm volatile("s_nop 1" :: "v"(v)); }
template <int CTRL> DI unsigned dpp_xor(unsigned v) { return v ^ __builtin_amdgcn_update_dpp(0u, v, CTRL, 0xf, 0xf, true); }
DI unsigned fold_xor(unsigned e, unsigned o) {
  auto r = __builtin_amdgcn_permlane32_swap(e, o, false, false);
  unsigned u = r[0] ^ r[1];
  auto q = __builtin_amdgcn_permlane16_swap(u, u, false, false);
  u = q[0] ^ q[1];
  u = dpp_xor<0x128>(u); u = dpp_xor<0x124>(u); u = dpp_xor<0x4e>(u); return dpp_xor<0xb1>(u);
}
DI bool seal_ok(unsigned e, unsigned o, u32x4 seal, bool has, u32x2 tag) {
  const unsigned bad = has ? (seal[1] ^ tag[0]) | (seal[3] ^ tag[1]) : 0u;
  return __builtin_amdgcn_ballot_w64((fold_xor(e ^ seal[0], o ^ seal[2]) | bad) != 0) == 0;
}
DI unsigned xor3(unsigned a, unsigned b, unsigned c) { return __builtin_amdgcn_bitop3_b32(a, b, c, 0x96); }
#define XOR_IN(e, o, v) do { e = xor3(e, (v)[0], (v)[2]); o = xor3(o, (v)[1], (v)[3]); } while (0)
#define MIXV6() _Pragma("unroll") for (int q_ = 0; q_ < 6; ++q_) { __builtin_amdgcn_sched_group_barrier(0x8, 1, 0); __builtin_amdgcn_sched_group_barrier(0x2, 1, 0); } SBAR()
template <int N> DI bool sealed(const u32x4 (&v)[N], u32x4 seal, bool has, u32x2 tag) {
  unsigned e = 0, o = 0;
#pragma unroll
  for (int i = 0; i < N; ++i) XOR_IN(e, o, v[i]);
  return seal_ok(e, o, seal, has, tag);
}
DI bool tags_ok(u32x4 f, u32x2 tag) { return f[0] == tag[0] && f[1] == tag[1] && f[2] == tag[0] && f[3] == tag[1]; }

__global__ __launch_bounds__(512, 2) void k_timesformer(const char* x_, const char* wqkv_t_, const char* wo_t_, const char* wqkv_s_, const char* wo_s_, char* ws_, char* out_) {
  unsigned dc;
  u64x4 ka; u64x2 kb; u64 kc;
  {
    u64 pc;
    asm volatile("s_load_dwordx8 %2, %6, 0x0\n\ts_load_dwordx4 %3, %6, 0x20\n\ts_load_dwordx2 %4, %6, 0x30\n\ts_getpc_b64 %1\n\tglobal_load_dword %0, %5, %1"
                 : "=v"(dc), "=&s"(pc), "=&s"(ka), "=&s"(kb), "=&s"(kc) : "v"(min(threadIdx.x * 128, CODE_BYTES)), "s"(__builtin_amdgcn_kernarg_segment_ptr()) : "memory");
  }
  constexpr unsigned lds0 = 0;
  const unsigned b = blockIdx.x;
  unsigned w8 = __builtin_amdgcn_readfirstlane(threadIdx.x >> 6);
  unsigned l = threadIdx.x & 63;
  const unsigned t = b & 7, qb = b >> 3;
  w8 = (w8 & 4) | ((w8 + qb / 6) & 3);
  ST_DECL;
  ST(0);
  const bool jqk = b < 144, job = b < 216;
  const unsigned jp = jqk ? b >> 1 : b - 144, jw = jp / 18, jks = (jp - jw * 18) / 3, jtau = jp - jw * 15 - jks * 3;
  const unsigned jrow = jks * 32 + (jqk ? (b & 1) * 16 : 0), jslot = WS_WM + (jqk ? b * 512 : 73728 + jp * 1024);
  unsigned fo[4][3];
  if (w8 < 4) {
#pragma unroll
    for (int i = 0; i < 3; ++i) { const unsigned p = i * 64 + l; fo[0][i] = p * 16 + (p >= 96 ? FRAME_B - 1536 : 0) + w8 * 2 * FRAME_B + b * 1536; fo[1][i] = fo[2][i] = fo[3][i] = 0; }
  } else {
    const unsigned ra = jrow * 1152 + (jqk ? 0 : 768), rb = jqk ? jtau * (16 * 1152) + 384 : ra + 16 * 1152;
#pragma unroll
    for (int i = 0; i < 3; ++i) {
      const unsigned pos = i * 64 + l, row = (pos * 2731) >> 16, o = row * 1152 + (((pos - row * 24) ^ row) * 16) - i * 1024;
      fo[0][i] = o + ra; fo[1][i] = o + (ra + 8 * 1152); fo[2][i] = o + rb; fo[3][i] = o + (rb + 8 * 1152);
    }
  }
  unsigned so_ = (l >> 1) * 384 + (l & 1) * 16 + jtau * 32;
  asm volatile("s_waitcnt lgkmcnt(0)" : "+s"(ka), "+s"(kb), "+s"(kc), "+v"(fo[0][0]), "+v"(fo[0][1]), "+v"(fo[0][2]), "+v"(fo[1][0]), "+v"(fo[1][1]), "+v"(fo[1][2]),
               "+v"(fo[2][0]), "+v"(fo[2][1]), "+v"(fo[2][2]), "+v"(fo[3][0]), "+v"(fo[3][1]), "+v"(fo[3][2]), "+v"(so_) :: "memory");
  char* const ws = (char*)kb[1];
  if (w8 < 4) {
    __builtin_amdgcn_s_sleep(X_DELAY);
#pragma unroll
    for (int i = 0; i < 3; ++i) DMA1(fo[0][i], ka[0], lds0 + L_YI + w8 * 3072 + i * 1024);
  } else if (w8 < 6 && job) {
    if (w8 == 5) { __builtin_amdgcn_s_sleep(JOB2_DELAY); if (!jqk) __builtin_amdgcn_s_sleep(2 * JOB2_DELAY); }
    const u64 wqkv = w8 == 4 ? ka[1] : ka[3], wo = w8 == 4 ? ka[2] : kb[0];
    const unsigned dst = (w8 - 4) * SLOT;
#pragma unroll
    for (int q = 0; q < 4; ++q) DMA3(fo[q][0], fo[q][1], fo[q][2], wqkv, dst + q * 3072);
    if (!jqk) {
#pragma unroll
      for (int i = 0; i < 6; ++i) DMA1(so_, wo + i * 12288, dst + (12 + i) * 1024);
    }
  }
  const auto rsWS = __builtin_amdgcn_make_buffer_rsrc((void*)ws, 0, WS_END, 0x00020000);
  const u32x4 pz = {POISON, POISON, POISON, POISON};
  u64 epoch;
  asm volatile("s_load_dwordx2 %0, %1, 0x0" : "=&s"(epoch) : "s"(ws + WS_EPOCH) : "memory");
  asm volatile("s_waitcnt lgkmcnt(0)" : "+s"(epoch) :: "memory");
  epoch += 1;
  const u32x2 tag = {(unsigned)epoch, (unsigned)(epoch >> 32)};
  asm volatile("" : "+s"(w8));
  const unsigned w = w8 & 3;
#define LANE_IDS const unsigned i16 = l & 15, g = l >> 4, r32 = l & 31, hh = l >> 5, tq = i16 >> 2, tp = l & 3
#define FRAG_POS unsigned fr[2], dp[2];                                                                       \
  fr[0] = L_X + fi_pos(i16, g); fr[1] = (fr[0] ^ 64) + 6144; dp[0] = L_X + fi_pos(i16, hh) + 8 * (g & 1) + w * 1536; dp[1] = (dp[0] ^ 64) + 6144; \
  OPAQUE(fr[0]); OPAQUE(fr[1]); OPAQUE(dp[0]); OPAQUE(dp[1])
  auto ldw = [&](u32x4 (&W)[18], const int m, const int k0, const int k1, const unsigned rot) {
#pragma unroll
    for (int kk = k0; kk < k1; ++kk) {
      const unsigned ks = kk + rot >= 6 ? kk + rot - 6 : kk + rot;
#pragma unroll
      for (int i = 0; i < 3; ++i) { SBAR(); W[kk * 3 + i] = __builtin_amdgcn_raw_buffer_load_b128(rsWS, l * 16 + i * 1024, WS_WM + m * 73728 + w * 18432 + ks * 3072, SC1); }
    }
  };
  auto ld_seal = [&](const int m) {
    return __builtin_amdgcn_raw_buffer_load_b128(rsWS, l < ((m & 1) ? 18 : 36) ? l * 128 : WS_END, WS_SEAL + (m >> 1) * 32768 + ((m & 1) ? 144 + 18 * w : 36 * w) * 128, SC1);
  };
  const unsigned rot = qb % 6;
  auto h_frags = [&](u32x4 (&H)[6][2], const unsigned (&fr)[2]) {
#pragma unroll
    for (int kk = 0; kk < 6; ++kk) {
      const unsigned ks = kk + rot >= 6 ? kk + rot - 6 : kk + rot;
      H[kk][0] = lds_r128(fr[0] + ks * 1024 + (L_H - L_X)); H[kk][1] = lds_r128(fr[1] + ks * 1024 + (L_H - L_X));
    }
  };
  auto low_sum = [&](const f32x4 a, const f32x4 c, float (&lt)[2]) {
    lt[0] = (a[0] + a[2]) + (c[0] + c[2]);
    lt[1] = (a[1] + a[3]) + (c[1] + c[3]);
    return (bool)__any(lt[0] < 8.7e-19f || lt[1] < 8.7e-19f);
  };

  if (w8 < 4) {
    const unsigned nl = l & 3, j = (l >> 2) & 7, tt = 2 * w + (l >> 5), r = nl * 8 + tt;
    unsigned rp = L_X + fi_pos(r, j);
    OPAQUE(rp);
    waitvm<0>();
    asm volatile("" :: "v"(dc));
    f32x2 xv[12];
    float mu1;
    {
      u32x4 h[3], xr[3];
#pragma unroll
      for (int i = 0; i < 3; ++i) xr[i] = lds_r128(lds0 + L_YI + tt * 1536 + nl * 384 + j * 16 + i * 128);
#pragma unroll
      for (int i = 0; i < 12; ++i) xv[i] = unpack2(xr[i >> 2][i & 3]);
      mu1 = row8_sum(sum24(xv)) * (1.0f / 192.0f);
#pragma unroll
      for (int i = 0; i < 12; ++i) xv[i] -= mu1;
      const float rs = __builtin_amdgcn_rsqf(row8_sum(sumsq24(xv)) * (1.0f / 192.0f) + 1e-5f);
#pragma unroll
      for (int i = 0; i < 12; ++i) { const f32x2 n = xv[i] * rs; h[i >> 2][i & 3] = cvtpk(n[0], n[1]); }
#pragma unroll
      for (int i = 0; i < 3; ++i) lds_w128(rp + (L_H - L_X) + i * 2048, h[i]);
    }
    SBAR();
    OPAQUE(l);
    unsigned so = tt * FRAME_B + (j >> 1) * 1024 + ((t ^ ((j >> 1) & 1)) * 128) + (((j & 1) ^ (t >> 2)) * 64) + nl * 16;
    const unsigned sso = qb * TILE_B;
    LANE_IDS;
    FRAG_POS;
    unsigned sto = j == 0 ? (tt * 1024 + nl) * 8 : WS_END;
    unsigned sa = L_SP + l * 16;
    unsigned keep = (hh == (i16 >> 3)) ? ~0u : 0u;
    OPAQUE(sto); OPAQUE(so); OPAQUE(sa); OPAQUE(keep);
    ST(1);
    BAR();
    ST(6);
    {
      u32x4 H[6][2], W[18];
      h_frags(H, fr);
      asm volatile("s_barrier" ::: "memory");
      ST(2);
#pragma unroll 1
      for (int sp = 0;; ++sp) {
        const u32x4 seal = ld_seal(3);
        ldw(W, 3, 0, 6, rot);
        SBAR();
        ST(3);
        if (sp == 0) asm volatile("s_barrier" ::: "memory");
        f32x4 vp[3][2], sq[4], e;
        float rd;
        u32x2 pt[2];
        unsigned he = 0, ho = 0;
#pragma unroll
        for (int ww = 0; ww < 4; ++ww) sq[ww] = lds_r128f(sa + ww * 1024);
        SBAR();
        ST(4);
#pragma unroll
        for (int kk = 0; kk < 6; ++kk) {
#pragma unroll
          for (int i = 0; i < 3; ++i) {
            vp[i][0] = mfma16(H[kk][0], W[kk * 3 + i], kk ? vp[i][0] : f32x4{0, 0, 0, 0});
            vp[i][1] = mfma16(H[kk][1], W[kk * 3 + i], kk ? vp[i][1] : f32x4{0, 0, 0, 0});
            XOR_IN(he, ho, W[kk * 3 + i]);
          }
          if (kk == 1) { e = (sq[0] + sq[1]) + (sq[2] + sq[3]); rd = fmaxf(fmaxf(e[0], e[1]), fmaxf(e[2], e[3])); OPAQUE(rd); }
          if (kk == 2) { e -= xor16_max(rd); OPAQUE(e); }
          if (kk == 3) {
#pragma unroll
            for (int k = 0; k < 4; ++k) e[k] = __builtin_amdgcn_exp2f(e[k]);
            rd = (e[0] + e[1]) + (e[2] + e[3]);
            OPAQUE(rd);
          }
          if (kk == 4) { pt[0] = pack4(e * __builtin_amdgcn_rcpf(xor16_add(rd))); OPAQUE(pt[0]); }
          if (kk == 5) {
#pragma unroll
            for (int k = 0; k < 2; ++k) {
              auto q = __builtin_amdgcn_permlane32_swap(pt[0][k], pt[0][k], false, false);
              pt[1][k] = (q[0] ^ q[1] ^ pt[0][k]) & keep;
              pt[0][k] &= keep;
            }
            OPAQUE(pt[0]); OPAQUE(pt[1]);
          }
#pragma unroll
          for (int q = 0; q < 6; ++q) { __builtin_amdgcn_sched_group_barrier(0x8, 1, 0); __builtin_amdgcn_sched_group_barrier(0x402, 3, 0); }
          SBAR();
        }
        OPAQUE(he); OPAQUE(ho);
        ST(7);
        f32x4 y[3][2];
#pragma unroll
        for (int hf = 0; hf < 2; ++hf)
#pragma unroll
          for (int i = 0; i < 3; ++i) y[i][hf] = mfma16k16(pack4(vp[i][hf]), pt[hf], f32x4{0, 0, 0, 0});
        SBAR();
#pragma unroll
        for (int hf = 0; hf < 2; ++hf)
#pragma unroll
          for (int i = 0; i < 3; ++i) lds_w64(dp[hf] + (L_YI - L_X) + i * 512, pack4(y[i][hf]));
        if (__builtin_expect(seal_ok(he, ho, seal, l < 18, tag), 1) || sp >= SPIN_CAP) break;
        asm volatile("" ::: "memory");
      }
    }
    ST(9);
    BAR();
    {
      u32x4 y[3], h[3];
#pragma unroll
      for (int i = 0; i < 3; ++i) y[i] = lds_r128(rp + (L_YI - L_X) + i * 2048);
#pragma unroll
      for (int i = 0; i < 12; ++i) xv[i] += unpack2(y[i >> 2][i & 3]);
      const float mu = row8_sum(sum24(xv)) * (1.0f / 192.0f), sq = row8_sum(sumsq24(xv));
      const float var = fmaxf(fmaf(sq, 1.0f / 192.0f, -mu * mu), 0.f) + 1e-5f;
      const float rs = __builtin_amdgcn_rsqf(var), nb = -mu * rs;
#pragma unroll
      for (int i = 0; i < 3; ++i) {
#pragma unroll
        for (int e = 0; e < 4; ++e) { const f32x2 n = xv[4 * i + e] * rs + nb; h[i][e] = cvtpk(n[0], n[1]); }
        if (i == 0) { OPAQUE(h[0]); ST(5); asm volatile("s_barrier" ::: "memory"); }
        st128(h[i], rsWS, so, WS_H2 + sso + i * 4096);
        SBAR();
      }
      const u32x2 stat = {asu(mu + mu1), asu(var)};
      __builtin_amdgcn_raw_buffer_store_b64(stat, rsWS, sto, WS_ST + b * 32, SC1);
    }
    ST(10);
    const unsigned ring = lds0 + w * SLOT;
    unsigned kr0 = ring + (r32 >> 2) * 128 + (hh ^ (r32 >> 4)) * 64 + (r32 & 3) * 16, kr1 = kr0 ^ 128;
    unsigned vr0 = ring + (g & 1) * 1024 + (hh ^ (g & 1)) * 128 + (tp >> 1) * 64 + tq * 16 + 8 * (tp & 1), vr1 = (vr0 ^ 64) + 512;
    unsigned lo[3] = {l * 16, l * 16 + 4096, l * 16 + 8192}, pw = ring + l * 16, lw = l < 16 ? L_L + (l * 4 + w) * 8 : L_L + 512 + l * 8;
    OPAQUE(kr0); OPAQUE(kr1); OPAQUE(vr0); OPAQUE(vr1); OPAQUE(lo[0]); OPAQUE(lo[1]); OPAQUE(lo[2]); OPAQUE(pw); OPAQUE(lw);
    const u64 hbu = (u64)(ws + WS_H2 + (size_t)t * FRAME_B);
    const u32x4 rs = {(unsigned)hbu, (unsigned)(hbu >> 32) & 0xffffu, (unsigned)FRAME_B, 0x00020000u};
    unsigned toff[8];
#pragma unroll
    for (int i = 0; i < 8; ++i) { toff[i] = (8 * w + ((i + qb) & 7)) * TILE_B; asm volatile("" : "+s"(toff[i])); }
    u32x4 sel;
    { const unsigned one = (tp == (g & 1)) ? 0x3f803f80u : 0u; sel = u32x4{one, one, one, one}; OPAQUE(sel); }
    auto piece = [&](auto Tc, auto Pc) {
      constexpr int T = decltype(Tc)::value, p = decltype(Pc)::value;
      if constexpr (T < 8) {
        if constexpr (p % 4 == 0) setm0<(T % 3) * TILE_B + p * 1024>(ring);
        bdma<(p % 4) * 1024>(lo[p / 4], rs, toff[T]);
      }
    };
#define P6(T, a) piece(IC(T), IC(a)); piece(IC(T), IC(a + 1)); piece(IC(T), IC(a + 2)); piece(IC(T), IC(a + 3)); piece(IC(T), IC(a + 4)); piece(IC(T), IC(a + 5));
    auto read_valid = [&](u32x4 (&KK)[12], const unsigned dst, unsigned off) {
      for (int sp = 0; sp < SPIN_CAP; ++sp) {
#pragma unroll
        for (int k = 0; k < 12; ++k) KK[k] = lds_r128(((k & 1) ? kr1 : kr0) + dst + k * 1024);
        if (__builtin_expect(!poisoned(KK), 1)) break;
#pragma unroll 1
        for (int k = 0; k < 12; ++k) { setm0<0>(ring + dst + k * 1024); bdma<0>(lo[0], rs, off + k * 1024); }
        waitvm<0>();
      }
    };
    unsigned qa = lds0 + L_Q + lo[0], na = lds0 + L_N + r32 * 16;
    OPAQUE(qa); OPAQUE(na);
    BARV(0);
    __builtin_amdgcn_s_sleep(TILE0_DELAY);
    P6(0, 0) P6(0, 6)
    if (w == 0 && l == 0) __builtin_amdgcn_raw_buffer_store_b64(tag, rsWS, 0, WS_FLAG2 + b * 8, SC1);
    ST(11);
    BAR();
    u32x4 K0[12];
    if (w == 0) waitvm<1>(); else waitvm<0>();
    ST(12);
    read_valid(K0, 0, toff[0]);
    ST(13);
    piece(IC(1), IC(0)); piece(IC(1), IC(1)); piece(IC(1), IC(2)); piece(IC(1), IC(3));
    BAR();
    ST(14);
    u32x4 Q[12];
    float mref;
    {
      const f32x4 n0 = lds_r128f(na), n1 = lds_r128f(na + 512);
#pragma unroll
      for (int s = 0; s < 6; ++s) Q[s] = lds_r128(qa + s * 1024);
      SBAR();
      const f32x4 s = n0 + n1;
      mref = __builtin_amdgcn_sqrtf(((s[0] + s[1]) + (s[2] + s[3])) * (192.0f * 1.02f));
      OPAQUE(mref);
      SBAR();
#pragma unroll
      for (int s = 6; s < 12; ++s) Q[s] = lds_r128(qa + s * 1024);
      SBAR();
    }
    constexpr int KW = 4, VW = 4;
    bool safe = false, exact = false, late = false;
#pragma unroll 1
    for (;;) {
      f32x16 S[2], O[6], negm;
      u32x4 K[12], V[12], P[2][2];
      f32x4 lsum = {0, 0, 0, 0};
      float pe[2][2];
#pragma unroll
      for (int i = 0; i < 16; ++i) negm[i] = -mref;
#define RK(k, T) K[k] = lds_r128((((k) & 1) ? kr1 : kr0) + ((T) % 3) * TILE_B + (k) * 1024);
#define RV(u, T) { const unsigned a_ = (((u) & 1) ? vr1 : vr0) + ((T) % 3) * TILE_B + ((u) >> 1) * 2048; V[u] = lds_tr2(a_, a_ + 256); }
      auto sm = [&](auto Tc, auto Hc, auto Gc) {
        constexpr int T = decltype(Tc)::value, H = decltype(Hc)::value, G = decltype(Gc)::value;
        if constexpr (G < 8) pe[(G >> 1) & 1][G & 1] = __builtin_amdgcn_exp2f(S[T & 1][8 * H + G]);
        if constexpr (G >= 2 && G <= 8 && G % 2 == 0) { constexpr int q = G / 2 - 1; P[T & 1][H][q] = cvtpk(pe[q & 1][0], pe[q & 1][1]); }
        if constexpr (G == 10) lsum = mfma16(sel, P[T & 1][H], lsum);
      };
      auto put_o = [&](const int un) {
        u32x4 pk;
#pragma unroll
        for (int e = 0; e < 4; ++e) pk[e] = cvtpk(O[un >> 1][8 * (un & 1) + 2 * e], O[un >> 1][8 * (un & 1) + 2 * e + 1]);
        lds_w128(pw + un * 1024, pk);
      };
      auto qkphase = [&](auto Ic) {
        constexpr int I = decltype(Ic)::value;
        auto step = [&](auto Kc) {
          constexpr int k = decltype(Kc)::value;
          if constexpr (I + 1 < 8) {
            S[(I + 1) & 1] = mfma32(I < 0 ? K0[k] : K[k], Q[k], k == 0 ? negm : S[(I + 1) & 1]);
            if constexpr (k + KW < 12 && I >= 0) RK(k + KW, I + 1)
          }
          if constexpr (I < 0 && k < 8) piece(IC(1), IC(4 + k));
          if constexpr (I >= 0) { if constexpr (k & 1) piece(IC(I + 2), IC(6 + k / 2)); if constexpr (k < 11) sm(IC(I), IC(1), IC(k)); }
          if constexpr (I >= 0 && I < 7 && k >= 12 - VW) RV(k - (12 - VW), I)
          if constexpr (I == 7 && k < VW) RV(k, I)
          SBAR();
        };
        step(IC(0)); step(IC(1)); step(IC(2)); step(IC(3)); step(IC(4)); step(IC(5)); step(IC(6)); step(IC(7)); step(IC(8)); step(IC(9)); step(IC(10)); step(IC(11));
      };
      auto pvphase = [&](auto Ic) {
        constexpr int I = decltype(Ic)::value;
        auto step = [&](auto Uc) {
          constexpr int u = decltype(Uc)::value;
          if constexpr (I >= 0) {
            f32x16 z;
#pragma unroll
            for (int e = 0; e < 16; ++e) z[e] = 0.f;
            O[u >> 1] = mfma32(V[u], P[I & 1][u & 1], (I == 0 && !(u & 1)) ? z : O[u >> 1]);
            if constexpr (u & 1) piece(IC(I + 3), IC(u / 2));
            if constexpr (u + VW < 12) RV(u + VW, I)
          }
          if constexpr (I + 1 < 8 && u >= 1) sm(IC(I + 1), IC(0), IC(u - 1));
          if constexpr (I < 0 && (u & 1)) piece(IC(2), IC(u / 2));
          if constexpr (I + 2 < 8) {
            if constexpr (u == 12 - KW) waitvm<(I < 0 ? 12 : I + 3 < 8 ? 6 + (12 - KW) / 2 : 6)>();
            if constexpr (u >= 12 - KW) RK(u - (12 - KW), I + 2)
            if constexpr (u == 11) waitvm<(I + 3 < 8 ? 6 : 0)>();
          }
          if constexpr (I == 7 && u >= 3) {
            put_o(u - 3);
          }
          SBAR();
        };
        step(IC(0)); step(IC(1)); step(IC(2)); step(IC(3)); step(IC(4)); step(IC(5)); step(IC(6)); step(IC(7)); step(IC(8)); step(IC(9)); step(IC(10)); step(IC(11));
      };
#define STALE(v) (__builtin_expect(!safe && __any((v) != (v)), 0))
      bool done = false;
      do {
        qkphase(IC(-1));
        ST(15);
        pvphase(IC(-1));
        ST(16);
        qkphase(IC(0)); pvphase(IC(0)); qkphase(IC(1)); pvphase(IC(1)); qkphase(IC(2));
        const float seen = lsum[0];
        pvphase(IC(2));
        if (STALE(seen)) break;
        qkphase(IC(3)); pvphase(IC(3)); qkphase(IC(4)); pvphase(IC(4)); qkphase(IC(5));
        if (!late) asm volatile("s_barrier" ::: "memory");
        late = true;
        pvphase(IC(5)); qkphase(IC(6)); pvphase(IC(6)); qkphase(IC(7)); pvphase(IC(7));
        if (STALE(lsum[0])) break;
        { const u32x2 ls = {asu(lsum[0]), asu(lsum[1])}; lds_w64(lw, ls); }
        put_o(9); put_o(10); put_o(11);
        done = true;
      } while (false);
#pragma unroll
      for (int k = 0; k < 12; ++k) asm volatile("" : "=v"(K0[k]));
      if (__builtin_expect(done, 1)) {
        ST(17);
        BAR();
        ST(18);
        float lt[2];
        unsigned ll = lo[0];
        OPAQUE(ll);
        ll >>= 4;
        if (__builtin_expect(!low_sum(lds_r128f(L_L + (ll & 15) * 32), lds_r128f(L_L + (ll & 15) * 32 + 16), lt) || exact, 1)) break;
        BAR();
        float mx = -3.0e38f;
#pragma unroll 1
        for (unsigned i = 0; i < 8; ++i) {
#pragma unroll 1
          for (int k = 0; k < 12; ++k) { setm0<0>(ring + k * 1024); bdma<0>(lo[0], rs, (8 * w + i) * TILE_B + k * 1024); }
          waitvm<0>();
          f32x16 s;
#pragma unroll
          for (int e = 0; e < 16; ++e) s[e] = 0.f;
#pragma unroll
          for (int k = 0; k < 12; ++k) s = mfma32(lds_r128(((k & 1) ? kr1 : kr0) + k * 1024), Q[k], s);
#pragma unroll
          for (int e = 0; e < 16; ++e) mx = fmaxf(mx, s[e]);
        }
        lds_w32(L_N + ((ll & 31) * 8 + w * 2 + (ll >> 5)) * 4, mx);
        BAR();
        const f32x4 c = lds_r128f(L_N + (ll & 31) * 32), d = lds_r128f(L_N + (ll & 31) * 32 + 16);
        mref = fmaxf(fmaxf(fmaxf(c[0], c[1]), fmaxf(c[2], c[3])), fmaxf(fmaxf(d[0], d[1]), fmaxf(d[2], d[3])));
        exact = true;
      } else {
        unsigned ll = lo[0];
        OPAQUE(ll);
        for (int sp = 0; sp < SPIN_CAP; ++sp) {
          asm volatile("" ::: "memory");
          const u32x2 f = __builtin_amdgcn_raw_buffer_load_b64(rsWS, ll >> 1, WS_FLAG2 + w * 512, SC1);
          if (__all(f[0] == tag[0] && f[1] == tag[1])) break;
        }
      }
      safe = true;
      asm volatile("s_waitcnt vmcnt(0) lgkmcnt(0)" ::: "memory");
      P6(0, 0) P6(0, 6)
      waitvm<0>();
      piece(IC(1), IC(0)); piece(IC(1), IC(1)); piece(IC(1), IC(2)); piece(IC(1), IC(3));
#pragma unroll
      for (int k = 0; k < 12; ++k) K0[k] = lds_r128(((k & 1) ? kr1 : kr0) + k * 1024);
    }
    ST(19);
    BAR();
    ST_OUT(ws, 0, 12);
  } else {
    LANE_IDS;
    auto job_run = [&](const unsigned at, const unsigned slot, const unsigned sealat, auto younger) {
      unsigned ja[2] = {at + i16 * 384 + ((g ^ (l & 7)) * 16), at + i16 * 384 + (((4 + g) ^ (l & 7)) * 16)}, jb = at + 12288 + (8 * g + tq) * 32 + tp * 8;
      OPAQUE(ja[0]); OPAQUE(ja[1]); OPAQUE(jb);
      waitvm<decltype(younger)::value>();
      ST(2);
      u32x4 a[6], c[6];
#pragma unroll
      for (int kk = 0; kk < 6; ++kk) { a[kk] = lds_r128(ja[kk & 1] + (kk >> 1) * 128); c[kk] = lds_r128(ja[kk & 1] + (kk >> 1) * 128 + 6144); }
      const unsigned o = (hh * 16 + i16) * 16 + (g & 1) * 8;
      f32x4 d = {0, 0, 0, 0};
      u32x2 r;
      if (jqk) {
#pragma unroll
        for (int kk = 0; kk < 6; ++kk) d = mfma16(a[kk], c[kk], d);
        r = pack4(d * QSCALE);
        __builtin_amdgcn_raw_buffer_store_b64(r, rsWS, o, slot, SC1);
      } else {
        f32x4 e = {0, 0, 0, 0};
#pragma unroll
        for (int kk = 0; kk < 6; ++kk) {
          const u32x4 bb = lds_tr2(jb + kk * 1024, jb + kk * 1024 + 128);
          d = mfma16(a[kk], bb, d); e = mfma16(c[kk], bb, e);
        }
        const u32x2 r0 = pack4(d), r1 = pack4(e);
        __builtin_amdgcn_raw_buffer_store_b64(r0, rsWS, o, slot, SC1);
        __builtin_amdgcn_raw_buffer_store_b64(r1, rsWS, o + 512, slot, SC1);
        r = r0 ^ r1;
      }
      const u32x2 half = {fold_xor(r[0], r[1]), hh ? tag[1] : tag[0]};
      __builtin_amdgcn_raw_buffer_store_b64(half, rsWS, r32 == 0 ? hh * 8 : WS_END, sealat, SC1);
    };
    FRAG_POS;
    if (w == 0 || (w == 1 && jqk)) {
      ST(1);
      if (job) job_run(w * SLOT, jslot + (1 - w) * 2 * 73728, WS_SEAL + (1 - w) * 32768 + b * 128, IC(0));
      ST(3);
      if (w == 0) {
        __builtin_amdgcn_raw_buffer_store_b32(POISON, rsWS, l * 4, WS_ST + (t * 1024 + qb * 32) * 8, SC1);
        const unsigned p0 = WS_H2 + t * FRAME_B + (jqk ? qb * 8 : 144 + (qb - 27) * 48) * 1024, np = jqk ? 2 : job ? 0 : 12;
#pragma unroll 1
        for (unsigned i = 0; i < np; ++i) {
#pragma unroll
          for (int q = 0; q < 4; ++q) __builtin_amdgcn_raw_buffer_store_b128(pz, rsWS, l * 16 + q * 1024, p0 + i * 4096, SC1);
        }
        asm volatile("s_nop 1" :: "v"(pz));
        if (jqk) { waitvm<9>(); __builtin_amdgcn_s_sleep(READ_DELAY); } else if (job) __builtin_amdgcn_s_sleep(VO_DELAY); else { waitvm<0>(); __builtin_amdgcn_s_sleep(IDLE_DELAY); }
      }
    }
    u32x4 wq[18], wv[18];
    {
      u32x4 H[6][2];
      u32x2 hs[3][2];
      unsigned spa = hh == (i16 >> 3) ? L_SP + w * 1024 + l * 16 : L_SP + 4096 + l * 16;
      OPAQUE(spa);
      __builtin_amdgcn_s_setprio(3);
      ST(4);
      BAR();
#pragma unroll 1
      for (int sp = 0;; ++sp) {
        const u32x4 seal = ld_seal(2);
        ldw(wq, 2, 0, 3, rot);
        SBAR();
        h_frags(H, fr);
#pragma unroll
        for (int i = 0; i < 3; ++i) { hs[i][0] = lds_r64(dp[0] + (L_H - L_X) + i * 512); hs[i][1] = lds_r64(dp[1] + (L_H - L_X) + i * 512); }
        ldw(wq, 2, 3, 6, rot);
        if (sp == 0) {
          if (w == 0) {
            waitvm<19>();
            if (l < 8) __builtin_amdgcn_raw_buffer_store_b64(tag, rsWS, l * 2048, WS_FLAG + b * 8, SC1);
          }
          asm volatile("s_barrier" ::: "memory");
        }
        SBAR();
        f32x4 d[3][2], st[2];
        unsigned he = 0, ho = 0;
#pragma unroll
        for (int kk = 0; kk < 6; ++kk) {
#pragma unroll
          for (int i = 0; i < 3; ++i) {
            d[i][0] = mfma16(wq[kk * 3 + i], H[kk][0], kk ? d[i][0] : f32x4{0, 0, 0, 0});
            d[i][1] = mfma16(wq[kk * 3 + i], H[kk][1], kk ? d[i][1] : f32x4{0, 0, 0, 0});
            XOR_IN(he, ho, wq[kk * 3 + i]);
          }
          MIXV6();
        }
        ST(19);
        const bool ok = seal_ok(he, ho, seal, l < 36, tag);
#pragma unroll
        for (int hf = 0; hf < 2; ++hf) {
          st[hf] = f32x4{0, 0, 0, 0};
#pragma unroll
          for (int i = 0; i < 3; ++i) st[hf] = mfma16k16(hs[i][hf], pack4(d[i][hf]), st[hf]);
          lds_w128f(spa ^ (hf * 512), st[hf]);
        }
        if (__builtin_expect(ok, 1) || sp >= SPIN_CAP) break;
        asm volatile("" ::: "memory");
      }
      asm volatile("" :: "v"(dc));
    }
    ST(10);
    BAR();
    __builtin_amdgcn_s_setprio(0);
    u32x4 seal = ld_seal(0);
    {
      ldw(wq, 0, 0, WQ_SPLIT, 0);
      u32x2 fl = __builtin_amdgcn_raw_buffer_load_b64(rsWS, l * 8, WS_FLAG + t * 2048 + w * 512, SC1);
      ST(5);
      BAR();
      ldw(wq, 0, WQ_SPLIT, 6, 0);
      if (__builtin_expect(!__all(fl[0] == tag[0] && fl[1] == tag[1]), 0)) {
        for (int sp = 0; sp < SPIN_CAP; ++sp) {
          asm volatile("" ::: "memory");
          const u32x2 f2 = __builtin_amdgcn_raw_buffer_load_b64(rsWS, l * 8, WS_FLAG + t * 2048 + w * 512, SC1);
          if (__all(f2[0] == tag[0] && f2[1] == tag[1])) break;
        }
      }
      ST(18);
      BAR();
    }
    unsigned hqa[2][2], own[2][2];
#pragma unroll
    for (int hf = 0; hf < 2; ++hf) {
      const unsigned in = lds0 + L_HQ + ((4 * hf + tq) ^ hh) * 128 + ((g & 1) ^ hf) * 64 + tp * 16;
      hqa[hf][0] = in + hh * 1024; hqa[hf][1] = in + 2048 + hh * (SLOT - 2048);
      OPAQUE(hqa[hf][0]); OPAQUE(hqa[hf][1]);
    }
    const unsigned tok = 16 * (g & 1) + i16;
    unsigned nw = lds0 + L_N + l * 16 + w * 4;
    OPAQUE(nw);
    unsigned qw = L_Q + (3 * w * 64 + hh * 32 + i16) * 16 + 8 * (g & 1);
    own[0][0] = w * SLOT + L_HQ + (tq ^ (w & 1)) * 128 + hh * 64 + tp * 16 + 8 * (g & 1); own[0][1] = (own[0][0] ^ 64) + 512;
    own[1][0] = own[0][0] ^ 128; own[1][1] = own[0][1] ^ 128;
    OPAQUE(qw); OPAQUE(own[0][0]); OPAQUE(own[0][1]); OPAQUE(own[1][0]); OPAQUE(own[1][1]);
    if (b == 0 && w == 3 && l == 0) __builtin_amdgcn_raw_buffer_store_b64(tag, rsWS, 0, WS_EPOCH, SC1);
    for (int sp = 0; sp < SPIN_CAP && !sealed(wq, seal, l < 36, tag); ++sp) {
      asm volatile("" ::: "memory");
      seal = ld_seal(0);
      ldw(wq, 0, 0, 6, 0);
    }
    if (w == 1 && job && !jqk) job_run(SLOT, jslot, WS_SEAL + b * 128, IC(0));
    ST(6);
    BAR();
    ST(7);
    u32x4 hq[3];
    {
      const u64 hbu = (u64)(ws + WS_H2 + (size_t)t * FRAME_B);
      const u32x4 rs = {(unsigned)hbu, (unsigned)(hbu >> 32) & 0xffffu, (unsigned)FRAME_B, 0x00020000u};
      for (int sp = 0; sp < SPIN_CAP; ++sp) {
        setm0<L_HQ>(lds0 + w * SLOT);
        bdma<0>(l * 16, rs, qb * TILE_B + w * 3072); bdma<1024>(l * 16, rs, qb * TILE_B + w * 3072); bdma<2048>(l * 16, rs, qb * TILE_B + w * 3072);
        waitvm<0>();
#pragma unroll
        for (int i = 0; i < 3; ++i) { const u32x2 u0 = lds_r64(own[i & 1][0] + i * 1024), u1 = lds_r64(own[i & 1][1] + i * 1024); hq[i] = u32x4{u0[0], u0[1], u1[0], u1[1]}; }
        if (!poisoned(hq)) break;
      }
    }
    ST(8);
    BAR();
    ST(9);
    auto gemm = [&](const u32x4 (&wf)[18], const u32x4 (&bf)[6][2], auto&& fin) {
      f32x4 d[3][2];
#pragma unroll
      for (int i = 0; i < 3; ++i) {
#pragma unroll
        for (int ks = 0; ks < 6; ++ks) {
          d[i][0] = mfma16(wf[ks * 3 + i], bf[ks][0], ks ? d[i][0] : f32x4{0, 0, 0, 0});
          d[i][1] = mfma16(wf[ks * 3 + i], bf[ks][1], ks ? d[i][1] : f32x4{0, 0, 0, 0});
          if (ks == 0) SBAR();
        }
        if (i > 0) fin(i - 1, d[i - 1]);
        SBAR();
      }
      fin(2, d[2]);
    };
    {
      u32x4 bf[6][2];
#pragma unroll
      for (int ks = 0; ks < 6; ++ks)
#pragma unroll
        for (int hf = 0; hf < 2; ++hf) {
          if (ks == 3) { hqa[hf][0] += 2 * SLOT; hqa[hf][1] += 2 * SLOT; }
          bf[ks][hf] = lds_r128(ks % 3 == 1 ? hqa[hf][1] : hqa[hf][0] + (ks % 3 == 2 ? SLOT + 1024 : 0));
        }
      SBAR();
      f32x2 n[2] = {{0.f, 0.f}, {0.f, 0.f}};
      gemm(wq, bf, [&](const int i, const f32x4 (&d)[2]) {
#pragma unroll
        for (int hf = 0; hf < 2; ++hf) {
          lds_w64(qw + i * 1024 + hf * 256, pack4(d[hf]));
          const f32x2 a = {d[hf][0], d[hf][1]}, c = {d[hf][2], d[hf][3]};
          n[hf] = __builtin_elementwise_fma(a, a, n[hf]); n[hf] = __builtin_elementwise_fma(c, c, n[hf]);
        }
      });
      auto rr = __builtin_amdgcn_permlane16_swap(asu(n[0][0] + n[0][1]), asu(n[1][0] + n[1][1]), false, false);
      lds_w32(nw, asf(rr[0]) + asf(rr[1]));
    }
    ST(11);
    BAR();
    unsigned oo = (t * 1024 + qb * 32 + tok) * 384 + (6 * w + hh) * 16;
    OPAQUE(oo);
    float lt[2];
    u32x4 pick;
#pragma unroll
    for (int e = 0; e < 4; ++e) {
      const unsigned d = 8 * (e >> 1) + 4 * (g & 1) + 2 * (e & 1);
      pick[e] = (i16 == d ? 0x3f80u : 0u) | (i16 == d + 1 ? 0x3f800000u : 0u);
    }
    unsigned pr[2], la = lds0 + L_L + i16 * 32;
    pr[0] = lds0 + hh * SLOT + 3 * w * 1024 + ((g & 1) * 32 + i16) * 16; pr[1] = pr[0] + 2 * SLOT;
    OPAQUE(pick); OPAQUE(pr[0]); OPAQUE(pr[1]); OPAQUE(la);
    f32x4 x1[3][2];
    {
      __builtin_amdgcn_s_sleep(40);
      u32x2 st[2];
      for (int sp = 0; sp < SPIN_CAP; ++sp) {
        asm volatile("" ::: "memory");
        st[0] = __builtin_amdgcn_raw_buffer_load_b64(rsWS, i16 * 8, WS_ST + (t * 1024 + qb * 32) * 8, SC1);
        st[1] = __builtin_amdgcn_raw_buffer_load_b64(rsWS, i16 * 8 + 128, WS_ST + (t * 1024 + qb * 32) * 8, SC1);
        if (!__any(max(max(st[0][0], st[0][1]), max(st[1][0], st[1][1])) == POISON)) break;
      }
#pragma unroll
      for (int hf = 0; hf < 2; ++hf) {
        const float mu = asf(st[hf][0]), sd = __builtin_amdgcn_sqrtf(asf(st[hf][1]));
#pragma unroll
        for (int i = 0; i < 3; ++i) {
          const f32x4 h = {bflo(hq[i][2 * hf]), bfhi(hq[i][2 * hf]), bflo(hq[i][2 * hf + 1]), bfhi(hq[i][2 * hf + 1])};
          x1[i][hf] = h * sd + mu;
          OPAQUE(x1[i][hf]);
        }
      }
    }
    BAR();
    ST(16);
    for (int sp = 0; sp < SPIN_CAP; ++sp) {
      asm volatile("" ::: "memory");
      seal = ld_seal(1);
      ldw(wv, 1, 0, 6, 0);
      if (sealed(wv, seal, l < 18, tag)) break;
    }
    ST(12);
#pragma unroll 1
    for (bool exact = false;; exact = true) {
      BAR();
      ST(13);
      u32x4 p[3][2][2];
#pragma unroll
      for (int i = 0; i < 3; ++i)
#pragma unroll
        for (int hf = 0; hf < 2; ++hf) { p[i][hf][0] = lds_r128(pr[0] + i * 1024 + hf * 256); p[i][hf][1] = lds_r128(pr[1] + i * 1024 + hf * 256); }
      const f32x4 l0 = lds_r128f(la), l1 = lds_r128f(la + 16);
      SBAR();
#pragma unroll
      for (int i = 0; i < 3; ++i)
#pragma unroll
        for (int hf = 0; hf < 2; ++hf) lds_w64(dp[hf] + (L_FI - L_X) + i * 512, pack4(mfma16(pick, p[i][hf][1], mfma16(pick, p[i][hf][0], f32x4{0, 0, 0, 0}))));
      if (__builtin_expect(!low_sum(l0, l1, lt) || exact, 1)) break;
      BAR();
      BAR();
    }
    ST(14);
    const float linv[2] = {__builtin_amdgcn_rcpf(lt[0]), __builtin_amdgcn_rcpf(lt[1])};
    BAR();
    ST(15);
    u32x4 bf[6][2];
#pragma unroll
    for (int ks = 0; ks < 6; ++ks) { bf[ks][0] = lds_r128(fr[0] + (L_FI - L_X) + ks * 1024); bf[ks][1] = lds_r128(fr[1] + (L_FI - L_X) + ks * 1024); }
    SBAR();
    f32x4 d[3][2];
#pragma unroll
    for (int ks = 0; ks < 6; ++ks)
#pragma unroll
      for (int i = 0; i < 3; ++i) {
        d[i][0] = mfma16(wv[ks * 3 + i], bf[ks][0], ks ? d[i][0] : f32x4{0, 0, 0, 0});
        d[i][1] = mfma16(wv[ks * 3 + i], bf[ks][1], ks ? d[i][1] : f32x4{0, 0, 0, 0});
      }
#pragma unroll
    for (int i = 0; i < 3; ++i) {
      const u32x2 y0 = pack4(d[i][0] * linv[0] + x1[i][0]), y1 = pack4(d[i][1] * linv[1] + x1[i][1]);
      auto s0 = __builtin_amdgcn_permlane16_swap(y0[0], y1[0], false, false);
      auto s1 = __builtin_amdgcn_permlane16_swap(y0[1], y1[1], false, false);
      const u32x4 o = {s0[0], s1[0], s0[1], s1[1]};
      *(__attribute__((address_space(1))) u32x4*)(kc + oo + i * 32) = o;
    }
    ST(17);
    ST_OUT(ws, 12, 20);
  }
}

extern "C" void kernel_launch(void* const* d_in, const int* in_sizes, int n_in, void* d_out, int out_size, void* d_ws, size_t ws_size, hipStream_t stream) {
  (void)hipFuncSetAttribute((const void*)k_timesformer, hipFuncAttributeMaxDynamicSharedMemorySize, LDS_BYTES);
  k_timesformer<<<256, 512, LDS_BYTES, stream>>>((const char*)d_in[0], (const char*)d_in[1], (const char*)d_in[2], (const char*)d_in[3], (const char*)d_in[4],
                                                 (char*)d_ws, (char*)d_out);
}
```

```cpp
#include <hip/hip_runtime.h>
#include <hip/hip_bf16.h>
#include <type_traits>

typedef short s16x4 __attribute__((ext_vector_type(4)));
typedef __bf16 b16x8 __attribute__((ext_vector_type(8)));
typedef __bf16 b16x2 __attribute__((ext_vector_type(2)));
typedef float f32x2 __attribute__((ext_vector_type(2)));
typedef float f32x4 __attribute__((ext_vector_type(4)));
typedef float f32x16 __attribute__((ext_vector_type(16)));
typedef unsigned u32x2 __attribute__((ext_vector_type(2)));
typedef unsigned u32x4 __attribute__((ext_vector_type(4)));
typedef unsigned long long u64;
typedef u64 u64x4 __attribute__((ext_vector_type(4)));
typedef u64 u64x2 __attribute__((ext_vector_type(2)));
#define LAS __attribute__((address_space(3)))
#define DI __device__ __forceinline__
#define SBAR() __builtin_amdgcn_sched_barrier(0)

constexpr int TILE_B = 12288;
constexpr int FRAME_B = 393216;
constexpr unsigned WS_H2 = 0, WS_ST = 8 * FRAME_B, WS_WM = 16 * FRAME_B, WS_SEAL = WS_WM + 4 * 73728, WS_FLAG = WS_SEAL + 2 * 256 * 128, WS_FLAG2 = WS_FLAG + 8 * 2048, WS_EPOCH = WS_FLAG2 + 2048, WS_END = WS_EPOCH + 256;
constexpr float QSCALE = 0.07216878364870322f * 1.4426950408889634f;
constexpr int SC1 = 16;
constexpr unsigned POISON = 0xffffffffu;
constexpr unsigned CODE_BYTES = 22528;
constexpr int TILE0_DELAY = 2, JOB2_DELAY = 6, X_DELAY = 1, READ_DELAY = 3, VO_DELAY = 1, IDLE_DELAY = 6, WQ_SPLIT = 4;
constexpr int SPIN_CAP = 1 << 16;

#define ST_DECL
#define ST(k)
#define ST_OUT(ws, k0, k1)

DI float asf(unsigned u) { return __builtin_bit_cast(float, u); }
DI unsigned asu(float f) { return __builtin_bit_cast(unsigned, f); }
DI unsigned cvtpk(float lo, float hi) { f32x2 v = {lo, hi}; return __builtin_bit_cast(unsigned, __builtin_convertvector(v, b16x2)); }
DI float bflo(unsigned u) { return asf(u << 16); }
DI float bfhi(unsigned u) { return asf(u & 0xffff0000u); }
DI f32x16 mfma32(u32x4 a, u32x4 b, f32x16 c) { return __builtin_amdgcn_mfma_f32_32x32x16_bf16(__builtin_bit_cast(b16x8, a), __builtin_bit_cast(b16x8, b), c, 0, 0, 0); }
DI f32x4 mfma16(u32x4 a, u32x4 b, f32x4 c) { return __builtin_amdgcn_mfma_f32_16x16x32_bf16(__builtin_bit_cast(b16x8, a), __builtin_bit_cast(b16x8, b), c, 0, 0, 0); }
DI f32x4 mfma16k16(u32x2 a, u32x2 b, f32x4 c) { return __builtin_amdgcn_mfma_f32_16x16x16bf16_1k(__builtin_bit_cast(s16x4, a), __builtin_bit_cast(s16x4, b), c, 0, 0, 0); }
DI u32x2 pack4(f32x4 v) { u32x2 r = {cvtpk(v[0], v[1]), cvtpk(v[2], v[3])}; return r; }
DI u32x4 lds_r128(unsigned a) { return *(LAS u32x4*)(size_t)a; }
DI f32x4 lds_r128f(unsigned a) { return *(LAS f32x4*)(size_t)a; }
DI u32x2 lds_r64(unsigned a) { return *(LAS u32x2*)(size_t)a; }
DI void lds_w128(unsigned a, u32x4 v) { *(LAS u32x4*)(size_t)a = v; }
DI void lds_w128f(unsigned a, f32x4 v) { *(LAS f32x4*)(size_t)a = v; }
DI void lds_w64(unsigned a, u32x2 v) { *(LAS u32x2*)(size_t)a = v; }
DI void lds_w32(unsigned a, float v) { *(LAS float*)(size_t)a = v; }
DI u32x2 lds_tr(unsigned a) { return __builtin_bit_cast(u32x2, __builtin_amdgcn_ds_read_tr16_b64_v4i16((LAS s16x4*)(size_t)a)); }
DI u32x4 lds_tr2(unsigned a0, unsigned a1) { u32x2 x = lds_tr(a0), y = lds_tr(a1); u32x4 r = {x[0], x[1], y[0], y[1]}; return r; }
#define BAR() asm volatile("s_waitcnt lgkmcnt(0)\n\ts_barrier" ::: "memory")
#define BARV(n) asm volatile("s_waitcnt vmcnt(" #n ") lgkmcnt(0)\n\ts_barrier" ::: "memory")
template <int CTRL> DI float dpp_add(float v) { return v + asf(__builtin_amdgcn_update_dpp(0u, asu(v), CTRL, 0xf, 0xf, true)); }
DI float xor16_add(float v) { auto r = __builtin_amdgcn_permlane16_swap(asu(v), asu(v), false, false); return asf(r[0]) + asf(r[1]); }
DI float xor16_max(float v) { auto r = __builtin_amdgcn_permlane16_swap(asu(v), asu(v), false, false); return __builtin_amdgcn_fmed3f(asf(r[0]), asf(r[1]), __builtin_inff()); }
DI unsigned umax3(unsigned a, unsigned b, unsigned c) { return max(max(a, b), c); }
template <int N> DI bool poisoned(const u32x4 (&v)[N]) {
  unsigned m = 0;
#pragma unroll
  for (int i = 0; i < N; ++i) { m = umax3(m, v[i][0], v[i][1]); m = umax3(m, v[i][2], v[i][3]); }
  return __any(m == POISON);
}
DI u32x4 unpoison(u32x4 v) { u32x4 r = {min(v[0], POISON - 1), min(v[1], POISON - 1), min(v[2], POISON - 1), min(v[3], POISON - 1)}; return r; }
DI unsigned fi_pos(unsigned r, unsigned c8) {
  return (((r >> 4) * 6 + (c8 >> 2)) * 64 + (c8 & 3) * 16 + ((r & 15) ^ ((c8 & 1) | (((r >> 3) & 1) << 1) | ((r >> 4) << 2)))) * 16;
}
DI float row8_sum(float s) { s = dpp_add<0x124>(s); s = dpp_add<0x128>(s); return xor16_add(s); }
DI float sum24(const f32x2 (&v)[12]) {
  f32x2 s0 = v[0] + v[1], s1 = v[2] + v[3];
#pragma unroll
  for (int i = 4; i < 12; i += 2) { s0 += v[i]; s1 += v[i + 1]; }
  s0 += s1;
  return s0[0] + s0[1];
}
DI float sumsq24(const f32x2 (&v)[12]) {
  f32x2 q0 = v[0] * v[0], q1 = v[1] * v[1];
#pragma unroll
  for (int i = 2; i < 12; i += 2) { q0 = __builtin_elementwise_fma(v[i], v[i], q0); q1 = __builtin_elementwise_fma(v[i + 1], v[i + 1], q1); }
  q0 += q1;
  return q0[0] + q0[1];
}
DI f32x2 unpack2(unsigned u) { f32x2 r = {bflo(u), bfhi(u)}; return r; }

constexpr unsigned SLOT = 36864, L_X = 3 * SLOT, L_H = 4 * SLOT, L_N = L_H + TILE_B, L_L = L_N + 1024, L_PT = L_L + 1024, LDS_BYTES = 163840;
constexpr unsigned L_VI = L_X, L_SP = L_X + TILE_B, L_YI = L_X + 20480;
constexpr unsigned L_HQ = 24576, L_Q = L_H, L_FI = L_H;

#define DMA3(v0, v1, v2, sbase, ldsa) \
  asm volatile("s_mov_b32 m0, %4\n\ts_nop 0\n\tglobal_load_lds_dwordx4 %0, %3\n\tglobal_load_lds_dwordx4 %1, %3 offset:1024\n\tglobal_load_lds_dwordx4 %2, %3 offset:2048" \
               :: "v"(v0), "v"(v1), "v"(v2), "s"(sbase), "s"(ldsa) : "memory")
#define DMA1(v0, sbase, ldsa) \
  asm volatile("s_mov_b32 m0, %2\n\ts_nop 0\n\tglobal_load_lds_dwordx4 %0, %1" :: "v"(v0), "s"(sbase), "s"(ldsa) : "memory")

template <int OFF> DI void bdma(unsigned voff, u32x4 rs, unsigned soff) { asm volatile("buffer_load_dwordx4 %0, %1, %2 offen offset:%3 sc1 lds" :: "v"(voff), "s"(rs), "s"(soff), "i"(OFF) : "memory"); }
template <int OFF> DI void setm0(unsigned base) { asm volatile("s_add_u32 m0, %0, %1\n\ts_nop 0" :: "s"(base), "i"(OFF) : "memory", "scc"); }
template <int N> DI void waitvm() { asm volatile("s_waitcnt vmcnt(%0)" :: "i"(N) : "memory"); }
#define IC(x) std::integral_constant<int, (x)>{}
#define OPAQUE(v) asm volatile("" : "+v"(v))
template <class R> DI void st128(u32x4 v, R rs, unsigned voff, unsigned soff) { __builtin_amdgcn_raw_buffer_store_b128(v, rs, voff, soff, SC1); asm volatile("s_nop 1" :: "v"(v)); }
template <int CTRL> DI unsigned dpp_xor(unsigned v) { return v ^ __builtin_amdgcn_update_dpp(0u, v, CTRL, 0xf, 0xf, true); }
DI unsigned fold_xor(unsigned e, unsigned o) {
  auto r = __builtin_amdgcn_permlane32_swap(e, o, false, false);
  unsigned u = r[0] ^ r[1];
  auto q = __builtin_amdgcn_permlane16_swap(u, u, false, false);
  u = q[0] ^ q[1];
  u = dpp_xor<0x128>(u); u = dpp_xor<0x124>(u); u = dpp_xor<0x4e>(u); return dpp_xor<0xb1>(u);
}
DI bool seal_ok(unsigned e, unsigned o, u32x4 seal, bool has, u32x2 tag) {
  const unsigned bad = has ? (seal[1] ^ tag[0]) | (seal[3] ^ tag[1]) : 0u;
  return __builtin_amdgcn_ballot_w64((fold_xor(e ^ seal[0], o ^ seal[2]) | bad) != 0) == 0;
}
DI unsigned xor3(unsigned a, unsigned b, unsigned c) { return __builtin_amdgcn_bitop3_b32(a, b, c, 0x96); }
#define XOR_IN(e, o, v) do { e = xor3(e, (v)[0], (v)[2]); o = xor3(o, (v)[1], (v)[3]); } while (0)
#define MIXV6() _Pragma("unroll") for (int q_ = 0; q_ < 6; ++q_) { __builtin_amdgcn_sched_group_barrier(0x8, 1, 0); __builtin_amdgcn_sched_group_barrier(0x2, 1, 0); } SBAR()
template <int N> DI bool sealed(const u32x4 (&v)[N], u32x4 seal, bool has, u32x2 tag) {
  unsigned e = 0, o = 0;
#pragma unroll
  for (int i = 0; i < N; ++i) XOR_IN(e, o, v[i]);
  return seal_ok(e, o, seal, has, tag);
}
DI bool tags_ok(u32x4 f, u32x2 tag) { return f[0] == tag[0] && f[1] == tag[1] && f[2] == tag[0] && f[3] == tag[1]; }

__global__ __launch_bounds__(512, 2) void k_timesformer(const char* x_, const char* wqkv_t_, const char* wo_t_, const char* wqkv_s_, const char* wo_s_, char* ws_, char* out_) {
  unsigned dc;
  u64x4 ka; u64x2 kb; u64 kc;
  {
    u64 pc;
    asm volatile("s_load_dwordx8 %2, %6, 0x0\n\ts_load_dwordx4 %3, %6, 0x20\n\ts_load_dwordx2 %4, %6, 0x30\n\ts_getpc_b64 %1\n\tglobal_load_dword %0, %5, %1"
                 : "=v"(dc), "=&s"(pc), "=&s"(ka), "=&s"(kb), "=&s"(kc) : "v"(min(threadIdx.x * 128, CODE_BYTES)), "s"(__builtin_amdgcn_kernarg_segment_ptr()) : "memory");
  }
  constexpr unsigned lds0 = 0;
  const unsigned b = blockIdx.x;
  unsigned w8 = __builtin_amdgcn_readfirstlane(threadIdx.x >> 6);
  unsigned l = threadIdx.x & 63;
  const unsigned t = b & 7, qb = b >> 3;
  w8 = (w8 & 4) | ((w8 + qb / 6) & 3);
  ST_DECL;
  ST(0);
  const bool jqk = b < 144, job = b < 216;
  const unsigned jp = jqk ? b >> 1 : b - 144, jw = jp / 18, jks = (jp - jw * 18) / 3, jtau = jp - jw * 15 - jks * 3;
  const unsigned jrow = jks * 32 + (jqk ? (b & 1) * 16 : 0), jslot = WS_WM + (jqk ? b * 512 : 73728 + jp * 1024);
  unsigned fo[4][3];
  if (w8 < 4) {
#pragma unroll
    for (int i = 0; i < 3; ++i) { const unsigned p = i * 64 + l; fo[0][i] = p * 16 + (p >= 96 ? FRAME_B - 1536 : 0) + w8 * 2 * FRAME_B + b * 1536; fo[1][i] = fo[2][i] = fo[3][i] = 0; }
  } else {
    const unsigned ra = jrow * 1152 + (jqk ? 0 : 768), rb = jqk ? jtau * (16 * 1152) + 384 : ra + 16 * 1152;
#pragma unroll
    for (int i = 0; i < 3; ++i) {
      const unsigned pos = i * 64 + l, row = (pos * 2731) >> 16, o = row * 1152 + (((pos - row * 24) ^ row) * 16) - i * 1024;
      fo[0][i] = o + ra; fo[1][i] = o + (ra + 8 * 1152); fo[2][i] = o + rb; fo[3][i] = o + (rb + 8 * 1152);
    }
  }
  unsigned so_ = (l >> 1) * 384 + (l & 1) * 16 + jtau * 32;
  asm volatile("s_waitcnt lgkmcnt(0)" : "+s"(ka), "+s"(kb), "+s"(kc), "+v"(fo[0][0]), "+v"(fo[0][1]), "+v"(fo[0][2]), "+v"(fo[1][0]), "+v"(fo[1][1]), "+v"(fo[1][2]),
               "+v"(fo[2][0]), "+v"(fo[2][1]), "+v"(fo[2][2]), "+v"(fo[3][0]), "+v"(fo[3][1]), "+v"(fo[3][2]), "+v"(so_) :: "memory");
  char* const ws = (char*)kb[1];
  unsigned dw = 0;
  if (w8 < 4) {
    __builtin_amdgcn_s_sleep(X_DELAY);
#pragma unroll
    for (int i = 0; i < 3; ++i) DMA1(fo[0][i], ka[0], lds0 + L_YI + w8 * 3072 + i * 1024);
  } else if (w8 < 6 && job) {
    if (w8 == 5) { __builtin_amdgcn_s_sleep(JOB2_DELAY); if (!jqk) __builtin_amdgcn_s_sleep(2 * JOB2_DELAY); }
    const u64 wqkv = w8 == 4 ? ka[1] : ka[3], wo = w8 == 4 ? ka[2] : kb[0];
    const unsigned dst = (w8 - 4) * SLOT;
#pragma unroll
    for (int q = 0; q < 4; ++q) DMA3(fo[q][0], fo[q][1], fo[q][2], wqkv, dst + q * 3072);
    if (!jqk) {
#pragma unroll
      for (int i = 0; i < 6; ++i) DMA1(so_, wo + i * 12288, dst + (12 + i) * 1024);
    }
  } else if (w8 >= 6) {
    asm volatile("global_load_dword %0, %1, %2" : "=v"(dw) : "v"((qb * 18 + min(l, 17u)) * 128), "s"(w8 == 6 ? ka[2] : kb[0]) : "memory");
  }
  const auto rsWS = __builtin_amdgcn_make_buffer_rsrc((void*)ws, 0, WS_END, 0x00020000);
  const u32x4 pz = {POISON, POISON, POISON, POISON};
  u64 epoch;
  asm volatile("s_load_dwordx2 %0, %1, 0x0" : "=&s"(epoch) : "s"(ws + WS_EPOCH) : "memory");
  asm volatile("s_waitcnt lgkmcnt(0)" : "+s"(epoch) :: "memory");
  epoch += 1;
  const u32x2 tag = {(unsigned)epoch, (unsigned)(epoch >> 32)};
  asm volatile("" : "+s"(w8));
  const unsigned w = w8 & 3;
#define LANE_IDS const unsigned i16 = l & 15, g = l >> 4, r32 = l & 31, hh = l >> 5, tq = i16 >> 2, tp = l & 3
#define FRAG_POS unsigned fr[2], dp[2];                                                                       \
  fr[0] = L_X + fi_pos(i16, g); fr[1] = (fr[0] ^ 64) + 6144; dp[0] = L_X + fi_pos(i16, hh) + 8 * (g & 1) + w * 1536; dp[1] = (dp[0] ^ 64) + 6144; \
  OPAQUE(fr[0]); OPAQUE(fr[1]); OPAQUE(dp[0]); OPAQUE(dp[1])
  auto ldw = [&](u32x4 (&W)[18], const int m, const int k0, const int k1, const unsigned rot) {
#pragma unroll
    for (int kk = k0; kk < k1; ++kk) {
      const unsigned ks = kk + rot >= 6 ? kk + rot - 6 : kk + rot;
#pragma unroll
      for (int i = 0; i < 3; ++i) { SBAR(); W[kk * 3 + i] = __builtin_amdgcn_raw_buffer_load_b128(rsWS, l * 16 + i * 1024, WS_WM + m * 73728 + w * 18432 + ks * 3072, SC1); }
    }
  };
  auto ld_seal = [&](const int m) {
    return __builtin_amdgcn_raw_buffer_load_b128(rsWS, l < ((m & 1) ? 18 : 36) ? l * 128 : WS_END, WS_SEAL + (m >> 1) * 32768 + ((m & 1) ? 144 + 18 * w : 36 * w) * 128, SC1);
  };
  const unsigned rot = qb % 6;
  auto h_frags = [&](u32x4 (&H)[6][2], const unsigned (&fr)[2]) {
#pragma unroll
    for (int kk = 0; kk < 6; ++kk) {
      const unsigned ks = kk + rot >= 6 ? kk + rot - 6 : kk + rot;
      H[kk][0] = lds_r128(fr[0] + ks * 1024 + (L_H - L_X)); H[kk][1] = lds_r128(fr[1] + ks * 1024 + (L_H - L_X));
    }
  };
  auto low_sum = [&](const f32x4 a, const f32x4 c, float (&lt)[2]) {
    lt[0] = (a[0] + a[2]) + (c[0] + c[2]);
    lt[1] = (a[1] + a[3]) + (c[1] + c[3]);
    return (bool)__any(lt[0] < 8.7e-19f || lt[1] < 8.7e-19f);
  };

  if (w8 < 4) {
    const unsigned nl = l & 3, j = (l >> 2) & 7, tt = 2 * w + (l >> 5), r = nl * 8 + tt;
    unsigned rp = L_X + fi_pos(r, j);
    OPAQUE(rp);
    waitvm<0>();
    asm volatile("" :: "v"(dc));
    f32x2 xv[12];
    float mu1;
    {
      u32x4 h[3], xr[3];
#pragma unroll
      for (int i = 0; i < 3; ++i) xr[i] = lds_r128(lds0 + L_YI + tt * 1536 + nl * 384 + j * 16 + i * 128);
#pragma unroll
      for (int i = 0; i < 12; ++i) xv[i] = unpack2(xr[i >> 2][i & 3]);
      mu1 = row8_sum(sum24(xv)) * (1.0f / 192.0f);
#pragma unroll
      for (int i = 0; i < 12; ++i) xv[i] -= mu1;
      const float rs = __builtin_amdgcn_rsqf(row8_sum(sumsq24(xv)) * (1.0f / 192.0f) + 1e-5f);
#pragma unroll
      for (int i = 0; i < 12; ++i) { const f32x2 n = xv[i] * rs; h[i >> 2][i & 3] = cvtpk(n[0], n[1]); }
#pragma unroll
      for (int i = 0; i < 3; ++i) lds_w128(rp + (L_H - L_X) + i * 2048, h[i]);
    }
    SBAR();
    OPAQUE(l);
    unsigned so = tt * FRAME_B + (j >> 1) * 1024 + ((t ^ ((j >> 1) & 1)) * 128) + (((j & 1) ^ (t >> 2)) * 64) + nl * 16;
    const unsigned sso = qb * TILE_B;
    LANE_IDS;
    FRAG_POS;
    unsigned sto = j == 0 ? (tt * 1024 + nl) * 8 : WS_END;
    unsigned sa = L_SP + l * 16;
    unsigned keep = (hh == (i16 >> 3)) ? ~0u : 0u;
    OPAQUE(sto); OPAQUE(so); OPAQUE(sa); OPAQUE(keep);
    ST(1);
    BAR();
    ST(6);
    {
      u32x4 H[6][2], W[18];
      h_frags(H, fr);
      asm volatile("s_barrier" ::: "memory");
      ST(2);
#pragma unroll 1
      for (int sp = 0;; ++sp) {
        const u32x4 seal = ld_seal(3);
        ldw(W, 3, 0, 6, rot);
        SBAR();
        ST(3);
        if (sp == 0) asm volatile("s_barrier" ::: "memory");
        f32x4 vp[3][2], sq[4], e;
        float rd;
        u32x2 pt[2];
        unsigned he = 0, ho = 0;
#pragma unroll
        for (int ww = 0; ww < 4; ++ww) sq[ww] = lds_r128f(sa + ww * 1024);
        SBAR();
        ST(4);
#pragma unroll
        for (int kk = 0; kk < 6; ++kk) {
#pragma unroll
          for (int i = 0; i < 3; ++i) {
            vp[i][0] = mfma16(H[kk][0], W[kk * 3 + i], kk ? vp[i][0] : f32x4{0, 0, 0, 0});
            vp[i][1] = mfma16(H[kk][1], W[kk * 3 + i], kk ? vp[i][1] : f32x4{0, 0, 0, 0});
            XOR_IN(he, ho, W[kk * 3 + i]);
          }
          if (kk == 1) { e = (sq[0] + sq[1]) + (sq[2] + sq[3]); rd = fmaxf(fmaxf(e[0], e[1]), fmaxf(e[2], e[3])); OPAQUE(rd); }
          if (kk == 2) { e -= xor16_max(rd); OPAQUE(e); }
          if (kk == 3) {
#pragma unroll
            for (int k = 0; k < 4; ++k) e[k] = __builtin_amdgcn_exp2f(e[k]);
            rd = (e[0] + e[1]) + (e[2] + e[3]);
            OPAQUE(rd);
          }
          if (kk == 4) { pt[0] = pack4(e * __builtin_amdgcn_rcpf(xor16_add(rd))); OPAQUE(pt[0]); }
          if (kk == 5) {
#pragma unroll
            for (int k = 0; k < 2; ++k) {
              auto q = __builtin_amdgcn_permlane32_swap(pt[0][k], pt[0][k], false, false);
              pt[1][k] = (q[0] ^ q[1] ^ pt[0][k]) & keep;
              pt[0][k] &= keep;
            }
            OPAQUE(pt[0]); OPAQUE(pt[1]);
          }
#pragma unroll
          for (int q = 0; q < 6; ++q) { __builtin_amdgcn_sched_group_barrier(0x8, 1, 0); __builtin_amdgcn_sched_group_barrier(0x402, 3, 0); }
          SBAR();
        }
        OPAQUE(he); OPAQUE(ho);
        ST(7);
        f32x4 y[3][2];
#pragma unroll
        for (int hf = 0; hf < 2; ++hf)
#pragma unroll
          for (int i = 0; i < 3; ++i) y[i][hf] = mfma16k16(pack4(vp[i][hf]), pt[hf], f32x4{0, 0, 0, 0});
        SBAR();
#pragma unroll
        for (int hf = 0; hf < 2; ++hf)
#pragma unroll
          for (int i = 0; i < 3; ++i) lds_w64(dp[hf] + (L_YI - L_X) + i * 512, pack4(y[i][hf]));
        if (__builtin_expect(seal_ok(he, ho, seal, l < 18, tag), 1) || sp >= SPIN_CAP) break;
        asm volatile("" ::: "memory");
      }
    }
    ST(9);
    BAR();
    {
      u32x4 y[3], h[3];
#pragma unroll
      for (int i = 0; i < 3; ++i) y[i] = lds_r128(rp + (L_YI - L_X) + i * 2048);
#pragma unroll
      for (int i = 0; i < 12; ++i) xv[i] += unpack2(y[i >> 2][i & 3]);
      const float mu = row8_sum(sum24(xv)) * (1.0f / 192.0f), sq = row8_sum(sumsq24(xv));
      const float var = fmaxf(fmaf(sq, 1.0f / 192.0f, -mu * mu), 0.f) + 1e-5f;
      const float rs = __builtin_amdgcn_rsqf(var), nb = -mu * rs;
#pragma unroll
      for (int i = 0; i < 3; ++i) {
#pragma unroll
        for (int e = 0; e < 4; ++e) { const f32x2 n = xv[4 * i + e] * rs + nb; h[i][e] = cvtpk(n[0], n[1]); }
        if (i == 0) { OPAQUE(h[0]); ST(5); asm volatile("s_barrier" ::: "memory"); }
        st128(h[i], rsWS, so, WS_H2 + sso + i * 4096);
        SBAR();
      }
      const u32x2 stat = {asu(mu + mu1), asu(var)};
      __builtin_amdgcn_raw_buffer_store_b64(stat, rsWS, sto, WS_ST + b * 32, SC1);
    }
    ST(10);
    const unsigned ring = lds0 + w * SLOT;
    unsigned kr0 = ring + (r32 >> 2) * 128 + (hh ^ (r32 >> 4)) * 64 + (r32 & 3) * 16, kr1 = kr0 ^ 128;
    unsigned vr0 = ring + (g & 1) * 1024 + (hh ^ (g & 1)) * 128 + (tp >> 1) * 64 + tq * 16 + 8 * (tp & 1), vr1 = (vr0 ^ 64) + 512;
    unsigned lo[3] = {l * 16, l * 16 + 4096, l * 16 + 8192}, pw = ring + l * 16, lw = l < 16 ? L_L + (l * 4 + w) * 8 : L_L + 512 + l * 8;
    OPAQUE(kr0); OPAQUE(kr1); OPAQUE(vr0); OPAQUE(vr1); OPAQUE(lo[0]); OPAQUE(lo[1]); OPAQUE(lo[2]); OPAQUE(pw); OPAQUE(lw);
    const u64 hbu = (u64)(ws + WS_H2 + (size_t)t * FRAME_B);
    const u32x4 rs = {(unsigned)hbu, (unsigned)(hbu >> 32) & 0xffffu, (unsigned)FRAME_B, 0x00020000u};
    unsigned toff[8];
#pragma unroll
    for (int i = 0; i < 8; ++i) { toff[i] = (8 * w + ((i + qb) & 7)) * TILE_B; asm volatile("" : "+s"(toff[i])); }
    u32x4 sel;
    { const unsigned one = (tp == (g & 1)) ? 0x3f803f80u : 0u; sel = u32x4{one, one, one, one}; OPAQUE(sel); }
    auto piece = [&](auto Tc, auto Pc) {
      constexpr int T = decltype(Tc)::value, p = decltype(Pc)::value;
      if constexpr (T < 8) {
        if constexpr (p % 4 == 0) setm0<(T % 3) * TILE_B + p * 1024>(ring);
        bdma<(p % 4) * 1024>(lo[p / 4], rs, toff[T]);
      }
    };
#define P6(T, a) piece(IC(T), IC(a)); piece(IC(T), IC(a + 1)); piece(IC(T), IC(a + 2)); piece(IC(T), IC(a + 3)); piece(IC(T), IC(a + 4)); piece(IC(T), IC(a + 5));
    auto read_valid = [&](u32x4 (&KK)[12], const unsigned dst, unsigned off) {
      for (int sp = 0; sp < SPIN_CAP; ++sp) {
#pragma unroll
        for (int k = 0; k < 12; ++k) KK[k] = lds_r128(((k & 1) ? kr1 : kr0) + dst + k * 1024);
        if (__builtin_expect(!poisoned(KK), 1)) break;
#pragma unroll 1
        for (int k = 0; k < 12; ++k) { setm0<0>(ring + dst + k * 1024); bdma<0>(lo[0], rs, off + k * 1024); }
        waitvm<0>();
      }
    };
    unsigned qa = lds0 + L_Q + lo[0], na = lds0 + L_N + r32 * 16;
    OPAQUE(qa); OPAQUE(na);
    BARV(0);
    __builtin_amdgcn_s_sleep(TILE0_DELAY);
    P6(0, 0) P6(0, 6)
    if (w == 0 && l == 0) __builtin_amdgcn_raw_buffer_store_b64(tag, rsWS, 0, WS_FLAG2 + b * 8, SC1);
    ST(11);
    BAR();
    u32x4 K0[12];
    if (w == 0) waitvm<1>(); else waitvm<0>();
    ST(12);
    read_valid(K0, 0, toff[0]);
    ST(13);
    piece(IC(1), IC(0)); piece(IC(1), IC(1)); piece(IC(1), IC(2)); piece(IC(1), IC(3));
    BAR();
    ST(14);
    u32x4 Q[12];
    float mref;
    {
      const f32x4 n0 = lds_r128f(na), n1 = lds_r128f(na + 512);
#pragma unroll
      for (int s = 0; s < 6; ++s) Q[s] = lds_r128(qa + s * 1024);
      SBAR();
      const f32x4 s = n0 + n1;
      mref = __builtin_amdgcn_sqrtf(((s[0] + s[1]) + (s[2] + s[3])) * (192.0f * 1.02f));
      OPAQUE(mref);
      SBAR();
#pragma unroll
      for (int s = 6; s < 12; ++s) Q[s] = lds_r128(qa + s * 1024);
      SBAR();
    }
    constexpr int KW = 4, VW = 4;
    bool safe = false, exact = false, late = false;
#pragma unroll 1
    for (;;) {
      f32x16 S[2], O[6], negm;
      u32x4 K[12], V[12], P[2][2];
      f32x4 lsum = {0, 0, 0, 0};
      float pe[2][2];
#pragma unroll
      for (int i = 0; i < 16; ++i) negm[i] = -mref;
#define RK(k, T) K[k] = lds_r128((((k) & 1) ? kr1 : kr0) + ((T) % 3) * TILE_B + (k) * 1024);
#define RV(u, T) { const unsigned a_ = (((u) & 1) ? vr1 : vr0) + ((T) % 3) * TILE_B + ((u) >> 1) * 2048; V[u] = lds_tr2(a_, a_ + 256); }
      auto sm = [&](auto Tc, auto Hc, auto Gc) {
        constexpr int T = decltype(Tc)::value, H = decltype(Hc)::value, G = decltype(Gc)::value;
        if constexpr (G < 8) pe[(G >> 1) & 1][G & 1] = __builtin_amdgcn_exp2f(S[T & 1][8 * H + G]);
        if constexpr (G >= 2 && G <= 8 && G % 2 == 0) { constexpr int q = G / 2 - 1; P[T & 1][H][q] = cvtpk(pe[q & 1][0], pe[q & 1][1]); }
        if constexpr (G == 10) lsum = mfma16(sel, P[T & 1][H], lsum);
      };
      auto put_o = [&](const int un) {
        u32x4 pk;
#pragma unroll
        for (int e = 0; e < 4; ++e) pk[e] = cvtpk(O[un >> 1][8 * (un & 1) + 2 * e], O[un >> 1][8 * (un & 1) + 2 * e + 1]);
        lds_w128(pw + un * 1024, pk);
      };
      auto qkphase = [&](auto Ic) {
        constexpr int I = decltype(Ic)::value;
        auto step = [&](auto Kc) {
          constexpr int k = decltype(Kc)::value;
          if constexpr (I + 1 < 8) {
            S[(I + 1) & 1] = mfma32(I < 0 ? K0[k] : K[k], Q[k], k == 0 ? negm : S[(I + 1) & 1]);
            if constexpr (k + KW < 12 && I >= 0) RK(k + KW, I + 1)
          }
          if constexpr (I < 0 && k < 8) piece(IC(1), IC(4 + k));
          if constexpr (I >= 0) { if constexpr (k & 1) piece(IC(I + 2), IC(6 + k / 2)); if constexpr (k < 11) sm(IC(I), IC(1), IC(k)); }
          if constexpr (I >= 0 && I < 7 && k >= 12 - VW) RV(k - (12 - VW), I)
          if constexpr (I == 7 && k < VW) RV(k, I)
          SBAR();
        };
        step(IC(0)); step(IC(1)); step(IC(2)); step(IC(3)); step(IC(4)); step(IC(5)); step(IC(6)); step(IC(7)); step(IC(8)); step(IC(9)); step(IC(10)); step(IC(11));
      };
      auto pvphase = [&](auto Ic) {
        constexpr int I = decltype(Ic)::value;
        auto step = [&](auto Uc) {
          constexpr int u = decltype(Uc)::value;
          if constexpr (I >= 0) {
            f32x16 z;
#pragma unroll
            for (int e = 0; e < 16; ++e) z[e] = 0.f;
            O[u >> 1] = mfma32(V[u], P[I & 1][u & 1], (I == 0 && !(u & 1)) ? z : O[u >> 1]);
            if constexpr (u & 1) piece(IC(I + 3), IC(u / 2));
            if constexpr (u + VW < 12) RV(u + VW, I)
          }
          if constexpr (I + 1 < 8 && u >= 1) sm(IC(I + 1), IC(0), IC(u - 1));
          if constexpr (I < 0 && (u & 1)) piece(IC(2), IC(u / 2));
          if constexpr (I + 2 < 8) {
            if constexpr (u == 12 - KW) waitvm<(I < 0 ? 12 : I + 3 < 8 ? 6 + (12 - KW) / 2 : 6)>();
            if constexpr (u >= 12 - KW) RK(u - (12 - KW), I + 2)
            if constexpr (u == 11) waitvm<(I + 3 < 8 ? 6 : 0)>();
          }
          if constexpr (I == 7 && u >= 3) {
            put_o(u - 3);
          }
          SBAR();
        };
        step(IC(0)); step(IC(1)); step(IC(2)); step(IC(3)); step(IC(4)); step(IC(5)); step(IC(6)); step(IC(7)); step(IC(8)); step(IC(9)); step(IC(10)); step(IC(11));
      };
#define STALE(v) (__builtin_expect(!safe && __any((v) != (v)), 0))
      bool done = false;
      do {
        qkphase(IC(-1));
        ST(15);
        pvphase(IC(-1));
        ST(16);
        qkphase(IC(0)); pvphase(IC(0)); qkphase(IC(1)); pvphase(IC(1)); qkphase(IC(2));
        const float seen = lsum[0];
        pvphase(IC(2));
        if (STALE(seen)) break;
        qkphase(IC(3)); pvphase(IC(3)); qkphase(IC(4)); pvphase(IC(4)); qkphase(IC(5));
        if (!late) asm volatile("s_barrier" ::: "memory");
        late = true;
        pvphase(IC(5)); qkphase(IC(6)); pvphase(IC(6)); qkphase(IC(7)); pvphase(IC(7));
        if (STALE(lsum[0])) break;
        { const u32x2 ls = {asu(lsum[0]), asu(lsum[1])}; lds_w64(lw, ls); }
        put_o(9); put_o(10); put_o(11);
        done = true;
      } while (false);
#pragma unroll
      for (int k = 0; k < 12; ++k) asm volatile("" : "=v"(K0[k]));
      if (__builtin_expect(done, 1)) {
        ST(17);
        BAR();
        ST(18);
        float lt[2];
        unsigned ll = lo[0];
        OPAQUE(ll);
        ll >>= 4;
        if (__builtin_expect(!low_sum(lds_r128f(L_L + (ll & 15) * 32), lds_r128f(L_L + (ll & 15) * 32 + 16), lt) || exact, 1)) break;
        BAR();
        float mx = -3.0e38f;
#pragma unroll 1
        for (unsigned i = 0; i < 8; ++i) {
#pragma unroll 1
          for (int k = 0; k < 12; ++k) { setm0<0>(ring + k * 1024); bdma<0>(lo[0], rs, (8 * w + i) * TILE_B + k * 1024); }
          waitvm<0>();
          f32x16 s;
#pragma unroll
          for (int e = 0; e < 16; ++e) s[e] = 0.f;
#pragma unroll
          for (int k = 0; k < 12; ++k) s = mfma32(lds_r128(((k & 1) ? kr1 : kr0) + k * 1024), Q[k], s);
#pragma unroll
          for (int e = 0; e < 16; ++e) mx = fmaxf(mx, s[e]);
        }
        lds_w32(L_N + ((ll & 31) * 8 + w * 2 + (ll >> 5)) * 4, mx);
        BAR();
        const f32x4 c = lds_r128f(L_N + (ll & 31) * 32), d = lds_r128f(L_N + (ll & 31) * 32 + 16);
        mref = fmaxf(fmaxf(fmaxf(c[0], c[1]), fmaxf(c[2], c[3])), fmaxf(fmaxf(d[0], d[1]), fmaxf(d[2], d[3])));
        exact = true;
      } else {
        unsigned ll = lo[0];
        OPAQUE(ll);
        for (int sp = 0; sp < SPIN_CAP; ++sp) {
          asm volatile("" ::: "memory");
          const u32x2 f = __builtin_amdgcn_raw_buffer_load_b64(rsWS, ll >> 1, WS_FLAG2 + w * 512, SC1);
          if (__all(f[0] == tag[0] && f[1] == tag[1])) break;
        }
      }
      safe = true;
      asm volatile("s_waitcnt vmcnt(0) lgkmcnt(0)" ::: "memory");
      P6(0, 0) P6(0, 6)
      waitvm<0>();
      piece(IC(1), IC(0)); piece(IC(1), IC(1)); piece(IC(1), IC(2)); piece(IC(1), IC(3));
#pragma unroll
      for (int k = 0; k < 12; ++k) K0[k] = lds_r128(((k & 1) ? kr1 : kr0) + k * 1024);
    }
    ST(19);
    BAR();
    ST_OUT(ws, 0, 12);
  } else {
    LANE_IDS;
    auto job_run = [&](const unsigned at, const unsigned slot, const unsigned sealat, auto younger) {
      unsigned ja[2] = {at + i16 * 384 + ((g ^ (l & 7)) * 16), at + i16 * 384 + (((4 + g) ^ (l & 7)) * 16)}, jb = at + 12288 + (8 * g + tq) * 32 + tp * 8;
      OPAQUE(ja[0]); OPAQUE(ja[1]); OPAQUE(jb);
      waitvm<decltype(younger)::value>();
      ST(2);
      u32x4 a[6], c[6];
#pragma unroll
      for (int kk = 0; kk < 6; ++kk) { a[kk] = lds_r128(ja[kk & 1] + (kk >> 1) * 128); c[kk] = lds_r128(ja[kk & 1] + (kk >> 1) * 128 + 6144); }
      const unsigned o = (hh * 16 + i16) * 16 + (g & 1) * 8;
      f32x4 d = {0, 0, 0, 0};
      u32x2 r;
      if (jqk) {
#pragma unroll
        for (int kk = 0; kk < 6; ++kk) d = mfma16(a[kk], c[kk], d);
        r = pack4(d * QSCALE);
        __builtin_amdgcn_raw_buffer_store_b64(r, rsWS, o, slot, SC1);
      } else {
        f32x4 e = {0, 0, 0, 0};
#pragma unroll
        for (int kk = 0; kk < 6; ++kk) {
          const u32x4 bb = lds_tr2(jb + kk * 1024, jb + kk * 1024 + 128);
          d = mfma16(a[kk], bb, d); e = mfma16(c[kk], bb, e);
        }
        const u32x2 r0 = pack4(d), r1 = pack4(e);
        __builtin_amdgcn_raw_buffer_store_b64(r0, rsWS, o, slot, SC1);
        __builtin_amdgcn_raw_buffer_store_b64(r1, rsWS, o + 512, slot, SC1);
        r = r0 ^ r1;
      }
      const u32x2 half = {fold_xor(r[0], r[1]), hh ? tag[1] : tag[0]};
      __builtin_amdgcn_raw_buffer_store_b64(half, rsWS, r32 == 0 ? hh * 8 : WS_END, sealat, SC1);
    };
    FRAG_POS;
    if (w == 0 || (w == 1 && jqk)) {
      ST(1);
      if (job) job_run(w * SLOT, jslot + (1 - w) * 2 * 73728, WS_SEAL + (1 - w) * 32768 + b * 128, IC(0));
      ST(3);
      if (w == 0) {
        __builtin_amdgcn_raw_buffer_store_b32(POISON, rsWS, l * 4, WS_ST + (t * 1024 + qb * 32) * 8, SC1);
        const unsigned p0 = WS_H2 + t * FRAME_B + (jqk ? qb * 8 : 144 + (qb - 27) * 48) * 1024, np = jqk ? 2 : job ? 0 : 12;
#pragma unroll 1
        for (unsigned i = 0; i < np; ++i) {
#pragma unroll
          for (int q = 0; q < 4; ++q) __builtin_amdgcn_raw_buffer_store_b128(pz, rsWS, l * 16 + q * 1024, p0 + i * 4096, SC1);
        }
        asm volatile("s_nop 1" :: "v"(pz));
        if (jqk) { waitvm<9>(); __builtin_amdgcn_s_sleep(READ_DELAY); } else if (job) __builtin_amdgcn_s_sleep(VO_DELAY); else { waitvm<0>(); __builtin_amdgcn_s_sleep(IDLE_DELAY); }
      }
    }
    u32x4 wq[18], wv[18];
    {
      u32x4 H[6][2];
      u32x2 hs[3][2];
      unsigned spa = hh == (i16 >> 3) ? L_SP + w * 1024 + l * 16 : L_SP + 4096 + l * 16;
      OPAQUE(spa);
      __builtin_amdgcn_s_setprio(3);
      ST(4);
      BAR();
#pragma unroll 1
      for (int sp = 0;; ++sp) {
        const u32x4 seal = ld_seal(2);
        ldw(wq, 2, 0, 3, rot);
        SBAR();
        h_frags(H, fr);
#pragma unroll
        for (int i = 0; i < 3; ++i) { hs[i][0] = lds_r64(dp[0] + (L_H - L_X) + i * 512); hs[i][1] = lds_r64(dp[1] + (L_H - L_X) + i * 512); }
        ldw(wq, 2, 3, 6, rot);
        if (sp == 0) {
          if (w == 0) {
            waitvm<19>();
            if (l < 8) __builtin_amdgcn_raw_buffer_store_b64(tag, rsWS, l * 2048, WS_FLAG + b * 8, SC1);
          }
          asm volatile("s_barrier" ::: "memory");
        }
        SBAR();
        f32x4 d[3][2], st[2];
        unsigned he = 0, ho = 0;
#pragma unroll
        for (int kk = 0; kk < 6; ++kk) {
#pragma unroll
          for (int i = 0; i < 3; ++i) {
            d[i][0] = mfma16(wq[kk * 3 + i], H[kk][0], kk ? d[i][0] : f32x4{0, 0, 0, 0});
            d[i][1] = mfma16(wq[kk * 3 + i], H[kk][1], kk ? d[i][1] : f32x4{0, 0, 0, 0});
            XOR_IN(he, ho, wq[kk * 3 + i]);
          }
          MIXV6();
        }
        ST(19);
        const bool ok = seal_ok(he, ho, seal, l < 36, tag);
#pragma unroll
        for (int hf = 0; hf < 2; ++hf) {
          st[hf] = f32x4{0, 0, 0, 0};
#pragma unroll
          for (int i = 0; i < 3; ++i) st[hf] = mfma16k16(hs[i][hf], pack4(d[i][hf]), st[hf]);
          lds_w128f(spa ^ (hf * 512), st[hf]);
        }
        if (__builtin_expect(ok, 1) || sp >= SPIN_CAP) break;
        asm volatile("" ::: "memory");
      }
      asm volatile("" :: "v"(dc), "v"(dw));
    }
    ST(10);
    BAR();
    __builtin_amdgcn_s_setprio(0);
    u32x4 seal = ld_seal(0);
    {
      ldw(wq, 0, 0, WQ_SPLIT, 0);
      u32x2 fl = __builtin_amdgcn_raw_buffer_load_b64(rsWS, l * 8, WS_FLAG + t * 2048 + w * 512, SC1);
      ST(5);
      BAR();
      ldw(wq, 0, WQ_SPLIT, 6, 0);
      if (__builtin_expect(!__all(fl[0] == tag[0] && fl[1] == tag[1]), 0)) {
        for (int sp = 0; sp < SPIN_CAP; ++sp) {
          asm volatile("" ::: "memory");
          const u32x2 f2 = __builtin_amdgcn_raw_buffer_load_b64(rsWS, l * 8, WS_FLAG + t * 2048 + w * 512, SC1);
          if (__all(f2[0] == tag[0] && f2[1] == tag[1])) break;
        }
      }
      ST(18);
      BAR();
    }
    unsigned hqa[2][2], own[2][2];
#pragma unroll
    for (int hf = 0; hf < 2; ++hf) {
      const unsigned in = lds0 + L_HQ + ((4 * hf + tq) ^ hh) * 128 + ((g & 1) ^ hf) * 64 + tp * 16;
      hqa[hf][0] = in + hh * 1024; hqa[hf][1] = in + 2048 + hh * (SLOT - 2048);
      OPAQUE(hqa[hf][0]); OPAQUE(hqa[hf][1]);
    }
    const unsigned tok = 16 * (g & 1) + i16;
    unsigned nw = lds0 + L_N + l * 16 + w * 4;
    OPAQUE(nw);
    unsigned qw = L_Q + (3 * w * 64 + hh * 32 + i16) * 16 + 8 * (g & 1);
    own[0][0] = w * SLOT + L_HQ + (tq ^ (w & 1)) * 128 + hh * 64 + tp * 16 + 8 * (g & 1); own[0][1] = (own[0][0] ^ 64) + 512;
    own[1][0] = own[0][0] ^ 128; own[1][1] = own[0][1] ^ 128;
    OPAQUE(qw); OPAQUE(own[0][0]); OPAQUE(own[0][1]); OPAQUE(own[1][0]); OPAQUE(own[1][1]);
    if (b == 0 && w == 3 && l == 0) __builtin_amdgcn_raw_buffer_store_b64(tag, rsWS, 0, WS_EPOCH, SC1);
    for (int sp = 0; sp < SPIN_CAP && !sealed(wq, seal, l < 36, tag); ++sp) {
      asm volatile("" ::: "memory");
      seal = ld_seal(0);
      ldw(wq, 0, 0, 6, 0);
    }
    if (w == 1 && job && !jqk) job_run(SLOT, jslot, WS_SEAL + b * 128, IC(0));
    ST(6);
    BAR();
    ST(7);
    u32x4 hq[3];
    {
      const u64 hbu = (u64)(ws + WS_H2 + (size_t)t * FRAME_B);
      const u32x4 rs = {(unsigned)hbu, (unsigned)(hbu >> 32) & 0xffffu, (unsigned)FRAME_B, 0x00020000u};
      for (int sp = 0; sp < SPIN_CAP; ++sp) {
        setm0<L_HQ>(lds0 + w * SLOT);
        bdma<0>(l * 16, rs, qb * TILE_B + w * 3072); bdma<1024>(l * 16, rs, qb * TILE_B + w * 3072); bdma<2048>(l * 16, rs, qb * TILE_B + w * 3072);
        waitvm<0>();
#pragma unroll
        for (int i = 0; i < 3; ++i) { const u32x2 u0 = lds_r64(own[i & 1][0] + i * 1024), u1 = lds_r64(own[i & 1][1] + i * 1024); hq[i] = u32x4{u0[0], u0[1], u1[0], u1[1]}; }
        if (!poisoned(hq)) break;
      }
    }
    ST(8);
    BAR();
    ST(9);
    auto gemm = [&](const u32x4 (&wf)[18], const u32x4 (&bf)[6][2], auto&& fin) {
      f32x4 d[3][2];
#pragma unroll
      for (int i = 0; i < 3; ++i) {
#pragma unroll
        for (int ks = 0; ks < 6; ++ks) {
          d[i][0] = mfma16(wf[ks * 3 + i], bf[ks][0], ks ? d[i][0] : f32x4{0, 0, 0, 0});
          d[i][1] = mfma16(wf[ks * 3 + i], bf[ks][1], ks ? d[i][1] : f32x4{0, 0, 0, 0});
          if (ks == 0) SBAR();
        }
        if (i > 0) fin(i - 1, d[i - 1]);
        SBAR();
      }
      fin(2, d[2]);
    };
    {
      u32x4 bf[6][2];
#pragma unroll
      for (int ks = 0; ks < 6; ++ks)
#pragma unroll
        for (int hf = 0; hf < 2; ++hf) {
          if (ks == 3) { hqa[hf][0] += 2 * SLOT; hqa[hf][1] += 2 * SLOT; }
          bf[ks][hf] = lds_r128(ks % 3 == 1 ? hqa[hf][1] : hqa[hf][0] + (ks % 3 == 2 ? SLOT + 1024 : 0));
        }
      SBAR();
      f32x2 n[2] = {{0.f, 0.f}, {0.f, 0.f}};
      gemm(wq, bf, [&](const int i, const f32x4 (&d)[2]) {
#pragma unroll
        for (int hf = 0; hf < 2; ++hf) {
          lds_w64(qw + i * 1024 + hf * 256, pack4(d[hf]));
          const f32x2 a = {d[hf][0], d[hf][1]}, c = {d[hf][2], d[hf][3]};
          n[hf] = __builtin_elementwise_fma(a, a, n[hf]); n[hf] = __builtin_elementwise_fma(c, c, n[hf]);
        }
      });
      auto rr = __builtin_amdgcn_permlane16_swap(asu(n[0][0] + n[0][1]), asu(n[1][0] + n[1][1]), false, false);
      lds_w32(nw, asf(rr[0]) + asf(rr[1]));
    }
    ST(11);
    BAR();
    unsigned oo = (t * 1024 + qb * 32 + tok) * 384 + (6 * w + hh) * 16;
    OPAQUE(oo);
    float lt[2];
    u32x4 pick;
#pragma unroll
    for (int e = 0; e < 4; ++e) {
      const unsigned d = 8 * (e >> 1) + 4 * (g & 1) + 2 * (e & 1);
      pick[e] = (i16 == d ? 0x3f80u : 0u) | (i16 == d + 1 ? 0x3f800000u : 0u);
    }
    unsigned pr[2], la = lds0 + L_L + i16 * 32;
    pr[0] = lds0 + hh * SLOT + 3 * w * 1024 + ((g & 1) * 32 + i16) * 16; pr[1] = pr[0] + 2 * SLOT;
    OPAQUE(pick); OPAQUE(pr[0]); OPAQUE(pr[1]); OPAQUE(la);
    f32x4 x1[3][2];
    {
      __builtin_amdgcn_s_sleep(40);
      u32x2 st[2];
      for (int sp = 0; sp < SPIN_CAP; ++sp) {
        asm volatile("" ::: "memory");
        st[0] = __builtin_amdgcn_raw_buffer_load_b64(rsWS, i16 * 8, WS_ST + (t * 1024 + qb * 32) * 8, SC1);
        st[1] = __builtin_amdgcn_raw_buffer_load_b64(rsWS, i16 * 8 + 128, WS_ST + (t * 1024 + qb * 32) * 8, SC1);
        if (!__any(max(max(st[0][0], st[0][1]), max(st[1][0], st[1][1])) == POISON)) break;
      }
#pragma unroll
      for (int hf = 0; hf < 2; ++hf) {
        const float mu = asf(st[hf][0]), sd = __builtin_amdgcn_sqrtf(asf(st[hf][1]));
#pragma unroll
        for (int i = 0; i < 3; ++i) {
          const f32x4 h = {bflo(hq[i][2 * hf]), bfhi(hq[i][2 * hf]), bflo(hq[i][2 * hf + 1]), bfhi(hq[i][2 * hf + 1])};
          x1[i][hf] = h * sd + mu;
          OPAQUE(x1[i][hf]);
        }
      }
    }
    BAR();
    ST(16);
    for (int sp = 0; sp < SPIN_CAP; ++sp) {
      asm volatile("" ::: "memory");
      seal = ld_seal(1);
      ldw(wv, 1, 0, 6, 0);
      if (sealed(wv, seal, l < 18, tag)) break;
    }
    ST(12);
#pragma unroll 1
    for (bool exact = false;; exact = true) {
      BAR();
      ST(13);
      u32x4 p[3][2][2];
#pragma unroll
      for (int i = 0; i < 3; ++i)
#pragma unroll
        for (int hf = 0; hf < 2; ++hf) { p[i][hf][0] = lds_r128(pr[0] + i * 1024 + hf * 256); p[i][hf][1] = lds_r128(pr[1] + i * 1024 + hf * 256); }
      const f32x4 l0 = lds_r128f(la), l1 = lds_r128f(la + 16);
      SBAR();
#pragma unroll
      for (int i = 0; i < 3; ++i)
#pragma unroll
        for (int hf = 0; hf < 2; ++hf) lds_w64(dp[hf] + (L_FI - L_X) + i * 512, pack4(mfma16(pick, p[i][hf][1], mfma16(pick, p[i][hf][0], f32x4{0, 0, 0, 0}))));
      if (__builtin_expect(!low_sum(l0, l1, lt) || exact, 1)) break;
      BAR();
      BAR();
    }
    ST(14);
    const float linv[2] = {__builtin_amdgcn_rcpf(lt[0]), __builtin_amdgcn_rcpf(lt[1])};
    BAR();
    ST(15);
    u32x4 bf[6][2];
#pragma unroll
    for (int ks = 0; ks < 6; ++ks) { bf[ks][0] = lds_r128(fr[0] + (L_FI - L_X) + ks * 1024); bf[ks][1] = lds_r128(fr[1] + (L_FI - L_X) + ks * 1024); }
    SBAR();
    f32x4 d[3][2];
#pragma unroll
    for (int ks = 0; ks < 6; ++ks)
#pragma unroll
      for (int i = 0; i < 3; ++i) {
        d[i][0] = mfma16(wv[ks * 3 + i], bf[ks][0], ks ? d[i][0] : f32x4{0, 0, 0, 0});
        d[i][1] = mfma16(wv[ks * 3 + i], bf[ks][1], ks ? d[i][1] : f32x4{0, 0, 0, 0});
      }
#pragma unroll
    for (int i = 0; i < 3; ++i) {
      const u32x2 y0 = pack4(d[i][0] * linv[0] + x1[i][0]), y1 = pack4(d[i][1] * linv[1] + x1[i][1]);
      auto s0 = __builtin_amdgcn_permlane16_swap(y0[0], y1[0], false, false);
      auto s1 = __builtin_amdgcn_permlane16_swap(y0[1], y1[1], false, false);
      const u32x4 o = {s0[0], s1[0], s0[1], s1[1]};
      *(__attribute__((address_space(1))) u32x4*)(kc + oo + i * 32) = o;
    }
    ST(17);
    ST_OUT(ws, 12, 20);
  }
}

extern "C" void kernel_launch(void* const* d_in, const int* in_sizes, int n_in, void* d_out, int out_size, void* d_ws, size_t ws_size, hipStream_t stream) {
  (void)hipFuncSetAttribute((const void*)k_timesformer, hipFuncAttributeMaxDynamicSharedMemorySize, LDS_BYTES);
  k_timesformer<<<256, 512, LDS_BYTES, stream>>>((const char*)d_in[0], (const char*)d_in[1], (const char*)d_in[2], (const char*)d_in[3], (const char*)d_in[4],
                                                 (char*)d_ws, (char*)d_out);
}
```

```cpp
#include <hip/hip_runtime.h>
#include <hip/hip_bf16.h>
#include <type_traits>

typedef short s16x4 __attribute__((ext_vector_type(4)));
typedef __bf16 b16x8 __attribute__((ext_vector_type(8)));
typedef __bf16 b16x2 __attribute__((ext_vector_type(2)));
typedef float f32x2 __attribute__((ext_vector_type(2)));
typedef float f32x4 __attribute__((ext_vector_type(4)));
typedef float f32x16 __attribute__((ext_vector_type(16)));
typedef unsigned u32x2 __attribute__((ext_vector_type(2)));
typedef unsigned u32x4 __attribute__((ext_vector_type(4)));
typedef unsigned long long u64;
typedef u64 u64x4 __attribute__((ext_vector_type(4)));
typedef u64 u64x2 __attribute__((ext_vector_type(2)));
#define LAS __attribute__((address_space(3)))
#define DI __device__ __forceinline__
#define SBAR() __builtin_amdgcn_sched_barrier(0)

constexpr int TILE_B = 12288;
constexpr int FRAME_B = 393216;
constexpr unsigned WS_H2 = 0, WS_ST = 8 * FRAME_B, WS_WM = 16 * FRAME_B, WS_SEAL = WS_WM + 4 * 73728, WS_FLAG = WS_SEAL + 2 * 256 * 128, WS_FLAG2 = WS_FLAG + 8 * 2048, WS_EPOCH = WS_FLAG2 + 2048, WS_END = WS_EPOCH + 256;
constexpr float QSCALE = 0.07216878364870322f * 1.4426950408889634f;
constexpr int SC1 = 16;
constexpr unsigned POISON = 0xffffffffu;
constexpr unsigned CODE_BYTES = 22528;
constexpr int TILE0_DELAY = 2, JOB2_DELAY = 6, X_DELAY = 1, READ_DELAY = 6, VO_DELAY = 6, IDLE_DELAY = 6, WQ_SPLIT = 4;
constexpr int SPIN_CAP = 1 << 16;


constexpr int PROBE = 17, ROLE = 1; constexpr size_t WS_T0 = 32u << 20;
#define ST_DECL u64 st_ = 0, t0_ = 0
#define ST(k) do { if ((k) == PROBE || (k) == 0) { SBAR(); u64 t_; asm volatile("s_memrealtime %0\n\ts_waitcnt lgkmcnt(0)" : "=s"(t_) :: "memory"); SBAR(); \
    if ((k) == PROBE) st_ = t_; \
    if ((k) == 0) t0_ = t_; } STO_##k } while (0)
#define STO_(k)
#define STO_0
#define STO_1
#define STO_2
#define STO_3
#define STO_4
#define STO_5
#define STO_6
#define STO_7
#define STO_8
#define STO_9
#define STO_10
#define STO_11
#define STO_12
#define STO_13
#define STO_14
#define STO_15
#define STO_16 if (threadIdx.x == 0) __hip_atomic_store((__attribute__((address_space(1))) u64*)(ws + WS_T0) + blockIdx.x, t0_, __ATOMIC_RELAXED, __HIP_MEMORY_SCOPE_AGENT);
#define STO_17
#define STO_18
#define STO_19
#define ST_OUT(ws, k0, k1) do { if (ROLE == ((k0) != 0)) { u64 m_ = ~0ull; \
    for (int q_ = 0; q_ < 4; ++q_) { u64 v_ = __hip_atomic_load((__attribute__((address_space(1))) u64*)(ws + WS_T0) + (threadIdx.x & 63) * 4 + q_, __ATOMIC_RELAXED, __HIP_MEMORY_SCOPE_AGENT); m_ = v_ < m_ ? v_ : m_; } \
    for (int o_ = 32; o_; o_ >>= 1) { u64 v_ = __shfl_xor(m_, o_); m_ = v_ < m_ ? v_ : m_; } \
    long long d_ = (long long)(st_ - m_); if (d_ < 0) d_ = 0; if (d_ > 5000) d_ = 5000; \
    const u64 e_ = m_ + 4000 + 4 * d_; while (__builtin_amdgcn_s_memrealtime() < e_) __builtin_amdgcn_s_sleep(1); } } while (0)


DI float asf(unsigned u) { return __builtin_bit_cast(float, u); }
DI unsigned asu(float f) { return __builtin_bit_cast(unsigned, f); }
DI unsigned cvtpk(float lo, float hi) { f32x2 v = {lo, hi}; return __builtin_bit_cast(unsigned, __builtin_convertvector(v, b16x2)); }
DI float bflo(unsigned u) { return asf(u << 16); }
DI float bfhi(unsigned u) { return asf(u & 0xffff0000u); }
DI f32x16 mfma32(u32x4 a, u32x4 b, f32x16 c) { return __builtin_amdgcn_mfma_f32_32x32x16_bf16(__builtin_bit_cast(b16x8, a), __builtin_bit_cast(b16x8, b), c, 0, 0, 0); }
DI f32x4 mfma16(u32x4 a, u32x4 b, f32x4 c) { return __builtin_amdgcn_mfma_f32_16x16x32_bf16(__builtin_bit_cast(b16x8, a), __builtin_bit_cast(b16x8, b), c, 0, 0, 0); }
DI f32x4 mfma16k16(u32x2 a, u32x2 b, f32x4 c) { return __builtin_amdgcn_mfma_f32_16x16x16bf16_1k(__builtin_bit_cast(s16x4, a), __builtin_bit_cast(s16x4, b), c, 0, 0, 0); }
DI u32x2 pack4(f32x4 v) { u32x2 r = {cvtpk(v[0], v[1]), cvtpk(v[2], v[3])}; return r; }
DI u32x4 lds_r128(unsigned a) { return *(LAS u32x4*)(size_t)a; }
DI f32x4 lds_r128f(unsigned a) { return *(LAS f32x4*)(size_t)a; }
DI u32x2 lds_r64(unsigned a) { return *(LAS u32x2*)(size_t)a; }
DI void lds_w128(unsigned a, u32x4 v) { *(LAS u32x4*)(size_t)a = v; }
DI void lds_w128f(unsigned a, f32x4 v) { *(LAS f32x4*)(size_t)a = v; }
DI void lds_w64(unsigned a, u32x2 v) { *(LAS u32x2*)(size_t)a = v; }
DI void lds_w32(unsigned a, float v) { *(LAS float*)(size_t)a = v; }
DI u32x2 lds_tr(unsigned a) { return __builtin_bit_cast(u32x2, __builtin_amdgcn_ds_read_tr16_b64_v4i16((LAS s16x4*)(size_t)a)); }
DI u32x4 lds_tr2(unsigned a0, unsigned a1) { u32x2 x = lds_tr(a0), y = lds_tr(a1); u32x4 r = {x[0], x[1], y[0], y[1]}; return r; }
#define BAR() asm volatile("s_waitcnt lgkmcnt(0)\n\ts_barrier" ::: "memory")
#define BARV(n) asm volatile("s_waitcnt vmcnt(" #n ") lgkmcnt(0)\n\ts_barrier" ::: "memory")
template <int CTRL> DI float dpp_add(float v) { return v + asf(__builtin_amdgcn_update_dpp(0u, asu(v), CTRL, 0xf, 0xf, true)); }
DI float xor16_add(float v) { auto r = __builtin_amdgcn_permlane16_swap(asu(v), asu(v), false, false); return asf(r[0]) + asf(r[1]); }
DI float xor16_max(float v) { auto r = __builtin_amdgcn_permlane16_swap(asu(v), asu(v), false, false); return __builtin_amdgcn_fmed3f(asf(r[0]), asf(r[1]), __builtin_inff()); }
DI unsigned umax3(unsigned a, unsigned b, unsigned c) { return max(max(a, b), c); }
template <int N> DI bool poisoned(const u32x4 (&v)[N]) {
  unsigned m = 0;
#pragma unroll
  for (int i = 0; i < N; ++i) { m = umax3(m, v[i][0], v[i][1]); m = umax3(m, v[i][2], v[i][3]); }
  return __any(m == POISON);
}
DI u32x4 unpoison(u32x4 v) { u32x4 r = {min(v[0], POISON - 1), min(v[1], POISON - 1), min(v[2], POISON - 1), min(v[3], POISON - 1)}; return r; }
DI unsigned fi_pos(unsigned r, unsigned c8) {
  return (((r >> 4) * 6 + (c8 >> 2)) * 64 + (c8 & 3) * 16 + ((r & 15) ^ ((c8 & 1) | (((r >> 3) & 1) << 1) | ((r >> 4) << 2)))) * 16;
}
DI float row8_sum(float s) { s = dpp_add<0x124>(s); s = dpp_add<0x128>(s); return xor16_add(s); }
DI float sum24(const f32x2 (&v)[12]) {
  f32x2 s0 = v[0] + v[1], s1 = v[2] + v[3];
#pragma unroll
  for (int i = 4; i < 12; i += 2) { s0 += v[i]; s1 += v[i + 1]; }
  s0 += s1;
  return s0[0] + s0[1];
}
DI float sumsq24(const f32x2 (&v)[12]) {
  f32x2 q0 = v[0] * v[0], q1 = v[1] * v[1];
#pragma unroll
  for (int i = 2; i < 12; i += 2) { q0 = __builtin_elementwise_fma(v[i], v[i], q0); q1 = __builtin_elementwise_fma(v[i + 1], v[i + 1], q1); }
  q0 += q1;
  return q0[0] + q0[1];
}
DI f32x2 unpack2(unsigned u) { f32x2 r = {bflo(u), bfhi(u)}; return r; }

constexpr unsigned SLOT = 36864, L_X = 3 * SLOT, L_H = 4 * SLOT, L_N = L_H + TILE_B, L_L = L_N + 1024, L_PT = L_L + 1024, LDS_BYTES = 163840;
constexpr unsigned L_VI = L_X, L_SP = L_X + TILE_B, L_YI = L_X + 20480;
constexpr unsigned L_HQ = 24576, L_Q = L_H, L_FI = L_H;

#define DMA3(v0, v1, v2, sbase, ldsa) \
  asm volatile("s_mov_b32 m0, %4\n\ts_nop 0\n\tglobal_load_lds_dwordx4 %0, %3\n\tglobal_load_lds_dwordx4 %1, %3 offset:1024\n\tglobal_load_lds_dwordx4 %2, %3 offset:2048" \
               :: "v"(v0), "v"(v1), "v"(v2), "s"(sbase), "s"(ldsa) : "memory")
#define DMA1(v0, sbase, ldsa) \
  asm volatile("s_mov_b32 m0, %2\n\ts_nop 0\n\tglobal_load_lds_dwordx4 %0, %1" :: "v"(v0), "s"(sbase), "s"(ldsa) : "memory")

template <int OFF> DI void bdma(unsigned voff, u32x4 rs, unsigned soff) { asm volatile("buffer_load_dwordx4 %0, %1, %2 offen offset:%3 sc1 lds" :: "v"(voff), "s"(rs), "s"(soff), "i"(OFF) : "memory"); }
template <int OFF> DI void setm0(unsigned base) { asm volatile("s_add_u32 m0, %0, %1\n\ts_nop 0" :: "s"(base), "i"(OFF) : "memory", "scc"); }
template <int N> DI void waitvm() { asm volatile("s_waitcnt vmcnt(%0)" :: "i"(N) : "memory"); }
#define IC(x) std::integral_constant<int, (x)>{}
#define OPAQUE(v) asm volatile("" : "+v"(v))
template <class R> DI void st128(u32x4 v, R rs, unsigned voff, unsigned soff) { __builtin_amdgcn_raw_buffer_store_b128(v, rs, voff, soff, SC1); asm volatile("s_nop 1" :: "v"(v)); }
template <int CTRL> DI unsigned dpp_xor(unsigned v) { return v ^ __builtin_amdgcn_update_dpp(0u, v, CTRL, 0xf, 0xf, true); }
DI unsigned fold_xor(unsigned e, unsigned o) {
  auto r = __builtin_amdgcn_permlane32_swap(e, o, false, false);
  unsigned u = r[0] ^ r[1];
  auto q = __builtin_amdgcn_permlane16_swap(u, u, false, false);
  u = q[0] ^ q[1];
  u = dpp_xor<0x128>(u); u = dpp_xor<0x124>(u); u = dpp_xor<0x4e>(u); return dpp_xor<0xb1>(u);
}
DI bool seal_ok(unsigned e, unsigned o, u32x4 seal, bool has, u32x2 tag) {
  const unsigned bad = has ? (seal[1] ^ tag[0]) | (seal[3] ^ tag[1]) : 0u;
  return __builtin_amdgcn_ballot_w64((fold_xor(e ^ seal[0], o ^ seal[2]) | bad) != 0) == 0;
}
DI unsigned xor3(unsigned a, unsigned b, unsigned c) { return __builtin_amdgcn_bitop3_b32(a, b, c, 0x96); }
#define XOR_IN(e, o, v) do { e = xor3(e, (v)[0], (v)[2]); o = xor3(o, (v)[1], (v)[3]); } while (0)
#define MIXV6() _Pragma("unroll") for (int q_ = 0; q_ < 6; ++q_) { __builtin_amdgcn_sched_group_barrier(0x8, 1, 0); __builtin_amdgcn_sched_group_barrier(0x2, 1, 0); } SBAR()
template <int N> DI bool sealed(const u32x4 (&v)[N], u32x4 seal, bool has, u32x2 tag) {
  unsigned e = 0, o = 0;
#pragma unroll
  for (int i = 0; i < N; ++i) XOR_IN(e, o, v[i]);
  return seal_ok(e, o, seal, has, tag);
}
DI bool tags_ok(u32x4 f, u32x2 tag) { return f[0] == tag[0] && f[1] == tag[1] && f[2] == tag[0] && f[3] == tag[1]; }

__global__ __launch_bounds__(512, 2) void k_timesformer(const char* x_, const char* wqkv_t_, const char* wo_t_, const char* wqkv_s_, const char* wo_s_, char* ws_, char* out_) {
  unsigned dc;
  u64x4 ka; u64x2 kb; u64 kc;
  {
    u64 pc;
    asm volatile("s_load_dwordx8 %2, %6, 0x0\n\ts_load_dwordx4 %3, %6, 0x20\n\ts_load_dwordx2 %4, %6, 0x30\n\ts_getpc_b64 %1\n\tglobal_load_dword %0, %5, %1"
                 : "=v"(dc), "=&s"(pc), "=&s"(ka), "=&s"(kb), "=&s"(kc) : "v"(min(threadIdx.x * 128, CODE_BYTES)), "s"(__builtin_amdgcn_kernarg_segment_ptr()) : "memory");
  }
  constexpr unsigned lds0 = 0;
  const unsigned b = blockIdx.x;
  unsigned w8 = __builtin_amdgcn_readfirstlane(threadIdx.x >> 6);
  unsigned l = threadIdx.x & 63;
  const unsigned t = b & 7, qb = b >> 3;
  w8 = (w8 & 4) | ((w8 + qb / 6) & 3);
  ST_DECL;
  ST(0);
  const bool jqk = b < 144, job = b < 216;
  const unsigned jp = jqk ? b >> 1 : b - 144, jw = jp / 18, jks = (jp - jw * 18) / 3, jtau = jp - jw * 15 - jks * 3;
  const unsigned jrow = jks * 32 + (jqk ? (b & 1) * 16 : 0), jslot = WS_WM + (jqk ? b * 512 : 73728 + jp * 1024);
  unsigned fo[4][3];
  if (w8 < 4) {
#pragma unroll
    for (int i = 0; i < 3; ++i) { const unsigned p = i * 64 + l; fo[0][i] = p * 16 + (p >= 96 ? FRAME_B - 1536 : 0) + w8 * 2 * FRAME_B + b * 1536; fo[1][i] = fo[2][i] = fo[3][i] = 0; }
  } else {
    const unsigned ra = jrow * 1152 + (jqk ? 0 : 768), rb = jqk ? jtau * (16 * 1152) + 384 : ra + 16 * 1152;
#pragma unroll
    for (int i = 0; i < 3; ++i) {
      const unsigned pos = i * 64 + l, row = (pos * 2731) >> 16, o = row * 1152 + (((pos - row * 24) ^ row) * 16) - i * 1024;
      fo[0][i] = o + ra; fo[1][i] = o + (ra + 8 * 1152); fo[2][i] = o + rb; fo[3][i] = o + (rb + 8 * 1152);
    }
  }
  unsigned so_ = (l >> 1) * 384 + (l & 1) * 16 + jtau * 32;
  asm volatile("s_waitcnt lgkmcnt(0)" : "+s"(ka), "+s"(kb), "+s"(kc), "+v"(fo[0][0]), "+v"(fo[0][1]), "+v"(fo[0][2]), "+v"(fo[1][0]), "+v"(fo[1][1]), "+v"(fo[1][2]),
               "+v"(fo[2][0]), "+v"(fo[2][1]), "+v"(fo[2][2]), "+v"(fo[3][0]), "+v"(fo[3][1]), "+v"(fo[3][2]), "+v"(so_) :: "memory");
  char* const ws = (char*)kb[1];
  if (w8 < 4) {
    __builtin_amdgcn_s_sleep(X_DELAY);
#pragma unroll
    for (int i = 0; i < 3; ++i) DMA1(fo[0][i], ka[0], lds0 + L_YI + w8 * 3072 + i * 1024);
  } else if (w8 < 6 && job) {
    if (w8 == 5) { __builtin_amdgcn_s_sleep(JOB2_DELAY); if (!jqk) __builtin_amdgcn_s_sleep(2 * JOB2_DELAY); }
    const u64 wqkv = w8 == 4 ? ka[1] : ka[3], wo = w8 == 4 ? ka[2] : kb[0];
    const unsigned dst = (w8 - 4) * SLOT;
#pragma unroll
    for (int q = 0; q < 4; ++q) DMA3(fo[q][0], fo[q][1], fo[q][2], wqkv, dst + q * 3072);
    if (!jqk) {
#pragma unroll
      for (int i = 0; i < 6; ++i) DMA1(so_, wo + i * 12288, dst + (12 + i) * 1024);
    }
  }
  const auto rsWS = __builtin_amdgcn_make_buffer_rsrc((void*)ws, 0, WS_END, 0x00020000);
  const u32x4 pz = {POISON, POISON, POISON, POISON};
  u64 epoch;
  asm volatile("s_load_dwordx2 %0, %1, 0x0" : "=&s"(epoch) : "s"(ws + WS_EPOCH) : "memory");
  asm volatile("s_waitcnt lgkmcnt(0)" : "+s"(epoch) :: "memory");
  epoch += 1;
  const u32x2 tag = {(unsigned)epoch, (unsigned)(epoch >> 32)};
  asm volatile("" : "+s"(w8));
  const unsigned w = w8 & 3;
#define LANE_IDS const unsigned i16 = l & 15, g = l >> 4, r32 = l & 31, hh = l >> 5, tq = i16 >> 2, tp = l & 3
#define FRAG_POS unsigned fr[2], dp[2];                                                                       \
  fr[0] = L_X + fi_pos(i16, g); fr[1] = (fr[0] ^ 64) + 6144; dp[0] = L_X + fi_pos(i16, hh) + 8 * (g & 1) + w * 1536; dp[1] = (dp[0] ^ 64) + 6144; \
  OPAQUE(fr[0]); OPAQUE(fr[1]); OPAQUE(dp[0]); OPAQUE(dp[1])
  auto ldw = [&](u32x4 (&W)[18], const int m, const int k0, const int k1, const unsigned rot) {
#pragma unroll
    for (int kk = k0; kk < k1; ++kk) {
      const unsigned ks = kk + rot >= 6 ? kk + rot - 6 : kk + rot;
#pragma unroll
      for (int i = 0; i < 3; ++i) { SBAR(); W[kk * 3 + i] = __builtin_amdgcn_raw_buffer_load_b128(rsWS, l * 16 + i * 1024, WS_WM + m * 73728 + w * 18432 + ks * 3072, SC1); }
    }
  };
  auto ld_seal = [&](const int m) {
    return __builtin_amdgcn_raw_buffer_load_b128(rsWS, l < ((m & 1) ? 18 : 36) ? l * 128 : WS_END, WS_SEAL + (m >> 1) * 32768 + ((m & 1) ? 144 + 18 * w : 36 * w) * 128, SC1);
  };
  const unsigned rot = qb % 6;
  auto h_frags = [&](u32x4 (&H)[6][2], const unsigned (&fr)[2]) {
#pragma unroll
    for (int kk = 0; kk < 6; ++kk) {
      const unsigned ks = kk + rot >= 6 ? kk + rot - 6 : kk + rot;
      H[kk][0] = lds_r128(fr[0] + ks * 1024 + (L_H - L_X)); H[kk][1] = lds_r128(fr[1] + ks * 1024 + (L_H - L_X));
    }
  };
  auto low_sum = [&](const f32x4 a, const f32x4 c, float (&lt)[2]) {
    lt[0] = (a[0] + a[2]) + (c[0] + c[2]);
    lt[1] = (a[1] + a[3]) + (c[1] + c[3]);
    return (bool)__any(lt[0] < 8.7e-19f || lt[1] < 8.7e-19f);
  };

  if (w8 < 4) {
    const unsigned nl = l & 3, j = (l >> 2) & 7, tt = 2 * w + (l >> 5), r = nl * 8 + tt;
    unsigned rp = L_X + fi_pos(r, j);
    OPAQUE(rp);
    waitvm<0>();
    asm volatile("" :: "v"(dc));
    f32x2 xv[12];
    float mu1;
    {
      u32x4 h[3], xr[3];
#pragma unroll
      for (int i = 0; i < 3; ++i) xr[i] = lds_r128(lds0 + L_YI + tt * 1536 + nl * 384 + j * 16 + i * 128);
#pragma unroll
      for (int i = 0; i < 12; ++i) xv[i] = unpack2(xr[i >> 2][i & 3]);
      mu1 = row8_sum(sum24(xv)) * (1.0f / 192.0f);
#pragma unroll
      for (int i = 0; i < 12; ++i) xv[i] -= mu1;
      const float rs = __builtin_amdgcn_rsqf(row8_sum(sumsq24(xv)) * (1.0f / 192.0f) + 1e-5f);
#pragma unroll
      for (int i = 0; i < 12; ++i) { const f32x2 n = xv[i] * rs; h[i >> 2][i & 3] = cvtpk(n[0], n[1]); }
#pragma unroll
      for (int i = 0; i < 3; ++i) lds_w128(rp + (L_H - L_X) + i * 2048, h[i]);
    }
    SBAR();
    OPAQUE(l);
    unsigned so = tt * FRAME_B + (j >> 1) * 1024 + ((t ^ ((j >> 1) & 1)) * 128) + (((j & 1) ^ (t >> 2)) * 64) + nl * 16;
    const unsigned sso = qb * TILE_B;
    LANE_IDS;
    FRAG_POS;
    unsigned sto = j == 0 ? (tt * 1024 + nl) * 8 : WS_END;
    unsigned sa = L_SP + l * 16;
    unsigned keep = (hh == (i16 >> 3)) ? ~0u : 0u;
    OPAQUE(sto); OPAQUE(so); OPAQUE(sa); OPAQUE(keep);
    ST(1);
    BAR();
    ST(6);
    {
      u32x4 H[6][2], W[18];
      h_frags(H, fr);
      asm volatile("s_barrier" ::: "memory");
      ST(2);
#pragma unroll 1
      for (int sp = 0;; ++sp) {
        const u32x4 seal = ld_seal(3);
        ldw(W, 3, 0, 6, rot);
        SBAR();
        ST(3);
        if (sp == 0) asm volatile("s_barrier" ::: "memory");
        f32x4 vp[3][2], sq[4], e;
        float rd;
        u32x2 pt[2];
        unsigned he = 0, ho = 0;
#pragma unroll
        for (int ww = 0; ww < 4; ++ww) sq[ww] = lds_r128f(sa + ww * 1024);
        SBAR();
        ST(4);
#pragma unroll
        for (int kk = 0; kk < 6; ++kk) {
#pragma unroll
          for (int i = 0; i < 3; ++i) {
            vp[i][0] = mfma16(H[kk][0], W[kk * 3 + i], kk ? vp[i][0] : f32x4{0, 0, 0, 0});
            vp[i][1] = mfma16(H[kk][1], W[kk * 3 + i], kk ? vp[i][1] : f32x4{0, 0, 0, 0});
            XOR_IN(he, ho, W[kk * 3 + i]);
          }
          if (kk == 1) { e = (sq[0] + sq[1]) + (sq[2] + sq[3]); rd = fmaxf(fmaxf(e[0], e[1]), fmaxf(e[2], e[3])); OPAQUE(rd); }
          if (kk == 2) { e -= xor16_max(rd); OPAQUE(e); }
          if (kk == 3) {
#pragma unroll
            for (int k = 0; k < 4; ++k) e[k] = __builtin_amdgcn_exp2f(e[k]);
            rd = (e[0] + e[1]) + (e[2] + e[3]);
            OPAQUE(rd);
          }
          if (kk == 4) { pt[0] = pack4(e * __builtin_amdgcn_rcpf(xor16_add(rd))); OPAQUE(pt[0]); }
          if (kk == 5) {
#pragma unroll
            for (int k = 0; k < 2; ++k) {
              auto q = __builtin_amdgcn_permlane32_swap(pt[0][k], pt[0][k], false, false);
              pt[1][k] = (q[0] ^ q[1] ^ pt[0][k]) & keep;
              pt[0][k] &= keep;
            }
            OPAQUE(pt[0]); OPAQUE(pt[1]);
          }
#pragma unroll
          for (int q = 0; q < 6; ++q) { __builtin_amdgcn_sched_group_barrier(0x8, 1, 0); __builtin_amdgcn_sched_group_barrier(0x402, 3, 0); }
          SBAR();
        }
        OPAQUE(he); OPAQUE(ho);
        ST(7);
        f32x4 y[3][2];
#pragma unroll
        for (int hf = 0; hf < 2; ++hf)
#pragma unroll
          for (int i = 0; i < 3; ++i) y[i][hf] = mfma16k16(pack4(vp[i][hf]), pt[hf], f32x4{0, 0, 0, 0});
        SBAR();
#pragma unroll
        for (int hf = 0; hf < 2; ++hf)
#pragma unroll
          for (int i = 0; i < 3; ++i) lds_w64(dp[hf] + (L_YI - L_X) + i * 512, pack4(y[i][hf]));
        if (__builtin_expect(seal_ok(he, ho, seal, l < 18, tag), 1) || sp >= SPIN_CAP) break;
        asm volatile("" ::: "memory");
      }
    }
    ST(9);
    BAR();
    {
      u32x4 y[3], h[3];
#pragma unroll
      for (int i = 0; i < 3; ++i) y[i] = lds_r128(rp + (L_YI - L_X) + i * 2048);
#pragma unroll
      for (int i = 0; i < 12; ++i) xv[i] += unpack2(y[i >> 2][i & 3]);
      const float mu = row8_sum(sum24(xv)) * (1.0f / 192.0f), sq = row8_sum(sumsq24(xv));
      const float var = fmaxf(fmaf(sq, 1.0f / 192.0f, -mu * mu), 0.f) + 1e-5f;
      const float rs = __builtin_amdgcn_rsqf(var), nb = -mu * rs;
#pragma unroll
      for (int i = 0; i < 3; ++i) {
#pragma unroll
        for (int e = 0; e < 4; ++e) { const f32x2 n = xv[4 * i + e] * rs + nb; h[i][e] = cvtpk(n[0], n[1]); }
        if (i == 0) { OPAQUE(h[0]); ST(5); asm volatile("s_barrier" ::: "memory"); }
        st128(h[i], rsWS, so, WS_H2 + sso + i * 4096);
        SBAR();
      }
      const u32x2 stat = {asu(mu + mu1), asu(var)};
      __builtin_amdgcn_raw_buffer_store_b64(stat, rsWS, sto, WS_ST + b * 32, SC1);
    }
    ST(10);
    const unsigned ring = lds0 + w * SLOT;
    unsigned kr0 = ring + (r32 >> 2) * 128 + (hh ^ (r32 >> 4)) * 64 + (r32 & 3) * 16, kr1 = kr0 ^ 128;
    unsigned vr0 = ring + (g & 1) * 1024 + (hh ^ (g & 1)) * 128 + (tp >> 1) * 64 + tq * 16 + 8 * (tp & 1), vr1 = (vr0 ^ 64) + 512;
    unsigned lo[3] = {l * 16, l * 16 + 4096, l * 16 + 8192}, pw = ring + l * 16, lw = l < 16 ? L_L + (l * 4 + w) * 8 : L_L + 512 + l * 8;
    OPAQUE(kr0); OPAQUE(kr1); OPAQUE(vr0); OPAQUE(vr1); OPAQUE(lo[0]); OPAQUE(lo[1]); OPAQUE(lo[2]); OPAQUE(pw); OPAQUE(lw);
    const u64 hbu = (u64)(ws + WS_H2 + (size_t)t * FRAME_B);
    const u32x4 rs = {(unsigned)hbu, (unsigned)(hbu >> 32) & 0xffffu, (unsigned)FRAME_B, 0x00020000u};
    unsigned toff[8];
#pragma unroll
    for (int i = 0; i < 8; ++i) { toff[i] = (8 * w + ((i + qb) & 7)) * TILE_B; asm volatile("" : "+s"(toff[i])); }
    u32x4 sel;
    { const unsigned one = (tp == (g & 1)) ? 0x3f803f80u : 0u; sel = u32x4{one, one, one, one}; OPAQUE(sel); }
    auto piece = [&](auto Tc, auto Pc) {
      constexpr int T = decltype(Tc)::value, p = decltype(Pc)::value;
      if constexpr (T < 8) {
        if constexpr (p % 4 == 0) setm0<(T % 3) * TILE_B + p * 1024>(ring);
        bdma<(p % 4) * 1024>(lo[p / 4], rs, toff[T]);
      }
    };
#define P6(T, a) piece(IC(T), IC(a)); piece(IC(T), IC(a + 1)); piece(IC(T), IC(a + 2)); piece(IC(T), IC(a + 3)); piece(IC(T), IC(a + 4)); piece(IC(T), IC(a + 5));
    auto read_valid = [&](u32x4 (&KK)[12], const unsigned dst, unsigned off) {
      for (int sp = 0; sp < SPIN_CAP; ++sp) {
#pragma unroll
        for (int k = 0; k < 12; ++k) KK[k] = lds_r128(((k & 1) ? kr1 : kr0) + dst + k * 1024);
        if (__builtin_expect(!poisoned(KK), 1)) break;
#pragma unroll 1
        for (int k = 0; k < 12; ++k) { setm0<0>(ring + dst + k * 1024); bdma<0>(lo[0], rs, off + k * 1024); }
        waitvm<0>();
      }
    };
    unsigned qa = lds0 + L_Q + lo[0], na = lds0 + L_N + r32 * 16;
    OPAQUE(qa); OPAQUE(na);
    BARV(0);
    __builtin_amdgcn_s_sleep(TILE0_DELAY);
    P6(0, 0) P6(0, 6)
    if (w == 0 && l == 0) __builtin_amdgcn_raw_buffer_store_b64(tag, rsWS, 0, WS_FLAG2 + b * 8, SC1);
    ST(11);
    BAR();
    u32x4 K0[12];
    if (w == 0) waitvm<1>(); else waitvm<0>();
    ST(12);
    read_valid(K0, 0, toff[0]);
    ST(13);
    piece(IC(1), IC(0)); piece(IC(1), IC(1)); piece(IC(1), IC(2)); piece(IC(1), IC(3));
    BAR();
    ST(14);
    u32x4 Q[12];
    float mref;
    {
      const f32x4 n0 = lds_r128f(na), n1 = lds_r128f(na + 512);
#pragma unroll
      for (int s = 0; s < 6; ++s) Q[s] = lds_r128(qa + s * 1024);
      SBAR();
      const f32x4 s = n0 + n1;
      mref = __builtin_amdgcn_sqrtf(((s[0] + s[1]) + (s[2] + s[3])) * (192.0f * 1.02f));
      OPAQUE(mref);
      SBAR();
#pragma unroll
      for (int s = 6; s < 12; ++s) Q[s] = lds_r128(qa + s * 1024);
      SBAR();
    }
    constexpr int KW = 4, VW = 4;
    bool safe = false, exact = false, late = false;
#pragma unroll 1
    for (;;) {
      f32x16 S[2], O[6], negm;
      u32x4 K[12], V[12], P[2][2];
      f32x4 lsum = {0, 0, 0, 0};
      float pe[2][2];
#pragma unroll
      for (int i = 0; i < 16; ++i) negm[i] = -mref;
#define RK(k, T) K[k] = lds_r128((((k) & 1) ? kr1 : kr0) + ((T) % 3) * TILE_B + (k) * 1024);
#define RV(u, T) { const unsigned a_ = (((u) & 1) ? vr1 : vr0) + ((T) % 3) * TILE_B + ((u) >> 1) * 2048; V[u] = lds_tr2(a_, a_ + 256); }
      auto sm = [&](auto Tc, auto Hc, auto Gc) {
        constexpr int T = decltype(Tc)::value, H = decltype(Hc)::value, G = decltype(Gc)::value;
        if constexpr (G < 8) pe[(G >> 1) & 1][G & 1] = __builtin_amdgcn_exp2f(S[T & 1][8 * H + G]);
        if constexpr (G >= 2 && G <= 8 && G % 2 == 0) { constexpr int q = G / 2 - 1; P[T & 1][H][q] = cvtpk(pe[q & 1][0], pe[q & 1][1]); }
        if constexpr (G == 10) lsum = mfma16(sel, P[T & 1][H], lsum);
      };
      auto put_o = [&](const int un) {
        u32x4 pk;
#pragma unroll
        for (int e = 0; e < 4; ++e) pk[e] = cvtpk(O[un >> 1][8 * (un & 1) + 2 * e], O[un >> 1][8 * (un & 1) + 2 * e + 1]);
        lds_w128(pw + un * 1024, pk);
      };
      auto qkphase = [&](auto Ic) {
        constexpr int I = decltype(Ic)::value;
        auto step = [&](auto Kc) {
          constexpr int k = decltype(Kc)::value;
          if constexpr (I + 1 < 8) {
            S[(I + 1) & 1] = mfma32(I < 0 ? K0[k] : K[k], Q[k], k == 0 ? negm : S[(I + 1) & 1]);
            if constexpr (k + KW < 12 && I >= 0) RK(k + KW, I + 1)
          }
          if constexpr (I < 0 && k < 8) piece(IC(1), IC(4 + k));
          if constexpr (I >= 0) { if constexpr (k & 1) piece(IC(I + 2), IC(6 + k / 2)); if constexpr (k < 11) sm(IC(I), IC(1), IC(k)); }
          if constexpr (I >= 0 && I < 7 && k >= 12 - VW) RV(k - (12 - VW), I)
          if constexpr (I == 7 && k < VW) RV(k, I)
          SBAR();
        };
        step(IC(0)); step(IC(1)); step(IC(2)); step(IC(3)); step(IC(4)); step(IC(5)); step(IC(6)); step(IC(7)); step(IC(8)); step(IC(9)); step(IC(10)); step(IC(11));
      };
      auto pvphase = [&](auto Ic) {
        constexpr int I = decltype(Ic)::value;
        auto step = [&](auto Uc) {
          constexpr int u = decltype(Uc)::value;
          if constexpr (I >= 0) {
            f32x16 z;
#pragma unroll
            for (int e = 0; e < 16; ++e) z[e] = 0.f;
            O[u >> 1] = mfma32(V[u], P[I & 1][u & 1], (I == 0 && !(u & 1)) ? z : O[u >> 1]);
            if constexpr (u & 1) piece(IC(I + 3), IC(u / 2));
            if constexpr (u + VW < 12) RV(u + VW, I)
          }
          if constexpr (I + 1 < 8 && u >= 1) sm(IC(I + 1), IC(0), IC(u - 1));
          if constexpr (I < 0 && (u & 1)) piece(IC(2), IC(u / 2));
          if constexpr (I + 2 < 8) {
            if constexpr (u == 12 - KW) waitvm<(I < 0 ? 12 : I + 3 < 8 ? 6 + (12 - KW) / 2 : 6)>();
            if constexpr (u >= 12 - KW) RK(u - (12 - KW), I + 2)
            if constexpr (u == 11) waitvm<(I + 3 < 8 ? 6 : 0)>();
          }
          if constexpr (I == 7 && u >= 3) {
            put_o(u - 3);
          }
          SBAR();
        };
        step(IC(0)); step(IC(1)); step(IC(2)); step(IC(3)); step(IC(4)); step(IC(5)); step(IC(6)); step(IC(7)); step(IC(8)); step(IC(9)); step(IC(10)); step(IC(11));
      };
#define STALE(v) (__builtin_expect(!safe && __any((v) != (v)), 0))
      bool done = false;
      do {
        qkphase(IC(-1));
        ST(15);
        pvphase(IC(-1));
        ST(16);
        qkphase(IC(0)); pvphase(IC(0)); qkphase(IC(1)); pvphase(IC(1)); qkphase(IC(2));
        const float seen = lsum[0];
        pvphase(IC(2));
        if (STALE(seen)) break;
        qkphase(IC(3)); pvphase(IC(3)); qkphase(IC(4)); pvphase(IC(4)); qkphase(IC(5));
        if (!late) asm volatile("s_barrier" ::: "memory");
        late = true;
        pvphase(IC(5)); qkphase(IC(6)); pvphase(IC(6)); qkphase(IC(7)); pvphase(IC(7));
        if (STALE(lsum[0])) break;
        { const u32x2 ls = {asu(lsum[0]), asu(lsum[1])}; lds_w64(lw, ls); }
        put_o(9); put_o(10); put_o(11);
        done = true;
      } while (false);
#pragma unroll
      for (int k = 0; k < 12; ++k) asm volatile("" : "=v"(K0[k]));
      if (__builtin_expect(done, 1)) {
        ST(17);
        BAR();
        ST(18);
        float lt[2];
        unsigned ll = lo[0];
        OPAQUE(ll);
        ll >>= 4;
        if (__builtin_expect(!low_sum(lds_r128f(L_L + (ll & 15) * 32), lds_r128f(L_L + (ll & 15) * 32 + 16), lt) || exact, 1)) break;
        BAR();
        float mx = -3.0e38f;
#pragma unroll 1
        for (unsigned i = 0; i < 8; ++i) {
#pragma unroll 1
          for (int k = 0; k < 12; ++k) { setm0<0>(ring + k * 1024); bdma<0>(lo[0], rs, (8 * w + i) * TILE_B + k * 1024); }
          waitvm<0>();
          f32x16 s;
#pragma unroll
          for (int e = 0; e < 16; ++e) s[e] = 0.f;
#pragma unroll
          for (int k = 0; k < 12; ++k) s = mfma32(lds_r128(((k & 1) ? kr1 : kr0) + k * 1024), Q[k], s);
#pragma unroll
          for (int e = 0; e < 16; ++e) mx = fmaxf(mx, s[e]);
        }
        lds_w32(L_N + ((ll & 31) * 8 + w * 2 + (ll >> 5)) * 4, mx);
        BAR();
        const f32x4 c = lds_r128f(L_N + (ll & 31) * 32), d = lds_r128f(L_N + (ll & 31) * 32 + 16);
        mref = fmaxf(fmaxf(fmaxf(c[0], c[1]), fmaxf(c[2], c[3])), fmaxf(fmaxf(d[0], d[1]), fmaxf(d[2], d[3])));
        exact = true;
      } else {
        unsigned ll = lo[0];
        OPAQUE(ll);
        for (int sp = 0; sp < SPIN_CAP; ++sp) {
          asm volatile("" ::: "memory");
          const u32x2 f = __builtin_amdgcn_raw_buffer_load_b64(rsWS, ll >> 1, WS_FLAG2 + w * 512, SC1);
          if (__all(f[0] == tag[0] && f[1] == tag[1])) break;
        }
      }
      safe = true;
      asm volatile("s_waitcnt vmcnt(0) lgkmcnt(0)" ::: "memory");
      P6(0, 0) P6(0, 6)
      waitvm<0>();
      piece(IC(1), IC(0)); piece(IC(1), IC(1)); piece(IC(1), IC(2)); piece(IC(1), IC(3));
#pragma unroll
      for (int k = 0; k < 12; ++k) K0[k] = lds_r128(((k & 1) ? kr1 : kr0) + k * 1024);
    }
    ST(19);
    BAR();
    ST_OUT(ws, 0, 12);
  } else {
    LANE_IDS;
    auto job_run = [&](const unsigned at, const unsigned slot, const unsigned sealat, auto younger) {
      unsigned ja[2] = {at + i16 * 384 + ((g ^ (l & 7)) * 16), at + i16 * 384 + (((4 + g) ^ (l & 7)) * 16)}, jb = at + 12288 + (8 * g + tq) * 32 + tp * 8;
      OPAQUE(ja[0]); OPAQUE(ja[1]); OPAQUE(jb);
      waitvm<decltype(younger)::value>();
      ST(2);
      u32x4 a[6], c[6];
#pragma unroll
      for (int kk = 0; kk < 6; ++kk) { a[kk] = lds_r128(ja[kk & 1] + (kk >> 1) * 128); c[kk] = lds_r128(ja[kk & 1] + (kk >> 1) * 128 + 6144); }
      const unsigned o = (hh * 16 + i16) * 16 + (g & 1) * 8;
      f32x4 d = {0, 0, 0, 0};
      u32x2 r;
      if (jqk) {
#pragma unroll
        for (int kk = 0; kk < 6; ++kk) d = mfma16(a[kk], c[kk], d);
        r = pack4(d * QSCALE);
        __builtin_amdgcn_raw_buffer_store_b64(r, rsWS, o, slot, SC1);
      } else {
        f32x4 e = {0, 0, 0, 0};
#pragma unroll
        for (int kk = 0; kk < 6; ++kk) {
          const u32x4 bb = lds_tr2(jb + kk * 1024, jb + kk * 1024 + 128);
          d = mfma16(a[kk], bb, d); e = mfma16(c[kk], bb, e);
        }
        const u32x2 r0 = pack4(d), r1 = pack4(e);
        __builtin_amdgcn_raw_buffer_store_b64(r0, rsWS, o, slot, SC1);
        __builtin_amdgcn_raw_buffer_store_b64(r1, rsWS, o + 512, slot, SC1);
        r = r0 ^ r1;
      }
      const u32x2 half = {fold_xor(r[0], r[1]), hh ? tag[1] : tag[0]};
      __builtin_amdgcn_raw_buffer_store_b64(half, rsWS, r32 == 0 ? hh * 8 : WS_END, sealat, SC1);
    };
    FRAG_POS;
    if (w == 0 || (w == 1 && jqk)) {
      ST(1);
      if (job) job_run(w * SLOT, jslot + (1 - w) * 2 * 73728, WS_SEAL + (1 - w) * 32768 + b * 128, IC(0));
      ST(3);
      if (w == 0) {
        __builtin_amdgcn_raw_buffer_store_b32(POISON, rsWS, l * 4, WS_ST + (t * 1024 + qb * 32) * 8, SC1);
        const unsigned p0 = WS_H2 + t * FRAME_B + (jqk ? qb * 8 : 144 + (qb - 27) * 48) * 1024, np = jqk ? 2 : job ? 0 : 12;
#pragma unroll 1
        for (unsigned i = 0; i < np; ++i) {
#pragma unroll
          for (int q = 0; q < 4; ++q) __builtin_amdgcn_raw_buffer_store_b128(pz, rsWS, l * 16 + q * 1024, p0 + i * 4096, SC1);
        }
        asm volatile("s_nop 1" :: "v"(pz));
        if (jqk) { waitvm<9>(); __builtin_amdgcn_s_sleep(READ_DELAY); } else if (job) __builtin_amdgcn_s_sleep(VO_DELAY); else { waitvm<0>(); __builtin_amdgcn_s_sleep(IDLE_DELAY); }
      }
    }
    u32x4 wq[18], wv[18];
    {
      u32x4 H[6][2];
      u32x2 hs[3][2];
      unsigned spa = hh == (i16 >> 3) ? L_SP + w * 1024 + l * 16 : L_SP + 4096 + l * 16;
      OPAQUE(spa);
      __builtin_amdgcn_s_setprio(3);
      ST(4);
      BAR();
#pragma unroll 1
      for (int sp = 0;; ++sp) {
        const u32x4 seal = ld_seal(2);
        ldw(wq, 2, 0, 3, rot);
        SBAR();
        h_frags(H, fr);
#pragma unroll
        for (int i = 0; i < 3; ++i) { hs[i][0] = lds_r64(dp[0] + (L_H - L_X) + i * 512); hs[i][1] = lds_r64(dp[1] + (L_H - L_X) + i * 512); }
        ldw(wq, 2, 3, 6, rot);
        if (sp == 0) {
          if (w == 0) {
            waitvm<19>();
            if (l < 8) __builtin_amdgcn_raw_buffer_store_b64(tag, rsWS, l * 2048, WS_FLAG + b * 8, SC1);
          }
          asm volatile("s_barrier" ::: "memory");
        }
        SBAR();
        f32x4 d[3][2], st[2];
        unsigned he = 0, ho = 0;
#pragma unroll
        for (int kk = 0; kk < 6; ++kk) {
#pragma unroll
          for (int i = 0; i < 3; ++i) {
            d[i][0] = mfma16(wq[kk * 3 + i], H[kk][0], kk ? d[i][0] : f32x4{0, 0, 0, 0});
            d[i][1] = mfma16(wq[kk * 3 + i], H[kk][1], kk ? d[i][1] : f32x4{0, 0, 0, 0});
            XOR_IN(he, ho, wq[kk * 3 + i]);
          }
          MIXV6();
        }
        ST(19);
        const bool ok = seal_ok(he, ho, seal, l < 36, tag);
#pragma unroll
        for (int hf = 0; hf < 2; ++hf) {
          st[hf] = f32x4{0, 0, 0, 0};
#pragma unroll
          for (int i = 0; i < 3; ++i) st[hf] = mfma16k16(hs[i][hf], pack4(d[i][hf]), st[hf]);
          lds_w128f(spa ^ (hf * 512), st[hf]);
        }
        if (__builtin_expect(ok, 1) || sp >= SPIN_CAP) break;
        asm volatile("" ::: "memory");
      }
      asm volatile("" :: "v"(dc));
    }
    ST(10);
    BAR();
    __builtin_amdgcn_s_setprio(0);
    u32x4 seal = ld_seal(0);
    {
      ldw(wq, 0, 0, WQ_SPLIT, 0);
      u32x2 fl = __builtin_amdgcn_raw_buffer_load_b64(rsWS, l * 8, WS_FLAG + t * 2048 + w * 512, SC1);
      ST(5);
      BAR();
      ldw(wq, 0, WQ_SPLIT, 6, 0);
      if (__builtin_expect(!__all(fl[0] == tag[0] && fl[1] == tag[1]), 0)) {
        for (int sp = 0; sp < SPIN_CAP; ++sp) {
          asm volatile("" ::: "memory");
          const u32x2 f2 = __builtin_amdgcn_raw_buffer_load_b64(rsWS, l * 8, WS_FLAG + t * 2048 + w * 512, SC1);
          if (__all(f2[0] == tag[0] && f2[1] == tag[1])) break;
        }
      }
      ST(18);
      BAR();
    }
    unsigned hqa[2][2], own[2][2];
#pragma unroll
    for (int hf = 0; hf < 2; ++hf) {
      const unsigned in = lds0 + L_HQ + ((4 * hf + tq) ^ hh) * 128 + ((g & 1) ^ hf) * 64 + tp * 16;
      hqa[hf][0] = in + hh * 1024; hqa[hf][1] = in + 2048 + hh * (SLOT - 2048);
      OPAQUE(hqa[hf][0]); OPAQUE(hqa[hf][1]);
    }
    const unsigned tok = 16 * (g & 1) + i16;
    unsigned nw = lds0 + L_N + l * 16 + w * 4;
    OPAQUE(nw);
    unsigned qw = L_Q + (3 * w * 64 + hh * 32 + i16) * 16 + 8 * (g & 1);
    own[0][0] = w * SLOT + L_HQ + (tq ^ (w & 1)) * 128 + hh * 64 + tp * 16 + 8 * (g & 1); own[0][1] = (own[0][0] ^ 64) + 512;
    own[1][0] = own[0][0] ^ 128; own[1][1] = own[0][1] ^ 128;
    OPAQUE(qw); OPAQUE(own[0][0]); OPAQUE(own[0][1]); OPAQUE(own[1][0]); OPAQUE(own[1][1]);
    if (b == 0 && w == 3 && l == 0) __builtin_amdgcn_raw_buffer_store_b64(tag, rsWS, 0, WS_EPOCH, SC1);
    for (int sp = 0; sp < SPIN_CAP && !sealed(wq, seal, l < 36, tag); ++sp) {
      asm volatile("" ::: "memory");
      seal = ld_seal(0);
      ldw(wq, 0, 0, 6, 0);
    }
    if (w == 1 && job && !jqk) job_run(SLOT, jslot, WS_SEAL + b * 128, IC(0));
    ST(6);
    BAR();
    ST(7);
    u32x4 hq[3];
    {
      const u64 hbu = (u64)(ws + WS_H2 + (size_t)t * FRAME_B);
      const u32x4 rs = {(unsigned)hbu, (unsigned)(hbu >> 32) & 0xffffu, (unsigned)FRAME_B, 0x00020000u};
      for (int sp = 0; sp < SPIN_CAP; ++sp) {
        setm0<L_HQ>(lds0 + w * SLOT);
        bdma<0>(l * 16, rs, qb * TILE_B + w * 3072); bdma<1024>(l * 16, rs, qb * TILE_B + w * 3072); bdma<2048>(l * 16, rs, qb * TILE_B + w * 3072);
        waitvm<0>();
#pragma unroll
        for (int i = 0; i < 3; ++i) { const u32x2 u0 = lds_r64(own[i & 1][0] + i * 1024), u1 = lds_r64(own[i & 1][1] + i * 1024); hq[i] = u32x4{u0[0], u0[1], u1[0], u1[1]}; }
        if (!poisoned(hq)) break;
      }
    }
    ST(8);
    BAR();
    ST(9);
    auto gemm = [&](const u32x4 (&wf)[18], const u32x4 (&bf)[6][2], auto&& fin) {
      f32x4 d[3][2];
#pragma unroll
      for (int i = 0; i < 3; ++i) {
#pragma unroll
        for (int ks = 0; ks < 6; ++ks) {
          d[i][0] = mfma16(wf[ks * 3 + i], bf[ks][0], ks ? d[i][0] : f32x4{0, 0, 0, 0});
          d[i][1] = mfma16(wf[ks * 3 + i], bf[ks][1], ks ? d[i][1] : f32x4{0, 0, 0, 0});
          if (ks == 0) SBAR();
        }
        if (i > 0) fin(i - 1, d[i - 1]);
        SBAR();
      }
      fin(2, d[2]);
    };
    {
      u32x4 bf[6][2];
#pragma unroll
      for (int ks = 0; ks < 6; ++ks)
#pragma unroll
        for (int hf = 0; hf < 2; ++hf) {
          if (ks == 3) { hqa[hf][0] += 2 * SLOT; hqa[hf][1] += 2 * SLOT; }
          bf[ks][hf] = lds_r128(ks % 3 == 1 ? hqa[hf][1] : hqa[hf][0] + (ks % 3 == 2 ? SLOT + 1024 : 0));
        }
      SBAR();
      f32x2 n[2] = {{0.f, 0.f}, {0.f, 0.f}};
      gemm(wq, bf, [&](const int i, const f32x4 (&d)[2]) {
#pragma unroll
        for (int hf = 0; hf < 2; ++hf) {
          lds_w64(qw + i * 1024 + hf * 256, pack4(d[hf]));
          const f32x2 a = {d[hf][0], d[hf][1]}, c = {d[hf][2], d[hf][3]};
          n[hf] = __builtin_elementwise_fma(a, a, n[hf]); n[hf] = __builtin_elementwise_fma(c, c, n[hf]);
        }
      });
      auto rr = __builtin_amdgcn_permlane16_swap(asu(n[0][0] + n[0][1]), asu(n[1][0] + n[1][1]), false, false);
      lds_w32(nw, asf(rr[0]) + asf(rr[1]));
    }
    ST(11);
    BAR();
    unsigned oo = (t * 1024 + qb * 32 + tok) * 384 + (6 * w + hh) * 16;
    OPAQUE(oo);
    float lt[2];
    u32x4 pick;
#pragma unroll
    for (int e = 0; e < 4; ++e) {
      const unsigned d = 8 * (e >> 1) + 4 * (g & 1) + 2 * (e & 1);
      pick[e] = (i16 == d ? 0x3f80u : 0u) | (i16 == d + 1 ? 0x3f800000u : 0u);
    }
    unsigned pr[2], la = lds0 + L_L + i16 * 32;
    pr[0] = lds0 + hh * SLOT + 3 * w * 1024 + ((g & 1) * 32 + i16) * 16; pr[1] = pr[0] + 2 * SLOT;
    OPAQUE(pick); OPAQUE(pr[0]); OPAQUE(pr[1]); OPAQUE(la);
    f32x4 x1[3][2];
    {
      __builtin_amdgcn_s_sleep(40);
      u32x2 st[2];
      for (int sp = 0; sp < SPIN_CAP; ++sp) {
        asm volatile("" ::: "memory");
        st[0] = __builtin_amdgcn_raw_buffer_load_b64(rsWS, i16 * 8, WS_ST + (t * 1024 + qb * 32) * 8, SC1);
        st[1] = __builtin_amdgcn_raw_buffer_load_b64(rsWS, i16 * 8 + 128, WS_ST + (t * 1024 + qb * 32) * 8, SC1);
        if (!__any(max(max(st[0][0], st[0][1]), max(st[1][0], st[1][1])) == POISON)) break;
      }
#pragma unroll
      for (int hf = 0; hf < 2; ++hf) {
        const float mu = asf(st[hf][0]), sd = __builtin_amdgcn_sqrtf(asf(st[hf][1]));
#pragma unroll
        for (int i = 0; i < 3; ++i) {
          const f32x4 h = {bflo(hq[i][2 * hf]), bfhi(hq[i][2 * hf]), bflo(hq[i][2 * hf + 1]), bfhi(hq[i][2 * hf + 1])};
          x1[i][hf] = h * sd + mu;
          OPAQUE(x1[i][hf]);
        }
      }
    }
    BAR();
    ST(16);
    for (int sp = 0; sp < SPIN_CAP; ++sp) {
      asm volatile("" ::: "memory");
      seal = ld_seal(1);
      ldw(wv, 1, 0, 6, 0);
      if (sealed(wv, seal, l < 18, tag)) break;
    }
    ST(12);
#pragma unroll 1
    for (bool exact = false;; exact = true) {
      BAR();
      ST(13);
      u32x4 p[3][2][2];
#pragma unroll
      for (int i = 0; i < 3; ++i)
#pragma unroll
        for (int hf = 0; hf < 2; ++hf) { p[i][hf][0] = lds_r128(pr[0] + i * 1024 + hf * 256); p[i][hf][1] = lds_r128(pr[1] + i * 1024 + hf * 256); }
      const f32x4 l0 = lds_r128f(la), l1 = lds_r128f(la + 16);
      SBAR();
#pragma unroll
      for (int i = 0; i < 3; ++i)
#pragma unroll
        for (int hf = 0; hf < 2; ++hf) lds_w64(dp[hf] + (L_FI - L_X) + i * 512, pack4(mfma16(pick, p[i][hf][1], mfma16(pick, p[i][hf][0], f32x4{0, 0, 0, 0}))));
      if (__builtin_expect(!low_sum(l0, l1, lt) || exact, 1)) break;
      BAR();
      BAR();
    }
    ST(14);
    const float linv[2] = {__builtin_amdgcn_rcpf(lt[0]), __builtin_amdgcn_rcpf(lt[1])};
    BAR();
    ST(15);
    u32x4 bf[6][2];
#pragma unroll
    for (int ks = 0; ks < 6; ++ks) { bf[ks][0] = lds_r128(fr[0] + (L_FI - L_X) + ks * 1024); bf[ks][1] = lds_r128(fr[1] + (L_FI - L_X) + ks * 1024); }
    SBAR();
    f32x4 d[3][2];
#pragma unroll
    for (int ks = 0; ks < 6; ++ks)
#pragma unroll
      for (int i = 0; i < 3; ++i) {
        d[i][0] = mfma16(wv[ks * 3 + i], bf[ks][0], ks ? d[i][0] : f32x4{0, 0, 0, 0});
        d[i][1] = mfma16(wv[ks * 3 + i], bf[ks][1], ks ? d[i][1] : f32x4{0, 0, 0, 0});
      }
#pragma unroll
    for (int i = 0; i < 3; ++i) {
      const u32x2 y0 = pack4(d[i][0] * linv[0] + x1[i][0]), y1 = pack4(d[i][1] * linv[1] + x1[i][1]);
      auto s0 = __builtin_amdgcn_permlane16_swap(y0[0], y1[0], false, false);
      auto s1 = __builtin_amdgcn_permlane16_swap(y0[1], y1[1], false, false);
      const u32x4 o = {s0[0], s1[0], s0[1], s1[1]};
      *(__attribute__((address_space(1))) u32x4*)(kc + oo + i * 32) = o;
    }
    ST(17);
    ST_OUT(ws, 12, 20);
  }
}

extern "C" void kernel_launch(void* const* d_in, const int* in_sizes, int n_in, void* d_out, int out_size, void* d_ws, size_t ws_size, hipStream_t stream) {
  (void)hipFuncSetAttribute((const void*)k_timesformer, hipFuncAttributeMaxDynamicSharedMemorySize, LDS_BYTES);
  k_timesformer<<<256, 512, LDS_BYTES, stream>>>((const char*)d_in[0], (const char*)d_in[1], (const char*)d_in[2], (const char*)d_in[3], (const char*)d_in[4],
                                                 (char*)d_ws, (char*)d_out);
}
```

```cpp
#include <hip/hip_runtime.h>
#include <hip/hip_bf16.h>
#include <type_traits>

typedef short s16x4 __attribute__((ext_vector_type(4)));
typedef __bf16 b16x8 __attribute__((ext_vector_type(8)));
typedef __bf16 b16x2 __attribute__((ext_vector_type(2)));
typedef float f32x2 __attribute__((ext_vector_type(2)));
typedef float f32x4 __attribute__((ext_vector_type(4)));
typedef float f32x16 __attribute__((ext_vector_type(16)));
typedef unsigned u32x2 __attribute__((ext_vector_type(2)));
typedef unsigned u32x4 __attribute__((ext_vector_type(4)));
typedef unsigned long long u64;
typedef u64 u64x4 __attribute__((ext_vector_type(4)));
typedef u64 u64x2 __attribute__((ext_vector_type(2)));
#define LAS __attribute__((address_space(3)))
#define DI __device__ __forceinline__
#define SBAR() __builtin_amdgcn_sched_barrier(0)

constexpr int TILE_B = 12288;
constexpr int FRAME_B = 393216;
constexpr unsigned WS_H2 = 0, WS_ST = 8 * FRAME_B, WS_WM = 16 * FRAME_B, WS_SEAL = WS_WM + 4 * 73728, WS_FLAG = WS_SEAL + 2 * 256 * 128, WS_FLAG2 = WS_FLAG + 8 * 2048, WS_EPOCH = WS_FLAG2 + 2048, WS_END = WS_EPOCH + 256;
constexpr float QSCALE = 0.07216878364870322f * 1.4426950408889634f;
constexpr int SC1 = 16;
constexpr unsigned POISON = 0xffffffffu;
constexpr unsigned CODE_BYTES = 22528;
constexpr int TILE0_DELAY = 2, JOB2_DELAY = 6, X_DELAY = 1, READ_DELAY = 14, VO_DELAY = 11, IDLE_DELAY = 18, WQ_SPLIT = 4, B9_DELAY = 4;
constexpr int SPIN_CAP = 1 << 16;


constexpr int PROBE = 17, ROLE = 1; constexpr size_t WS_T0 = 32u << 20;
#define ST_DECL u64 st_ = 0, t0_ = 0
#define ST(k) do { if ((k) == PROBE || (k) == 0) { SBAR(); u64 t_; asm volatile("s_memrealtime %0\n\ts_waitcnt lgkmcnt(0)" : "=s"(t_) :: "memory"); SBAR(); \
    if ((k) == PROBE) st_ = t_; \
    if ((k) == 0) t0_ = t_; } STO_##k } while (0)
#define STO_(k)
#define STO_0
#define STO_1
#define STO_2
#define STO_3
#define STO_4
#define STO_5
#define STO_6
#define STO_7
#define STO_8
#define STO_9
#define STO_10
#define STO_11
#define STO_12
#define STO_13
#define STO_14
#define STO_15
#define STO_16 if (threadIdx.x == 0) __hip_atomic_store((__attribute__((address_space(1))) u64*)(ws + WS_T0) + blockIdx.x, t0_, __ATOMIC_RELAXED, __HIP_MEMORY_SCOPE_AGENT);
#define STO_17
#define STO_18
#define STO_19
#define ST_OUT(ws, k0, k1) do { if (ROLE == ((k0) != 0)) { u64 m_ = ~0ull; \
    for (int q_ = 0; q_ < 4; ++q_) { u64 v_ = __hip_atomic_load((__attribute__((address_space(1))) u64*)(ws + WS_T0) + (threadIdx.x & 63) * 4 + q_, __ATOMIC_RELAXED, __HIP_MEMORY_SCOPE_AGENT); m_ = v_ < m_ ? v_ : m_; } \
    for (int o_ = 32; o_; o_ >>= 1) { u64 v_ = __shfl_xor(m_, o_); m_ = v_ < m_ ? v_ : m_; } \
    long long d_ = (long long)(st_ - m_); if (d_ < 0) d_ = 0; if (d_ > 5000) d_ = 5000; \
    const u64 e_ = m_ + 4000 + 4 * d_; while (__builtin_amdgcn_s_memrealtime() < e_) __builtin_amdgcn_s_sleep(1); } } while (0)


DI float asf(unsigned u) { return __builtin_bit_cast(float, u); }
DI unsigned asu(float f) { return __builtin_bit_cast(unsigned, f); }
DI unsigned cvtpk(float lo, float hi) { f32x2 v = {lo, hi}; return __builtin_bit_cast(unsigned, __builtin_convertvector(v, b16x2)); }
DI float bflo(unsigned u) { return asf(u << 16); }
DI float bfhi(unsigned u) { return asf(u & 0xffff0000u); }
DI f32x16 mfma32(u32x4 a, u32x4 b, f32x16 c) { return __builtin_amdgcn_mfma_f32_32x32x16_bf16(__builtin_bit_cast(b16x8, a), __builtin_bit_cast(b16x8, b), c, 0, 0, 0); }
DI f32x4 mfma16(u32x4 a, u32x4 b, f32x4 c) { return __builtin_amdgcn_mfma_f32_16x16x32_bf16(__builtin_bit_cast(b16x8, a), __builtin_bit_cast(b16x8, b), c, 0, 0, 0); }
DI f32x4 mfma16k16(u32x2 a, u32x2 b, f32x4 c) { return __builtin_amdgcn_mfma_f32_16x16x16bf16_1k(__builtin_bit_cast(s16x4, a), __builtin_bit_cast(s16x4, b), c, 0, 0, 0); }
DI u32x2 pack4(f32x4 v) { u32x2 r = {cvtpk(v[0], v[1]), cvtpk(v[2], v[3])}; return r; }
DI u32x4 lds_r128(unsigned a) { return *(LAS u32x4*)(size_t)a; }
DI f32x4 lds_r128f(unsigned a) { return *(LAS f32x4*)(size_t)a; }
DI u32x2 lds_r64(unsigned a) { return *(LAS u32x2*)(size_t)a; }
DI void lds_w128(unsigned a, u32x4 v) { *(LAS u32x4*)(size_t)a = v; }
DI void lds_w128f(unsigned a, f32x4 v) { *(LAS f32x4*)(size_t)a = v; }
DI void lds_w64(unsigned a, u32x2 v) { *(LAS u32x2*)(size_t)a = v; }
DI void lds_w32(unsigned a, float v) { *(LAS float*)(size_t)a = v; }
DI u32x2 lds_tr(unsigned a) { return __builtin_bit_cast(u32x2, __builtin_amdgcn_ds_read_tr16_b64_v4i16((LAS s16x4*)(size_t)a)); }
DI u32x4 lds_tr2(unsigned a0, unsigned a1) { u32x2 x = lds_tr(a0), y = lds_tr(a1); u32x4 r = {x[0], x[1], y[0], y[1]}; return r; }
#define BAR() asm volatile("s_waitcnt lgkmcnt(0)\n\ts_barrier" ::: "memory")
#define BARV(n) asm volatile("s_waitcnt vmcnt(" #n ") lgkmcnt(0)\n\ts_barrier" ::: "memory")
template <int CTRL> DI float dpp_add(float v) { return v + asf(__builtin_amdgcn_update_dpp(0u, asu(v), CTRL, 0xf, 0xf, true)); }
DI float xor16_add(float v) { auto r = __builtin_amdgcn_permlane16_swap(asu(v), asu(v), false, false); return asf(r[0]) + asf(r[1]); }
DI float xor16_max(float v) { auto r = __builtin_amdgcn_permlane16_swap(asu(v), asu(v), false, false); return __builtin_amdgcn_fmed3f(asf(r[0]), asf(r[1]), __builtin_inff()); }
DI unsigned umax3(unsigned a, unsigned b, unsigned c) { return max(max(a, b), c); }
template <int N> DI bool poisoned(const u32x4 (&v)[N]) {
  unsigned m = 0;
#pragma unroll
  for (int i = 0; i < N; ++i) { m = umax3(m, v[i][0], v[i][1]); m = umax3(m, v[i][2], v[i][3]); }
  return __any(m == POISON);
}
DI u32x4 unpoison(u32x4 v) { u32x4 r = {min(v[0], POISON - 1), min(v[1], POISON - 1), min(v[2], POISON - 1), min(v[3], POISON - 1)}; return r; }
DI unsigned fi_pos(unsigned r, unsigned c8) {
  return (((r >> 4) * 6 + (c8 >> 2)) * 64 + (c8 & 3) * 16 + ((r & 15) ^ ((c8 & 1) | (((r >> 3) & 1) << 1) | ((r >> 4) << 2)))) * 16;
}
DI float row8_sum(float s) { s = dpp_add<0x124>(s); s = dpp_add<0x128>(s); return xor16_add(s); }
DI float sum24(const f32x2 (&v)[12]) {
  f32x2 s0 = v[0] + v[1], s1 = v[2] + v[3];
#pragma unroll
  for (int i = 4; i < 12; i += 2) { s0 += v[i]; s1 += v[i + 1]; }
  s0 += s1;
  return s0[0] + s0[1];
}
DI float sumsq24(const f32x2 (&v)[12]) {
  f32x2 q0 = v[0] * v[0], q1 = v[1] * v[1];
#pragma unroll
  for (int i = 2; i < 12; i += 2) { q0 = __builtin_elementwise_fma(v[i], v[i], q0); q1 = __builtin_elementwise_fma(v[i + 1], v[i + 1], q1); }
  q0 += q1;
  return q0[0] + q0[1];
}
DI f32x2 unpack2(unsigned u) { f32x2 r = {bflo(u), bfhi(u)}; return r; }

constexpr unsigned SLOT = 36864, L_X = 3 * SLOT, L_H = 4 * SLOT, L_N = L_H + TILE_B, L_L = L_N + 1024, L_PT = L_L + 1024, LDS_BYTES = 163840;
constexpr unsigned L_VI = L_X, L_SP = L_X + TILE_B, L_YI = L_X + 20480;
constexpr unsigned L_HQ = 24576, L_Q = L_H, L_FI = L_H;

#define DMA3(v0, v1, v2, sbase, ldsa) \
  asm volatile("s_mov_b32 m0, %4\n\ts_nop 0\n\tglobal_load_lds_dwordx4 %0, %3\n\tglobal_load_lds_dwordx4 %1, %3 offset:1024\n\tglobal_load_lds_dwordx4 %2, %3 offset:2048" \
               :: "v"(v0), "v"(v1), "v"(v2), "s"(sbase), "s"(ldsa) : "memory")
#define DMA1(v0, sbase, ldsa) \
  asm volatile("s_mov_b32 m0, %2\n\ts_nop 0\n\tglobal_load_lds_dwordx4 %0, %1" :: "v"(v0), "s"(sbase), "s"(ldsa) : "memory")

template <int OFF> DI void bdma(unsigned voff, u32x4 rs, unsigned soff) { asm volatile("buffer_load_dwordx4 %0, %1, %2 offen offset:%3 sc1 lds" :: "v"(voff), "s"(rs), "s"(soff), "i"(OFF) : "memory"); }
template <int OFF> DI void setm0(unsigned base) { asm volatile("s_add_u32 m0, %0, %1\n\ts_nop 0" :: "s"(base), "i"(OFF) : "memory", "scc"); }
template <int N> DI void waitvm() { asm volatile("s_waitcnt vmcnt(%0)" :: "i"(N) : "memory"); }
#define IC(x) std::integral_constant<int, (x)>{}
#define OPAQUE(v) asm volatile("" : "+v"(v))
template <class R> DI void st128(u32x4 v, R rs, unsigned voff, unsigned soff) { __builtin_amdgcn_raw_buffer_store_b128(v, rs, voff, soff, SC1); asm volatile("s_nop 1" :: "v"(v)); }
template <int CTRL> DI unsigned dpp_xor(unsigned v) { return v ^ __builtin_amdgcn_update_dpp(0u, v, CTRL, 0xf, 0xf, true); }
DI unsigned fold_xor(unsigned e, unsigned o) {
  auto r = __builtin_amdgcn_permlane32_swap(e, o, false, false);
  unsigned u = r[0] ^ r[1];
  auto q = __builtin_amdgcn_permlane16_swap(u, u, false, false);
  u = q[0] ^ q[1];
  u = dpp_xor<0x128>(u); u = dpp_xor<0x124>(u); u = dpp_xor<0x4e>(u); return dpp_xor<0xb1>(u);
}
DI bool seal_ok(unsigned e, unsigned o, u32x4 seal, bool has, u32x2 tag) {
  const unsigned bad = has ? (seal[1] ^ tag[0]) | (seal[3] ^ tag[1]) : 0u;
  return __builtin_amdgcn_ballot_w64((fold_xor(e ^ seal[0], o ^ seal[2]) | bad) != 0) == 0;
}
DI unsigned xor3(unsigned a, unsigned b, unsigned c) { return __builtin_amdgcn_bitop3_b32(a, b, c, 0x96); }
#define XOR_IN(e, o, v) do { e = xor3(e, (v)[0], (v)[2]); o = xor3(o, (v)[1], (v)[3]); } while (0)
#define MIXV6() _Pragma("unroll") for (int q_ = 0; q_ < 6; ++q_) { __builtin_amdgcn_sched_group_barrier(0x8, 1, 0); __builtin_amdgcn_sched_group_barrier(0x2, 1, 0); } SBAR()
template <int N> DI bool sealed(const u32x4 (&v)[N], u32x4 seal, bool has, u32x2 tag) {
  unsigned e = 0, o = 0;
#pragma unroll
  for (int i = 0; i < N; ++i) XOR_IN(e, o, v[i]);
  return seal_ok(e, o, seal, has, tag);
}
DI bool tags_ok(u32x4 f, u32x2 tag) { return f[0] == tag[0] && f[1] == tag[1] && f[2] == tag[0] && f[3] == tag[1]; }

__global__ __launch_bounds__(512, 2) void k_timesformer(const char* x_, const char* wqkv_t_, const char* wo_t_, const char* wqkv_s_, const char* wo_s_, char* ws_, char* out_) {
  unsigned dc;
  u64x4 ka; u64x2 kb; u64 kc;
  {
    u64 pc;
    asm volatile("s_load_dwordx8 %2, %6, 0x0\n\ts_load_dwordx4 %3, %6, 0x20\n\ts_load_dwordx2 %4, %6, 0x30\n\ts_getpc_b64 %1\n\tglobal_load_dword %0, %5, %1"
                 : "=v"(dc), "=&s"(pc), "=&s"(ka), "=&s"(kb), "=&s"(kc) : "v"(min(threadIdx.x * 128, CODE_BYTES)), "s"(__builtin_amdgcn_kernarg_segment_ptr()) : "memory");
  }
  constexpr unsigned lds0 = 0;
  const unsigned b = blockIdx.x;
  unsigned w8 = __builtin_amdgcn_readfirstlane(threadIdx.x >> 6);
  unsigned l = threadIdx.x & 63;
  const unsigned t = b & 7, qb = b >> 3;
  w8 = (w8 & 4) | ((w8 + qb / 6) & 3);
  ST_DECL;
  ST(0);
  const bool jqk = b < 144, job = b < 216;
  const unsigned jp = jqk ? b >> 1 : b - 144, jw = jp / 18, jks = (jp - jw * 18) / 3, jtau = jp - jw * 15 - jks * 3;
  const unsigned jrow = jks * 32 + (jqk ? (b & 1) * 16 : 0), jslot = WS_WM + (jqk ? b * 512 : 73728 + jp * 1024);
  unsigned fo[4][3];
  if (w8 < 4) {
#pragma unroll
    for (int i = 0; i < 3; ++i) { const unsigned p = i * 64 + l; fo[0][i] = p * 16 + (p >= 96 ? FRAME_B - 1536 : 0) + w8 * 2 * FRAME_B + b * 1536; fo[1][i] = fo[2][i] = fo[3][i] = 0; }
  } else {
    const unsigned ra = jrow * 1152 + (jqk ? 0 : 768), rb = jqk ? jtau * (16 * 1152) + 384 : ra + 16 * 1152;
#pragma unroll
    for (int i = 0; i < 3; ++i) {
      const unsigned pos = i * 64 + l, row = (pos * 2731) >> 16, o = row * 1152 + (((pos - row * 24) ^ row) * 16) - i * 1024;
      fo[0][i] = o + ra; fo[1][i] = o + (ra + 8 * 1152); fo[2][i] = o + rb; fo[3][i] = o + (rb + 8 * 1152);
    }
  }
  unsigned so_ = (l >> 1) * 384 + (l & 1) * 16 + jtau * 32;
  asm volatile("s_waitcnt lgkmcnt(0)" : "+s"(ka), "+s"(kb), "+s"(kc), "+v"(fo[0][0]), "+v"(fo[0][1]), "+v"(fo[0][2]), "+v"(fo[1][0]), "+v"(fo[1][1]), "+v"(fo[1][2]),
               "+v"(fo[2][0]), "+v"(fo[2][1]), "+v"(fo[2][2]), "+v"(fo[3][0]), "+v"(fo[3][1]), "+v"(fo[3][2]), "+v"(so_) :: "memory");
  char* const ws = (char*)kb[1];
  unsigned dw = 0;
  if (w8 < 4) {
    __builtin_amdgcn_s_sleep(X_DELAY);
#pragma unroll
    for (int i = 0; i < 3; ++i) DMA1(fo[0][i], ka[0], lds0 + L_YI + w8 * 3072 + i * 1024);
  } else if (w8 < 6 && job) {
    if (w8 == 5) { __builtin_amdgcn_s_sleep(JOB2_DELAY); if (!jqk) __builtin_amdgcn_s_sleep(2 * JOB2_DELAY); }
    const u64 wqkv = w8 == 4 ? ka[1] : ka[3], wo = w8 == 4 ? ka[2] : kb[0];
    const unsigned dst = (w8 - 4) * SLOT;
#pragma unroll
    for (int q = 0; q < 4; ++q) DMA3(fo[q][0], fo[q][1], fo[q][2], wqkv, dst + q * 3072);
    if (!jqk) {
#pragma unroll
      for (int i = 0; i < 6; ++i) DMA1(so_, wo + i * 12288, dst + (12 + i) * 1024);
    }
  } else if (w8 >= 6) {
    asm volatile("global_load_dword %0, %1, %2" : "=v"(dw) : "v"((qb * 18 + min(l, 17u)) * 128), "s"(w8 == 6 ? ka[2] : kb[0]) : "memory");
  }
  const auto rsWS = __builtin_amdgcn_make_buffer_rsrc((void*)ws, 0, WS_END, 0x00020000);
  const u32x4 pz = {POISON, POISON, POISON, POISON};
  u64 epoch;
  asm volatile("s_load_dwordx2 %0, %1, 0x0" : "=&s"(epoch) : "s"(ws + WS_EPOCH) : "memory");
  asm volatile("s_waitcnt lgkmcnt(0)" : "+s"(epoch) :: "memory");
  epoch += 1;
  const u32x2 tag = {(unsigned)epoch, (unsigned)(epoch >> 32)};
  asm volatile("" : "+s"(w8));
  const unsigned w = w8 & 3;
#define LANE_IDS const unsigned i16 = l & 15, g = l >> 4, r32 = l & 31, hh = l >> 5, tq = i16 >> 2, tp = l & 3
#define FRAG_POS unsigned fr[2], dp[2];                                                                       \
  fr[0] = L_X + fi_pos(i16, g); fr[1] = (fr[0] ^ 64) + 6144; dp[0] = L_X + fi_pos(i16, hh) + 8 * (g & 1) + w * 1536; dp[1] = (dp[0] ^ 64) + 6144; \
  OPAQUE(fr[0]); OPAQUE(fr[1]); OPAQUE(dp[0]); OPAQUE(dp[1])
  auto ldw = [&](u32x4 (&W)[18], const int m, const int k0, const int k1, const unsigned rot) {
#pragma unroll
    for (int kk = k0; kk < k1; ++kk) {
      const unsigned ks = kk + rot >= 6 ? kk + rot - 6 : kk + rot;
#pragma unroll
      for (int i = 0; i < 3; ++i) { SBAR(); W[kk * 3 + i] = __builtin_amdgcn_raw_buffer_load_b128(rsWS, l * 16 + i * 1024, WS_WM + m * 73728 + w * 18432 + ks * 3072, SC1); }
    }
  };
  auto ld_seal = [&](const int m) {
    return __builtin_amdgcn_raw_buffer_load_b128(rsWS, l < ((m & 1) ? 18 : 36) ? l * 128 : WS_END, WS_SEAL + (m >> 1) * 32768 + ((m & 1) ? 144 + 18 * w : 36 * w) * 128, SC1);
  };
  const unsigned rot = qb % 6;
  auto h_frags = [&](u32x4 (&H)[6][2], const unsigned (&fr)[2]) {
#pragma unroll
    for (int kk = 0; kk < 6; ++kk) {
      const unsigned ks = kk + rot >= 6 ? kk + rot - 6 : kk + rot;
      H[kk][0] = lds_r128(fr[0] + ks * 1024 + (L_H - L_X)); H[kk][1] = lds_r128(fr[1] + ks * 1024 + (L_H - L_X));
    }
  };
  auto low_sum = [&](const f32x4 a, const f32x4 c, float (&lt)[2]) {
    lt[0] = (a[0] + a[2]) + (c[0] + c[2]);
    lt[1] = (a[1] + a[3]) + (c[1] + c[3]);
    return (bool)__any(lt[0] < 8.7e-19f || lt[1] < 8.7e-19f);
  };

  if (w8 < 4) {
    const unsigned nl = l & 3, j = (l >> 2) & 7, tt = 2 * w + (l >> 5), r = nl * 8 + tt;
    unsigned rp = L_X + fi_pos(r, j);
    OPAQUE(rp);
    waitvm<0>();
    asm volatile("" :: "v"(dc));
    f32x2 xv[12];
    float mu1;
    {
      u32x4 h[3], xr[3];
#pragma unroll
      for (int i = 0; i < 3; ++i) xr[i] = lds_r128(lds0 + L_YI + tt * 1536 + nl * 384 + j * 16 + i * 128);
#pragma unroll
      for (int i = 0; i < 12; ++i) xv[i] = unpack2(xr[i >> 2][i & 3]);
      mu1 = row8_sum(sum24(xv)) * (1.0f / 192.0f);
#pragma unroll
      for (int i = 0; i < 12; ++i) xv[i] -= mu1;
      const float rs = __builtin_amdgcn_rsqf(row8_sum(sumsq24(xv)) * (1.0f / 192.0f) + 1e-5f);
#pragma unroll
      for (int i = 0; i < 12; ++i) { const f32x2 n = xv[i] * rs; h[i >> 2][i & 3] = cvtpk(n[0], n[1]); }
#pragma unroll
      for (int i = 0; i < 3; ++i) lds_w128(rp + (L_H - L_X) + i * 2048, h[i]);
    }
    SBAR();
    OPAQUE(l);
    unsigned so = tt * FRAME_B + (j >> 1) * 1024 + ((t ^ ((j >> 1) & 1)) * 128) + (((j & 1) ^ (t >> 2)) * 64) + nl * 16;
    const unsigned sso = qb * TILE_B;
    LANE_IDS;
    FRAG_POS;
    unsigned sto = j == 0 ? (tt * 1024 + nl) * 8 : WS_END;
    unsigned sa = L_SP + l * 16;
    unsigned keep = (hh == (i16 >> 3)) ? ~0u : 0u;
    OPAQUE(sto); OPAQUE(so); OPAQUE(sa); OPAQUE(keep);
    ST(1);
    BAR();
    ST(6);
    {
      u32x4 H[6][2], W[18];
      h_frags(H, fr);
      asm volatile("s_barrier" ::: "memory");
      ST(2);
#pragma unroll 1
      for (int sp = 0;; ++sp) {
        const u32x4 seal = ld_seal(3);
        ldw(W, 3, 0, 6, rot);
        SBAR();
        ST(3);
        if (sp == 0) asm volatile("s_barrier" ::: "memory");
        f32x4 vp[3][2], sq[4], e;
        float rd;
        u32x2 pt[2];
        unsigned he = 0, ho = 0;
#pragma unroll
        for (int ww = 0; ww < 4; ++ww) sq[ww] = lds_r128f(sa + ww * 1024);
        SBAR();
        ST(4);
#pragma unroll
        for (int kk = 0; kk < 6; ++kk) {
#pragma unroll
          for (int i = 0; i < 3; ++i) {
            vp[i][0] = mfma16(H[kk][0], W[kk * 3 + i], kk ? vp[i][0] : f32x4{0, 0, 0, 0});
            vp[i][1] = mfma16(H[kk][1], W[kk * 3 + i], kk ? vp[i][1] : f32x4{0, 0, 0, 0});
            XOR_IN(he, ho, W[kk * 3 + i]);
          }
          if (kk == 1) { e = (sq[0] + sq[1]) + (sq[2] + sq[3]); rd = fmaxf(fmaxf(e[0], e[1]), fmaxf(e[2], e[3])); OPAQUE(rd); }
          if (kk == 2) { e -= xor16_max(rd); OPAQUE(e); }
          if (kk == 3) {
#pragma unroll
            for (int k = 0; k < 4; ++k) e[k] = __builtin_amdgcn_exp2f(e[k]);
            rd = (e[0] + e[1]) + (e[2] + e[3]);
            OPAQUE(rd);
          }
          if (kk == 4) { pt[0] = pack4(e * __builtin_amdgcn_rcpf(xor16_add(rd))); OPAQUE(pt[0]); }
          if (kk == 5) {
#pragma unroll
            for (int k = 0; k < 2; ++k) {
              auto q = __builtin_amdgcn_permlane32_swap(pt[0][k], pt[0][k], false, false);
              pt[1][k] = (q[0] ^ q[1] ^ pt[0][k]) & keep;
              pt[0][k] &= keep;
            }
            OPAQUE(pt[0]); OPAQUE(pt[1]);
          }
#pragma unroll
          for (int q = 0; q < 6; ++q) { __builtin_amdgcn_sched_group_barrier(0x8, 1, 0); __builtin_amdgcn_sched_group_barrier(0x402, 3, 0); }
          SBAR();
        }
        OPAQUE(he); OPAQUE(ho);
        ST(7);
        f32x4 y[3][2];
#pragma unroll
        for (int hf = 0; hf < 2; ++hf)
#pragma unroll
          for (int i = 0; i < 3; ++i) y[i][hf] = mfma16k16(pack4(vp[i][hf]), pt[hf], f32x4{0, 0, 0, 0});
        SBAR();
#pragma unroll
        for (int hf = 0; hf < 2; ++hf)
#pragma unroll
          for (int i = 0; i < 3; ++i) lds_w64(dp[hf] + (L_YI - L_X) + i * 512, pack4(y[i][hf]));
        if (__builtin_expect(seal_ok(he, ho, seal, l < 18, tag), 1) || sp >= SPIN_CAP) break;
        asm volatile("" ::: "memory");
      }
    }
    ST(9);
    BAR();
    {
      u32x4 y[3], h[3];
#pragma unroll
      for (int i = 0; i < 3; ++i) y[i] = lds_r128(rp + (L_YI - L_X) + i * 2048);
#pragma unroll
      for (int i = 0; i < 12; ++i) xv[i] += unpack2(y[i >> 2][i & 3]);
      const float mu = row8_sum(sum24(xv)) * (1.0f / 192.0f), sq = row8_sum(sumsq24(xv));
      const float var = fmaxf(fmaf(sq, 1.0f / 192.0f, -mu * mu), 0.f) + 1e-5f;
      const float rs = __builtin_amdgcn_rsqf(var), nb = -mu * rs;
#pragma unroll
      for (int i = 0; i < 3; ++i) {
#pragma unroll
        for (int e = 0; e < 4; ++e) { const f32x2 n = xv[4 * i + e] * rs + nb; h[i][e] = cvtpk(n[0], n[1]); }
        if (i == 0) { OPAQUE(h[0]); ST(5); asm volatile("s_barrier" ::: "memory"); }
        st128(h[i], rsWS, so, WS_H2 + sso + i * 4096);
        SBAR();
      }
      const u32x2 stat = {asu(mu + mu1), asu(var)};
      __builtin_amdgcn_raw_buffer_store_b64(stat, rsWS, sto, WS_ST + b * 32, SC1);
    }
    ST(10);
    const unsigned ring = lds0 + w * SLOT;
    unsigned kr0 = ring + (r32 >> 2) * 128 + (hh ^ (r32 >> 4)) * 64 + (r32 & 3) * 16, kr1 = kr0 ^ 128;
    unsigned vr0 = ring + (g & 1) * 1024 + (hh ^ (g & 1)) * 128 + (tp >> 1) * 64 + tq * 16 + 8 * (tp & 1), vr1 = (vr0 ^ 64) + 512;
    unsigned lo[3] = {l * 16, l * 16 + 4096, l * 16 + 8192}, pw = ring + l * 16, lw = l < 16 ? L_L + (l * 4 + w) * 8 : L_L + 512 + l * 8;
    OPAQUE(kr0); OPAQUE(kr1); OPAQUE(vr0); OPAQUE(vr1); OPAQUE(lo[0]); OPAQUE(lo[1]); OPAQUE(lo[2]); OPAQUE(pw); OPAQUE(lw);
    const u64 hbu = (u64)(ws + WS_H2 + (size_t)t * FRAME_B);
    const u32x4 rs = {(unsigned)hbu, (unsigned)(hbu >> 32) & 0xffffu, (unsigned)FRAME_B, 0x00020000u};
    unsigned toff[8];
#pragma unroll
    for (int i = 0; i < 8; ++i) { toff[i] = (8 * w + ((i + qb) & 7)) * TILE_B; asm volatile("" : "+s"(toff[i])); }
    u32x4 sel;
    { const unsigned one = (tp == (g & 1)) ? 0x3f803f80u : 0u; sel = u32x4{one, one, one, one}; OPAQUE(sel); }
    auto piece = [&](auto Tc, auto Pc) {
      constexpr int T = decltype(Tc)::value, p = decltype(Pc)::value;
      if constexpr (T < 8) {
        if constexpr (p % 4 == 0) setm0<(T % 3) * TILE_B + p * 1024>(ring);
        bdma<(p % 4) * 1024>(lo[p / 4], rs, toff[T]);
      }
    };
#define P6(T, a) piece(IC(T), IC(a)); piece(IC(T), IC(a + 1)); piece(IC(T), IC(a + 2)); piece(IC(T), IC(a + 3)); piece(IC(T), IC(a + 4)); piece(IC(T), IC(a + 5));
    auto read_valid = [&](u32x4 (&KK)[12], const unsigned dst, unsigned off) {
      for (int sp = 0; sp < SPIN_CAP; ++sp) {
#pragma unroll
        for (int k = 0; k < 12; ++k) KK[k] = lds_r128(((k & 1) ? kr1 : kr0) + dst + k * 1024);
        if (__builtin_expect(!poisoned(KK), 1)) break;
#pragma unroll 1
        for (int k = 0; k < 12; ++k) { setm0<0>(ring + dst + k * 1024); bdma<0>(lo[0], rs, off + k * 1024); }
        waitvm<0>();
      }
    };
    unsigned qa = lds0 + L_Q + lo[0], na = lds0 + L_N + r32 * 16;
    OPAQUE(qa); OPAQUE(na);
    waitvm<0>();
    __builtin_amdgcn_s_sleep(B9_DELAY);
    BAR();
    __builtin_amdgcn_s_sleep(TILE0_DELAY);
    P6(0, 0) P6(0, 6)
    if (w == 0 && l == 0) __builtin_amdgcn_raw_buffer_store_b64(tag, rsWS, 0, WS_FLAG2 + b * 8, SC1);
    ST(11);
    BAR();
    u32x4 K0[12];
    if (w == 0) waitvm<1>(); else waitvm<0>();
    ST(12);
    read_valid(K0, 0, toff[0]);
    ST(13);
    piece(IC(1), IC(0)); piece(IC(1), IC(1)); piece(IC(1), IC(2)); piece(IC(1), IC(3));
    BAR();
    ST(14);
    u32x4 Q[12];
    float mref;
    {
      const f32x4 n0 = lds_r128f(na), n1 = lds_r128f(na + 512);
#pragma unroll
      for (int s = 0; s < 6; ++s) Q[s] = lds_r128(qa + s * 1024);
      SBAR();
      const f32x4 s = n0 + n1;
      mref = __builtin_amdgcn_sqrtf(((s[0] + s[1]) + (s[2] + s[3])) * (192.0f * 1.02f));
      OPAQUE(mref);
      SBAR();
#pragma unroll
      for (int s = 6; s < 12; ++s) Q[s] = lds_r128(qa + s * 1024);
      SBAR();
    }
    constexpr int KW = 4, VW = 4;
    bool safe = false, exact = false, late = false;
#pragma unroll 1
    for (;;) {
      f32x16 S[2], O[6], negm;
      u32x4 K[12], V[12], P[2][2];
      f32x4 lsum = {0, 0, 0, 0};
      float pe[2][2];
#pragma unroll
      for (int i = 0; i < 16; ++i) negm[i] = -mref;
#define RK(k, T) K[k] = lds_r128((((k) & 1) ? kr1 : kr0) + ((T) % 3) * TILE_B + (k) * 1024);
#define RV(u, T) { const unsigned a_ = (((u) & 1) ? vr1 : vr0) + ((T) % 3) * TILE_B + ((u) >> 1) * 2048; V[u] = lds_tr2(a_, a_ + 256); }
      auto sm = [&](auto Tc, auto Hc, auto Gc) {
        constexpr int T = decltype(Tc)::value, H = decltype(Hc)::value, G = decltype(Gc)::value;
        if constexpr (G < 8) pe[(G >> 1) & 1][G & 1] = __builtin_amdgcn_exp2f(S[T & 1][8 * H + G]);
        if constexpr (G >= 2 && G <= 8 && G % 2 == 0) { constexpr int q = G / 2 - 1; P[T & 1][H][q] = cvtpk(pe[q & 1][0], pe[q & 1][1]); }
        if constexpr (G == 10) lsum = mfma16(sel, P[T & 1][H], lsum);
      };
      auto put_o = [&](const int un) {
        u32x4 pk;
#pragma unroll
        for (int e = 0; e < 4; ++e) pk[e] = cvtpk(O[un >> 1][8 * (un & 1) + 2 * e], O[un >> 1][8 * (un & 1) + 2 * e + 1]);
        lds_w128(pw + un * 1024, pk);
      };
      auto qkphase = [&](auto Ic) {
        constexpr int I = decltype(Ic)::value;
        auto step = [&](auto Kc) {
          constexpr int k = decltype(Kc)::value;
          if constexpr (I + 1 < 8) {
            S[(I + 1) & 1] = mfma32(I < 0 ? K0[k] : K[k], Q[k], k == 0 ? negm : S[(I + 1) & 1]);
            if constexpr (k + KW < 12 && I >= 0) RK(k + KW, I + 1)
          }
          if constexpr (I < 0 && k < 8) piece(IC(1), IC(4 + k));
          if constexpr (I >= 0) { if constexpr (k & 1) piece(IC(I + 2), IC(6 + k / 2)); if constexpr (k < 11) sm(IC(I), IC(1), IC(k)); }
          if constexpr (I >= 0 && I < 7 && k >= 12 - VW) RV(k - (12 - VW), I)
          if constexpr (I == 7 && k < VW) RV(k, I)
          SBAR();
        };
        step(IC(0)); step(IC(1)); step(IC(2)); step(IC(3)); step(IC(4)); step(IC(5)); step(IC(6)); step(IC(7)); step(IC(8)); step(IC(9)); step(IC(10)); step(IC(11));
      };
      auto pvphase = [&](auto Ic) {
        constexpr int I = decltype(Ic)::value;
        auto step = [&](auto Uc) {
          constexpr int u = decltype(Uc)::value;
          if constexpr (I >= 0) {
            f32x16 z;
#pragma unroll
            for (int e = 0; e < 16; ++e) z[e] = 0.f;
            O[u >> 1] = mfma32(V[u], P[I & 1][u & 1], (I == 0 && !(u & 1)) ? z : O[u >> 1]);
            if constexpr (u & 1) piece(IC(I + 3), IC(u / 2));
            if constexpr (u + VW < 12) RV(u + VW, I)
          }
          if constexpr (I + 1 < 8 && u >= 1) sm(IC(I + 1), IC(0), IC(u - 1));
          if constexpr (I < 0 && (u & 1)) piece(IC(2), IC(u / 2));
          if constexpr (I + 2 < 8) {
            if constexpr (u == 12 - KW) waitvm<(I < 0 ? 12 : I + 3 < 8 ? 6 + (12 - KW) / 2 : 6)>();
            if constexpr (u >= 12 - KW) RK(u - (12 - KW), I + 2)
            if constexpr (u == 11) waitvm<(I + 3 < 8 ? 6 : 0)>();
          }
          if constexpr (I == 7 && u >= 3) {
            put_o(u - 3);
          }
          SBAR();
        };
        step(IC(0)); step(IC(1)); step(IC(2)); step(IC(3)); step(IC(4)); step(IC(5)); step(IC(6)); step(IC(7)); step(IC(8)); step(IC(9)); step(IC(10)); step(IC(11));
      };
#define STALE(v) (__builtin_expect(!safe && __any((v) != (v)), 0))
      bool done = false;
      do {
        qkphase(IC(-1));
        ST(15);
        pvphase(IC(-1));
        ST(16);
        qkphase(IC(0)); pvphase(IC(0)); qkphase(IC(1)); pvphase(IC(1)); qkphase(IC(2));
        const float seen = lsum[0];
        pvphase(IC(2));
        if (STALE(seen)) break;
        qkphase(IC(3)); pvphase(IC(3)); qkphase(IC(4)); pvphase(IC(4)); qkphase(IC(5));
        if (!late) asm volatile("s_barrier" ::: "memory");
        late = true;
        pvphase(IC(5)); qkphase(IC(6)); pvphase(IC(6)); qkphase(IC(7)); pvphase(IC(7));
        if (STALE(lsum[0])) break;
        { const u32x2 ls = {asu(lsum[0]), asu(lsum[1])}; lds_w64(lw, ls); }
        put_o(9); put_o(10); put_o(11);
        done = true;
      } while (false);
#pragma unroll
      for (int k = 0; k < 12; ++k) asm volatile("" : "=v"(K0[k]));
      if (__builtin_expect(done, 1)) {
        ST(17);
        BAR();
        ST(18);
        float lt[2];
        unsigned ll = lo[0];
        OPAQUE(ll);
        ll >>= 4;
        if (__builtin_expect(!low_sum(lds_r128f(L_L + (ll & 15) * 32), lds_r128f(L_L + (ll & 15) * 32 + 16), lt) || exact, 1)) break;
        BAR();
        float mx = -3.0e38f;
#pragma unroll 1
        for (unsigned i = 0; i < 8; ++i) {
#pragma unroll 1
          for (int k = 0; k < 12; ++k) { setm0<0>(ring + k * 1024); bdma<0>(lo[0], rs, (8 * w + i) * TILE_B + k * 1024); }
          waitvm<0>();
          f32x16 s;
#pragma unroll
          for (int e = 0; e < 16; ++e) s[e] = 0.f;
#pragma unroll
          for (int k = 0; k < 12; ++k) s = mfma32(lds_r128(((k & 1) ? kr1 : kr0) + k * 1024), Q[k], s);
#pragma unroll
          for (int e = 0; e < 16; ++e) mx = fmaxf(mx, s[e]);
        }
        lds_w32(L_N + ((ll & 31) * 8 + w * 2 + (ll >> 5)) * 4, mx);
        BAR();
        const f32x4 c = lds_r128f(L_N + (ll & 31) * 32), d = lds_r128f(L_N + (ll & 31) * 32 + 16);
        mref = fmaxf(fmaxf(fmaxf(c[0], c[1]), fmaxf(c[2], c[3])), fmaxf(fmaxf(d[0], d[1]), fmaxf(d[2], d[3])));
        exact = true;
      } else {
        unsigned ll = lo[0];
        OPAQUE(ll);
        for (int sp = 0; sp < SPIN_CAP; ++sp) {
          asm volatile("" ::: "memory");
          const u32x2 f = __builtin_amdgcn_raw_buffer_load_b64(rsWS, ll >> 1, WS_FLAG2 + w * 512, SC1);
          if (__all(f[0] == tag[0] && f[1] == tag[1])) break;
        }
      }
      safe = true;
      asm volatile("s_waitcnt vmcnt(0) lgkmcnt(0)" ::: "memory");
      P6(0, 0) P6(0, 6)
      waitvm<0>();
      piece(IC(1), IC(0)); piece(IC(1), IC(1)); piece(IC(1), IC(2)); piece(IC(1), IC(3));
#pragma unroll
      for (int k = 0; k < 12; ++k) K0[k] = lds_r128(((k & 1) ? kr1 : kr0) + k * 1024);
    }
    ST(19);
    BAR();
    ST_OUT(ws, 0, 12);
  } else {
    LANE_IDS;
    auto job_run = [&](const unsigned at, const unsigned slot, const unsigned sealat, auto younger) {
      unsigned ja[2] = {at + i16 * 384 + ((g ^ (l & 7)) * 16), at + i16 * 384 + (((4 + g) ^ (l & 7)) * 16)}, jb = at + 12288 + (8 * g + tq) * 32 + tp * 8;
      OPAQUE(ja[0]); OPAQUE(ja[1]); OPAQUE(jb);
      waitvm<decltype(younger)::value>();
      ST(2);
      u32x4 a[6], c[6];
#pragma unroll
      for (int kk = 0; kk < 6; ++kk) { a[kk] = lds_r128(ja[kk & 1] + (kk >> 1) * 128); c[kk] = lds_r128(ja[kk & 1] + (kk >> 1) * 128 + 6144); }
      const unsigned o = (hh * 16 + i16) * 16 + (g & 1) * 8;
      f32x4 d = {0, 0, 0, 0};
      u32x2 r;
      if (jqk) {
#pragma unroll
        for (int kk = 0; kk < 6; ++kk) d = mfma16(a[kk], c[kk], d);
        r = pack4(d * QSCALE);
        __builtin_amdgcn_raw_buffer_store_b64(r, rsWS, o, slot, SC1);
      } else {
        f32x4 e = {0, 0, 0, 0};
#pragma unroll
        for (int kk = 0; kk < 6; ++kk) {
          const u32x4 bb = lds_tr2(jb + kk * 1024, jb + kk * 1024 + 128);
          d = mfma16(a[kk], bb, d); e = mfma16(c[kk], bb, e);
        }
        const u32x2 r0 = pack4(d), r1 = pack4(e);
        __builtin_amdgcn_raw_buffer_store_b64(r0, rsWS, o, slot, SC1);
        __builtin_amdgcn_raw_buffer_store_b64(r1, rsWS, o + 512, slot, SC1);
        r = r0 ^ r1;
      }
      const u32x2 half = {fold_xor(r[0], r[1]), hh ? tag[1] : tag[0]};
      __builtin_amdgcn_raw_buffer_store_b64(half, rsWS, r32 == 0 ? hh * 8 : WS_END, sealat, SC1);
    };
    FRAG_POS;
    if (w == 0 || (w == 1 && jqk)) {
      ST(1);
      if (job) job_run(w * SLOT, jslot + (1 - w) * 2 * 73728, WS_SEAL + (1 - w) * 32768 + b * 128, IC(0));
      ST(3);
      if (w == 0) {
        __builtin_amdgcn_raw_buffer_store_b32(POISON, rsWS, l * 4, WS_ST + (t * 1024 + qb * 32) * 8, SC1);
        const unsigned p0 = WS_H2 + t * FRAME_B + (jqk ? qb * 8 : 144 + (qb - 27) * 48) * 1024, np = jqk ? 2 : job ? 0 : 12;
#pragma unroll 1
        for (unsigned i = 0; i < np; ++i) {
#pragma unroll
          for (int q = 0; q < 4; ++q) __builtin_amdgcn_raw_buffer_store_b128(pz, rsWS, l * 16 + q * 1024, p0 + i * 4096, SC1);
        }
        asm volatile("s_nop 1" :: "v"(pz));
        if (jqk) { waitvm<9>(); __builtin_amdgcn_s_sleep(READ_DELAY); } else if (job) { waitvm<1>(); __builtin_amdgcn_s_sleep(VO_DELAY); } else { waitvm<0>(); __builtin_amdgcn_s_sleep(IDLE_DELAY); }
      }
    }
    u32x4 wq[18], wv[18];
    {
      u32x4 H[6][2];
      u32x2 hs[3][2];
      unsigned spa = hh == (i16 >> 3) ? L_SP + w * 1024 + l * 16 : L_SP + 4096 + l * 16;
      OPAQUE(spa);
      __builtin_amdgcn_s_setprio(3);
      ST(4);
      BAR();
#pragma unroll 1
      for (int sp = 0;; ++sp) {
        const u32x4 seal = ld_seal(2);
        ldw(wq, 2, 0, 3, rot);
        SBAR();
        h_frags(H, fr);
#pragma unroll
        for (int i = 0; i < 3; ++i) { hs[i][0] = lds_r64(dp[0] + (L_H - L_X) + i * 512); hs[i][1] = lds_r64(dp[1] + (L_H - L_X) + i * 512); }
        ldw(wq, 2, 3, 6, rot);
        if (sp == 0) {
          if (w == 0) {
            waitvm<19>();
            if (l < 8) __builtin_amdgcn_raw_buffer_store_b64(tag, rsWS, l * 2048, WS_FLAG + b * 8, SC1);
          }
          asm volatile("s_barrier" ::: "memory");
        }
        SBAR();
        f32x4 d[3][2], st[2];
        unsigned he = 0, ho = 0;
#pragma unroll
        for (int kk = 0; kk < 6; ++kk) {
#pragma unroll
          for (int i = 0; i < 3; ++i) {
            d[i][0] = mfma16(wq[kk * 3 + i], H[kk][0], kk ? d[i][0] : f32x4{0, 0, 0, 0});
            d[i][1] = mfma16(wq[kk * 3 + i], H[kk][1], kk ? d[i][1] : f32x4{0, 0, 0, 0});
            XOR_IN(he, ho, wq[kk * 3 + i]);
          }
          MIXV6();
        }
        ST(19);
        const bool ok = seal_ok(he, ho, seal, l < 36, tag);
#pragma unroll
        for (int hf = 0; hf < 2; ++hf) {
          st[hf] = f32x4{0, 0, 0, 0};
#pragma unroll
          for (int i = 0; i < 3; ++i) st[hf] = mfma16k16(hs[i][hf], pack4(d[i][hf]), st[hf]);
          lds_w128f(spa ^ (hf * 512), st[hf]);
        }
        if (__builtin_expect(ok, 1) || sp >= SPIN_CAP) break;
        asm volatile("" ::: "memory");
      }
      asm volatile("" :: "v"(dc), "v"(dw));
    }
    ST(10);
    BAR();
    __builtin_amdgcn_s_setprio(0);
    u32x4 seal = ld_seal(0);
    {
      ldw(wq, 0, 0, WQ_SPLIT, 0);
      u32x2 fl = __builtin_amdgcn_raw_buffer_load_b64(rsWS, l * 8, WS_FLAG + t * 2048 + w * 512, SC1);
      ST(5);
      BAR();
      ldw(wq, 0, WQ_SPLIT, 6, 0);
      if (__builtin_expect(!__all(fl[0] == tag[0] && fl[1] == tag[1]), 0)) {
        for (int sp = 0; sp < SPIN_CAP; ++sp) {
          asm volatile("" ::: "memory");
          const u32x2 f2 = __builtin_amdgcn_raw_buffer_load_b64(rsWS, l * 8, WS_FLAG + t * 2048 + w * 512, SC1);
          if (__all(f2[0] == tag[0] && f2[1] == tag[1])) break;
        }
      }
      ST(18);
      BAR();
    }
    unsigned hqa[2][2], own[2][2];
#pragma unroll
    for (int hf = 0; hf < 2; ++hf) {
      const unsigned in = lds0 + L_HQ + ((4 * hf + tq) ^ hh) * 128 + ((g & 1) ^ hf) * 64 + tp * 16;
      hqa[hf][0] = in + hh * 1024; hqa[hf][1] = in + 2048 + hh * (SLOT - 2048);
      OPAQUE(hqa[hf][0]); OPAQUE(hqa[hf][1]);
    }
    const unsigned tok = 16 * (g & 1) + i16;
    unsigned nw = lds0 + L_N + l * 16 + w * 4;
    OPAQUE(nw);
    unsigned qw = L_Q + (3 * w * 64 + hh * 32 + i16) * 16 + 8 * (g & 1);
    own[0][0] = w * SLOT + L_HQ + (tq ^ (w & 1)) * 128 + hh * 64 + tp * 16 + 8 * (g & 1); own[0][1] = (own[0][0] ^ 64) + 512;
    own[1][0] = own[0][0] ^ 128; own[1][1] = own[0][1] ^ 128;
    OPAQUE(qw); OPAQUE(own[0][0]); OPAQUE(own[0][1]); OPAQUE(own[1][0]); OPAQUE(own[1][1]);
    if (b == 0 && w == 3 && l == 0) __builtin_amdgcn_raw_buffer_store_b64(tag, rsWS, 0, WS_EPOCH, SC1);
    for (int sp = 0; sp < SPIN_CAP && !sealed(wq, seal, l < 36, tag); ++sp) {
      asm volatile("" ::: "memory");
      seal = ld_seal(0);
      ldw(wq, 0, 0, 6, 0);
    }
    if (w == 1 && job && !jqk) job_run(SLOT, jslot, WS_SEAL + b * 128, IC(0));
    ST(6);
    BAR();
    ST(7);
    u32x4 hq[3];
    {
      const u64 hbu = (u64)(ws + WS_H2 + (size_t)t * FRAME_B);
      const u32x4 rs = {(unsigned)hbu, (unsigned)(hbu >> 32) & 0xffffu, (unsigned)FRAME_B, 0x00020000u};
      for (int sp = 0; sp < SPIN_CAP; ++sp) {
        setm0<L_HQ>(lds0 + w * SLOT);
        bdma<0>(l * 16, rs, qb * TILE_B + w * 3072); bdma<1024>(l * 16, rs, qb * TILE_B + w * 3072); bdma<2048>(l * 16, rs, qb * TILE_B + w * 3072);
        waitvm<0>();
#pragma unroll
        for (int i = 0; i < 3; ++i) { const u32x2 u0 = lds_r64(own[i & 1][0] + i * 1024), u1 = lds_r64(own[i & 1][1] + i * 1024); hq[i] = u32x4{u0[0], u0[1], u1[0], u1[1]}; }
        if (!poisoned(hq)) break;
      }
    }
    ST(8);
    BAR();
    ST(9);
    auto gemm = [&](const u32x4 (&wf)[18], const u32x4 (&bf)[6][2], auto&& fin) {
      f32x4 d[3][2];
#pragma unroll
      for (int i = 0; i < 3; ++i) {
#pragma unroll
        for (int ks = 0; ks < 6; ++ks) {
          d[i][0] = mfma16(wf[ks * 3 + i], bf[ks][0], ks ? d[i][0] : f32x4{0, 0, 0, 0});
          d[i][1] = mfma16(wf[ks * 3 + i], bf[ks][1], ks ? d[i][1] : f32x4{0, 0, 0, 0});
          if (ks == 0) SBAR();
        }
        if (i > 0) fin(i - 1, d[i - 1]);
        SBAR();
      }
      fin(2, d[2]);
    };
    {
      u32x4 bf[6][2];
#pragma unroll
      for (int ks = 0; ks < 6; ++ks)
#pragma unroll
        for (int hf = 0; hf < 2; ++hf) {
          if (ks == 3) { hqa[hf][0] += 2 * SLOT; hqa[hf][1] += 2 * SLOT; }
          bf[ks][hf] = lds_r128(ks % 3 == 1 ? hqa[hf][1] : hqa[hf][0] + (ks % 3 == 2 ? SLOT + 1024 : 0));
        }
      SBAR();
      f32x2 n[2] = {{0.f, 0.f}, {0.f, 0.f}};
      gemm(wq, bf, [&](const int i, const f32x4 (&d)[2]) {
#pragma unroll
        for (int hf = 0; hf < 2; ++hf) {
          lds_w64(qw + i * 1024 + hf * 256, pack4(d[hf]));
          const f32x2 a = {d[hf][0], d[hf][1]}, c = {d[hf][2], d[hf][3]};
          n[hf] = __builtin_elementwise_fma(a, a, n[hf]); n[hf] = __builtin_elementwise_fma(c, c, n[hf]);
        }
      });
      auto rr = __builtin_amdgcn_permlane16_swap(asu(n[0][0] + n[0][1]), asu(n[1][0] + n[1][1]), false, false);
      lds_w32(nw, asf(rr[0]) + asf(rr[1]));
    }
    ST(11);
    BAR();
    unsigned oo = (t * 1024 + qb * 32 + tok) * 384 + (6 * w + hh) * 16;
    OPAQUE(oo);
    float lt[2];
    u32x4 pick;
#pragma unroll
    for (int e = 0; e < 4; ++e) {
      const unsigned d = 8 * (e >> 1) + 4 * (g & 1) + 2 * (e & 1);
      pick[e] = (i16 == d ? 0x3f80u : 0u) | (i16 == d + 1 ? 0x3f800000u : 0u);
    }
    unsigned pr[2], la = lds0 + L_L + i16 * 32;
    pr[0] = lds0 + hh * SLOT + 3 * w * 1024 + ((g & 1) * 32 + i16) * 16; pr[1] = pr[0] + 2 * SLOT;
    OPAQUE(pick); OPAQUE(pr[0]); OPAQUE(pr[1]); OPAQUE(la);
    f32x4 x1[3][2];
    {
      __builtin_amdgcn_s_sleep(40);
      u32x2 st[2];
      for (int sp = 0; sp < SPIN_CAP; ++sp) {
        asm volatile("" ::: "memory");
        st[0] = __builtin_amdgcn_raw_buffer_load_b64(rsWS, i16 * 8, WS_ST + (t * 1024 + qb * 32) * 8, SC1);
        st[1] = __builtin_amdgcn_raw_buffer_load_b64(rsWS, i16 * 8 + 128, WS_ST + (t * 1024 + qb * 32) * 8, SC1);
        if (!__any(max(max(st[0][0], st[0][1]), max(st[1][0], st[1][1])) == POISON)) break;
      }
#pragma unroll
      for (int hf = 0; hf < 2; ++hf) {
        const float mu = asf(st[hf][0]), sd = __builtin_amdgcn_sqrtf(asf(st[hf][1]));
#pragma unroll
        for (int i = 0; i < 3; ++i) {
          const f32x4 h = {bflo(hq[i][2 * hf]), bfhi(hq[i][2 * hf]), bflo(hq[i][2 * hf + 1]), bfhi(hq[i][2 * hf + 1])};
          x1[i][hf] = h * sd + mu;
          OPAQUE(x1[i][hf]);
        }
      }
    }
    BAR();
    ST(16);
    for (int sp = 0; sp < SPIN_CAP; ++sp) {
      asm volatile("" ::: "memory");
      seal = ld_seal(1);
      ldw(wv, 1, 0, 6, 0);
      if (sealed(wv, seal, l < 18, tag)) break;
    }
    ST(12);
#pragma unroll 1
    for (bool exact = false;; exact = true) {
      BAR();
      ST(13);
      u32x4 p[3][2][2];
#pragma unroll
      for (int i = 0; i < 3; ++i)
#pragma unroll
        for (int hf = 0; hf < 2; ++hf) { p[i][hf][0] = lds_r128(pr[0] + i * 1024 + hf * 256); p[i][hf][1] = lds_r128(pr[1] + i * 1024 + hf * 256); }
      const f32x4 l0 = lds_r128f(la), l1 = lds_r128f(la + 16);
      SBAR();
#pragma unroll
      for (int i = 0; i < 3; ++i)
#pragma unroll
        for (int hf = 0; hf < 2; ++hf) lds_w64(dp[hf] + (L_FI - L_X) + i * 512, pack4(mfma16(pick, p[i][hf][1], mfma16(pick, p[i][hf][0], f32x4{0, 0, 0, 0}))));
      if (__builtin_expect(!low_sum(l0, l1, lt) || exact, 1)) break;
      BAR();
      BAR();
    }
    ST(14);
    const float linv[2] = {__builtin_amdgcn_rcpf(lt[0]), __builtin_amdgcn_rcpf(lt[1])};
    BAR();
    ST(15);
    u32x4 bf[6][2];
#pragma unroll
    for (int ks = 0; ks < 6; ++ks) { bf[ks][0] = lds_r128(fr[0] + (L_FI - L_X) + ks * 1024); bf[ks][1] = lds_r128(fr[1] + (L_FI - L_X) + ks * 1024); }
    SBAR();
    f32x4 d[3][2];
#pragma unroll
    for (int ks = 0; ks < 6; ++ks)
#pragma unroll
      for (int i = 0; i < 3; ++i) {
        d[i][0] = mfma16(wv[ks * 3 + i], bf[ks][0], ks ? d[i][0] : f32x4{0, 0, 0, 0});
        d[i][1] = mfma16(wv[ks * 3 + i], bf[ks][1], ks ? d[i][1] : f32x4{0, 0, 0, 0});
      }
#pragma unroll
    for (int i = 0; i < 3; ++i) {
      const u32x2 y0 = pack4(d[i][0] * linv[0] + x1[i][0]), y1 = pack4(d[i][1] * linv[1] + x1[i][1]);
      auto s0 = __builtin_amdgcn_permlane16_swap(y0[0], y1[0], false, false);
      auto s1 = __builtin_amdgcn_permlane16_swap(y0[1], y1[1], false, false);
      const u32x4 o = {s0[0], s1[0], s0[1], s1[1]};
      *(__attribute__((address_space(1))) u32x4*)(kc + oo + i * 32) = o;
    }
    ST(17);
    ST_OUT(ws, 12, 20);
  }
}

extern "C" void kernel_launch(void* const* d_in, const int* in_sizes, int n_in, void* d_out, int out_size, void* d_ws, size_t ws_size, hipStream_t stream) {
  (void)hipFuncSetAttribute((const void*)k_timesformer, hipFuncAttributeMaxDynamicSharedMemorySize, LDS_BYTES);
  k_timesformer<<<256, 512, LDS_BYTES, stream>>>((const char*)d_in[0], (const char*)d_in[1], (const char*)d_in[2], (const char*)d_in[3], (const char*)d_in[4],
                                                 (char*)d_ws, (char*)d_out);
}
```

```cpp
#include <hip/hip_runtime.h>
#include <hip/hip_bf16.h>
#include <type_traits>

typedef short s16x4 __attribute__((ext_vector_type(4)));
typedef __bf16 b16x8 __attribute__((ext_vector_type(8)));
typedef __bf16 b16x2 __attribute__((ext_vector_type(2)));
typedef float f32x2 __attribute__((ext_vector_type(2)));
typedef float f32x4 __attribute__((ext_vector_type(4)));
typedef float f32x16 __attribute__((ext_vector_type(16)));
typedef unsigned u32x2 __attribute__((ext_vector_type(2)));
typedef unsigned u32x4 __attribute__((ext_vector_type(4)));
typedef unsigned long long u64;
typedef u64 u64x4 __attribute__((ext_vector_type(4)));
typedef u64 u64x2 __attribute__((ext_vector_type(2)));
#define LAS __attribute__((address_space(3)))
#define DI __device__ __forceinline__
#define SBAR() __builtin_amdgcn_sched_barrier(0)

constexpr int TILE_B = 12288;
constexpr int FRAME_B = 393216;
constexpr unsigned WS_H2 = 0, WS_ST = 8 * FRAME_B, WS_WM = 16 * FRAME_B, WS_SEAL = WS_WM + 4 * 73728, WS_FLAG = WS_SEAL + 2 * 256 * 128, WS_FLAG2 = WS_FLAG + 8 * 2048, WS_EPOCH = WS_FLAG2 + 2048, WS_END = WS_EPOCH + 256;
constexpr float QSCALE = 0.07216878364870322f * 1.4426950408889634f;
constexpr int SC1 = 16;
constexpr unsigned POISON = 0xffffffffu;
constexpr unsigned CODE_BYTES = 22528;
constexpr int TILE0_DELAY = 2, JOB2_DELAY = 6, X_DELAY = 1, READ_DELAY = 13, VO_DELAY = 10, IDLE_DELAY = 17, WQ_SPLIT = 4, B9_DELAY = 6;
constexpr int SPIN_CAP = 1 << 16;


constexpr int PROBE = 17, ROLE = 1; constexpr size_t WS_T0 = 32u << 20;
#define ST_DECL u64 st_ = 0, t0_ = 0
#define ST(k) do { if ((k) == PROBE || (k) == 0) { SBAR(); u64 t_; asm volatile("s_memrealtime %0\n\ts_waitcnt lgkmcnt(0)" : "=s"(t_) :: "memory"); SBAR(); \
    if ((k) == PROBE) st_ = t_; \
    if ((k) == 0) t0_ = t_; } STO_##k } while (0)
#define STO_(k)
#define STO_0
#define STO_1
#define STO_2
#define STO_3
#define STO_4
#define STO_5
#define STO_6
#define STO_7
#define STO_8
#define STO_9
#define STO_10
#define STO_11
#define STO_12
#define STO_13
#define STO_14
#define STO_15
#define STO_16 if (threadIdx.x == 0) __hip_atomic_store((__attribute__((address_space(1))) u64*)(ws + WS_T0) + blockIdx.x, t0_, __ATOMIC_RELAXED, __HIP_MEMORY_SCOPE_AGENT);
#define STO_17
#define STO_18
#define STO_19
#define ST_OUT(ws, k0, k1) do { if (ROLE == ((k0) != 0)) { u64 m_ = ~0ull; \
    for (int q_ = 0; q_ < 4; ++q_) { u64 v_ = __hip_atomic_load((__attribute__((address_space(1))) u64*)(ws + WS_T0) + (threadIdx.x & 63) * 4 + q_, __ATOMIC_RELAXED, __HIP_MEMORY_SCOPE_AGENT); m_ = v_ < m_ ? v_ : m_; } \
    for (int o_ = 32; o_; o_ >>= 1) { u64 v_ = __shfl_xor(m_, o_); m_ = v_ < m_ ? v_ : m_; } \
    long long d_ = (long long)(st_ - m_); if (d_ < 0) d_ = 0; if (d_ > 5000) d_ = 5000; \
    const u64 e_ = m_ + 4000 + 4 * d_; while (__builtin_amdgcn_s_memrealtime() < e_) __builtin_amdgcn_s_sleep(1); } } while (0)


DI float asf(unsigned u) { return __builtin_bit_cast(float, u); }
DI unsigned asu(float f) { return __builtin_bit_cast(unsigned, f); }
DI unsigned cvtpk(float lo, float hi) { f32x2 v = {lo, hi}; return __builtin_bit_cast(unsigned, __builtin_convertvector(v, b16x2)); }
DI float bflo(unsigned u) { return asf(u << 16); }
DI float bfhi(unsigned u) { return asf(u & 0xffff0000u); }
DI f32x16 mfma32(u32x4 a, u32x4 b, f32x16 c) { return __builtin_amdgcn_mfma_f32_32x32x16_bf16(__builtin_bit_cast(b16x8, a), __builtin_bit_cast(b16x8, b), c, 0, 0, 0); }
DI f32x4 mfma16(u32x4 a, u32x4 b, f32x4 c) { return __builtin_amdgcn_mfma_f32_16x16x32_bf16(__builtin_bit_cast(b16x8, a), __builtin_bit_cast(b16x8, b), c, 0, 0, 0); }
DI f32x4 mfma16k16(u32x2 a, u32x2 b, f32x4 c) { return __builtin_amdgcn_mfma_f32_16x16x16bf16_1k(__builtin_bit_cast(s16x4, a), __builtin_bit_cast(s16x4, b), c, 0, 0, 0); }
DI u32x2 pack4(f32x4 v) { u32x2 r = {cvtpk(v[0], v[1]), cvtpk(v[2], v[3])}; return r; }
DI u32x4 lds_r128(unsigned a) { return *(LAS u32x4*)(size_t)a; }
DI f32x4 lds_r128f(unsigned a) { return *(LAS f32x4*)(size_t)a; }
DI u32x2 lds_r64(unsigned a) { return *(LAS u32x2*)(size_t)a; }
DI void lds_w128(unsigned a, u32x4 v) { *(LAS u32x4*)(size_t)a = v; }
DI void lds_w128f(unsigned a, f32x4 v) { *(LAS f32x4*)(size_t)a = v; }
DI void lds_w64(unsigned a, u32x2 v) { *(LAS u32x2*)(size_t)a = v; }
DI void lds_w32(unsigned a, float v) { *(LAS float*)(size_t)a = v; }
DI u32x2 lds_tr(unsigned a) { return __builtin_bit_cast(u32x2, __builtin_amdgcn_ds_read_tr16_b64_v4i16((LAS s16x4*)(size_t)a)); }
DI u32x4 lds_tr2(unsigned a0, unsigned a1) { u32x2 x = lds_tr(a0), y = lds_tr(a1); u32x4 r = {x[0], x[1], y[0], y[1]}; return r; }
#define BAR() asm volatile("s_waitcnt lgkmcnt(0)\n\ts_barrier" ::: "memory")
#define BARV(n) asm volatile("s_waitcnt vmcnt(" #n ") lgkmcnt(0)\n\ts_barrier" ::: "memory")
template <int CTRL> DI float dpp_add(float v) { return v + asf(__builtin_amdgcn_update_dpp(0u, asu(v), CTRL, 0xf, 0xf, true)); }
DI float xor16_add(float v) { auto r = __builtin_amdgcn_permlane16_swap(asu(v), asu(v), false, false); return asf(r[0]) + asf(r[1]); }
DI float xor16_max(float v) { auto r = __builtin_amdgcn_permlane16_swap(asu(v), asu(v), false, false); return __builtin_amdgcn_fmed3f(asf(r[0]), asf(r[1]), __builtin_inff()); }
DI unsigned umax3(unsigned a, unsigned b, unsigned c) { return max(max(a, b), c); }
template <int N> DI bool poisoned(const u32x4 (&v)[N]) {
  unsigned m = 0;
#pragma unroll
  for (int i = 0; i < N; ++i) { m = umax3(m, v[i][0], v[i][1]); m = umax3(m, v[i][2], v[i][3]); }
  return __any(m == POISON);
}
DI u32x4 unpoison(u32x4 v) { u32x4 r = {min(v[0], POISON - 1), min(v[1], POISON - 1), min(v[2], POISON - 1), min(v[3], POISON - 1)}; return r; }
DI unsigned fi_pos(unsigned r, unsigned c8) {
  return (((r >> 4) * 6 + (c8 >> 2)) * 64 + (c8 & 3) * 16 + ((r & 15) ^ ((c8 & 1) | (((r >> 3) & 1) << 1) | ((r >> 4) << 2)))) * 16;
}
DI float row8_sum(float s) { s = dpp_add<0x124>(s); s = dpp_add<0x128>(s); return xor16_add(s); }
DI float sum24(const f32x2 (&v)[12]) {
  f32x2 s0 = v[0] + v[1], s1 = v[2] + v[3];
#pragma unroll
  for (int i = 4; i < 12; i += 2) { s0 += v[i]; s1 += v[i + 1]; }
  s0 += s1;
  return s0[0] + s0[1];
}
DI float sumsq24(const f32x2 (&v)[12]) {
  f32x2 q0 = v[0] * v[0], q1 = v[1] * v[1];
#pragma unroll
  for (int i = 2; i < 12; i += 2) { q0 = __builtin_elementwise_fma(v[i], v[i], q0); q1 = __builtin_elementwise_fma(v[i + 1], v[i + 1], q1); }
  q0 += q1;
  return q0[0] + q0[1];
}
DI f32x2 unpack2(unsigned u) { f32x2 r = {bflo(u), bfhi(u)}; return r; }

constexpr unsigned SLOT = 36864, L_X = 3 * SLOT, L_H = 4 * SLOT, L_N = L_H + TILE_B, L_L = L_N + 1024, L_PT = L_L + 1024, LDS_BYTES = 163840;
constexpr unsigned L_VI = L_X, L_SP = L_X + TILE_B, L_YI = L_X + 20480;
constexpr unsigned L_Q = L_H, L_FI = L_H;

#define DMA3(v0, v1, v2, sbase, ldsa) \
  asm volatile("s_mov_b32 m0, %4\n\ts_nop 0\n\tglobal_load_lds_dwordx4 %0, %3\n\tglobal_load_lds_dwordx4 %1, %3 offset:1024\n\tglobal_load_lds_dwordx4 %2, %3 offset:2048" \
               :: "v"(v0), "v"(v1), "v"(v2), "s"(sbase), "s"(ldsa) : "memory")
#define DMA1(v0, sbase, ldsa) \
  asm volatile("s_mov_b32 m0, %2\n\ts_nop 0\n\tglobal_load_lds_dwordx4 %0, %1" :: "v"(v0), "s"(sbase), "s"(ldsa) : "memory")

template <int OFF> DI void bdma(unsigned voff, u32x4 rs, unsigned soff) { asm volatile("buffer_load_dwordx4 %0, %1, %2 offen offset:%3 sc1 lds" :: "v"(voff), "s"(rs), "s"(soff), "i"(OFF) : "memory"); }
template <int OFF> DI void setm0(unsigned base) { asm volatile("s_add_u32 m0, %0, %1\n\ts_nop 0" :: "s"(base), "i"(OFF) : "memory", "scc"); }
template <int N> DI void waitvm() { asm volatile("s_waitcnt vmcnt(%0)" :: "i"(N) : "memory"); }
#define IC(x) std::integral_constant<int, (x)>{}
#define OPAQUE(v) asm volatile("" : "+v"(v))
template <class R> DI void st128(u32x4 v, R rs, unsigned voff, unsigned soff) { __builtin_amdgcn_raw_buffer_store_b128(v, rs, voff, soff, SC1); asm volatile("s_nop 1" :: "v"(v)); }
template <int CTRL> DI unsigned dpp_xor(unsigned v) { return v ^ __builtin_amdgcn_update_dpp(0u, v, CTRL, 0xf, 0xf, true); }
DI unsigned fold_xor(unsigned e, unsigned o) {
  auto r = __builtin_amdgcn_permlane32_swap(e, o, false, false);
  unsigned u = r[0] ^ r[1];
  auto q = __builtin_amdgcn_permlane16_swap(u, u, false, false);
  u = q[0] ^ q[1];
  u = dpp_xor<0x128>(u); u = dpp_xor<0x124>(u); u = dpp_xor<0x4e>(u); return dpp_xor<0xb1>(u);
}
DI bool seal_ok(unsigned e, unsigned o, u32x4 seal, bool has, u32x2 tag) {
  const unsigned bad = has ? (seal[1] ^ tag[0]) | (seal[3] ^ tag[1]) : 0u;
  return __builtin_amdgcn_ballot_w64((fold_xor(e ^ seal[0], o ^ seal[2]) | bad) != 0) == 0;
}
DI unsigned xor3(unsigned a, unsigned b, unsigned c) { return __builtin_amdgcn_bitop3_b32(a, b, c, 0x96); }
#define XOR_IN(e, o, v) do { e = xor3(e, (v)[0], (v)[2]); o = xor3(o, (v)[1], (v)[3]); } while (0)
#define MIXV6() _Pragma("unroll") for (int q_ = 0; q_ < 6; ++q_) { __builtin_amdgcn_sched_group_barrier(0x8, 1, 0); __builtin_amdgcn_sched_group_barrier(0x2, 1, 0); } SBAR()
template <int N> DI bool sealed(const u32x4 (&v)[N], u32x4 seal, bool has, u32x2 tag) {
  unsigned e = 0, o = 0;
#pragma unroll
  for (int i = 0; i < N; ++i) XOR_IN(e, o, v[i]);
  return seal_ok(e, o, seal, has, tag);
}
DI bool tags_ok(u32x4 f, u32x2 tag) { return f[0] == tag[0] && f[1] == tag[1] && f[2] == tag[0] && f[3] == tag[1]; }

__global__ __launch_bounds__(512, 2) void k_timesformer(const char* x_, const char* wqkv_t_, const char* wo_t_, const char* wqkv_s_, const char* wo_s_, char* ws_, char* out_) {
  unsigned dc;
  u64x4 ka; u64x2 kb; u64 kc;
  {
    u64 pc;
    asm volatile("s_load_dwordx8 %2, %6, 0x0\n\ts_load_dwordx4 %3, %6, 0x20\n\ts_load_dwordx2 %4, %6, 0x30\n\ts_getpc_b64 %1\n\tglobal_load_dword %0, %5, %1"
                 : "=v"(dc), "=&s"(pc), "=&s"(ka), "=&s"(kb), "=&s"(kc) : "v"(min(threadIdx.x * 128, CODE_BYTES)), "s"(__builtin_amdgcn_kernarg_segment_ptr()) : "memory");
  }
  constexpr unsigned lds0 = 0;
  const unsigned b = blockIdx.x;
  unsigned w8 = __builtin_amdgcn_readfirstlane(threadIdx.x >> 6);
  unsigned l = threadIdx.x & 63;
  const unsigned t = b & 7, qb = b >> 3;
  w8 = (w8 & 4) | ((w8 + qb / 6) & 3);
  ST_DECL;
  ST(0);
  const bool jqk = b < 144, job = b < 216;
  const unsigned jp = jqk ? b >> 1 : b - 144, jw = jp / 18, jks = (jp - jw * 18) / 3, jtau = jp - jw * 15 - jks * 3;
  const unsigned jrow = jks * 32 + (jqk ? (b & 1) * 16 : 0), jslot = WS_WM + (jqk ? b * 512 : 73728 + jp * 1024);
  unsigned fo[4][3];
  if (w8 < 4) {
#pragma unroll
    for (int i = 0; i < 3; ++i) { const unsigned p = i * 64 + l; fo[0][i] = p * 16 + (p >= 96 ? FRAME_B - 1536 : 0) + w8 * 2 * FRAME_B + b * 1536; fo[1][i] = fo[2][i] = fo[3][i] = 0; }
  } else {
    const unsigned ra = jrow * 1152 + (jqk ? 0 : 768), rb = jqk ? jtau * (16 * 1152) + 384 : ra + 16 * 1152;
#pragma unroll
    for (int i = 0; i < 3; ++i) {
      const unsigned pos = i * 64 + l, row = (pos * 2731) >> 16, o = row * 1152 + (((pos - row * 24) ^ row) * 16) - i * 1024;
      fo[0][i] = o + ra; fo[1][i] = o + (ra + 8 * 1152); fo[2][i] = o + rb; fo[3][i] = o + (rb + 8 * 1152);
    }
  }
  unsigned so_ = (l >> 1) * 384 + (l & 1) * 16 + jtau * 32;
  asm volatile("s_waitcnt lgkmcnt(0)" : "+s"(ka), "+s"(kb), "+s"(kc), "+v"(fo[0][0]), "+v"(fo[0][1]), "+v"(fo[0][2]), "+v"(fo[1][0]), "+v"(fo[1][1]), "+v"(fo[1][2]),
               "+v"(fo[2][0]), "+v"(fo[2][1]), "+v"(fo[2][2]), "+v"(fo[3][0]), "+v"(fo[3][1]), "+v"(fo[3][2]), "+v"(so_) :: "memory");
  char* const ws = (char*)kb[1];
  unsigned dw = 0;
  if (w8 < 4) {
    __builtin_amdgcn_s_sleep(X_DELAY);
#pragma unroll
    for (int i = 0; i < 3; ++i) DMA1(fo[0][i], ka[0], lds0 + L_YI + w8 * 3072 + i * 1024);
  } else if (w8 < 6 && job) {
    if (w8 == 5) { __builtin_amdgcn_s_sleep(JOB2_DELAY); if (!jqk) __builtin_amdgcn_s_sleep(2 * JOB2_DELAY); }
    const u64 wqkv = w8 == 4 ? ka[1] : ka[3], wo = w8 == 4 ? ka[2] : kb[0];
    const unsigned dst = (w8 - 4) * SLOT;
#pragma unroll
    for (int q = 0; q < 4; ++q) DMA3(fo[q][0], fo[q][1], fo[q][2], wqkv, dst + q * 3072);
    if (!jqk) {
#pragma unroll
      for (int i = 0; i < 6; ++i) DMA1(so_, wo + i * 12288, dst + (12 + i) * 1024);
    }
  } else if (w8 >= 6) {
    asm volatile("global_load_dword %0, %1, %2" : "=v"(dw) : "v"((qb * 18 + min(l, 17u)) * 128), "s"(w8 == 6 ? ka[2] : kb[0]) : "memory");
  }
  const auto rsWS = __builtin_amdgcn_make_buffer_rsrc((void*)ws, 0, WS_END, 0x00020000);
  const u32x4 pz = {POISON, POISON, POISON, POISON};
  u64 epoch;
  asm volatile("s_load_dwordx2 %0, %1, 0x0" : "=&s"(epoch) : "s"(ws + WS_EPOCH) : "memory");
  asm volatile("s_waitcnt lgkmcnt(0)" : "+s"(epoch) :: "memory");
  epoch += 1;
  const u32x2 tag = {(unsigned)epoch, (unsigned)(epoch >> 32)};
  asm volatile("" : "+s"(w8));
  const unsigned w = w8 & 3;
#define LANE_IDS const unsigned i16 = l & 15, g = l >> 4, r32 = l & 31, hh = l >> 5, tq = i16 >> 2, tp = l & 3
#define FRAG_POS unsigned fr[2], dp[2];                                                                       \
  fr[0] = L_X + fi_pos(i16, g); fr[1] = (fr[0] ^ 64) + 6144; dp[0] = L_X + fi_pos(i16, hh) + 8 * (g & 1) + w * 1536; dp[1] = (dp[0] ^ 64) + 6144; \
  OPAQUE(fr[0]); OPAQUE(fr[1]); OPAQUE(dp[0]); OPAQUE(dp[1])
  auto ldw = [&](u32x4 (&W)[18], const int m, const int k0, const int k1, const unsigned rot) {
#pragma unroll
    for (int kk = k0; kk < k1; ++kk) {
      const unsigned ks = kk + rot >= 6 ? kk + rot - 6 : kk + rot;
#pragma unroll
      for (int i = 0; i < 3; ++i) { SBAR(); W[kk * 3 + i] = __builtin_amdgcn_raw_buffer_load_b128(rsWS, l * 16 + i * 1024, WS_WM + m * 73728 + w * 18432 + ks * 3072, SC1); }
    }
  };
  auto ld_seal = [&](const int m) {
    return __builtin_amdgcn_raw_buffer_load_b128(rsWS, l < ((m & 1) ? 18 : 36) ? l * 128 : WS_END, WS_SEAL + (m >> 1) * 32768 + ((m & 1) ? 144 + 18 * w : 36 * w) * 128, SC1);
  };
  const unsigned rot = qb % 6;
  auto h_frags = [&](u32x4 (&H)[6][2], const unsigned (&fr)[2]) {
#pragma unroll
    for (int kk = 0; kk < 6; ++kk) {
      const unsigned ks = kk + rot >= 6 ? kk + rot - 6 : kk + rot;
      H[kk][0] = lds_r128(fr[0] + ks * 1024 + (L_H - L_X)); H[kk][1] = lds_r128(fr[1] + ks * 1024 + (L_H - L_X));
    }
  };
  auto low_sum = [&](const f32x4 a, const f32x4 c, float (&lt)[2]) {
    lt[0] = (a[0] + a[2]) + (c[0] + c[2]);
    lt[1] = (a[1] + a[3]) + (c[1] + c[3]);
    return (bool)__any(lt[0] < 8.7e-19f || lt[1] < 8.7e-19f);
  };

  if (w8 < 4) {
    const unsigned nl = l & 3, j = (l >> 2) & 7, tt = 2 * w + (l >> 5), r = nl * 8 + tt;
    unsigned rp = L_X + fi_pos(r, j);
    OPAQUE(rp);
    waitvm<0>();
    asm volatile("" :: "v"(dc));
    f32x2 xv[12];
    float mu1;
    {
      u32x4 h[3], xr[3];
#pragma unroll
      for (int i = 0; i < 3; ++i) xr[i] = lds_r128(lds0 + L_YI + tt * 1536 + nl * 384 + j * 16 + i * 128);
#pragma unroll
      for (int i = 0; i < 12; ++i) xv[i] = unpack2(xr[i >> 2][i & 3]);
      mu1 = row8_sum(sum24(xv)) * (1.0f / 192.0f);
#pragma unroll
      for (int i = 0; i < 12; ++i) xv[i] -= mu1;
      const float rs = __builtin_amdgcn_rsqf(row8_sum(sumsq24(xv)) * (1.0f / 192.0f) + 1e-5f);
#pragma unroll
      for (int i = 0; i < 12; ++i) { const f32x2 n = xv[i] * rs; h[i >> 2][i & 3] = cvtpk(n[0], n[1]); }
#pragma unroll
      for (int i = 0; i < 3; ++i) lds_w128(rp + (L_H - L_X) + i * 2048, h[i]);
    }
    SBAR();
    OPAQUE(l);
    unsigned so = tt * FRAME_B + (j >> 1) * 1024 + ((t ^ ((j >> 1) & 1)) * 128) + (((j & 1) ^ (t >> 2)) * 64) + nl * 16;
    const unsigned sso = qb * TILE_B;
    LANE_IDS;
    FRAG_POS;
    unsigned sto = j == 0 ? (tt * 1024 + nl) * 8 : WS_END;
    unsigned sa = L_SP + l * 16;
    unsigned keep = (hh == (i16 >> 3)) ? ~0u : 0u;
    OPAQUE(sto); OPAQUE(so); OPAQUE(sa); OPAQUE(keep);
    ST(1);
    BAR();
    ST(6);
    {
      u32x4 H[6][2], W[18];
      h_frags(H, fr);
      asm volatile("s_barrier" ::: "memory");
      ST(2);
#pragma unroll 1
      for (int sp = 0;; ++sp) {
        const u32x4 seal = ld_seal(3);
        ldw(W, 3, 0, 6, rot);
        SBAR();
        ST(3);
        if (sp == 0) asm volatile("s_barrier" ::: "memory");
        f32x4 vp[3][2], sq[4], e;
        float rd;
        u32x2 pt[2];
        unsigned he = 0, ho = 0;
#pragma unroll
        for (int ww = 0; ww < 4; ++ww) sq[ww] = lds_r128f(sa + ww * 1024);
        SBAR();
        ST(4);
#pragma unroll
        for (int kk = 0; kk < 6; ++kk) {
#pragma unroll
          for (int i = 0; i < 3; ++i) {
            vp[i][0] = mfma16(H[kk][0], W[kk * 3 + i], kk ? vp[i][0] : f32x4{0, 0, 0, 0});
            vp[i][1] = mfma16(H[kk][1], W[kk * 3 + i], kk ? vp[i][1] : f32x4{0, 0, 0, 0});
            XOR_IN(he, ho, W[kk * 3 + i]);
          }
          if (kk == 1) { e = (sq[0] + sq[1]) + (sq[2] + sq[3]); rd = fmaxf(fmaxf(e[0], e[1]), fmaxf(e[2], e[3])); OPAQUE(rd); }
          if (kk == 2) { e -= xor16_max(rd); OPAQUE(e); }
          if (kk == 3) {
#pragma unroll
            for (int k = 0; k < 4; ++k) e[k] = __builtin_amdgcn_exp2f(e[k]);
            rd = (e[0] + e[1]) + (e[2] + e[3]);
            OPAQUE(rd);
          }
          if (kk == 4) { pt[0] = pack4(e * __builtin_amdgcn_rcpf(xor16_add(rd))); OPAQUE(pt[0]); }
          if (kk == 5) {
#pragma unroll
            for (int k = 0; k < 2; ++k) {
              auto q = __builtin_amdgcn_permlane32_swap(pt[0][k], pt[0][k], false, false);
              pt[1][k] = (q[0] ^ q[1] ^ pt[0][k]) & keep;
              pt[0][k] &= keep;
            }
            OPAQUE(pt[0]); OPAQUE(pt[1]);
          }
#pragma unroll
          for (int q = 0; q < 6; ++q) { __builtin_amdgcn_sched_group_barrier(0x8, 1, 0); __builtin_amdgcn_sched_group_barrier(0x402, 3, 0); }
          SBAR();
        }
        OPAQUE(he); OPAQUE(ho);
        ST(7);
        f32x4 y[3][2];
#pragma unroll
        for (int hf = 0; hf < 2; ++hf)
#pragma unroll
          for (int i = 0; i < 3; ++i) y[i][hf] = mfma16k16(pack4(vp[i][hf]), pt[hf], f32x4{0, 0, 0, 0});
        SBAR();
#pragma unroll
        for (int hf = 0; hf < 2; ++hf)
#pragma unroll
          for (int i = 0; i < 3; ++i) lds_w64(dp[hf] + (L_YI - L_X) + i * 512, pack4(y[i][hf]));
        if (__builtin_expect(seal_ok(he, ho, seal, l < 18, tag), 1) || sp >= SPIN_CAP) break;
        asm volatile("" ::: "memory");
      }
    }
    ST(9);
    BAR();
    {
      u32x4 y[3], h[3];
#pragma unroll
      for (int i = 0; i < 3; ++i) y[i] = lds_r128(rp + (L_YI - L_X) + i * 2048);
#pragma unroll
      for (int i = 0; i < 12; ++i) xv[i] += unpack2(y[i >> 2][i & 3]);
      const float mu = row8_sum(sum24(xv)) * (1.0f / 192.0f), sq = row8_sum(sumsq24(xv));
      const float var = fmaxf(fmaf(sq, 1.0f / 192.0f, -mu * mu), 0.f) + 1e-5f;
      const float rs = __builtin_amdgcn_rsqf(var), nb = -mu * rs;
#pragma unroll
      for (int i = 0; i < 3; ++i) {
#pragma unroll
        for (int e = 0; e < 4; ++e) { const f32x2 n = xv[4 * i + e] * rs + nb; h[i][e] = cvtpk(n[0], n[1]); }
        if (i == 0) { OPAQUE(h[0]); ST(5); asm volatile("s_barrier" ::: "memory"); }
        st128(h[i], rsWS, so, WS_H2 + sso + i * 4096);
        SBAR();
      }
      const u32x2 stat = {asu(mu + mu1), asu(var)};
      __builtin_amdgcn_raw_buffer_store_b64(stat, rsWS, sto, WS_ST + b * 32, SC1);
    }
    ST(10);
    const unsigned ring = lds0 + w * SLOT;
    unsigned kr0 = ring + (r32 >> 2) * 128 + (hh ^ (r32 >> 4)) * 64 + (r32 & 3) * 16, kr1 = kr0 ^ 128;
    unsigned vr0 = ring + (g & 1) * 1024 + (hh ^ (g & 1)) * 128 + (tp >> 1) * 64 + tq * 16 + 8 * (tp & 1), vr1 = (vr0 ^ 64) + 512;
    unsigned lo[3] = {l * 16, l * 16 + 4096, l * 16 + 8192}, pw = ring + 2 * TILE_B + l * 16, lw = l < 16 ? L_L + (l * 4 + w) * 8 : L_L + 512 + l * 8;
    OPAQUE(kr0); OPAQUE(kr1); OPAQUE(vr0); OPAQUE(vr1); OPAQUE(lo[0]); OPAQUE(lo[1]); OPAQUE(lo[2]); OPAQUE(pw); OPAQUE(lw);
    const u64 hbu = (u64)(ws + WS_H2 + (size_t)t * FRAME_B);
    const u32x4 rs = {(unsigned)hbu, (unsigned)(hbu >> 32) & 0xffffu, (unsigned)FRAME_B, 0x00020000u};
    unsigned toff[8];
#pragma unroll
    for (int i = 0; i < 8; ++i) { toff[i] = (8 * w + ((i + qb) & 7)) * TILE_B; asm volatile("" : "+s"(toff[i])); }
    u32x4 sel;
    { const unsigned one = (tp == (g & 1)) ? 0x3f803f80u : 0u; sel = u32x4{one, one, one, one}; OPAQUE(sel); }
    auto piece = [&](auto Tc, auto Pc) {
      constexpr int T = decltype(Tc)::value, p = decltype(Pc)::value;
      if constexpr (T < 8) {
        if constexpr (p % 4 == 0) setm0<(T % 3) * TILE_B + p * 1024>(ring);
        bdma<(p % 4) * 1024>(lo[p / 4], rs, toff[T]);
      }
    };
#define P6(T, a) piece(IC(T), IC(a)); piece(IC(T), IC(a + 1)); piece(IC(T), IC(a + 2)); piece(IC(T), IC(a + 3)); piece(IC(T), IC(a + 4)); piece(IC(T), IC(a + 5));
    auto read_valid = [&](u32x4 (&KK)[12], const unsigned dst, unsigned off) {
      for (int sp = 0; sp < SPIN_CAP; ++sp) {
#pragma unroll
        for (int k = 0; k < 12; ++k) KK[k] = lds_r128(((k & 1) ? kr1 : kr0) + dst + k * 1024);
        if (__builtin_expect(!poisoned(KK), 1)) break;
#pragma unroll 1
        for (int k = 0; k < 12; ++k) { setm0<0>(ring + dst + k * 1024); bdma<0>(lo[0], rs, off + k * 1024); }
        waitvm<0>();
      }
    };
    unsigned qa = lds0 + L_Q + lo[0], na = lds0 + L_N + r32 * 16;
    OPAQUE(qa); OPAQUE(na);
    waitvm<0>();
    __builtin_amdgcn_s_sleep(B9_DELAY);
    BAR();
    __builtin_amdgcn_s_sleep(TILE0_DELAY);
    if (w != (qb >> 3)) { P6(0, 0) P6(0, 6) }
    else if (l == 0) __builtin_amdgcn_raw_buffer_store_b64(tag, rsWS, 0, WS_FLAG2 + b * 8, SC1);
    ST(11);
    BAR();
    u32x4 K0[12];
    if (w != (qb >> 3)) waitvm<0>();
    ST(12);
    read_valid(K0, 0, toff[0]);
    ST(13);
    piece(IC(1), IC(0)); piece(IC(1), IC(1)); piece(IC(1), IC(2)); piece(IC(1), IC(3));
    BAR();
    ST(14);
    u32x4 Q[12];
    float mref;
    {
      const f32x4 n0 = lds_r128f(na), n1 = lds_r128f(na + 512);
#pragma unroll
      for (int s = 0; s < 6; ++s) Q[s] = lds_r128(qa + s * 1024);
      SBAR();
      const f32x4 s = n0 + n1;
      mref = __builtin_amdgcn_sqrtf(((s[0] + s[1]) + (s[2] + s[3])) * (192.0f * 1.02f));
      OPAQUE(mref);
      SBAR();
#pragma unroll
      for (int s = 6; s < 12; ++s) Q[s] = lds_r128(qa + s * 1024);
      SBAR();
    }
    constexpr int KW = 4, VW = 4;
    bool safe = false, exact = false, late = false;
#pragma unroll 1
    for (;;) {
      f32x16 S[2], O[6], negm;
      u32x4 K[12], V[12], P[2][2];
      f32x4 lsum = {0, 0, 0, 0};
      float pe[2][2];
#pragma unroll
      for (int i = 0; i < 16; ++i) negm[i] = -mref;
#define RK(k, T) K[k] = lds_r128((((k) & 1) ? kr1 : kr0) + ((T) % 3) * TILE_B + (k) * 1024);
#define RV(u, T) { const unsigned a_ = (((u) & 1) ? vr1 : vr0) + ((T) % 3) * TILE_B + ((u) >> 1) * 2048; V[u] = lds_tr2(a_, a_ + 256); }
      auto sm = [&](auto Tc, auto Hc, auto Gc) {
        constexpr int T = decltype(Tc)::value, H = decltype(Hc)::value, G = decltype(Gc)::value;
        if constexpr (G < 8) pe[(G >> 1) & 1][G & 1] = __builtin_amdgcn_exp2f(S[T & 1][8 * H + G]);
        if constexpr (G >= 2 && G <= 8 && G % 2 == 0) { constexpr int q = G / 2 - 1; P[T & 1][H][q] = cvtpk(pe[q & 1][0], pe[q & 1][1]); }
        if constexpr (G == 10) lsum = mfma16(sel, P[T & 1][H], lsum);
      };
      auto put_o = [&](const int un) {
        u32x4 pk;
#pragma unroll
        for (int e = 0; e < 4; ++e) pk[e] = cvtpk(O[un >> 1][8 * (un & 1) + 2 * e], O[un >> 1][8 * (un & 1) + 2 * e + 1]);
        lds_w128(pw + un * 1024, pk);
      };
      auto qkphase = [&](auto Ic) {
        constexpr int I = decltype(Ic)::value;
        auto step = [&](auto Kc) {
          constexpr int k = decltype(Kc)::value;
          if constexpr (I + 1 < 8) {
            S[(I + 1) & 1] = mfma32(I < 0 ? K0[k] : K[k], Q[k], k == 0 ? negm : S[(I + 1) & 1]);
            if constexpr (k + KW < 12 && I >= 0) RK(k + KW, I + 1)
          }
          if constexpr (I < 0 && k < 8) piece(IC(1), IC(4 + k));
          if constexpr (I >= 0) { if constexpr (k & 1) piece(IC(I + 2), IC(6 + k / 2)); if constexpr (k < 11) sm(IC(I), IC(1), IC(k)); }
          if constexpr (I >= 0 && I < 7 && k >= 12 - VW) RV(k - (12 - VW), I)
          SBAR();
        };
        step(IC(0)); step(IC(1)); step(IC(2)); step(IC(3)); step(IC(4)); step(IC(5)); step(IC(6)); step(IC(7)); step(IC(8)); step(IC(9)); step(IC(10)); step(IC(11));
      };
      auto pvphase = [&](auto Ic) {
        constexpr int I = decltype(Ic)::value;
        auto step = [&](auto Uc) {
          constexpr int u = decltype(Uc)::value;
          if constexpr (I >= 0) {
            f32x16 z;
#pragma unroll
            for (int e = 0; e < 16; ++e) z[e] = 0.f;
            O[u >> 1] = mfma32(V[u], P[I & 1][u & 1], (I == 0 && !(u & 1)) ? z : O[u >> 1]);
            if constexpr (u & 1) piece(IC(I + 3), IC(u / 2));
            if constexpr (u + VW < 12) RV(u + VW, I)
          }
          if constexpr (I + 1 < 8 && u >= 1) sm(IC(I + 1), IC(0), IC(u - 1));
          if constexpr (I < 0 && (u & 1)) piece(IC(2), IC(u / 2));
          if constexpr (I + 2 < 8) {
            if constexpr (u == 12 - KW) waitvm<(I < 0 ? 12 : I + 3 < 8 ? 6 + (12 - KW) / 2 : 6)>();
            if constexpr (u >= 12 - KW) RK(u - (12 - KW), I + 2)
            if constexpr (u == 11) waitvm<(I + 3 < 8 ? 6 : 0)>();
          }
          SBAR();
        };
        step(IC(0)); step(IC(1)); step(IC(2)); step(IC(3)); step(IC(4)); step(IC(5)); step(IC(6)); step(IC(7)); step(IC(8)); step(IC(9)); step(IC(10)); step(IC(11));
      };
      auto drain = [&]() {
        constexpr int tl[24] = {6, 6, 6, 6, 6, 6, 6, 6, 7, 7, 7, 7, 6, 6, 7, 7, 6, 6, 7, 7, 7, 7, 7, 7};
        constexpr int fu[24] = {0, 1, 2, 3, 4, 5, 6, 7, 0, 1, 2, 3, 8, 9, 4, 5, 10, 11, 6, 7, 8, 9, 10, 11};
        constexpr int wr[24] = {-1, -1, -1, -1, -1, -1, -1, -1, -1, -1, -1, 0, 1, 2, -1, 3, -1, 4, 5, -1, -1, 6, 7, 8};
        u32x4 X[24];
#pragma unroll
        for (int n = 0; n < VW; ++n) X[n] = V[n];
        auto step = [&](auto Nc) {
          constexpr int n = decltype(Nc)::value;
          O[fu[n] >> 1] = mfma32(X[n], P[tl[n] & 1][fu[n] & 1], O[fu[n] >> 1]);
          if constexpr (n + VW < 24) {
            const unsigned a_ = ((fu[n + VW] & 1) ? vr1 : vr0) + (tl[n + VW] % 3) * TILE_B + (fu[n + VW] >> 1) * 2048;
            X[n + VW] = lds_tr2(a_, a_ + 256);
          }
          if constexpr (n >= 1 && n <= 3) { sm(IC(7), IC(0), IC(3 * n - 3)); sm(IC(7), IC(0), IC(3 * n - 2)); sm(IC(7), IC(0), IC(3 * n - 1)); }
          if constexpr (n >= 4 && n <= 6) { sm(IC(7), IC(1), IC(3 * n - 12)); sm(IC(7), IC(1), IC(3 * n - 11)); sm(IC(7), IC(1), IC(3 * n - 10)); }
          if constexpr (n == 7) sm(IC(7), IC(0), IC(10));
          if constexpr (n == 9) sm(IC(7), IC(1), IC(10));
          if constexpr (wr[n] >= 0) put_o(wr[n]);
          SBAR();
        };
        step(IC(0)); step(IC(1)); step(IC(2)); step(IC(3)); step(IC(4)); step(IC(5)); step(IC(6)); step(IC(7)); step(IC(8)); step(IC(9)); step(IC(10)); step(IC(11));
        step(IC(12)); step(IC(13)); step(IC(14)); step(IC(15)); step(IC(16)); step(IC(17)); step(IC(18)); step(IC(19)); step(IC(20)); step(IC(21)); step(IC(22)); step(IC(23));
      };
#define STALE(v) (__builtin_expect(!safe && __any((v) != (v)), 0))
      bool done = false;
      do {
        qkphase(IC(-1));
        ST(15);
        pvphase(IC(-1));
        ST(16);
        qkphase(IC(0)); pvphase(IC(0)); qkphase(IC(1)); pvphase(IC(1)); qkphase(IC(2));
        const float seen = lsum[0];
        pvphase(IC(2));
        if (STALE(seen)) break;
        qkphase(IC(3)); pvphase(IC(3)); qkphase(IC(4)); pvphase(IC(4)); qkphase(IC(5));
        if (!late) asm volatile("s_barrier" ::: "memory");
        late = true;
        pvphase(IC(5)); qkphase(IC(6)); drain();
        if (STALE(lsum[0])) break;
        { const u32x2 ls = {asu(lsum[0]), asu(lsum[1])}; lds_w64(lw, ls); }
        put_o(9); put_o(10); put_o(11);
        done = true;
      } while (false);
#pragma unroll
      for (int k = 0; k < 12; ++k) asm volatile("" : "=v"(K0[k]));
      if (__builtin_expect(done, 1)) {
        ST(17);
        BAR();
        ST(18);
        float lt[2];
        unsigned ll = lo[0];
        OPAQUE(ll);
        ll >>= 4;
        if (__builtin_expect(!low_sum(lds_r128f(L_L + (ll & 15) * 32), lds_r128f(L_L + (ll & 15) * 32 + 16), lt) || exact, 1)) break;
        BAR();
        float mx = -3.0e38f;
#pragma unroll 1
        for (unsigned i = 0; i < 8; ++i) {
#pragma unroll 1
          for (int k = 0; k < 12; ++k) { setm0<0>(ring + k * 1024); bdma<0>(lo[0], rs, (8 * w + i) * TILE_B + k * 1024); }
          waitvm<0>();
          f32x16 s;
#pragma unroll
          for (int e = 0; e < 16; ++e) s[e] = 0.f;
#pragma unroll
          for (int k = 0; k < 12; ++k) s = mfma32(lds_r128(((k & 1) ? kr1 : kr0) + k * 1024), Q[k], s);
#pragma unroll
          for (int e = 0; e < 16; ++e) mx = fmaxf(mx, s[e]);
        }
        lds_w32(L_N + ((ll & 31) * 8 + w * 2 + (ll >> 5)) * 4, mx);
        BAR();
        const f32x4 c = lds_r128f(L_N + (ll & 31) * 32), d = lds_r128f(L_N + (ll & 31) * 32 + 16);
        mref = fmaxf(fmaxf(fmaxf(c[0], c[1]), fmaxf(c[2], c[3])), fmaxf(fmaxf(d[0], d[1]), fmaxf(d[2], d[3])));
        exact = true;
      } else {
        unsigned ll = lo[0];
        OPAQUE(ll);
        for (int sp = 0; sp < SPIN_CAP; ++sp) {
          asm volatile("" ::: "memory");
          const u32x2 f = __builtin_amdgcn_raw_buffer_load_b64(rsWS, ll >> 1, WS_FLAG2 + w * 512, SC1);
          if (__all(f[0] == tag[0] && f[1] == tag[1])) break;
        }
      }
      safe = true;
      asm volatile("s_waitcnt vmcnt(0) lgkmcnt(0)" ::: "memory");
      P6(0, 0) P6(0, 6)
      waitvm<0>();
      piece(IC(1), IC(0)); piece(IC(1), IC(1)); piece(IC(1), IC(2)); piece(IC(1), IC(3));
#pragma unroll
      for (int k = 0; k < 12; ++k) K0[k] = lds_r128(((k & 1) ? kr1 : kr0) + k * 1024);
    }
    ST(19);
    BAR();
    ST_OUT(ws, 0, 12);
  } else {
    LANE_IDS;
    auto job_run = [&](const unsigned at, const unsigned slot, const unsigned sealat, auto younger) {
      unsigned ja[2] = {at + i16 * 384 + ((g ^ (l & 7)) * 16), at + i16 * 384 + (((4 + g) ^ (l & 7)) * 16)}, jb = at + 12288 + (8 * g + tq) * 32 + tp * 8;
      OPAQUE(ja[0]); OPAQUE(ja[1]); OPAQUE(jb);
      waitvm<decltype(younger)::value>();
      ST(2);
      u32x4 a[6], c[6];
#pragma unroll
      for (int kk = 0; kk < 6; ++kk) { a[kk] = lds_r128(ja[kk & 1] + (kk >> 1) * 128); c[kk] = lds_r128(ja[kk & 1] + (kk >> 1) * 128 + 6144); }
      const unsigned o = (hh * 16 + i16) * 16 + (g & 1) * 8;
      f32x4 d = {0, 0, 0, 0};
      u32x2 r;
      if (jqk) {
#pragma unroll
        for (int kk = 0; kk < 6; ++kk) d = mfma16(a[kk], c[kk], d);
        r = pack4(d * QSCALE);
        __builtin_amdgcn_raw_buffer_store_b64(r, rsWS, o, slot, SC1);
      } else {
        f32x4 e = {0, 0, 0, 0};
#pragma unroll
        for (int kk = 0; kk < 6; ++kk) {
          const u32x4 bb = lds_tr2(jb + kk * 1024, jb + kk * 1024 + 128);
          d = mfma16(a[kk], bb, d); e = mfma16(c[kk], bb, e);
        }
        const u32x2 r0 = pack4(d), r1 = pack4(e);
        __builtin_amdgcn_raw_buffer_store_b64(r0, rsWS, o, slot, SC1);
        __builtin_amdgcn_raw_buffer_store_b64(r1, rsWS, o + 512, slot, SC1);
        r = r0 ^ r1;
      }
      const u32x2 half = {fold_xor(r[0], r[1]), hh ? tag[1] : tag[0]};
      __builtin_amdgcn_raw_buffer_store_b64(half, rsWS, r32 == 0 ? hh * 8 : WS_END, sealat, SC1);
    };
    FRAG_POS;
    if (w == 0 || (w == 1 && jqk)) {
      ST(1);
      if (job) job_run(w * SLOT, jslot + (1 - w) * 2 * 73728, WS_SEAL + (1 - w) * 32768 + b * 128, IC(0));
      ST(3);
      if (w == 0) {
        __builtin_amdgcn_raw_buffer_store_b32(POISON, rsWS, l * 4, WS_ST + (t * 1024 + qb * 32) * 8, SC1);
        const unsigned p0 = WS_H2 + t * FRAME_B + (jqk ? qb * 8 : 144 + (qb - 27) * 48) * 1024, np = jqk ? 2 : job ? 0 : 12;
#pragma unroll 1
        for (unsigned i = 0; i < np; ++i) {
#pragma unroll
          for (int q = 0; q < 4; ++q) __builtin_amdgcn_raw_buffer_store_b128(pz, rsWS, l * 16 + q * 1024, p0 + i * 4096, SC1);
        }
        asm volatile("s_nop 1" :: "v"(pz));
        if (jqk) { waitvm<9>(); __builtin_amdgcn_s_sleep(READ_DELAY); } else if (job) { waitvm<1>(); __builtin_amdgcn_s_sleep(VO_DELAY); } else { waitvm<0>(); __builtin_amdgcn_s_sleep(IDLE_DELAY); }
      }
    }
    u32x4 wq[18], wv[18];
    {
      u32x4 H[6][2];
      u32x2 hs[3][2];
      unsigned spa = hh == (i16 >> 3) ? L_SP + w * 1024 + l * 16 : L_SP + 4096 + l * 16;
      OPAQUE(spa);
      __builtin_amdgcn_s_setprio(3);
      ST(4);
      BAR();
#pragma unroll 1
      for (int sp = 0;; ++sp) {
        const u32x4 seal = ld_seal(2);
        ldw(wq, 2, 0, 3, rot);
        SBAR();
        h_frags(H, fr);
#pragma unroll
        for (int i = 0; i < 3; ++i) { hs[i][0] = lds_r64(dp[0] + (L_H - L_X) + i * 512); hs[i][1] = lds_r64(dp[1] + (L_H - L_X) + i * 512); }
        ldw(wq, 2, 3, 6, rot);
        if (sp == 0) {
          if (w == 0) {
            waitvm<19>();
            if (l < 8) __builtin_amdgcn_raw_buffer_store_b64(tag, rsWS, l * 2048, WS_FLAG + b * 8, SC1);
          }
          asm volatile("s_barrier" ::: "memory");
        }
        SBAR();
        f32x4 d[3][2], st[2];
        unsigned he = 0, ho = 0;
#pragma unroll
        for (int kk = 0; kk < 6; ++kk) {
#pragma unroll
          for (int i = 0; i < 3; ++i) {
            d[i][0] = mfma16(wq[kk * 3 + i], H[kk][0], kk ? d[i][0] : f32x4{0, 0, 0, 0});
            d[i][1] = mfma16(wq[kk * 3 + i], H[kk][1], kk ? d[i][1] : f32x4{0, 0, 0, 0});
            XOR_IN(he, ho, wq[kk * 3 + i]);
          }
          MIXV6();
        }
        ST(19);
        const bool ok = seal_ok(he, ho, seal, l < 36, tag);
#pragma unroll
        for (int hf = 0; hf < 2; ++hf) {
          st[hf] = f32x4{0, 0, 0, 0};
#pragma unroll
          for (int i = 0; i < 3; ++i) st[hf] = mfma16k16(hs[i][hf], pack4(d[i][hf]), st[hf]);
          lds_w128f(spa ^ (hf * 512), st[hf]);
        }
        if (__builtin_expect(ok, 1) || sp >= SPIN_CAP) break;
        asm volatile("" ::: "memory");
      }
      asm volatile("" :: "v"(dc), "v"(dw));
    }
    ST(10);
    BAR();
    __builtin_amdgcn_s_setprio(0);
    u32x4 seal = ld_seal(0);
    {
      ldw(wq, 0, 0, WQ_SPLIT, 0);
      u32x2 fl = __builtin_amdgcn_raw_buffer_load_b64(rsWS, l * 8, WS_FLAG + t * 2048 + w * 512, SC1);
      ST(5);
      BAR();
      ldw(wq, 0, WQ_SPLIT, 6, 0);
      if (__builtin_expect(!__all(fl[0] == tag[0] && fl[1] == tag[1]), 0)) {
        for (int sp = 0; sp < SPIN_CAP; ++sp) {
          asm volatile("" ::: "memory");
          const u32x2 f2 = __builtin_amdgcn_raw_buffer_load_b64(rsWS, l * 8, WS_FLAG + t * 2048 + w * 512, SC1);
          if (__all(f2[0] == tag[0] && f2[1] == tag[1])) break;
        }
      }
      ST(18);
      BAR();
    }
    const unsigned qt = (qb >> 3) * SLOT;
    unsigned hqa[2], own[2][2];
    hqa[0] = qt + hh * 1024 + (tq ^ hh) * 128 + (g & 1) * 64 + tp * 16; hqa[1] = (hqa[0] ^ 64) + 512;
    OPAQUE(hqa[0]); OPAQUE(hqa[1]);
    const unsigned tok = 16 * (g & 1) + i16;
    unsigned nw = lds0 + L_N + l * 16 + w * 4;
    OPAQUE(nw);
    unsigned qw = L_Q + (3 * w * 64 + hh * 32 + i16) * 16 + 8 * (g & 1);
    own[0][0] = qt + w * 3072 + (tq ^ (w & 1)) * 128 + hh * 64 + tp * 16 + 8 * (g & 1); own[0][1] = (own[0][0] ^ 64) + 512;
    own[1][0] = own[0][0] ^ 128; own[1][1] = own[0][1] ^ 128;
    OPAQUE(qw); OPAQUE(own[0][0]); OPAQUE(own[0][1]); OPAQUE(own[1][0]); OPAQUE(own[1][1]);
    if (b == 0 && w == 3 && l == 0) __builtin_amdgcn_raw_buffer_store_b64(tag, rsWS, 0, WS_EPOCH, SC1);
    for (int sp = 0; sp < SPIN_CAP && !sealed(wq, seal, l < 36, tag); ++sp) {
      asm volatile("" ::: "memory");
      seal = ld_seal(0);
      ldw(wq, 0, 0, 6, 0);
    }
    if (w == 1 && job && !jqk) job_run(SLOT, jslot, WS_SEAL + b * 128, IC(0));
    u32x4 hq[3];
    {
      const u64 hbu = (u64)(ws + WS_H2 + (size_t)t * FRAME_B);
      u32x4 rs = {(unsigned)hbu, (unsigned)(hbu >> 32) & 0xffffu, (unsigned)FRAME_B, 0x00020000u};
      asm volatile("" : "+s"(rs));
      __builtin_amdgcn_s_setprio(3);
      ST(6);
      BAR();
      ST(7);
      for (int sp = 0; sp < SPIN_CAP; ++sp) {
        setm0<0>(qt + w * 3072);
        bdma<0>(l * 16, rs, qb * TILE_B + w * 3072); bdma<1024>(l * 16, rs, qb * TILE_B + w * 3072); bdma<2048>(l * 16, rs, qb * TILE_B + w * 3072);
        waitvm<0>();
#pragma unroll
        for (int i = 0; i < 3; ++i) { const u32x2 u0 = lds_r64(own[i & 1][0] + i * 1024), u1 = lds_r64(own[i & 1][1] + i * 1024); hq[i] = u32x4{u0[0], u0[1], u1[0], u1[1]}; }
        if (!poisoned(hq)) break;
      }
    }
    ST(8);
    BAR();
    ST(9);
    {
      u32x4 bf[6][2];
      f32x4 d[3][2];
      f32x2 n[2] = {{0.f, 0.f}, {0.f, 0.f}};
      auto fin = [&](const int i) {
#pragma unroll
        for (int hf = 0; hf < 2; ++hf) {
          lds_w64(qw + i * 1024 + hf * 256, pack4(d[i][hf]));
          const f32x2 a = {d[i][hf][0], d[i][hf][1]}, c = {d[i][hf][2], d[i][hf][3]};
          n[hf] = __builtin_elementwise_fma(a, a, n[hf]); n[hf] = __builtin_elementwise_fma(c, c, n[hf]);
        }
      };
      auto mm = [&](const int i, const int ks) {
        d[i][0] = mfma16(wq[ks * 3 + i], bf[ks][0], ks ? d[i][0] : f32x4{0, 0, 0, 0});
        d[i][1] = mfma16(wq[ks * 3 + i], bf[ks][1], ks ? d[i][1] : f32x4{0, 0, 0, 0});
      };
#pragma unroll
      for (int ks = 0; ks < 3; ++ks) { bf[ks][0] = lds_r128(hqa[0] + ks * 2048); bf[ks][1] = lds_r128(hqa[1] + ks * 2048); SBAR(); }
#pragma unroll
      for (int ks = 0; ks < 3; ++ks) {
        mm(0, ks);
        bf[ks + 3][0] = lds_r128(hqa[0] + (ks + 3) * 2048); bf[ks + 3][1] = lds_r128(hqa[1] + (ks + 3) * 2048);
        SBAR();
        mm(1, ks);
        SBAR();
      }
      mm(0, 3); mm(0, 4); mm(0, 5); mm(1, 3);
      SBAR();
      mm(1, 4); mm(1, 5); mm(2, 0); fin(0);
      SBAR();
      mm(2, 1); mm(2, 2); mm(2, 3); mm(2, 4); mm(2, 5); fin(1);
      SBAR();
      fin(2);
      auto rr = __builtin_amdgcn_permlane16_swap(asu(n[0][0] + n[0][1]), asu(n[1][0] + n[1][1]), false, false);
      lds_w32(nw, asf(rr[0]) + asf(rr[1]));
    }
    SBAR();
    ST(11);
    BAR();
    __builtin_amdgcn_s_setprio(0);
    unsigned oo = (t * 1024 + qb * 32 + tok) * 384 + (6 * w + hh) * 16;
    OPAQUE(oo);
    float lt[2];
    u32x4 pick;
#pragma unroll
    for (int e = 0; e < 4; ++e) {
      const unsigned d = 8 * (e >> 1) + 4 * (g & 1) + 2 * (e & 1);
      pick[e] = (i16 == d ? 0x3f80u : 0u) | (i16 == d + 1 ? 0x3f800000u : 0u);
    }
    unsigned pr[2], la = lds0 + L_L + i16 * 32;
    pr[0] = lds0 + 2 * TILE_B + hh * SLOT + 3 * w * 1024 + ((g & 1) * 32 + i16) * 16; pr[1] = pr[0] + 2 * SLOT;
    OPAQUE(pick); OPAQUE(pr[0]); OPAQUE(pr[1]); OPAQUE(la);
    f32x4 x1[3][2];
    {
      __builtin_amdgcn_s_sleep(40);
      u32x2 st[2];
      for (int sp = 0; sp < SPIN_CAP; ++sp) {
        asm volatile("" ::: "memory");
        st[0] = __builtin_amdgcn_raw_buffer_load_b64(rsWS, i16 * 8, WS_ST + (t * 1024 + qb * 32) * 8, SC1);
        st[1] = __builtin_amdgcn_raw_buffer_load_b64(rsWS, i16 * 8 + 128, WS_ST + (t * 1024 + qb * 32) * 8, SC1);
        if (!__any(max(max(st[0][0], st[0][1]), max(st[1][0], st[1][1])) == POISON)) break;
      }
#pragma unroll
      for (int hf = 0; hf < 2; ++hf) {
        const float mu = asf(st[hf][0]), sd = __builtin_amdgcn_sqrtf(asf(st[hf][1]));
#pragma unroll
        for (int i = 0; i < 3; ++i) {
          const f32x4 h = {bflo(hq[i][2 * hf]), bfhi(hq[i][2 * hf]), bflo(hq[i][2 * hf + 1]), bfhi(hq[i][2 * hf + 1])};
          x1[i][hf] = h * sd + mu;
          OPAQUE(x1[i][hf]);
        }
      }
    }
    BAR();
    ST(16);
    for (int sp = 0; sp < SPIN_CAP; ++sp) {
      asm volatile("" ::: "memory");
      seal = ld_seal(1);
      ldw(wv, 1, 0, 6, 0);
      if (sealed(wv, seal, l < 18, tag)) break;
    }
    ST(12);
#pragma unroll 1
    for (bool exact = false;; exact = true) {
      BAR();
      ST(13);
      u32x4 p[3][2][2];
#pragma unroll
      for (int i = 0; i < 3; ++i)
#pragma unroll
        for (int hf = 0; hf < 2; ++hf) { p[i][hf][0] = lds_r128(pr[0] + i * 1024 + hf * 256); p[i][hf][1] = lds_r128(pr[1] + i * 1024 + hf * 256); }
      const f32x4 l0 = lds_r128f(la), l1 = lds_r128f(la + 16);
      SBAR();
#pragma unroll
      for (int i = 0; i < 3; ++i)
#pragma unroll
        for (int hf = 0; hf < 2; ++hf) lds_w64(dp[hf] + (L_FI - L_X) + i * 512, pack4(mfma16(pick, p[i][hf][1], mfma16(pick, p[i][hf][0], f32x4{0, 0, 0, 0}))));
      if (__builtin_expect(!low_sum(l0, l1, lt) || exact, 1)) break;
      BAR();
      BAR();
    }
    ST(14);
    const float linv[2] = {__builtin_amdgcn_rcpf(lt[0]), __builtin_amdgcn_rcpf(lt[1])};
    BAR();
    ST(15);
    u32x4 bf[6][2];
#pragma unroll
    for (int ks = 0; ks < 6; ++ks) { bf[ks][0] = lds_r128(fr[0] + (L_FI - L_X) + ks * 1024); bf[ks][1] = lds_r128(fr[1] + (L_FI - L_X) + ks * 1024); }
    SBAR();
    f32x4 d[3][2];
#pragma unroll
    for (int ks = 0; ks < 6; ++ks)
#pragma unroll
      for (int i = 0; i < 3; ++i) {
        d[i][0] = mfma16(wv[ks * 3 + i], bf[ks][0], ks ? d[i][0] : f32x4{0, 0, 0, 0});
        d[i][1] = mfma16(wv[ks * 3 + i], bf[ks][1], ks ? d[i][1] : f32x4{0, 0, 0, 0});
      }
#pragma unroll
    for (int i = 0; i < 3; ++i) {
      const u32x2 y0 = pack4(d[i][0] * linv[0] + x1[i][0]), y1 = pack4(d[i][1] * linv[1] + x1[i][1]);
      auto s0 = __builtin_amdgcn_permlane16_swap(y0[0], y1[0], false, false);
      auto s1 = __builtin_amdgcn_permlane16_swap(y0[1], y1[1], false, false);
      const u32x4 o = {s0[0], s1[0], s0[1], s1[1]};
      *(__attribute__((address_space(1))) u32x4*)(kc + oo + i * 32) = o;
    }
    ST(17);
    ST_OUT(ws, 12, 20);
  }
}

extern "C" void kernel_launch(void* const* d_in, const int* in_sizes, int n_in, void* d_out, int out_size, void* d_ws, size_t ws_size, hipStream_t stream) {
  (void)hipFuncSetAttribute((const void*)k_timesformer, hipFuncAttributeMaxDynamicSharedMemorySize, LDS_BYTES);
  k_timesformer<<<256, 512, LDS_BYTES, stream>>>((const char*)d_in[0], (const char*)d_in[1], (const char*)d_in[2], (const char*)d_in[3], (const char*)d_in[4],
                                                 (char*)d_ws, (char*)d_out);
}
```

```cpp
#include <hip/hip_runtime.h>
#include <hip/hip_bf16.h>
#include <type_traits>

typedef short s16x4 __attribute__((ext_vector_type(4)));
typedef __bf16 b16x8 __attribute__((ext_vector_type(8)));
typedef __bf16 b16x2 __attribute__((ext_vector_type(2)));
typedef float f32x2 __attribute__((ext_vector_type(2)));
typedef float f32x4 __attribute__((ext_vector_type(4)));
typedef float f32x16 __attribute__((ext_vector_type(16)));
typedef unsigned u32x2 __attribute__((ext_vector_type(2)));
typedef unsigned u32x4 __attribute__((ext_vector_type(4)));
typedef unsigned long long u64;
typedef u64 u64x4 __attribute__((ext_vector_type(4)));
typedef u64 u64x2 __attribute__((ext_vector_type(2)));
#define LAS __attribute__((address_space(3)))
#define DI __device__ __forceinline__
#define SBAR() __builtin_amdgcn_sched_barrier(0)

constexpr int TILE_B = 12288;
constexpr int FRAME_B = 393216;
constexpr unsigned WS_H2 = 0, WS_ST = 8 * FRAME_B, WS_WM = 16 * FRAME_B, WS_SEAL = WS_WM + 3 * 73728, WS_FLAG = WS_SEAL + 216 * 128, WS_FLAG2 = WS_FLAG + 8 * 2048, WS_EPOCH = WS_FLAG2 + 2048, WS_END = WS_EPOCH + 256;
constexpr float QSCALE = 0.07216878364870322f * 1.4426950408889634f;
constexpr int SC1 = 16;
constexpr unsigned POISON = 0xffffffffu;
constexpr unsigned CODE_BYTES = 26112;
constexpr int TILE0_DELAY = 2, LOAD_DELAY = 2, WQ_SPLIT = 4, B9_DELAY = 2, VO_1 = 1, VO_2 = 3, MM_EARLY = 0, PQ = 12, PV = 9, PI = 23;
static_assert(9 * PQ + 18 * PV + 5 * PI >= 384, "the whole frame is poisoned");
constexpr int SPIN_CAP = 1 << 16;


constexpr int PROBE = 1, ROLE = 0; constexpr size_t WS_T0 = 32u << 20;
#define ST_DECL u64 st_ = 0, t0_ = 0
#define ST(k) do { if ((k) == PROBE || (k) == 0) { SBAR(); u64 t_; asm volatile("s_memrealtime %0\n\ts_waitcnt lgkmcnt(0)" : "=s"(t_) :: "memory"); SBAR(); \
    if ((k) == PROBE) st_ = t_; \
    if ((k) == 0) t0_ = t_; } STO_##k } while (0)
#define STO_(k)
#define STO_0
#define STO_1
#define STO_2
#define STO_3
#define STO_4
#define STO_5
#define STO_6
#define STO_7
#define STO_8
#define STO_9
#define STO_10
#define STO_11
#define STO_12
#define STO_13
#define STO_14
#define STO_15
#define STO_16 if (threadIdx.x == 0) __hip_atomic_store((__attribute__((address_space(1))) u64*)(ws + WS_T0) + blockIdx.x, t0_, __ATOMIC_RELAXED, __HIP_MEMORY_SCOPE_AGENT);
#define STO_17
#define STO_18
#define STO_19
#define ST_OUT(ws, k0, k1) do { if (ROLE == ((k0) != 0)) { u64 m_ = ~0ull; \
    for (int q_ = 0; q_ < 4; ++q_) { u64 v_ = __hip_atomic_load((__attribute__((address_space(1))) u64*)(ws + WS_T0) + (threadIdx.x & 63) * 4 + q_, __ATOMIC_RELAXED, __HIP_MEMORY_SCOPE_AGENT); m_ = v_ < m_ ? v_ : m_; } \
    for (int o_ = 32; o_; o_ >>= 1) { u64 v_ = __shfl_xor(m_, o_); m_ = v_ < m_ ? v_ : m_; } \
    long long d_ = (long long)(st_ - m_); if (d_ < 0) d_ = 0; if (d_ > 5000) d_ = 5000; \
    const u64 e_ = m_ + 4000 + 4 * d_; while (__builtin_amdgcn_s_memrealtime() < e_) __builtin_amdgcn_s_sleep(1); } } while (0)


DI float asf(unsigned u) { return __builtin_bit_cast(float, u); }
DI unsigned asu(float f) { return __builtin_bit_cast(unsigned, f); }
DI unsigned cvtpk(float lo, float hi) { f32x2 v = {lo, hi}; return __builtin_bit_cast(unsigned, __builtin_convertvector(v, b16x2)); }
DI float bflo(unsigned u) { return asf(u << 16); }
DI float bfhi(unsigned u) { return asf(u & 0xffff0000u); }
DI f32x16 mfma32(u32x4 a, u32x4 b, f32x16 c) { return __builtin_amdgcn_mfma_f32_32x32x16_bf16(__builtin_bit_cast(b16x8, a), __builtin_bit_cast(b16x8, b), c, 0, 0, 0); }
DI f32x4 mfma16(u32x4 a, u32x4 b, f32x4 c) { return __builtin_amdgcn_mfma_f32_16x16x32_bf16(__builtin_bit_cast(b16x8, a), __builtin_bit_cast(b16x8, b), c, 0, 0, 0); }
DI f32x4 mfma16k16(u32x2 a, u32x2 b, f32x4 c) { return __builtin_amdgcn_mfma_f32_16x16x16bf16_1k(__builtin_bit_cast(s16x4, a), __builtin_bit_cast(s16x4, b), c, 0, 0, 0); }
DI u32x2 pack4(f32x4 v) { u32x2 r = {cvtpk(v[0], v[1]), cvtpk(v[2], v[3])}; return r; }
DI u32x4 lds_r128(unsigned a) { return *(LAS u32x4*)(size_t)a; }
DI f32x4 lds_r128f(unsigned a) { return *(LAS f32x4*)(size_t)a; }
DI u32x2 lds_r64(unsigned a) { return *(LAS u32x2*)(size_t)a; }
DI void lds_w128(unsigned a, u32x4 v) { *(LAS u32x4*)(size_t)a = v; }
DI void lds_w128f(unsigned a, f32x4 v) { *(LAS f32x4*)(size_t)a = v; }
DI void lds_w64(unsigned a, u32x2 v) { *(LAS u32x2*)(size_t)a = v; }
DI void lds_w32(unsigned a, float v) { *(LAS float*)(size_t)a = v; }
DI u32x2 lds_tr(unsigned a) { return __builtin_bit_cast(u32x2, __builtin_amdgcn_ds_read_tr16_b64_v4i16((LAS s16x4*)(size_t)a)); }
DI u32x4 lds_tr2(unsigned a0, unsigned a1) { u32x2 x = lds_tr(a0), y = lds_tr(a1); u32x4 r = {x[0], x[1], y[0], y[1]}; return r; }
#define BAR() asm volatile("s_waitcnt lgkmcnt(0)\n\ts_barrier" ::: "memory")
#define BARV(n) asm volatile("s_waitcnt vmcnt(" #n ") lgkmcnt(0)\n\ts_barrier" ::: "memory")
template <int CTRL> DI float dpp_add(float v) { return v + asf(__builtin_amdgcn_update_dpp(0u, asu(v), CTRL, 0xf, 0xf, true)); }
DI float xor16_add(float v) { auto r = __builtin_amdgcn_permlane16_swap(asu(v), asu(v), false, false); return asf(r[0]) + asf(r[1]); }
DI float xor16_max(float v) { auto r = __builtin_amdgcn_permlane16_swap(asu(v), asu(v), false, false); return __builtin_amdgcn_fmed3f(asf(r[0]), asf(r[1]), __builtin_inff()); }
DI unsigned umax3(unsigned a, unsigned b, unsigned c) { return max(max(a, b), c); }
template <int N> DI bool poisoned(const u32x4 (&v)[N]) {
  unsigned m = 0;
#pragma unroll
  for (int i = 0; i < N; ++i) { m = umax3(m, v[i][0], v[i][1]); m = umax3(m, v[i][2], v[i][3]); }
  return __any(m == POISON);
}
DI u32x4 unpoison(u32x4 v) { u32x4 r = {min(v[0], POISON - 1), min(v[1], POISON - 1), min(v[2], POISON - 1), min(v[3], POISON - 1)}; return r; }
DI unsigned fi_pos(unsigned r, unsigned c8) {
  return (((r >> 4) * 6 + (c8 >> 2)) * 64 + (c8 & 3) * 16 + ((r & 15) ^ ((c8 & 1) | (((r >> 3) & 1) << 1) | ((r >> 4) << 2)))) * 16;
}
DI float row8_sum(float s) { s = dpp_add<0x124>(s); s = dpp_add<0x128>(s); return xor16_add(s); }
DI float sum24(const f32x2 (&v)[12]) {
  f32x2 s0 = v[0] + v[1], s1 = v[2] + v[3];
#pragma unroll
  for (int i = 4; i < 12; i += 2) { s0 += v[i]; s1 += v[i + 1]; }
  s0 += s1;
  return s0[0] + s0[1];
}
DI float sumsq24(const f32x2 (&v)[12]) {
  f32x2 q0 = v[0] * v[0], q1 = v[1] * v[1];
#pragma unroll
  for (int i = 2; i < 12; i += 2) { q0 = __builtin_elementwise_fma(v[i], v[i], q0); q1 = __builtin_elementwise_fma(v[i + 1], v[i + 1], q1); }
  q0 += q1;
  return q0[0] + q0[1];
}
DI f32x2 unpack2(unsigned u) { f32x2 r = {bflo(u), bfhi(u)}; return r; }

constexpr unsigned SLOT = 36864, L_X = 3 * SLOT, L_H = 4 * SLOT, L_N = L_H + TILE_B, L_L = L_N + 1024, L_PT = L_L + 1024, LDS_BYTES = 163840;
constexpr unsigned QK = 24576, L_JOB = 4 * QK;
constexpr unsigned L_VI = L_X, L_SP = L_X + TILE_B, L_YI = L_X + 20480;
constexpr unsigned L_Q = L_H, L_FI = L_H;

#define DMA3(v0, v1, v2, sbase, ldsa) \
  asm volatile("s_mov_b32 m0, %4\n\ts_nop 0\n\tglobal_load_lds_dwordx4 %0, %3\n\tglobal_load_lds_dwordx4 %1, %3 offset:1024\n\tglobal_load_lds_dwordx4 %2, %3 offset:2048" \
               :: "v"(v0), "v"(v1), "v"(v2), "s"(sbase), "s"(ldsa) : "memory")
#define DMA1(v0, sbase, ldsa) \
  asm volatile("s_mov_b32 m0, %2\n\ts_nop 0\n\tglobal_load_lds_dwordx4 %0, %1" :: "v"(v0), "s"(sbase), "s"(ldsa) : "memory")

template <int OFF> DI void bdma(unsigned voff, u32x4 rs, unsigned soff) { asm volatile("buffer_load_dwordx4 %0, %1, %2 offen offset:%3 sc1 lds" :: "v"(voff), "s"(rs), "s"(soff), "i"(OFF) : "memory"); }
template <int OFF> DI void setm0(unsigned base) { asm volatile("s_add_u32 m0, %0, %1\n\ts_nop 0" :: "s"(base), "i"(OFF) : "memory", "scc"); }
template <int N> DI void waitvm() { asm volatile("s_waitcnt vmcnt(%0)" :: "i"(N) : "memory"); }
#define IC(x) std::integral_constant<int, (x)>{}
#define OPAQUE(v) asm volatile("" : "+v"(v))
template <class R> DI void st128(u32x4 v, R rs, unsigned voff, unsigned soff) { __builtin_amdgcn_raw_buffer_store_b128(v, rs, voff, soff, SC1); asm volatile("s_nop 1" :: "v"(v)); }
template <int CTRL> DI unsigned dpp_xor(unsigned v) { return v ^ __builtin_amdgcn_update_dpp(0u, v, CTRL, 0xf, 0xf, true); }
DI unsigned fold_xor(unsigned e, unsigned o) {
  auto r = __builtin_amdgcn_permlane32_swap(e, o, false, false);
  unsigned u = r[0] ^ r[1];
  auto q = __builtin_amdgcn_permlane16_swap(u, u, false, false);
  u = q[0] ^ q[1];
  u = dpp_xor<0x128>(u); u = dpp_xor<0x124>(u); u = dpp_xor<0x4e>(u); return dpp_xor<0xb1>(u);
}
DI bool seal_ok(unsigned e, unsigned o, u32x4 seal, bool has, u32x2 tag) {
  const unsigned bad = has ? (seal[1] ^ tag[0]) | (seal[3] ^ tag[1]) : 0u;
  return __builtin_amdgcn_ballot_w64((fold_xor(e ^ seal[0], o ^ seal[2]) | bad) != 0) == 0;
}
DI unsigned xor3(unsigned a, unsigned b, unsigned c) { return __builtin_amdgcn_bitop3_b32(a, b, c, 0x96); }
#define XOR_IN(e, o, v) do { e = xor3(e, (v)[0], (v)[2]); o = xor3(o, (v)[1], (v)[3]); } while (0)
#define MIXV6() _Pragma("unroll") for (int q_ = 0; q_ < 6; ++q_) { __builtin_amdgcn_sched_group_barrier(0x8, 1, 0); __builtin_amdgcn_sched_group_barrier(0x2, 1, 0); } SBAR()
template <int N> DI bool sealed(const u32x4 (&v)[N], u32x4 seal, bool has, u32x2 tag) {
  unsigned e = 0, o = 0;
#pragma unroll
  for (int i = 0; i < N; ++i) XOR_IN(e, o, v[i]);
  return seal_ok(e, o, seal, has, tag);
}
DI bool tags_ok(u32x4 f, u32x2 tag) { return f[0] == tag[0] && f[1] == tag[1] && f[2] == tag[0] && f[3] == tag[1]; }

__global__ __launch_bounds__(512, 2) void k_timesformer(const char* x_, const char* wqkv_t_, const char* wo_t_, const char* wqkv_s_, const char* wo_s_, char* ws_, char* out_) {
  unsigned dc;
  u64x4 ka; u64x2 kb; u64 kc;
  {
    u64 pc;
    asm volatile("s_load_dwordx8 %2, %6, 0x0\n\ts_load_dwordx4 %3, %6, 0x20\n\ts_load_dwordx2 %4, %6, 0x30\n\ts_getpc_b64 %1\n\tglobal_load_dword %0, %5, %1"
                 : "=v"(dc), "=&s"(pc), "=&s"(ka), "=&s"(kb), "=&s"(kc) : "v"(min(threadIdx.x * 128, CODE_BYTES)), "s"(__builtin_amdgcn_kernarg_segment_ptr()) : "memory");
  }
  constexpr unsigned lds0 = 0;
  const unsigned b = blockIdx.x;
  unsigned w8 = __builtin_amdgcn_readfirstlane(threadIdx.x >> 6);
  unsigned l = threadIdx.x & 63;
  const unsigned t = b & 7, qb = b >> 3;
  w8 = (w8 & 4) | ((w8 + qb / 6) & 3);
  ST_DECL;
  ST(0);
  const bool jqk = b < 72, job = b < 216;
  const unsigned jp = b % 72, jw = jp / 18, jks = (jp - jw * 18) / 3, jtau = jp - jw * 15 - jks * 3;
  const unsigned rot = qb % 6;
  unsigned fo[2][3];
#pragma unroll
  for (int i = 0; i < 3; ++i) {
    const unsigned p = i * 64 + l;
    if (w8 < 4) { fo[0][i] = p * 16 + (p >= 96 ? FRAME_B - 1536 : 0) + w8 * 2 * FRAME_B + b * 1536 - i * 1024; fo[1][i] = 0; }
    else if (w8 == 7) {
      const unsigned row = (p * 1366) >> 16;
      fo[0][i] = row * 1152 + (((p - row * 48) ^ (row << 1)) * 16) - i * 1024; fo[1][i] = row * 1152 + (((p - row * 48) ^ (row << 1) ^ 8) * 16) - i * 1024;
    } else {
      const unsigned row = (p * 2731) >> 16, o = row * 1152 + (((p - row * 24) ^ row) * 16) - i * 1024;
      fo[0][i] = o + jks * (32 * 1152) + (jqk ? 0 : 768);
      fo[1][i] = jqk ? o + jtau * (16 * 1152) + 384 : (l >> 1) * 384 + (l & 1) * 16 + jtau * 32 + i * (12288 - 1024);
    }
  }
  asm volatile("s_waitcnt lgkmcnt(0)" : "+s"(ka), "+s"(kb), "+s"(kc), "+v"(fo[0][0]), "+v"(fo[0][1]), "+v"(fo[0][2]), "+v"(fo[1][0]), "+v"(fo[1][1]), "+v"(fo[1][2]) :: "memory");
  char* const ws = (char*)kb[1];
  auto chunk = [&](const int n) {
    const u64 src = ka[1] + (n + rot >= 6 ? n + rot - 6 : n + rot) * (32 * 1152);
#pragma unroll
    for (int q = 0; q < 8; ++q) DMA3(fo[(q >> 1) & 1][0], fo[(q >> 1) & 1][1], fo[(q >> 1) & 1][2], src + q * 4608, (n & 3) * QK + q * 3072);
  };
  unsigned dw = 0, dx = 0;
  if (w8 < 4) {
    DMA3(fo[0][0], fo[0][1], fo[0][2], ka[0], L_YI + w8 * 3072);
  } else if (w8 == 4) {
    if (job) {
      __builtin_amdgcn_s_setprio(3);
      const u64 wqkv = b >= 144 ? ka[1] : ka[3], second = jqk ? wqkv : b >= 144 ? ka[2] : kb[0];
#pragma unroll
      for (int q = 0; q < 4; ++q) DMA3(fo[0][0], fo[0][1], fo[0][2], wqkv + q * (8 * 1152), L_JOB + q * 3072);
#pragma unroll
      for (int q = 0; q < 2; ++q) DMA3(fo[1][0], fo[1][1], fo[1][2], second + q * (jqk ? 8 * 1152 : 36864), L_JOB + 12288 + q * 3072);
    }
  } else if (w8 == 7) {
    __builtin_amdgcn_s_sleep(LOAD_DELAY);
    chunk(0); chunk(1);
  } else {
    if (w8 == 6) asm volatile("global_load_dword %0, %1, %2" : "=v"(dx) : "v"((qb * 6 + min(l, 35u) / 6) * 1152 + (min(l, 35u) % 6) * 128), "s"(ka[1]) : "memory");
    asm volatile("global_load_dword %0, %1, %2" : "=v"(dw) : "v"((qb * 18 + min(l, 17u)) * 128), "s"(w8 == 6 ? ka[2] : kb[0]) : "memory");
  }
  const auto rsWS = __builtin_amdgcn_make_buffer_rsrc((void*)ws, 0, WS_END, 0x00020000);
  const u32x4 pz = {POISON, POISON, POISON, POISON};
  u64 epoch;
  asm volatile("s_load_dwordx2 %0, %1, 0x0" : "=&s"(epoch) : "s"(ws + WS_EPOCH) : "memory");
  asm volatile("s_waitcnt lgkmcnt(0)" : "+s"(epoch) :: "memory");
  epoch += 1;
  const u32x2 tag = {(unsigned)epoch, (unsigned)(epoch >> 32)};
  asm volatile("" : "+s"(w8));
  const unsigned w = w8 & 3;
#define LANE_IDS const unsigned i16 = l & 15, g = l >> 4, r32 = l & 31, hh = l >> 5, tq = i16 >> 2, tp = l & 3
#define FRAG_POS unsigned fr[2], dp[2];                                                                       \
  fr[0] = L_X + fi_pos(i16, g); fr[1] = (fr[0] ^ 64) + 6144; dp[0] = L_X + fi_pos(i16, hh) + 8 * (g & 1) + w * 1536; dp[1] = (dp[0] ^ 64) + 6144; \
  OPAQUE(fr[0]); OPAQUE(fr[1]); OPAQUE(dp[0]); OPAQUE(dp[1])
  auto ldw = [&](u32x4 (&W)[18], const int m, const int k0, const int k1, const unsigned rot) {
#pragma unroll
    for (int kk = k0; kk < k1; ++kk) {
      const unsigned ks = kk + rot >= 6 ? kk + rot - 6 : kk + rot;
#pragma unroll
      for (int i = 0; i < 3; ++i) { SBAR(); W[kk * 3 + i] = __builtin_amdgcn_raw_buffer_load_b128(rsWS, l * 16 + i * 1024, WS_WM + m * 73728 + w * 18432 + ks * 3072, SC1); }
    }
  };
  auto ld_seal = [&](const int m) {
    return __builtin_amdgcn_raw_buffer_load_b128(rsWS, l < 18 ? l * 128 : WS_END, WS_SEAL + (m * 72 + 18 * w) * 128, SC1);
  };
  auto h_frags = [&](u32x4 (&H)[6][2], const unsigned (&fr)[2], const int k0, const int k1) {
#pragma unroll
    for (int kk = k0; kk < k1; ++kk) {
      const unsigned ks = kk + rot >= 6 ? kk + rot - 6 : kk + rot;
      H[kk][0] = lds_r128(fr[0] + ks * 1024 + (L_H - L_X)); H[kk][1] = lds_r128(fr[1] + ks * 1024 + (L_H - L_X));
    }
  };
  auto low_sum = [&](const f32x4 a, const f32x4 c, float (&lt)[2]) {
    lt[0] = (a[0] + a[2]) + (c[0] + c[2]);
    lt[1] = (a[1] + a[3]) + (c[1] + c[3]);
    return (bool)__any(lt[0] < 8.7e-19f || lt[1] < 8.7e-19f);
  };

  if (w8 < 4) {
    const unsigned nl = l & 3, j = (l >> 2) & 7, tt = 2 * w + (l >> 5), r = nl * 8 + tt;
    unsigned rp = L_X + fi_pos(r, j);
    OPAQUE(rp);
    waitvm<0>();
    asm volatile("" :: "v"(dc));
    f32x2 xv[12];
    float mu1;
    {
      u32x4 h[3], xr[3];
#pragma unroll
      for (int i = 0; i < 3; ++i) xr[i] = lds_r128(lds0 + L_YI + tt * 1536 + nl * 384 + j * 16 + i * 128);
#pragma unroll
      for (int i = 0; i < 12; ++i) xv[i] = unpack2(xr[i >> 2][i & 3]);
      mu1 = row8_sum(sum24(xv)) * (1.0f / 192.0f);
#pragma unroll
      for (int i = 0; i < 12; ++i) xv[i] -= mu1;
      const float rs = __builtin_amdgcn_rsqf(row8_sum(sumsq24(xv)) * (1.0f / 192.0f) + 1e-5f);
#pragma unroll
      for (int i = 0; i < 12; ++i) { const f32x2 n = xv[i] * rs; h[i >> 2][i & 3] = cvtpk(n[0], n[1]); }
#pragma unroll
      for (int i = 0; i < 3; ++i) lds_w128(rp + (L_H - L_X) + i * 2048, h[i]);
    }
    SBAR();
    OPAQUE(l);
    unsigned so = tt * FRAME_B + (j >> 1) * 1024 + ((t ^ ((j >> 1) & 1)) * 128) + (((j & 1) ^ (t >> 2)) * 64) + nl * 16;
    const unsigned sso = qb * TILE_B;
    LANE_IDS;
    FRAG_POS;
    unsigned sto = j == 0 ? (tt * 1024 + nl) * 8 : WS_END;
    unsigned sa = L_SP + l * 16;
    unsigned keep = (hh == (i16 >> 3)) ? ~0u : 0u;
    OPAQUE(sto); OPAQUE(so); OPAQUE(sa); OPAQUE(keep);
    ST(1);
    BAR();
    ST(6);
    {
      u32x4 H[6][2], W[18];
      BAR();
      h_frags(H, fr, 0, 6);
      asm volatile("s_barrier" ::: "memory");
      ST(2);
#pragma unroll 1
      for (int sp = 0;; ++sp) {
        const u32x4 seal = ld_seal(2);
        ldw(W, 2, 0, VO_1, rot);
        if (sp == 0) asm volatile("s_barrier" ::: "memory");
        ldw(W, 2, VO_1, VO_2, rot);
        if (sp == 0) asm volatile("s_barrier" ::: "memory");
        ldw(W, 2, VO_2, 6, rot);
        SBAR();
        f32x4 vp[3][2];
        unsigned he = 0, ho = 0;
        auto mm = [&](const int kk) {
#pragma unroll
          for (int i = 0; i < 3; ++i) {
            vp[i][0] = mfma16(H[kk][0], W[kk * 3 + i], kk ? vp[i][0] : f32x4{0, 0, 0, 0});
            vp[i][1] = mfma16(H[kk][1], W[kk * 3 + i], kk ? vp[i][1] : f32x4{0, 0, 0, 0});
            XOR_IN(he, ho, W[kk * 3 + i]);
          }
          MIXV6();
        };
        ST(3);
#pragma unroll
        for (int kk = 0; kk < MM_EARLY; ++kk) mm(kk);
        OPAQUE(he); OPAQUE(ho);
        ST(8);
        if (sp == 0) asm volatile("s_barrier" ::: "memory");
        u32x2 pt[2];
        {
          f32x4 e = (lds_r128f(sa) + lds_r128f(sa + 1024)) + lds_r128f(sa + 2048);
          const float mq = xor16_max(fmaxf(fmaxf(e[0], e[1]), fmaxf(e[2], e[3]))) * -QSCALE;
#pragma unroll
          for (int k = 0; k < 4; ++k) e[k] = __builtin_amdgcn_exp2f(fmaf(e[k], QSCALE, mq));
          pt[0] = pack4(e * __builtin_amdgcn_rcpf(xor16_add((e[0] + e[1]) + (e[2] + e[3]))));
#pragma unroll
          for (int k = 0; k < 2; ++k) {
            auto q = __builtin_amdgcn_permlane32_swap(pt[0][k], pt[0][k], false, false);
            pt[1][k] = (q[0] ^ q[1] ^ pt[0][k]) & keep;
            pt[0][k] &= keep;
          }
          OPAQUE(pt[0]); OPAQUE(pt[1]);
        }
        SBAR();
        ST(4);
#pragma unroll
        for (int kk = MM_EARLY; kk < 6; ++kk) mm(kk);
        OPAQUE(he); OPAQUE(ho);
        ST(7);
        f32x4 y[3][2];
#pragma unroll
        for (int hf = 0; hf < 2; ++hf)
#pragma unroll
          for (int i = 0; i < 3; ++i) y[i][hf] = mfma16k16(pack4(vp[i][hf]), pt[hf], f32x4{0, 0, 0, 0});
        SBAR();
#pragma unroll
        for (int hf = 0; hf < 2; ++hf)
#pragma unroll
          for (int i = 0; i < 3; ++i) lds_w64(dp[hf] + (L_YI - L_X) + i * 512, pack4(y[i][hf]));
        if (__builtin_expect(seal_ok(he, ho, seal, l < 18, tag), 1) || sp >= SPIN_CAP) break;
        asm volatile("" ::: "memory");
      }
    }
    ST(9);
    BAR();
    {
      u32x4 y[3], h[3];
#pragma unroll
      for (int i = 0; i < 3; ++i) y[i] = lds_r128(rp + (L_YI - L_X) + i * 2048);
#pragma unroll
      for (int i = 0; i < 12; ++i) xv[i] += unpack2(y[i >> 2][i & 3]);
      const float mu = row8_sum(sum24(xv)) * (1.0f / 192.0f), sq = row8_sum(sumsq24(xv));
      const float var = fmaxf(fmaf(sq, 1.0f / 192.0f, -mu * mu), 0.f) + 1e-5f;
      const float rs = __builtin_amdgcn_rsqf(var), nb = -mu * rs;
#pragma unroll
      for (int i = 0; i < 3; ++i) {
#pragma unroll
        for (int e = 0; e < 4; ++e) { const f32x2 n = xv[4 * i + e] * rs + nb; h[i][e] = cvtpk(n[0], n[1]); }
        if (i == 0) { OPAQUE(h[0]); ST(5); asm volatile("s_barrier" ::: "memory"); }
        st128(h[i], rsWS, so, WS_H2 + sso + i * 4096);
        SBAR();
      }
      const u32x2 stat = {asu(mu + mu1), asu(var)};
      __builtin_amdgcn_raw_buffer_store_b64(stat, rsWS, sto, WS_ST + b * 32, SC1);
    }
    ST(10);
    const unsigned ring = lds0 + w * SLOT;
    unsigned kr0 = ring + (r32 >> 2) * 128 + (hh ^ (r32 >> 4)) * 64 + (r32 & 3) * 16, kr1 = kr0 ^ 128;
    unsigned vr0 = ring + (g & 1) * 1024 + (hh ^ (g & 1)) * 128 + (tp >> 1) * 64 + tq * 16 + 8 * (tp & 1), vr1 = (vr0 ^ 64) + 512;
    unsigned lo[3] = {l * 16, l * 16 + 4096, l * 16 + 8192}, pw = ring + 2 * TILE_B + l * 16, lw = l < 16 ? L_L + (l * 4 + w) * 8 : L_L + 512 + l * 8;
    OPAQUE(kr0); OPAQUE(kr1); OPAQUE(vr0); OPAQUE(vr1); OPAQUE(lo[0]); OPAQUE(lo[1]); OPAQUE(lo[2]); OPAQUE(pw); OPAQUE(lw);
    const u64 hbu = (u64)(ws + WS_H2 + (size_t)t * FRAME_B);
    const u32x4 rs = {(unsigned)hbu, (unsigned)(hbu >> 32) & 0xffffu, (unsigned)FRAME_B, 0x00020000u};
    unsigned toff[8];
#pragma unroll
    for (int i = 0; i < 8; ++i) { toff[i] = (8 * w + ((i + qb) & 7)) * TILE_B; asm volatile("" : "+s"(toff[i])); }
    u32x4 sel;
    { const unsigned one = (tp == (g & 1)) ? 0x3f803f80u : 0u; sel = u32x4{one, one, one, one}; OPAQUE(sel); }
    auto piece = [&](auto Tc, auto Pc) {
      constexpr int T = decltype(Tc)::value, p = decltype(Pc)::value;
      if constexpr (T < 8) {
        if constexpr (p % 4 == 0) setm0<(T % 3) * TILE_B + p * 1024>(ring);
        bdma<(p % 4) * 1024>(lo[p / 4], rs, toff[T]);
      }
    };
#define P6(T, a) piece(IC(T), IC(a)); piece(IC(T), IC(a + 1)); piece(IC(T), IC(a + 2)); piece(IC(T), IC(a + 3)); piece(IC(T), IC(a + 4)); piece(IC(T), IC(a + 5));
    auto read_valid = [&](u32x4 (&KK)[12], const unsigned dst, unsigned off) {
      for (int sp = 0; sp < SPIN_CAP; ++sp) {
#pragma unroll
        for (int k = 0; k < 12; ++k) KK[k] = lds_r128(((k & 1) ? kr1 : kr0) + dst + k * 1024);
        if (__builtin_expect(!poisoned(KK), 1)) break;
#pragma unroll 1
        for (int k = 0; k < 12; ++k) { setm0<0>(ring + dst + k * 1024); bdma<0>(lo[0], rs, off + k * 1024); }
        waitvm<0>();
      }
    };
    unsigned qa = lds0 + L_Q + lo[0], na = lds0 + L_N + r32 * 16;
    OPAQUE(qa); OPAQUE(na);
    waitvm<0>();
    __builtin_amdgcn_s_sleep(B9_DELAY);
    BAR();
    __builtin_amdgcn_s_sleep(TILE0_DELAY);
    if (w != (qb >> 3)) { P6(0, 0) P6(0, 6) }
    else if (l == 0) __builtin_amdgcn_raw_buffer_store_b64(tag, rsWS, 0, WS_FLAG2 + b * 8, SC1);
    ST(11);
    BAR();
    u32x4 K0[12];
    if (w != (qb >> 3)) waitvm<0>();
    ST(12);
    read_valid(K0, 0, toff[0]);
    ST(13);
    piece(IC(1), IC(0)); piece(IC(1), IC(1)); piece(IC(1), IC(2)); piece(IC(1), IC(3));
    BAR();
    ST(14);
    u32x4 Q[12];
    float mref;
    {
      const f32x4 n0 = lds_r128f(na), n1 = lds_r128f(na + 512);
#pragma unroll
      for (int s = 0; s < 6; ++s) Q[s] = lds_r128(qa + s * 1024);
      SBAR();
      const f32x4 s = n0 + n1;
      mref = __builtin_amdgcn_sqrtf(((s[0] + s[1]) + (s[2] + s[3])) * (192.0f * 1.02f));
      OPAQUE(mref);
      SBAR();
#pragma unroll
      for (int s = 6; s < 12; ++s) Q[s] = lds_r128(qa + s * 1024);
      SBAR();
    }
    constexpr int KW = 4, VW = 4;
    bool safe = false, exact = false, late = false;
#pragma unroll 1
    for (;;) {
      f32x16 S[2], O[6], negm;
      u32x4 K[12], V[12], P[2][2];
      f32x4 lsum = {0, 0, 0, 0};
      float pe[2][2];
#pragma unroll
      for (int i = 0; i < 16; ++i) negm[i] = -mref;
#define RK(k, T) K[k] = lds_r128((((k) & 1) ? kr1 : kr0) + ((T) % 3) * TILE_B + (k) * 1024);
#define RV(u, T) { const unsigned a_ = (((u) & 1) ? vr1 : vr0) + ((T) % 3) * TILE_B + ((u) >> 1) * 2048; V[u] = lds_tr2(a_, a_ + 256); }
      auto sm = [&](auto Tc, auto Hc, auto Gc) {
        constexpr int T = decltype(Tc)::value, H = decltype(Hc)::value, G = decltype(Gc)::value;
        if constexpr (G < 8) pe[(G >> 1) & 1][G & 1] = __builtin_amdgcn_exp2f(S[T & 1][8 * H + G]);
        if constexpr (G >= 2 && G <= 8 && G % 2 == 0) { constexpr int q = G / 2 - 1; P[T & 1][H][q] = cvtpk(pe[q & 1][0], pe[q & 1][1]); }
        if constexpr (G == 10) lsum = mfma16(sel, P[T & 1][H], lsum);
      };
      auto put_o = [&](const int un) {
        u32x4 pk;
#pragma unroll
        for (int e = 0; e < 4; ++e) pk[e] = cvtpk(O[un >> 1][8 * (un & 1) + 2 * e], O[un >> 1][8 * (un & 1) + 2 * e + 1]);
        lds_w128(pw + un * 1024, pk);
      };
      auto qkphase = [&](auto Ic) {
        constexpr int I = decltype(Ic)::value;
        auto step = [&](auto Kc) {
          constexpr int k = decltype(Kc)::value;
          if constexpr (I + 1 < 8) {
            S[(I + 1) & 1] = mfma32(I < 0 ? K0[k] : K[k], Q[k], k == 0 ? negm : S[(I + 1) & 1]);
            if constexpr (k + KW < 12 && I >= 0) RK(k + KW, I + 1)
          }
          if constexpr (I < 0 && k < 8) piece(IC(1), IC(4 + k));
          if constexpr (I >= 0) { if constexpr (k & 1) piece(IC(I + 2), IC(6 + k / 2)); if constexpr (k < 11) sm(IC(I), IC(1), IC(k)); }
          if constexpr (I >= 0 && I < 7 && k >= 12 - VW) RV(k - (12 - VW), I)
          SBAR();
        };
        step(IC(0)); step(IC(1)); step(IC(2)); step(IC(3)); step(IC(4)); step(IC(5)); step(IC(6)); step(IC(7)); step(IC(8)); step(IC(9)); step(IC(10)); step(IC(11));
      };
      auto pvphase = [&](auto Ic) {
        constexpr int I = decltype(Ic)::value;
        auto step = [&](auto Uc) {
          constexpr int u = decltype(Uc)::value;
          if constexpr (I >= 0) {
            f32x16 z;
#pragma unroll
            for (int e = 0; e < 16; ++e) z[e] = 0.f;
            O[u >> 1] = mfma32(V[u], P[I & 1][u & 1], (I == 0 && !(u & 1)) ? z : O[u >> 1]);
            if constexpr (u & 1) piece(IC(I + 3), IC(u / 2));
            if constexpr (u + VW < 12) RV(u + VW, I)
          }
          if constexpr (I + 1 < 8 && u >= 1) sm(IC(I + 1), IC(0), IC(u - 1));
          if constexpr (I < 0 && (u & 1)) piece(IC(2), IC(u / 2));
          if constexpr (I + 2 < 8) {
            if constexpr (u == 12 - KW) waitvm<(I < 0 ? 12 : I + 3 < 8 ? 6 + (12 - KW) / 2 : 6)>();
            if constexpr (u >= 12 - KW) RK(u - (12 - KW), I + 2)
            if constexpr (u == 11) waitvm<(I + 3 < 8 ? 6 : 0)>();
          }
          SBAR();
        };
        step(IC(0)); step(IC(1)); step(IC(2)); step(IC(3)); step(IC(4)); step(IC(5)); step(IC(6)); step(IC(7)); step(IC(8)); step(IC(9)); step(IC(10)); step(IC(11));
      };
      auto drain = [&]() {
        constexpr int tl[24] = {6, 6, 6, 6, 6, 6, 6, 6, 7, 7, 7, 7, 6, 6, 7, 7, 6, 6, 7, 7, 7, 7, 7, 7};
        constexpr int fu[24] = {0, 1, 2, 3, 4, 5, 6, 7, 0, 1, 2, 3, 8, 9, 4, 5, 10, 11, 6, 7, 8, 9, 10, 11};
        constexpr int wr[24] = {-1, -1, -1, -1, -1, -1, -1, -1, -1, -1, -1, 0, 1, 2, -1, 3, -1, 4, 5, -1, -1, 6, 7, 8};
        u32x4 X[24];
#pragma unroll
        for (int n = 0; n < VW; ++n) X[n] = V[n];
        auto step = [&](auto Nc) {
          constexpr int n = decltype(Nc)::value;
          O[fu[n] >> 1] = mfma32(X[n], P[tl[n] & 1][fu[n] & 1], O[fu[n] >> 1]);
          if constexpr (n + VW < 24) {
            const unsigned a_ = ((fu[n + VW] & 1) ? vr1 : vr0) + (tl[n + VW] % 3) * TILE_B + (fu[n + VW] >> 1) * 2048;
            X[n + VW] = lds_tr2(a_, a_ + 256);
          }
          if constexpr (n >= 1 && n <= 3) { sm(IC(7), IC(0), IC(3 * n - 3)); sm(IC(7), IC(0), IC(3 * n - 2)); sm(IC(7), IC(0), IC(3 * n - 1)); }
          if constexpr (n >= 4 && n <= 6) { sm(IC(7), IC(1), IC(3 * n - 12)); sm(IC(7), IC(1), IC(3 * n - 11)); sm(IC(7), IC(1), IC(3 * n - 10)); }
          if constexpr (n == 7) sm(IC(7), IC(0), IC(10));
          if constexpr (n == 9) sm(IC(7), IC(1), IC(10));
          if constexpr (wr[n] >= 0) put_o(wr[n]);
          SBAR();
        };
        step(IC(0)); step(IC(1)); step(IC(2)); step(IC(3)); step(IC(4)); step(IC(5)); step(IC(6)); step(IC(7)); step(IC(8)); step(IC(9)); step(IC(10)); step(IC(11));
        step(IC(12)); step(IC(13)); step(IC(14)); step(IC(15)); step(IC(16)); step(IC(17)); step(IC(18)); step(IC(19)); step(IC(20)); step(IC(21)); step(IC(22)); step(IC(23));
      };
#define STALE(v) (__builtin_expect(!safe && __any((v) != (v)), 0))
      bool done = false;
      do {
        qkphase(IC(-1));
        ST(15);
        pvphase(IC(-1));
        ST(16);
        qkphase(IC(0)); pvphase(IC(0)); qkphase(IC(1)); pvphase(IC(1)); qkphase(IC(2));
        const float seen = lsum[0];
        pvphase(IC(2));
        if (STALE(seen)) break;
        qkphase(IC(3)); pvphase(IC(3)); qkphase(IC(4)); pvphase(IC(4)); qkphase(IC(5));
        if (!late) asm volatile("s_barrier" ::: "memory");
        late = true;
        pvphase(IC(5)); qkphase(IC(6)); drain();
        if (STALE(lsum[0])) break;
        { const u32x2 ls = {asu(lsum[0]), asu(lsum[1])}; lds_w64(lw, ls); }
        put_o(9); put_o(10); put_o(11);
        done = true;
      } while (false);
#pragma unroll
      for (int k = 0; k < 12; ++k) asm volatile("" : "=v"(K0[k]));
      if (__builtin_expect(done, 1)) {
        ST(17);
        BAR();
        ST(18);
        float lt[2];
        unsigned ll = lo[0];
        OPAQUE(ll);
        ll >>= 4;
        if (__builtin_expect(!low_sum(lds_r128f(L_L + (ll & 15) * 32), lds_r128f(L_L + (ll & 15) * 32 + 16), lt) || exact, 1)) break;
        BAR();
        float mx = -3.0e38f;
#pragma unroll 1
        for (unsigned i = 0; i < 8; ++i) {
#pragma unroll 1
          for (int k = 0; k < 12; ++k) { setm0<0>(ring + k * 1024); bdma<0>(lo[0], rs, (8 * w + i) * TILE_B + k * 1024); }
          waitvm<0>();
          f32x16 s;
#pragma unroll
          for (int e = 0; e < 16; ++e) s[e] = 0.f;
#pragma unroll
          for (int k = 0; k < 12; ++k) s = mfma32(lds_r128(((k & 1) ? kr1 : kr0) + k * 1024), Q[k], s);
#pragma unroll
          for (int e = 0; e < 16; ++e) mx = fmaxf(mx, s[e]);
        }
        lds_w32(L_N + ((ll & 31) * 8 + w * 2 + (ll >> 5)) * 4, mx);
        BAR();
        const f32x4 c = lds_r128f(L_N + (ll & 31) * 32), d = lds_r128f(L_N + (ll & 31) * 32 + 16);
        mref = fmaxf(fmaxf(fmaxf(c[0], c[1]), fmaxf(c[2], c[3])), fmaxf(fmaxf(d[0], d[1]), fmaxf(d[2], d[3])));
        exact = true;
      } else {
        unsigned ll = lo[0];
        OPAQUE(ll);
        for (int sp = 0; sp < SPIN_CAP; ++sp) {
          asm volatile("" ::: "memory");
          const u32x2 f = __builtin_amdgcn_raw_buffer_load_b64(rsWS, ll >> 1, WS_FLAG2 + w * 512, SC1);
          if (__all(f[0] == tag[0] && f[1] == tag[1])) break;
        }
      }
      safe = true;
      asm volatile("s_waitcnt vmcnt(0) lgkmcnt(0)" ::: "memory");
      P6(0, 0) P6(0, 6)
      waitvm<0>();
      piece(IC(1), IC(0)); piece(IC(1), IC(1)); piece(IC(1), IC(2)); piece(IC(1), IC(3));
#pragma unroll
      for (int k = 0; k < 12; ++k) K0[k] = lds_r128(((k & 1) ? kr1 : kr0) + k * 1024);
    }
    ST(19);
    BAR();
    ST_OUT(ws, 0, 12);
  } else {
    LANE_IDS;
    FRAG_POS;
    u32x4 wq[18], wv[18];
    if (w == 3) {
      {
      __builtin_amdgcn_raw_buffer_store_b32(POISON, rsWS, l * 4, WS_ST + (t * 1024 + qb * 32) * 8, SC1);
      const unsigned p0 = WS_H2 + t * FRAME_B + (jqk ? qb * PQ : job ? 9 * (PQ - PV) + qb * PV : 384 + (qb - 32) * PI) * 1024, np = jqk ? PQ : job ? PV : PI;
#pragma unroll 1
      for (unsigned i = 0; i < np; ++i) __builtin_amdgcn_raw_buffer_store_b128(pz, rsWS, l * 16, p0 + i * 1024, SC1);
      asm volatile("s_nop 1" :: "v"(pz));
    }
      chunk(2);
      ST(1);
      if (jqk) waitvm<25 + PQ>(); else if (job) waitvm<25 + PV>(); else waitvm<25 + PI>();
      BAR();
      chunk(3);
      ST(2);
      BARV(24);
      __builtin_amdgcn_raw_buffer_store_b64(tag, rsWS, l < 8 ? l * 2048 : WS_END, WS_FLAG + b * 8, SC1);
      chunk(4); chunk(5);
      ST(3);
      BARV(49);
      BARV(24);
      BARV(0);
    } else {
      unsigned ta[2][4][2];
#pragma unroll
      for (int m = 0; m < 2; ++m)
#pragma unroll
        for (int i = 0; i < 4; ++i) {
          ta[m][i][0] = (8 * g + tq) * 768 + (((12 * m + 4 * w + i) ^ (tq | ((g & 1) << 2))) * 2 + (tp >> 1)) * 16 + 8 * (tp & 1);
          ta[m][i][1] = ta[m][i][0] + 2 * QK;
          OPAQUE(ta[m][i][0]); OPAQUE(ta[m][i][1]);
        }
      unsigned spa = hh == (i16 >> 3) ? L_SP + w * 1024 + l * 16 : L_SP + 4096 + l * 16;
      OPAQUE(spa);
      if (w == 0 && job) {
        unsigned ja[2] = {L_JOB + i16 * 384 + ((g ^ (l & 7)) * 16), L_JOB + i16 * 384 + (((4 + g) ^ (l & 7)) * 16)}, jb = L_JOB + 12288 + (8 * g + tq) * 32 + tp * 8;
        unsigned jo = (g & 1) * 512 + (hh * 16 + i16) * 16;
        OPAQUE(ja[0]); OPAQUE(ja[1]); OPAQUE(jb); OPAQUE(jo);
        waitvm<0>();
        ST(2);
        u32x4 bb[6];
        if (jqk) {
#pragma unroll
          for (int kk = 0; kk < 6; ++kk) bb[kk] = lds_r128(ja[kk & 1] + (kk >> 1) * 128 + 12288);
        } else {
#pragma unroll
          for (int kk = 0; kk < 6; ++kk) bb[kk] = lds_tr2(jb + kk * 1024, jb + kk * 1024 + 128);
        }
        f32x4 d = {0, 0, 0, 0}, e = {0, 0, 0, 0};
#pragma unroll
        for (int kk = 0; kk < 6; ++kk) {
          d = mfma16(lds_r128(ja[kk & 1] + (kk >> 1) * 128), bb[kk], d);
          e = mfma16(lds_r128(ja[kk & 1] + (kk >> 1) * 128 + 6144), bb[kk], e);
        }
        const float sc = jqk ? QSCALE : 1.0f;
        const u32x2 r0 = pack4(d * sc), r1 = pack4(e * sc);
        auto s0 = __builtin_amdgcn_permlane16_swap(r0[0], r1[0], false, false);
        auto s1 = __builtin_amdgcn_permlane16_swap(r0[1], r1[1], false, false);
        st128(u32x4{s0[0], s1[0], s0[1], s1[1]}, rsWS, jo, WS_WM + b * 1024);
        const u32x2 half = {fold_xor(r0[0] ^ r1[0], r0[1] ^ r1[1]), hh ? tag[1] : tag[0]};
        __builtin_amdgcn_raw_buffer_store_b64(half, rsWS, r32 == 0 ? hh * 8 : WS_END, WS_SEAL + b * 128, SC1);
        ST(3);
      }
      __builtin_amdgcn_s_setprio(3);
      u32x4 H[6][2];
      f32x4 acc[2][4][2];
      auto step = [&](const int n) {
        u32x4 a[8];
#pragma unroll
        for (int u = 0; u < 8; ++u) { const unsigned at = ta[u >> 2][u & 3][(n >> 1) & 1] + (n & 1) * QK; a[u] = lds_tr2(at, at + 3072); }
#pragma unroll
        for (int u = 0; u < 8; ++u) {
          acc[u >> 2][u & 3][0] = mfma16(a[u], H[n][0], n ? acc[u >> 2][u & 3][0] : f32x4{0, 0, 0, 0});
          acc[u >> 2][u & 3][1] = mfma16(a[u], H[n][1], n ? acc[u >> 2][u & 3][1] : f32x4{0, 0, 0, 0});
        }
        __builtin_amdgcn_sched_group_barrier(0x100, 8, 0);
#pragma unroll
        for (int q = 0; q < 4; ++q) { __builtin_amdgcn_sched_group_barrier(0x8, 2, 0); __builtin_amdgcn_sched_group_barrier(0x100, 2, 0); }
        SBAR();
      };
      ST(4);
      BAR();
      h_frags(H, fr, 0, 2);
      SBAR();
      step(0); step(1);
      BAR();
      h_frags(H, fr, 2, 6);
      SBAR();
      step(2);
      BAR();
      step(3);
      BAR();
      step(4);
      BAR();
      step(5);
      ST(19);
#pragma unroll
      for (int hf = 0; hf < 2; ++hf) {
        f32x4 st = {0, 0, 0, 0};
#pragma unroll
        for (int i = 0; i < 4; ++i) st = mfma16k16(pack4(acc[1][i][hf]), pack4(acc[0][i][hf]), st);
        lds_w128f(spa ^ (hf * 512), st);
      }
      waitvm<0>();
    }
    asm volatile("" :: "v"(dc), "v"(dw), "v"(dx));
    ST(10);
    BAR();
    __builtin_amdgcn_s_setprio(0);
    u32x4 seal = ld_seal(0);
    {
      ldw(wq, 0, 0, WQ_SPLIT, 0);
      u32x2 fl = __builtin_amdgcn_raw_buffer_load_b64(rsWS, l * 8, WS_FLAG + t * 2048 + w * 512, SC1);
      ST(5);
      BAR();
      ldw(wq, 0, WQ_SPLIT, 6, 0);
      if (__builtin_expect(!__all(fl[0] == tag[0] && fl[1] == tag[1]), 0)) {
        for (int sp = 0; sp < SPIN_CAP; ++sp) {
          asm volatile("" ::: "memory");
          const u32x2 f2 = __builtin_amdgcn_raw_buffer_load_b64(rsWS, l * 8, WS_FLAG + t * 2048 + w * 512, SC1);
          if (__all(f2[0] == tag[0] && f2[1] == tag[1])) break;
        }
      }
      ST(18);
      BAR();
    }
    const unsigned qt = (qb >> 3) * SLOT;
    unsigned hqa[2], own[2][2];
    hqa[0] = qt + hh * 1024 + (tq ^ hh) * 128 + (g & 1) * 64 + tp * 16; hqa[1] = (hqa[0] ^ 64) + 512;
    OPAQUE(hqa[0]); OPAQUE(hqa[1]);
    const unsigned tok = 16 * (g & 1) + i16;
    unsigned nw = lds0 + L_N + l * 16 + w * 4;
    OPAQUE(nw);
    unsigned qw = L_Q + (3 * w * 64 + hh * 32 + i16) * 16 + 8 * (g & 1);
    own[0][0] = qt + w * 3072 + (tq ^ (w & 1)) * 128 + hh * 64 + tp * 16 + 8 * (g & 1); own[0][1] = (own[0][0] ^ 64) + 512;
    own[1][0] = own[0][0] ^ 128; own[1][1] = own[0][1] ^ 128;
    OPAQUE(qw); OPAQUE(own[0][0]); OPAQUE(own[0][1]); OPAQUE(own[1][0]); OPAQUE(own[1][1]);
    if (b == 0 && w == 3 && l == 0) __builtin_amdgcn_raw_buffer_store_b64(tag, rsWS, 0, WS_EPOCH, SC1);
    for (int sp = 0; sp < SPIN_CAP && !sealed(wq, seal, l < 18, tag); ++sp) {
      asm volatile("" ::: "memory");
      seal = ld_seal(0);
      ldw(wq, 0, 0, 6, 0);
    }
    u32x4 hq[3];
    {
      const u64 hbu = (u64)(ws + WS_H2 + (size_t)t * FRAME_B);
      u32x4 rs = {(unsigned)hbu, (unsigned)(hbu >> 32) & 0xffffu, (unsigned)FRAME_B, 0x00020000u};
      asm volatile("" : "+s"(rs));
      __builtin_amdgcn_s_setprio(3);
      ST(6);
      BAR();
      ST(7);
      for (int sp = 0; sp < SPIN_CAP; ++sp) {
        setm0<0>(qt + w * 3072);
        bdma<0>(l * 16, rs, qb * TILE_B + w * 3072); bdma<1024>(l * 16, rs, qb * TILE_B + w * 3072); bdma<2048>(l * 16, rs, qb * TILE_B + w * 3072);
        waitvm<0>();
#pragma unroll
        for (int i = 0; i < 3; ++i) { const u32x2 u0 = lds_r64(own[i & 1][0] + i * 1024), u1 = lds_r64(own[i & 1][1] + i * 1024); hq[i] = u32x4{u0[0], u0[1], u1[0], u1[1]}; }
        if (!poisoned(hq)) break;
      }
    }
    ST(8);
    BAR();
    ST(9);
    {
      u32x4 bf[6][2];
      f32x4 d[3][2];
      f32x2 n[2] = {{0.f, 0.f}, {0.f, 0.f}};
      auto fin = [&](const int i) {
#pragma unroll
        for (int hf = 0; hf < 2; ++hf) {
          lds_w64(qw + i * 1024 + hf * 256, pack4(d[i][hf]));
          const f32x2 a = {d[i][hf][0], d[i][hf][1]}, c = {d[i][hf][2], d[i][hf][3]};
          n[hf] = __builtin_elementwise_fma(a, a, n[hf]); n[hf] = __builtin_elementwise_fma(c, c, n[hf]);
        }
      };
      auto mm = [&](const int i, const int ks) {
        d[i][0] = mfma16(wq[ks * 3 + i], bf[ks][0], ks ? d[i][0] : f32x4{0, 0, 0, 0});
        d[i][1] = mfma16(wq[ks * 3 + i], bf[ks][1], ks ? d[i][1] : f32x4{0, 0, 0, 0});
      };
#pragma unroll
      for (int ks = 0; ks < 3; ++ks) { bf[ks][0] = lds_r128(hqa[0] + ks * 2048); bf[ks][1] = lds_r128(hqa[1] + ks * 2048); SBAR(); }
#pragma unroll
      for (int ks = 0; ks < 3; ++ks) {
        mm(0, ks);
        bf[ks + 3][0] = lds_r128(hqa[0] + (ks + 3) * 2048); bf[ks + 3][1] = lds_r128(hqa[1] + (ks + 3) * 2048);
        SBAR();
        mm(1, ks);
        SBAR();
      }
      mm(0, 3); mm(0, 4); mm(0, 5); mm(1, 3);
      SBAR();
      mm(1, 4); mm(1, 5); mm(2, 0); fin(0);
      SBAR();
      mm(2, 1); mm(2, 2); mm(2, 3); mm(2, 4); mm(2, 5); fin(1);
      SBAR();
      fin(2);
      auto rr = __builtin_amdgcn_permlane16_swap(asu(n[0][0] + n[0][1]), asu(n[1][0] + n[1][1]), false, false);
      lds_w32(nw, asf(rr[0]) + asf(rr[1]));
    }
    SBAR();
    ST(11);
    BAR();
    __builtin_amdgcn_s_setprio(0);
    unsigned oo = (t * 1024 + qb * 32 + tok) * 384 + (6 * w + hh) * 16;
    OPAQUE(oo);
    float lt[2];
    u32x4 pick;
#pragma unroll
    for (int e = 0; e < 4; ++e) {
      const unsigned d = 8 * (e >> 1) + 4 * (g & 1) + 2 * (e & 1);
      pick[e] = (i16 == d ? 0x3f80u : 0u) | (i16 == d + 1 ? 0x3f800000u : 0u);
    }
    unsigned pr[2], la = lds0 + L_L + i16 * 32;
    pr[0] = lds0 + 2 * TILE_B + hh * SLOT + 3 * w * 1024 + ((g & 1) * 32 + i16) * 16; pr[1] = pr[0] + 2 * SLOT;
    OPAQUE(pick); OPAQUE(pr[0]); OPAQUE(pr[1]); OPAQUE(la);
    f32x4 x1[3][2];
    {
      __builtin_amdgcn_s_sleep(40);
      u32x2 st[2];
      for (int sp = 0; sp < SPIN_CAP; ++sp) {
        asm volatile("" ::: "memory");
        st[0] = __builtin_amdgcn_raw_buffer_load_b64(rsWS, i16 * 8, WS_ST + (t * 1024 + qb * 32) * 8, SC1);
        st[1] = __builtin_amdgcn_raw_buffer_load_b64(rsWS, i16 * 8 + 128, WS_ST + (t * 1024 + qb * 32) * 8, SC1);
        if (!__any(max(max(st[0][0], st[0][1]), max(st[1][0], st[1][1])) == POISON)) break;
      }
#pragma unroll
      for (int hf = 0; hf < 2; ++hf) {
        const float mu = asf(st[hf][0]), sd = __builtin_amdgcn_sqrtf(asf(st[hf][1]));
#pragma unroll
        for (int i = 0; i < 3; ++i) {
          const f32x4 h = {bflo(hq[i][2 * hf]), bfhi(hq[i][2 * hf]), bflo(hq[i][2 * hf + 1]), bfhi(hq[i][2 * hf + 1])};
          x1[i][hf] = h * sd + mu;
          OPAQUE(x1[i][hf]);
        }
      }
    }
    BAR();
    ST(16);
    for (int sp = 0; sp < SPIN_CAP; ++sp) {
      asm volatile("" ::: "memory");
      seal = ld_seal(1);
      ldw(wv, 1, 0, 6, 0);
      if (sealed(wv, seal, l < 18, tag)) break;
    }
    ST(12);
#pragma unroll 1
    for (bool exact = false;; exact = true) {
      BAR();
      ST(13);
      u32x4 p[3][2][2];
#pragma unroll
      for (int i = 0; i < 3; ++i)
#pragma unroll
        for (int hf = 0; hf < 2; ++hf) { p[i][hf][0] = lds_r128(pr[0] + i * 1024 + hf * 256); p[i][hf][1] = lds_r128(pr[1] + i * 1024 + hf * 256); }
      const f32x4 l0 = lds_r128f(la), l1 = lds_r128f(la + 16);
      SBAR();
#pragma unroll
      for (int i = 0; i < 3; ++i)
#pragma unroll
        for (int hf = 0; hf < 2; ++hf) lds_w64(dp[hf] + (L_FI - L_X) + i * 512, pack4(mfma16(pick, p[i][hf][1], mfma16(pick, p[i][hf][0], f32x4{0, 0, 0, 0}))));
      if (__builtin_expect(!low_sum(l0, l1, lt) || exact, 1)) break;
      BAR();
      BAR();
    }
    ST(14);
    const float linv[2] = {__builtin_amdgcn_rcpf(lt[0]), __builtin_amdgcn_rcpf(lt[1])};
    BAR();
    ST(15);
    u32x4 bf[6][2];
#pragma unroll
    for (int ks = 0; ks < 6; ++ks) { bf[ks][0] = lds_r128(fr[0] + (L_FI - L_X) + ks * 1024); bf[ks][1] = lds_r128(fr[1] + (L_FI - L_X) + ks * 1024); }
    SBAR();
    f32x4 d[3][2];
#pragma unroll
    for (int ks = 0; ks < 6; ++ks)
#pragma unroll
      for (int i = 0; i < 3; ++i) {
        d[i][0] = mfma16(wv[ks * 3 + i], bf[ks][0], ks ? d[i][0] : f32x4{0, 0, 0, 0});
        d[i][1] = mfma16(wv[ks * 3 + i], bf[ks][1], ks ? d[i][1] : f32x4{0, 0, 0, 0});
      }
#pragma unroll
    for (int i = 0; i < 3; ++i) {
      const u32x2 y0 = pack4(d[i][0] * linv[0] + x1[i][0]), y1 = pack4(d[i][1] * linv[1] + x1[i][1]);
      auto s0 = __builtin_amdgcn_permlane16_swap(y0[0], y1[0], false, false);
      auto s1 = __builtin_amdgcn_permlane16_swap(y0[1], y1[1], false, false);
      const u32x4 o = {s0[0], s1[0], s0[1], s1[1]};
      *(__attribute__((address_space(1))) u32x4*)(kc + oo + i * 32) = o;
    }
    ST(17);
    ST_OUT(ws, 12, 20);
  }
}

extern "C" void kernel_launch(void* const* d_in, const int* in_sizes, int n_in, void* d_out, int out_size, void* d_ws, size_t ws_size, hipStream_t stream) {
  (void)hipFuncSetAttribute((const void*)k_timesformer, hipFuncAttributeMaxDynamicSharedMemorySize, LDS_BYTES);
  k_timesformer<<<256, 512, LDS_BYTES, stream>>>((const char*)d_in[0], (const char*)d_in[1], (const char*)d_in[2], (const char*)d_in[3], (const char*)d_in[4],
                                                 (char*)d_ws, (char*)d_out);
}
```

```cpp
#include <hip/hip_runtime.h>
#include <hip/hip_bf16.h>
#include <type_traits>

typedef short s16x4 __attribute__((ext_vector_type(4)));
typedef __bf16 b16x8 __attribute__((ext_vector_type(8)));
typedef __bf16 b16x2 __attribute__((ext_vector_type(2)));
typedef float f32x2 __attribute__((ext_vector_type(2)));
typedef float f32x4 __attribute__((ext_vector_type(4)));
typedef float f32x16 __attribute__((ext_vector_type(16)));
typedef unsigned u32x2 __attribute__((ext_vector_type(2)));
typedef unsigned u32x4 __attribute__((ext_vector_type(4)));
typedef unsigned long long u64;
typedef u64 u64x4 __attribute__((ext_vector_type(4)));
typedef u64 u64x2 __attribute__((ext_vector_type(2)));
#define LAS __attribute__((address_space(3)))
#define DI __device__ __forceinline__
#define SBAR() __builtin_amdgcn_sched_barrier(0)

constexpr int TILE_B = 12288;
constexpr int FRAME_B = 393216;
constexpr unsigned WS_H2 = 0, WS_ST = 8 * FRAME_B, WS_WM = 16 * FRAME_B, WS_SEAL = WS_WM + 3 * 73728, WS_FLAG = WS_SEAL + 216 * 128, WS_FLAG2 = WS_FLAG + 8 * 2048, WS_EPOCH = WS_FLAG2 + 2048, WS_END = WS_EPOCH + 256;
constexpr float QSCALE = 0.07216878364870322f * 1.4426950408889634f;
constexpr int SC1 = 16;
constexpr unsigned POISON = 0xffffffffu;
constexpr unsigned CODE_BYTES = 26112;
constexpr int TILE0_DELAY = 2, LOAD_DELAY = 2, WQ_SPLIT = 4, B9_DELAY = 2, VO_1 = 1, VO_2 = 3, MM_EARLY = 0, PQ = 12, PV = 9, PI = 23;
static_assert(9 * PQ + 18 * PV + 5 * PI >= 384, "the whole frame is poisoned");
constexpr int SPIN_CAP = 1 << 16;


constexpr int PA = 2, PB = 3, ROLE = 1; constexpr size_t WS_T0 = 32u << 20;
#define ST_DECL u64 sa_ = 0, sb_ = 0, t0_ = 0
#define ST(k) do { if ((k) == 0) { SBAR(); u64 t_; asm volatile("s_memrealtime %0\n\ts_waitcnt lgkmcnt(0)" : "=s"(t_) :: "memory"); SBAR(); \
    t0_ = t_; } STO_##k \
    if ((k) == PA || (k) == PB) { SBAR(); u64 t_; asm volatile("s_memrealtime %0\n\ts_waitcnt lgkmcnt(0)" : "=s"(t_) :: "memory"); SBAR(); if ((k) == PA) sa_ = t_; else if (sb_ == 0) sb_ = t_; } } while (0)
#define STO_(k)
#define STO_0
#define STO_1
#define STO_2
#define STO_3
#define STO_4
#define STO_5
#define STO_6
#define STO_7
#define STO_8
#define STO_9
#define STO_10
#define STO_11
#define STO_12
#define STO_13
#define STO_14
#define STO_15
#define STO_16 if (threadIdx.x == 0) __hip_atomic_store((__attribute__((address_space(1))) u64*)(ws + WS_T0) + blockIdx.x, t0_, __ATOMIC_RELAXED, __HIP_MEMORY_SCOPE_AGENT);
#define STO_17
#define STO_18
#define STO_19
#define ST_OUT(ws, k0, k1) do { if (ROLE == ((k0) != 0)) { u64 m_ = ~0ull; \
    for (int q_ = 0; q_ < 4; ++q_) { u64 v_ = __hip_atomic_load((__attribute__((address_space(1))) u64*)(ws + WS_T0) + (threadIdx.x & 63) * 4 + q_, __ATOMIC_RELAXED, __HIP_MEMORY_SCOPE_AGENT); m_ = v_ < m_ ? v_ : m_; } \
    for (int o_ = 32; o_; o_ >>= 1) { u64 v_ = __shfl_xor(m_, o_); m_ = v_ < m_ ? v_ : m_; } \
    int d_ = sb_ ? (int)(sb_ - sa_) / 1 : 0; if (d_ > 600 || !(w==0)) d_ = 0; if (d_ < 0) d_ = 0; if (d_ > 5000) d_ = 5000; \
    const u64 e_ = m_ + 4000 + 4 * d_; while (__builtin_amdgcn_s_memrealtime() < e_) __builtin_amdgcn_s_sleep(1); } } while (0)


DI float asf(unsigned u) { return __builtin_bit_cast(float, u); }
DI unsigned asu(float f) { return __builtin_bit_cast(unsigned, f); }
DI unsigned cvtpk(float lo, float hi) { f32x2 v = {lo, hi}; return __builtin_bit_cast(unsigned, __builtin_convertvector(v, b16x2)); }
DI float bflo(unsigned u) { return asf(u << 16); }
DI float bfhi(unsigned u) { return asf(u & 0xffff0000u); }
DI f32x16 mfma32(u32x4 a, u32x4 b, f32x16 c) { return __builtin_amdgcn_mfma_f32_32x32x16_bf16(__builtin_bit_cast(b16x8, a), __builtin_bit_cast(b16x8, b), c, 0, 0, 0); }
DI f32x4 mfma16(u32x4 a, u32x4 b, f32x4 c) { return __builtin_amdgcn_mfma_f32_16x16x32_bf16(__builtin_bit_cast(b16x8, a), __builtin_bit_cast(b16x8, b), c, 0, 0, 0); }
DI f32x4 mfma16k16(u32x2 a, u32x2 b, f32x4 c) { return __builtin_amdgcn_mfma_f32_16x16x16bf16_1k(__builtin_bit_cast(s16x4, a), __builtin_bit_cast(s16x4, b), c, 0, 0, 0); }
DI u32x2 pack4(f32x4 v) { u32x2 r = {cvtpk(v[0], v[1]), cvtpk(v[2], v[3])}; return r; }
DI u32x4 lds_r128(unsigned a) { return *(LAS u32x4*)(size_t)a; }
DI f32x4 lds_r128f(unsigned a) { return *(LAS f32x4*)(size_t)a; }
DI u32x2 lds_r64(unsigned a) { return *(LAS u32x2*)(size_t)a; }
DI void lds_w128(unsigned a, u32x4 v) { *(LAS u32x4*)(size_t)a = v; }
DI void lds_w128f(unsigned a, f32x4 v) { *(LAS f32x4*)(size_t)a = v; }
DI void lds_w64(unsigned a, u32x2 v) { *(LAS u32x2*)(size_t)a = v; }
DI void lds_w32(unsigned a, float v) { *(LAS float*)(size_t)a = v; }
DI u32x2 lds_tr(unsigned a) { return __builtin_bit_cast(u32x2, __builtin_amdgcn_ds_read_tr16_b64_v4i16((LAS s16x4*)(size_t)a)); }
DI u32x4 lds_tr2(unsigned a0, unsigned a1) { u32x2 x = lds_tr(a0), y = lds_tr(a1); u32x4 r = {x[0], x[1], y[0], y[1]}; return r; }
#define BAR() asm volatile("s_waitcnt lgkmcnt(0)\n\ts_barrier" ::: "memory")
#define BARV(n) asm volatile("s_waitcnt vmcnt(" #n ") lgkmcnt(0)\n\ts_barrier" ::: "memory")
template <int CTRL> DI float dpp_add(float v) { return v + asf(__builtin_amdgcn_update_dpp(0u, asu(v), CTRL, 0xf, 0xf, true)); }
DI float xor16_add(float v) { auto r = __builtin_amdgcn_permlane16_swap(asu(v), asu(v), false, false); return asf(r[0]) + asf(r[1]); }
DI float xor16_max(float v) { auto r = __builtin_amdgcn_permlane16_swap(asu(v), asu(v), false, false); return __builtin_amdgcn_fmed3f(asf(r[0]), asf(r[1]), __builtin_inff()); }
DI unsigned umax3(unsigned a, unsigned b, unsigned c) { return max(max(a, b), c); }
template <int N> DI bool poisoned(const u32x4 (&v)[N]) {
  unsigned m = 0;
#pragma unroll
  for (int i = 0; i < N; ++i) { m = umax3(m, v[i][0], v[i][1]); m = umax3(m, v[i][2], v[i][3]); }
  return __any(m == POISON);
}
DI u32x4 unpoison(u32x4 v) { u32x4 r = {min(v[0], POISON - 1), min(v[1], POISON - 1), min(v[2], POISON - 1), min(v[3], POISON - 1)}; return r; }
DI unsigned fi_pos(unsigned r, unsigned c8) {
  return (((r >> 4) * 6 + (c8 >> 2)) * 64 + (c8 & 3) * 16 + ((r & 15) ^ ((c8 & 1) | (((r >> 3) & 1) << 1) | ((r >> 4) << 2)))) * 16;
}
DI float row8_sum(float s) { s = dpp_add<0x124>(s); s = dpp_add<0x128>(s); return xor16_add(s); }
DI float sum24(const f32x2 (&v)[12]) {
  f32x2 s0 = v[0] + v[1], s1 = v[2] + v[3];
#pragma unroll
  for (int i = 4; i < 12; i += 2) { s0 += v[i]; s1 += v[i + 1]; }
  s0 += s1;
  return s0[0] + s0[1];
}
DI float sumsq24(const f32x2 (&v)[12]) {
  f32x2 q0 = v[0] * v[0], q1 = v[1] * v[1];
#pragma unroll
  for (int i = 2; i < 12; i += 2) { q0 = __builtin_elementwise_fma(v[i], v[i], q0); q1 = __builtin_elementwise_fma(v[i + 1], v[i + 1], q1); }
  q0 += q1;
  return q0[0] + q0[1];
}
DI f32x2 unpack2(unsigned u) { f32x2 r = {bflo(u), bfhi(u)}; return r; }

constexpr unsigned SLOT = 36864, L_X = 3 * SLOT, L_H = 4 * SLOT, L_N = L_H + TILE_B, L_L = L_N + 1024, L_PT = L_L + 1024, LDS_BYTES = 163840;
constexpr unsigned QK = 24576, L_JOB = 4 * QK;
constexpr unsigned L_VI = L_X, L_SP = L_X + TILE_B, L_YI = L_X + 20480;
constexpr unsigned L_Q = L_H, L_FI = L_H;

#define DMA3(v0, v1, v2, sbase, ldsa) \
  asm volatile("s_mov_b32 m0, %4\n\ts_nop 0\n\tglobal_load_lds_dwordx4 %0, %3\n\tglobal_load_lds_dwordx4 %1, %3 offset:1024\n\tglobal_load_lds_dwordx4 %2, %3 offset:2048" \
               :: "v"(v0), "v"(v1), "v"(v2), "s"(sbase), "s"(ldsa) : "memory")
#define DMA1(v0, sbase, ldsa) \
  asm volatile("s_mov_b32 m0, %2\n\ts_nop 0\n\tglobal_load_lds_dwordx4 %0, %1" :: "v"(v0), "s"(sbase), "s"(ldsa) : "memory")

template <int OFF> DI void bdma(unsigned voff, u32x4 rs, unsigned soff) { asm volatile("buffer_load_dwordx4 %0, %1, %2 offen offset:%3 sc1 lds" :: "v"(voff), "s"(rs), "s"(soff), "i"(OFF) : "memory"); }
template <int OFF> DI void setm0(unsigned base) { asm volatile("s_add_u32 m0, %0, %1\n\ts_nop 0" :: "s"(base), "i"(OFF) : "memory", "scc"); }
template <int N> DI void waitvm() { asm volatile("s_waitcnt vmcnt(%0)" :: "i"(N) : "memory"); }
#define IC(x) std::integral_constant<int, (x)>{}
#define OPAQUE(v) asm volatile("" : "+v"(v))
template <class R> DI void st128(u32x4 v, R rs, unsigned voff, unsigned soff) { __builtin_amdgcn_raw_buffer_store_b128(v, rs, voff, soff, SC1); asm volatile("s_nop 1" :: "v"(v)); }
template <int CTRL> DI unsigned dpp_xor(unsigned v) { return v ^ __builtin_amdgcn_update_dpp(0u, v, CTRL, 0xf, 0xf, true); }
DI unsigned fold_xor(unsigned e, unsigned o) {
  auto r = __builtin_amdgcn_permlane32_swap(e, o, false, false);
  unsigned u = r[0] ^ r[1];
  auto q = __builtin_amdgcn_permlane16_swap(u, u, false, false);
  u = q[0] ^ q[1];
  u = dpp_xor<0x128>(u); u = dpp_xor<0x124>(u); u = dpp_xor<0x4e>(u); return dpp_xor<0xb1>(u);
}
DI bool seal_ok(unsigned e, unsigned o, u32x4 seal, bool has, u32x2 tag) {
  const unsigned bad = has ? (seal[1] ^ tag[0]) | (seal[3] ^ tag[1]) : 0u;
  return __builtin_amdgcn_ballot_w64((fold_xor(e ^ seal[0], o ^ seal[2]) | bad) != 0) == 0;
}
DI unsigned xor3(unsigned a, unsigned b, unsigned c) { return __builtin_amdgcn_bitop3_b32(a, b, c, 0x96); }
#define XOR_IN(e, o, v) do { e = xor3(e, (v)[0], (v)[2]); o = xor3(o, (v)[1], (v)[3]); } while (0)
#define MIXV6() _Pragma("unroll") for (int q_ = 0; q_ < 6; ++q_) { __builtin_amdgcn_sched_group_barrier(0x8, 1, 0); __builtin_amdgcn_sched_group_barrier(0x2, 1, 0); } SBAR()
template <int N> DI bool sealed(const u32x4 (&v)[N], u32x4 seal, bool has, u32x2 tag) {
  unsigned e = 0, o = 0;
#pragma unroll
  for (int i = 0; i < N; ++i) XOR_IN(e, o, v[i]);
  return seal_ok(e, o, seal, has, tag);
}
DI bool tags_ok(u32x4 f, u32x2 tag) { return f[0] == tag[0] && f[1] == tag[1] && f[2] == tag[0] && f[3] == tag[1]; }

__global__ __launch_bounds__(512, 2) void k_timesformer(const char* x_, const char* wqkv_t_, const char* wo_t_, const char* wqkv_s_, const char* wo_s_, char* ws_, char* out_) {
  unsigned dc;
  u64x4 ka; u64x2 kb; u64 kc;
  {
    u64 pc;
    asm volatile("s_load_dwordx8 %2, %6, 0x0\n\ts_load_dwordx4 %3, %6, 0x20\n\ts_load_dwordx2 %4, %6, 0x30\n\ts_getpc_b64 %1\n\tglobal_load_dword %0, %5, %1"
                 : "=v"(dc), "=&s"(pc), "=&s"(ka), "=&s"(kb), "=&s"(kc) : "v"(min(threadIdx.x * 128, CODE_BYTES)), "s"(__builtin_amdgcn_kernarg_segment_ptr()) : "memory");
  }
  constexpr unsigned lds0 = 0;
  const unsigned b = blockIdx.x;
  unsigned w8 = __builtin_amdgcn_readfirstlane(threadIdx.x >> 6);
  unsigned l = threadIdx.x & 63;
  const unsigned t = b & 7, qb = b >> 3;
  w8 = (w8 & 4) | ((w8 + qb / 6) & 3);
  ST_DECL;
  ST(0);
  const bool jqk = b < 72, job = b < 216;
  const unsigned jp = b % 72, jw = jp / 18, jks = (jp - jw * 18) / 3, jtau = jp - jw * 15 - jks * 3;
  const unsigned rot = qb % 6;
  unsigned fo[2][3];
#pragma unroll
  for (int i = 0; i < 3; ++i) {
    const unsigned p = i * 64 + l;
    if (w8 < 4) { fo[0][i] = p * 16 + (p >= 96 ? FRAME_B - 1536 : 0) + w8 * 2 * FRAME_B + b * 1536 - i * 1024; fo[1][i] = 0; }
    else if (w8 == 7) {
      const unsigned row = (p * 1366) >> 16;
      fo[0][i] = row * 1152 + (((p - row * 48) ^ (row << 1)) * 16) - i * 1024; fo[1][i] = row * 1152 + (((p - row * 48) ^ (row << 1) ^ 8) * 16) - i * 1024;
    } else {
      const unsigned row = (p * 2731) >> 16, o = row * 1152 + (((p - row * 24) ^ row) * 16) - i * 1024;
      fo[0][i] = o + jks * (32 * 1152) + (jqk ? 0 : 768);
      fo[1][i] = jqk ? o + jtau * (16 * 1152) + 384 : (l >> 1) * 384 + (l & 1) * 16 + jtau * 32 + i * (12288 - 1024);
    }
  }
  asm volatile("s_waitcnt lgkmcnt(0)" : "+s"(ka), "+s"(kb), "+s"(kc), "+v"(fo[0][0]), "+v"(fo[0][1]), "+v"(fo[0][2]), "+v"(fo[1][0]), "+v"(fo[1][1]), "+v"(fo[1][2]) :: "memory");
  char* const ws = (char*)kb[1];
  auto chunk = [&](const int n) {
    const u64 src = ka[1] + (n + rot >= 6 ? n + rot - 6 : n + rot) * (32 * 1152);
#pragma unroll
    for (int q = 0; q < 8; ++q) DMA3(fo[(q >> 1) & 1][0], fo[(q >> 1) & 1][1], fo[(q >> 1) & 1][2], src + q * 4608, (n & 3) * QK + q * 3072);
  };
  unsigned dw = 0, dx = 0;
  if (w8 < 4) {
    DMA3(fo[0][0], fo[0][1], fo[0][2], ka[0], L_YI + w8 * 3072);
  } else if (w8 == 4) {
    if (job) {
      __builtin_amdgcn_s_setprio(3);
      const u64 wqkv = b >= 144 ? ka[1] : ka[3], second = jqk ? wqkv : b >= 144 ? ka[2] : kb[0];
#pragma unroll
      for (int q = 0; q < 4; ++q) DMA3(fo[0][0], fo[0][1], fo[0][2], wqkv + q * (8 * 1152), L_JOB + q * 3072);
#pragma unroll
      for (int q = 0; q < 2; ++q) DMA3(fo[1][0], fo[1][1], fo[1][2], second + q * (jqk ? 8 * 1152 : 36864), L_JOB + 12288 + q * 3072);
    }
  } else if (w8 == 7) {
    __builtin_amdgcn_s_sleep(LOAD_DELAY);
    chunk(0); chunk(1);
  } else {
    if (w8 == 6) asm volatile("global_load_dword %0, %1, %2" : "=v"(dx) : "v"((qb * 6 + min(l, 35u) / 6) * 1152 + (min(l, 35u) % 6) * 128), "s"(ka[1]) : "memory");
    asm volatile("global_load_dword %0, %1, %2" : "=v"(dw) : "v"((qb * 18 + min(l, 17u)) * 128), "s"(w8 == 6 ? ka[2] : kb[0]) : "memory");
  }
  const auto rsWS = __builtin_amdgcn_make_buffer_rsrc((void*)ws, 0, WS_END, 0x00020000);
  const u32x4 pz = {POISON, POISON, POISON, POISON};
  u64 epoch;
  asm volatile("s_load_dwordx2 %0, %1, 0x0" : "=&s"(epoch) : "s"(ws + WS_EPOCH) : "memory");
  asm volatile("s_waitcnt lgkmcnt(0)" : "+s"(epoch) :: "memory");
  epoch += 1;
  const u32x2 tag = {(unsigned)epoch, (unsigned)(epoch >> 32)};
  asm volatile("" : "+s"(w8));
  const unsigned w = w8 & 3;
#define LANE_IDS const unsigned i16 = l & 15, g = l >> 4, r32 = l & 31, hh = l >> 5, tq = i16 >> 2, tp = l & 3
#define FRAG_POS unsigned fr[2], dp[2];                                                                       \
  fr[0] = L_X + fi_pos(i16, g); fr[1] = (fr[0] ^ 64) + 6144; dp[0] = L_X + fi_pos(i16, hh) + 8 * (g & 1) + w * 1536; dp[1] = (dp[0] ^ 64) + 6144; \
  OPAQUE(fr[0]); OPAQUE(fr[1]); OPAQUE(dp[0]); OPAQUE(dp[1])
  auto ldw = [&](u32x4 (&W)[18], const int m, const int k0, const int k1, const unsigned rot) {
#pragma unroll
    for (int kk = k0; kk < k1; ++kk) {
      const unsigned ks = kk + rot >= 6 ? kk + rot - 6 : kk + rot;
#pragma unroll
      for (int i = 0; i < 3; ++i) { SBAR(); W[kk * 3 + i] = __builtin_amdgcn_raw_buffer_load_b128(rsWS, l * 16 + i * 1024, WS_WM + m * 73728 + w * 18432 + ks * 3072, SC1); }
    }
  };
  auto ld_seal = [&](const int m) {
    return __builtin_amdgcn_raw_buffer_load_b128(rsWS, l < 18 ? l * 128 : WS_END, WS_SEAL + (m * 72 + 18 * w) * 128, SC1);
  };
  auto h_frags = [&](u32x4 (&H)[6][2], const unsigned (&fr)[2], const int k0, const int k1) {
#pragma unroll
    for (int kk = k0; kk < k1; ++kk) {
      const unsigned ks = kk + rot >= 6 ? kk + rot - 6 : kk + rot;
      H[kk][0] = lds_r128(fr[0] + ks * 1024 + (L_H - L_X)); H[kk][1] = lds_r128(fr[1] + ks * 1024 + (L_H - L_X));
    }
  };
  auto low_sum = [&](const f32x4 a, const f32x4 c, float (&lt)[2]) {
    lt[0] = (a[0] + a[2]) + (c[0] + c[2]);
    lt[1] = (a[1] + a[3]) + (c[1] + c[3]);
    return (bool)__any(lt[0] < 8.7e-19f || lt[1] < 8.7e-19f);
  };

  if (w8 < 4) {
    const unsigned nl = l & 3, j = (l >> 2) & 7, tt = 2 * w + (l >> 5), r = nl * 8 + tt;
    unsigned rp = L_X + fi_pos(r, j);
    OPAQUE(rp);
    waitvm<0>();
    asm volatile("" :: "v"(dc));
    f32x2 xv[12];
    float mu1;
    {
      u32x4 h[3], xr[3];
#pragma unroll
      for (int i = 0; i < 3; ++i) xr[i] = lds_r128(lds0 + L_YI + tt * 1536 + nl * 384 + j * 16 + i * 128);
#pragma unroll
      for (int i = 0; i < 12; ++i) xv[i] = unpack2(xr[i >> 2][i & 3]);
      mu1 = row8_sum(sum24(xv)) * (1.0f / 192.0f);
#pragma unroll
      for (int i = 0; i < 12; ++i) xv[i] -= mu1;
      const float rs = __builtin_amdgcn_rsqf(row8_sum(sumsq24(xv)) * (1.0f / 192.0f) + 1e-5f);
#pragma unroll
      for (int i = 0; i < 12; ++i) { const f32x2 n = xv[i] * rs; h[i >> 2][i & 3] = cvtpk(n[0], n[1]); }
#pragma unroll
      for (int i = 0; i < 3; ++i) lds_w128(rp + (L_H - L_X) + i * 2048, h[i]);
    }
    SBAR();
    OPAQUE(l);
    unsigned so = tt * FRAME_B + (j >> 1) * 1024 + ((t ^ ((j >> 1) & 1)) * 128) + (((j & 1) ^ (t >> 2)) * 64) + nl * 16;
    const unsigned sso = qb * TILE_B;
    LANE_IDS;
    FRAG_POS;
    unsigned sto = j == 0 ? (tt * 1024 + nl) * 8 : WS_END;
    unsigned sa = L_SP + l * 16;
    unsigned keep = (hh == (i16 >> 3)) ? ~0u : 0u;
    OPAQUE(sto); OPAQUE(so); OPAQUE(sa); OPAQUE(keep);
    ST(1);
    BAR();
    ST(6);
    {
      u32x4 H[6][2], W[18];
      BAR();
      h_frags(H, fr, 0, 6);
      asm volatile("s_barrier" ::: "memory");
      ST(2);
#pragma unroll 1
      for (int sp = 0;; ++sp) {
        const u32x4 seal = ld_seal(2);
        ldw(W, 2, 0, VO_1, rot);
        if (sp == 0) asm volatile("s_barrier" ::: "memory");
        ldw(W, 2, VO_1, VO_2, rot);
        if (sp == 0) asm volatile("s_barrier" ::: "memory");
        ldw(W, 2, VO_2, 6, rot);
        SBAR();
        f32x4 vp[3][2];
        unsigned he = 0, ho = 0;
        auto mm = [&](const int kk) {
#pragma unroll
          for (int i = 0; i < 3; ++i) {
            vp[i][0] = mfma16(H[kk][0], W[kk * 3 + i], kk ? vp[i][0] : f32x4{0, 0, 0, 0});
            vp[i][1] = mfma16(H[kk][1], W[kk * 3 + i], kk ? vp[i][1] : f32x4{0, 0, 0, 0});
            XOR_IN(he, ho, W[kk * 3 + i]);
          }
          MIXV6();
        };
        ST(3);
#pragma unroll
        for (int kk = 0; kk < MM_EARLY; ++kk) mm(kk);
        OPAQUE(he); OPAQUE(ho);
        ST(8);
        if (sp == 0) asm volatile("s_barrier" ::: "memory");
        u32x2 pt[2];
        {
          f32x4 e = (lds_r128f(sa) + lds_r128f(sa + 1024)) + lds_r128f(sa + 2048);
          const float mq = xor16_max(fmaxf(fmaxf(e[0], e[1]), fmaxf(e[2], e[3]))) * -QSCALE;
#pragma unroll
          for (int k = 0; k < 4; ++k) e[k] = __builtin_amdgcn_exp2f(fmaf(e[k], QSCALE, mq));
          pt[0] = pack4(e * __builtin_amdgcn_rcpf(xor16_add((e[0] + e[1]) + (e[2] + e[3]))));
#pragma unroll
          for (int k = 0; k < 2; ++k) {
            auto q = __builtin_amdgcn_permlane32_swap(pt[0][k], pt[0][k], false, false);
            pt[1][k] = (q[0] ^ q[1] ^ pt[0][k]) & keep;
            pt[0][k] &= keep;
          }
          OPAQUE(pt[0]); OPAQUE(pt[1]);
        }
        SBAR();
        ST(4);
#pragma unroll
        for (int kk = MM_EARLY; kk < 6; ++kk) mm(kk);
        OPAQUE(he); OPAQUE(ho);
        ST(7);
        f32x4 y[3][2];
#pragma unroll
        for (int hf = 0; hf < 2; ++hf)
#pragma unroll
          for (int i = 0; i < 3; ++i) y[i][hf] = mfma16k16(pack4(vp[i][hf]), pt[hf], f32x4{0, 0, 0, 0});
        SBAR();
#pragma unroll
        for (int hf = 0; hf < 2; ++hf)
#pragma unroll
          for (int i = 0; i < 3; ++i) lds_w64(dp[hf] + (L_YI - L_X) + i * 512, pack4(y[i][hf]));
        if (__builtin_expect(seal_ok(he, ho, seal, l < 18, tag), 1) || sp >= SPIN_CAP) break;
        asm volatile("" ::: "memory");
      }
    }
    ST(9);
    BAR();
    {
      u32x4 y[3], h[3];
#pragma unroll
      for (int i = 0; i < 3; ++i) y[i] = lds_r128(rp + (L_YI - L_X) + i * 2048);
#pragma unroll
      for (int i = 0; i < 12; ++i) xv[i] += unpack2(y[i >> 2][i & 3]);
      const float mu = row8_sum(sum24(xv)) * (1.0f / 192.0f), sq = row8_sum(sumsq24(xv));
      const float var = fmaxf(fmaf(sq, 1.0f / 192.0f, -mu * mu), 0.f) + 1e-5f;
      const float rs = __builtin_amdgcn_rsqf(var), nb = -mu * rs;
#pragma unroll
      for (int i = 0; i < 3; ++i) {
#pragma unroll
        for (int e = 0; e < 4; ++e) { const f32x2 n = xv[4 * i + e] * rs + nb; h[i][e] = cvtpk(n[0], n[1]); }
        if (i == 0) { OPAQUE(h[0]); ST(5); asm volatile("s_barrier" ::: "memory"); }
        st128(h[i], rsWS, so, WS_H2 + sso + i * 4096);
        SBAR();
      }
      const u32x2 stat = {asu(mu + mu1), asu(var)};
      __builtin_amdgcn_raw_buffer_store_b64(stat, rsWS, sto, WS_ST + b * 32, SC1);
    }
    ST(10);
    const unsigned ring = lds0 + w * SLOT;
    unsigned kr0 = ring + (r32 >> 2) * 128 + (hh ^ (r32 >> 4)) * 64 + (r32 & 3) * 16, kr1 = kr0 ^ 128;
    unsigned vr0 = ring + (g & 1) * 1024 + (hh ^ (g & 1)) * 128 + (tp >> 1) * 64 + tq * 16 + 8 * (tp & 1), vr1 = (vr0 ^ 64) + 512;
    unsigned lo[3] = {l * 16, l * 16 + 4096, l * 16 + 8192}, pw = ring + 2 * TILE_B + l * 16, lw = l < 16 ? L_L + (l * 4 + w) * 8 : L_L + 512 + l * 8;
    OPAQUE(kr0); OPAQUE(kr1); OPAQUE(vr0); OPAQUE(vr1); OPAQUE(lo[0]); OPAQUE(lo[1]); OPAQUE(lo[2]); OPAQUE(pw); OPAQUE(lw);
    const u64 hbu = (u64)(ws + WS_H2 + (size_t)t * FRAME_B);
    const u32x4 rs = {(unsigned)hbu, (unsigned)(hbu >> 32) & 0xffffu, (unsigned)FRAME_B, 0x00020000u};
    unsigned toff[8];
#pragma unroll
    for (int i = 0; i < 8; ++i) { toff[i] = (8 * w + ((i + qb) & 7)) * TILE_B; asm volatile("" : "+s"(toff[i])); }
    u32x4 sel;
    { const unsigned one = (tp == (g & 1)) ? 0x3f803f80u : 0u; sel = u32x4{one, one, one, one}; OPAQUE(sel); }
    auto piece = [&](auto Tc, auto Pc) {
      constexpr int T = decltype(Tc)::value, p = decltype(Pc)::value;
      if constexpr (T < 8) {
        if constexpr (p % 4 == 0) setm0<(T % 3) * TILE_B + p * 1024>(ring);
        bdma<(p % 4) * 1024>(lo[p / 4], rs, toff[T]);
      }
    };
#define P6(T, a) piece(IC(T), IC(a)); piece(IC(T), IC(a + 1)); piece(IC(T), IC(a + 2)); piece(IC(T), IC(a + 3)); piece(IC(T), IC(a + 4)); piece(IC(T), IC(a + 5));
    auto read_valid = [&](u32x4 (&KK)[12], const unsigned dst, unsigned off) {
      for (int sp = 0; sp < SPIN_CAP; ++sp) {
#pragma unroll
        for (int k = 0; k < 12; ++k) KK[k] = lds_r128(((k & 1) ? kr1 : kr0) + dst + k * 1024);
        if (__builtin_expect(!poisoned(KK), 1)) break;
#pragma unroll 1
        for (int k = 0; k < 12; ++k) { setm0<0>(ring + dst + k * 1024); bdma<0>(lo[0], rs, off + k * 1024); }
        waitvm<0>();
      }
    };
    unsigned qa = lds0 + L_Q + lo[0], na = lds0 + L_N + r32 * 16;
    OPAQUE(qa); OPAQUE(na);
    waitvm<0>();
    __builtin_amdgcn_s_sleep(B9_DELAY);
    BAR();
    __builtin_amdgcn_s_sleep(TILE0_DELAY);
    if (w != (qb >> 3)) { P6(0, 0) P6(0, 6) }
    else if (l == 0) __builtin_amdgcn_raw_buffer_store_b64(tag, rsWS, 0, WS_FLAG2 + b * 8, SC1);
    ST(11);
    BAR();
    u32x4 K0[12];
    if (w != (qb >> 3)) waitvm<0>();
    ST(12);
    read_valid(K0, 0, toff[0]);
    ST(13);
    piece(IC(1), IC(0)); piece(IC(1), IC(1)); piece(IC(1), IC(2)); piece(IC(1), IC(3));
    BAR();
    ST(14);
    u32x4 Q[12];
    float mref;
    {
      const f32x4 n0 = lds_r128f(na), n1 = lds_r128f(na + 512);
#pragma unroll
      for (int s = 0; s < 6; ++s) Q[s] = lds_r128(qa + s * 1024);
      SBAR();
      const f32x4 s = n0 + n1;
      mref = __builtin_amdgcn_sqrtf(((s[0] + s[1]) + (s[2] + s[3])) * (192.0f * 1.02f));
      OPAQUE(mref);
      SBAR();
#pragma unroll
      for (int s = 6; s < 12; ++s) Q[s] = lds_r128(qa + s * 1024);
      SBAR();
    }
    constexpr int KW = 4, VW = 4;
    bool safe = false, exact = false, late = false;
#pragma unroll 1
    for (;;) {
      f32x16 S[2], O[6], negm;
      u32x4 K[12], V[12], P[2][2];
      f32x4 lsum = {0, 0, 0, 0};
      float pe[2][2];
#pragma unroll
      for (int i = 0; i < 16; ++i) negm[i] = -mref;
#define RK(k, T) K[k] = lds_r128((((k) & 1) ? kr1 : kr0) + ((T) % 3) * TILE_B + (k) * 1024);
#define RV(u, T) { const unsigned a_ = (((u) & 1) ? vr1 : vr0) + ((T) % 3) * TILE_B + ((u) >> 1) * 2048; V[u] = lds_tr2(a_, a_ + 256); }
      auto sm = [&](auto Tc, auto Hc, auto Gc) {
        constexpr int T = decltype(Tc)::value, H = decltype(Hc)::value, G = decltype(Gc)::value;
        if constexpr (G < 8) pe[(G >> 1) & 1][G & 1] = __builtin_amdgcn_exp2f(S[T & 1][8 * H + G]);
        if constexpr (G >= 2 && G <= 8 && G % 2 == 0) { constexpr int q = G / 2 - 1; P[T & 1][H][q] = cvtpk(pe[q & 1][0], pe[q & 1][1]); }
        if constexpr (G == 10) lsum = mfma16(sel, P[T & 1][H], lsum);
      };
      auto put_o = [&](const int un) {
        u32x4 pk;
#pragma unroll
        for (int e = 0; e < 4; ++e) pk[e] = cvtpk(O[un >> 1][8 * (un & 1) + 2 * e], O[un >> 1][8 * (un & 1) + 2 * e + 1]);
        lds_w128(pw + un * 1024, pk);
      };
      auto qkphase = [&](auto Ic) {
        constexpr int I = decltype(Ic)::value;
        auto step = [&](auto Kc) {
          constexpr int k = decltype(Kc)::value;
          if constexpr (I + 1 < 8) {
            S[(I + 1) & 1] = mfma32(I < 0 ? K0[k] : K[k], Q[k], k == 0 ? negm : S[(I + 1) & 1]);
            if constexpr (k + KW < 12 && I >= 0) RK(k + KW, I + 1)
          }
          if constexpr (I < 0 && k < 8) piece(IC(1), IC(4 + k));
          if constexpr (I >= 0) { if constexpr (k & 1) piece(IC(I + 2), IC(6 + k / 2)); if constexpr (k < 11) sm(IC(I), IC(1), IC(k)); }
          if constexpr (I >= 0 && I < 7 && k >= 12 - VW) RV(k - (12 - VW), I)
          SBAR();
        };
        step(IC(0)); step(IC(1)); step(IC(2)); step(IC(3)); step(IC(4)); step(IC(5)); step(IC(6)); step(IC(7)); step(IC(8)); step(IC(9)); step(IC(10)); step(IC(11));
      };
      auto pvphase = [&](auto Ic) {
        constexpr int I = decltype(Ic)::value;
        auto step = [&](auto Uc) {
          constexpr int u = decltype(Uc)::value;
          if constexpr (I >= 0) {
            f32x16 z;
#pragma unroll
            for (int e = 0; e < 16; ++e) z[e] = 0.f;
            O[u >> 1] = mfma32(V[u], P[I & 1][u & 1], (I == 0 && !(u & 1)) ? z : O[u >> 1]);
            if constexpr (u & 1) piece(IC(I + 3), IC(u / 2));
            if constexpr (u + VW < 12) RV(u + VW, I)
          }
          if constexpr (I + 1 < 8 && u >= 1) sm(IC(I + 1), IC(0), IC(u - 1));
          if constexpr (I < 0 && (u & 1)) piece(IC(2), IC(u / 2));
          if constexpr (I + 2 < 8) {
            if constexpr (u == 12 - KW) waitvm<(I < 0 ? 12 : I + 3 < 8 ? 6 + (12 - KW) / 2 : 6)>();
            if constexpr (u >= 12 - KW) RK(u - (12 - KW), I + 2)
            if constexpr (u == 11) waitvm<(I + 3 < 8 ? 6 : 0)>();
          }
          SBAR();
        };
        step(IC(0)); step(IC(1)); step(IC(2)); step(IC(3)); step(IC(4)); step(IC(5)); step(IC(6)); step(IC(7)); step(IC(8)); step(IC(9)); step(IC(10)); step(IC(11));
      };
      auto drain = [&]() {
        constexpr int tl[24] = {6, 6, 6, 6, 6, 6, 6, 6, 7, 7, 7, 7, 6, 6, 7, 7, 6, 6, 7, 7, 7, 7, 7, 7};
        constexpr int fu[24] = {0, 1, 2, 3, 4, 5, 6, 7, 0, 1, 2, 3, 8, 9, 4, 5, 10, 11, 6, 7, 8, 9, 10, 11};
        constexpr int wr[24] = {-1, -1, -1, -1, -1, -1, -1, -1, -1, -1, -1, 0, 1, 2, -1, 3, -1, 4, 5, -1, -1, 6, 7, 8};
        u32x4 X[24];
#pragma unroll
        for (int n = 0; n < VW; ++n) X[n] = V[n];
        auto step = [&](auto Nc) {
          constexpr int n = decltype(Nc)::value;
          O[fu[n] >> 1] = mfma32(X[n], P[tl[n] & 1][fu[n] & 1], O[fu[n] >> 1]);
          if constexpr (n + VW < 24) {
            const unsigned a_ = ((fu[n + VW] & 1) ? vr1 : vr0) + (tl[n + VW] % 3) * TILE_B + (fu[n + VW] >> 1) * 2048;
            X[n + VW] = lds_tr2(a_, a_ + 256);
          }
          if constexpr (n >= 1 && n <= 3) { sm(IC(7), IC(0), IC(3 * n - 3)); sm(IC(7), IC(0), IC(3 * n - 2)); sm(IC(7), IC(0), IC(3 * n - 1)); }
          if constexpr (n >= 4 && n <= 6) { sm(IC(7), IC(1), IC(3 * n - 12)); sm(IC(7), IC(1), IC(3 * n - 11)); sm(IC(7), IC(1), IC(3 * n - 10)); }
          if constexpr (n == 7) sm(IC(7), IC(0), IC(10));
          if constexpr (n == 9) sm(IC(7), IC(1), IC(10));
          if constexpr (wr[n] >= 0) put_o(wr[n]);
          SBAR();
        };
        step(IC(0)); step(IC(1)); step(IC(2)); step(IC(3)); step(IC(4)); step(IC(5)); step(IC(6)); step(IC(7)); step(IC(8)); step(IC(9)); step(IC(10)); step(IC(11));
        step(IC(12)); step(IC(13)); step(IC(14)); step(IC(15)); step(IC(16)); step(IC(17)); step(IC(18)); step(IC(19)); step(IC(20)); step(IC(21)); step(IC(22)); step(IC(23));
      };
#define STALE(v) (__builtin_expect(!safe && __any((v) != (v)), 0))
      bool done = false;
      do {
        qkphase(IC(-1));
        ST(15);
        pvphase(IC(-1));
        ST(16);
        qkphase(IC(0)); pvphase(IC(0)); qkphase(IC(1)); pvphase(IC(1)); qkphase(IC(2));
        const float seen = lsum[0];
        pvphase(IC(2));
        if (STALE(seen)) break;
        qkphase(IC(3)); pvphase(IC(3)); qkphase(IC(4)); pvphase(IC(4)); qkphase(IC(5));
        if (!late) asm volatile("s_barrier" ::: "memory");
        late = true;
        pvphase(IC(5)); qkphase(IC(6)); drain();
        if (STALE(lsum[0])) break;
        { const u32x2 ls = {asu(lsum[0]), asu(lsum[1])}; lds_w64(lw, ls); }
        put_o(9); put_o(10); put_o(11);
        done = true;
      } while (false);
#pragma unroll
      for (int k = 0; k < 12; ++k) asm volatile("" : "=v"(K0[k]));
      if (__builtin_expect(done, 1)) {
        ST(17);
        BAR();
        ST(18);
        float lt[2];
        unsigned ll = lo[0];
        OPAQUE(ll);
        ll >>= 4;
        if (__builtin_expect(!low_sum(lds_r128f(L_L + (ll & 15) * 32), lds_r128f(L_L + (ll & 15) * 32 + 16), lt) || exact, 1)) break;
        BAR();
        float mx = -3.0e38f;
#pragma unroll 1
        for (unsigned i = 0; i < 8; ++i) {
#pragma unroll 1
          for (int k = 0; k < 12; ++k) { setm0<0>(ring + k * 1024); bdma<0>(lo[0], rs, (8 * w + i) * TILE_B + k * 1024); }
          waitvm<0>();
          f32x16 s;
#pragma unroll
          for (int e = 0; e < 16; ++e) s[e] = 0.f;
#pragma unroll
          for (int k = 0; k < 12; ++k) s = mfma32(lds_r128(((k & 1) ? kr1 : kr0) + k * 1024), Q[k], s);
#pragma unroll
          for (int e = 0; e < 16; ++e) mx = fmaxf(mx, s[e]);
        }
        lds_w32(L_N + ((ll & 31) * 8 + w * 2 + (ll >> 5)) * 4, mx);
        BAR();
        const f32x4 c = lds_r128f(L_N + (ll & 31) * 32), d = lds_r128f(L_N + (ll & 31) * 32 + 16);
        mref = fmaxf(fmaxf(fmaxf(c[0], c[1]), fmaxf(c[2], c[3])), fmaxf(fmaxf(d[0], d[1]), fmaxf(d[2], d[3])));
        exact = true;
      } else {
        unsigned ll = lo[0];
        OPAQUE(ll);
        for (int sp = 0; sp < SPIN_CAP; ++sp) {
          asm volatile("" ::: "memory");
          const u32x2 f = __builtin_amdgcn_raw_buffer_load_b64(rsWS, ll >> 1, WS_FLAG2 + w * 512, SC1);
          if (__all(f[0] == tag[0] && f[1] == tag[1])) break;
        }
      }
      safe = true;
      asm volatile("s_waitcnt vmcnt(0) lgkmcnt(0)" ::: "memory");
      P6(0, 0) P6(0, 6)
      waitvm<0>();
      piece(IC(1), IC(0)); piece(IC(1), IC(1)); piece(IC(1), IC(2)); piece(IC(1), IC(3));
#pragma unroll
      for (int k = 0; k < 12; ++k) K0[k] = lds_r128(((k & 1) ? kr1 : kr0) + k * 1024);
    }
    ST(19);
    BAR();
    ST_OUT(ws, 0, 12);
  } else {
    LANE_IDS;
    FRAG_POS;
    u32x4 wq[18], wv[18];
    if (w == 3) {
      {
      __builtin_amdgcn_raw_buffer_store_b32(POISON, rsWS, l * 4, WS_ST + (t * 1024 + qb * 32) * 8, SC1);
      const unsigned p0 = WS_H2 + t * FRAME_B + (jqk ? qb * PQ : job ? 9 * (PQ - PV) + qb * PV : 384 + (qb - 32) * PI) * 1024, np = jqk ? PQ : job ? PV : PI;
#pragma unroll 1
      for (unsigned i = 0; i < np; ++i) __builtin_amdgcn_raw_buffer_store_b128(pz, rsWS, l * 16, p0 + i * 1024, SC1);
      asm volatile("s_nop 1" :: "v"(pz));
    }
      chunk(2);
      ST(1);
      if (jqk) waitvm<25 + PQ>(); else if (job) waitvm<25 + PV>(); else waitvm<25 + PI>();
      BAR();
      chunk(3);
      ST(2);
      BARV(24);
      __builtin_amdgcn_raw_buffer_store_b64(tag, rsWS, l < 8 ? l * 2048 : WS_END, WS_FLAG + b * 8, SC1);
      chunk(4); chunk(5);
      ST(3);
      BARV(49);
      BARV(24);
      BARV(0);
    } else {
      unsigned ta[2][4][2];
#pragma unroll
      for (int m = 0; m < 2; ++m)
#pragma unroll
        for (int i = 0; i < 4; ++i) {
          ta[m][i][0] = (8 * g + tq) * 768 + (((12 * m + 4 * w + i) ^ (tq | ((g & 1) << 2))) * 2 + (tp >> 1)) * 16 + 8 * (tp & 1);
          ta[m][i][1] = ta[m][i][0] + 2 * QK;
          OPAQUE(ta[m][i][0]); OPAQUE(ta[m][i][1]);
        }
      unsigned spa = hh == (i16 >> 3) ? L_SP + w * 1024 + l * 16 : L_SP + 4096 + l * 16;
      OPAQUE(spa);
      if (w == 0 && job) {
        unsigned ja[2] = {L_JOB + i16 * 384 + ((g ^ (l & 7)) * 16), L_JOB + i16 * 384 + (((4 + g) ^ (l & 7)) * 16)}, jb = L_JOB + 12288 + (8 * g + tq) * 32 + tp * 8;
        unsigned jo = (g & 1) * 512 + (hh * 16 + i16) * 16;
        OPAQUE(ja[0]); OPAQUE(ja[1]); OPAQUE(jb); OPAQUE(jo);
        waitvm<0>();
        ST(2);
        u32x4 bb[6];
        if (jqk) {
#pragma unroll
          for (int kk = 0; kk < 6; ++kk) bb[kk] = lds_r128(ja[kk & 1] + (kk >> 1) * 128 + 12288);
        } else {
#pragma unroll
          for (int kk = 0; kk < 6; ++kk) bb[kk] = lds_tr2(jb + kk * 1024, jb + kk * 1024 + 128);
        }
        f32x4 d = {0, 0, 0, 0}, e = {0, 0, 0, 0};
#pragma unroll
        for (int kk = 0; kk < 6; ++kk) {
          d = mfma16(lds_r128(ja[kk & 1] + (kk >> 1) * 128), bb[kk], d);
          e = mfma16(lds_r128(ja[kk & 1] + (kk >> 1) * 128 + 6144), bb[kk], e);
        }
        const float sc = jqk ? QSCALE : 1.0f;
        const u32x2 r0 = pack4(d * sc), r1 = pack4(e * sc);
        auto s0 = __builtin_amdgcn_permlane16_swap(r0[0], r1[0], false, false);
        auto s1 = __builtin_amdgcn_permlane16_swap(r0[1], r1[1], false, false);
        st128(u32x4{s0[0], s1[0], s0[1], s1[1]}, rsWS, jo, WS_WM + b * 1024);
        const u32x2 half = {fold_xor(r0[0] ^ r1[0], r0[1] ^ r1[1]), hh ? tag[1] : tag[0]};
        __builtin_amdgcn_raw_buffer_store_b64(half, rsWS, r32 == 0 ? hh * 8 : WS_END, WS_SEAL + b * 128, SC1);
        ST(3);
      }
      __builtin_amdgcn_s_setprio(3);
      u32x4 H[6][2];
      f32x4 acc[2][4][2];
      auto step = [&](const int n) {
        u32x4 a[8];
#pragma unroll
        for (int u = 0; u < 8; ++u) { const unsigned at = ta[u >> 2][u & 3][(n >> 1) & 1] + (n & 1) * QK; a[u] = lds_tr2(at, at + 3072); }
#pragma unroll
        for (int u = 0; u < 8; ++u) {
          acc[u >> 2][u & 3][0] = mfma16(a[u], H[n][0], n ? acc[u >> 2][u & 3][0] : f32x4{0, 0, 0, 0});
          acc[u >> 2][u & 3][1] = mfma16(a[u], H[n][1], n ? acc[u >> 2][u & 3][1] : f32x4{0, 0, 0, 0});
        }
        __builtin_amdgcn_sched_group_barrier(0x100, 8, 0);
#pragma unroll
        for (int q = 0; q < 4; ++q) { __builtin_amdgcn_sched_group_barrier(0x8, 2, 0); __builtin_amdgcn_sched_group_barrier(0x100, 2, 0); }
        SBAR();
      };
      ST(4);
      BAR();
      h_frags(H, fr, 0, 2);
      SBAR();
      step(0); step(1);
      BAR();
      h_frags(H, fr, 2, 6);
      SBAR();
      step(2);
      BAR();
      step(3);
      BAR();
      step(4);
      BAR();
      step(5);
      ST(19);
#pragma unroll
      for (int hf = 0; hf < 2; ++hf) {
        f32x4 st = {0, 0, 0, 0};
#pragma unroll
        for (int i = 0; i < 4; ++i) st = mfma16k16(pack4(acc[1][i][hf]), pack4(acc[0][i][hf]), st);
        lds_w128f(spa ^ (hf * 512), st);
      }
      waitvm<0>();
    }
    asm volatile("" :: "v"(dc), "v"(dw), "v"(dx));
    ST(10);
    BAR();
    __builtin_amdgcn_s_setprio(0);
    u32x4 seal = ld_seal(0);
    {
      ldw(wq, 0, 0, WQ_SPLIT, 0);
      u32x2 fl = __builtin_amdgcn_raw_buffer_load_b64(rsWS, l * 8, WS_FLAG + t * 2048 + w * 512, SC1);
      ST(5);
      BAR();
      ldw(wq, 0, WQ_SPLIT, 6, 0);
      if (__builtin_expect(!__all(fl[0] == tag[0] && fl[1] == tag[1]), 0)) {
        for (int sp = 0; sp < SPIN_CAP; ++sp) {
          asm volatile("" ::: "memory");
          const u32x2 f2 = __builtin_amdgcn_raw_buffer_load_b64(rsWS, l * 8, WS_FLAG + t * 2048 + w * 512, SC1);
          if (__all(f2[0] == tag[0] && f2[1] == tag[1])) break;
        }
      }
      ST(18);
      BAR();
    }
    const unsigned qt = (qb >> 3) * SLOT;
    unsigned hqa[2], own[2][2];
    hqa[0] = qt + hh * 1024 + (tq ^ hh) * 128 + (g & 1) * 64 + tp * 16; hqa[1] = (hqa[0] ^ 64) + 512;
    OPAQUE(hqa[0]); OPAQUE(hqa[1]);
    const unsigned tok = 16 * (g & 1) + i16;
    unsigned nw = lds0 + L_N + l * 16 + w * 4;
    OPAQUE(nw);
    unsigned qw = L_Q + (3 * w * 64 + hh * 32 + i16) * 16 + 8 * (g & 1);
    own[0][0] = qt + w * 3072 + (tq ^ (w & 1)) * 128 + hh * 64 + tp * 16 + 8 * (g & 1); own[0][1] = (own[0][0] ^ 64) + 512;
    own[1][0] = own[0][0] ^ 128; own[1][1] = own[0][1] ^ 128;
    OPAQUE(qw); OPAQUE(own[0][0]); OPAQUE(own[0][1]); OPAQUE(own[1][0]); OPAQUE(own[1][1]);
    if (b == 0 && w == 3 && l == 0) __builtin_amdgcn_raw_buffer_store_b64(tag, rsWS, 0, WS_EPOCH, SC1);
    for (int sp = 0; sp < SPIN_CAP && !sealed(wq, seal, l < 18, tag); ++sp) {
      asm volatile("" ::: "memory");
      seal = ld_seal(0);
      ldw(wq, 0, 0, 6, 0);
    }
    u32x4 hq[3];
    {
      const u64 hbu = (u64)(ws + WS_H2 + (size_t)t * FRAME_B);
      u32x4 rs = {(unsigned)hbu, (unsigned)(hbu >> 32) & 0xffffu, (unsigned)FRAME_B, 0x00020000u};
      asm volatile("" : "+s"(rs));
      __builtin_amdgcn_s_setprio(3);
      ST(6);
      BAR();
      ST(7);
      for (int sp = 0; sp < SPIN_CAP; ++sp) {
        setm0<0>(qt + w * 3072);
        bdma<0>(l * 16, rs, qb * TILE_B + w * 3072); bdma<1024>(l * 16, rs, qb * TILE_B + w * 3072); bdma<2048>(l * 16, rs, qb * TILE_B + w * 3072);
        waitvm<0>();
#pragma unroll
        for (int i = 0; i < 3; ++i) { const u32x2 u0 = lds_r64(own[i & 1][0] + i * 1024), u1 = lds_r64(own[i & 1][1] + i * 1024); hq[i] = u32x4{u0[0], u0[1], u1[0], u1[1]}; }
        if (!poisoned(hq)) break;
      }
    }
    ST(8);
    BAR();
    ST(9);
    {
      u32x4 bf[6][2];
      f32x4 d[3][2];
      f32x2 n[2] = {{0.f, 0.f}, {0.f, 0.f}};
      auto fin = [&](const int i) {
#pragma unroll
        for (int hf = 0; hf < 2; ++hf) {
          lds_w64(qw + i * 1024 + hf * 256, pack4(d[i][hf]));
          const f32x2 a = {d[i][hf][0], d[i][hf][1]}, c = {d[i][hf][2], d[i][hf][3]};
          n[hf] = __builtin_elementwise_fma(a, a, n[hf]); n[hf] = __builtin_elementwise_fma(c, c, n[hf]);
        }
      };
      auto mm = [&](const int i, const int ks) {
        d[i][0] = mfma16(wq[ks * 3 + i], bf[ks][0], ks ? d[i][0] : f32x4{0, 0, 0, 0});
        d[i][1] = mfma16(wq[ks * 3 + i], bf[ks][1], ks ? d[i][1] : f32x4{0, 0, 0, 0});
      };
#pragma unroll
      for (int ks = 0; ks < 3; ++ks) { bf[ks][0] = lds_r128(hqa[0] + ks * 2048); bf[ks][1] = lds_r128(hqa[1] + ks * 2048); SBAR(); }
#pragma unroll
      for (int ks = 0; ks < 3; ++ks) {
        mm(0, ks);
        bf[ks + 3][0] = lds_r128(hqa[0] + (ks + 3) * 2048); bf[ks + 3][1] = lds_r128(hqa[1] + (ks + 3) * 2048);
        SBAR();
        mm(1, ks);
        SBAR();
      }
      mm(0, 3); mm(0, 4); mm(0, 5); mm(1, 3);
      SBAR();
      mm(1, 4); mm(1, 5); mm(2, 0); fin(0);
      SBAR();
      mm(2, 1); mm(2, 2); mm(2, 3); mm(2, 4); mm(2, 5); fin(1);
      SBAR();
      fin(2);
      auto rr = __builtin_amdgcn_permlane16_swap(asu(n[0][0] + n[0][1]), asu(n[1][0] + n[1][1]), false, false);
      lds_w32(nw, asf(rr[0]) + asf(rr[1]));
    }
    SBAR();
    ST(11);
    BAR();
    __builtin_amdgcn_s_setprio(0);
    unsigned oo = (t * 1024 + qb * 32 + tok) * 384 + (6 * w + hh) * 16;
    OPAQUE(oo);
    float lt[2];
    u32x4 pick;
#pragma unroll
    for (int e = 0; e < 4; ++e) {
      const unsigned d = 8 * (e >> 1) + 4 * (g & 1) + 2 * (e & 1);
      pick[e] = (i16 == d ? 0x3f80u : 0u) | (i16 == d + 1 ? 0x3f800000u : 0u);
    }
    unsigned pr[2], la = lds0 + L_L + i16 * 32;
    pr[0] = lds0 + 2 * TILE_B + hh * SLOT + 3 * w * 1024 + ((g & 1) * 32 + i16) * 16; pr[1] = pr[0] + 2 * SLOT;
    OPAQUE(pick); OPAQUE(pr[0]); OPAQUE(pr[1]); OPAQUE(la);
    f32x4 x1[3][2];
    {
      __builtin_amdgcn_s_sleep(40);
      u32x2 st[2];
      for (int sp = 0; sp < SPIN_CAP; ++sp) {
        asm volatile("" ::: "memory");
        st[0] = __builtin_amdgcn_raw_buffer_load_b64(rsWS, i16 * 8, WS_ST + (t * 1024 + qb * 32) * 8, SC1);
        st[1] = __builtin_amdgcn_raw_buffer_load_b64(rsWS, i16 * 8 + 128, WS_ST + (t * 1024 + qb * 32) * 8, SC1);
        if (!__any(max(max(st[0][0], st[0][1]), max(st[1][0], st[1][1])) == POISON)) break;
      }
#pragma unroll
      for (int hf = 0; hf < 2; ++hf) {
        const float mu = asf(st[hf][0]), sd = __builtin_amdgcn_sqrtf(asf(st[hf][1]));
#pragma unroll
        for (int i = 0; i < 3; ++i) {
          const f32x4 h = {bflo(hq[i][2 * hf]), bfhi(hq[i][2 * hf]), bflo(hq[i][2 * hf + 1]), bfhi(hq[i][2 * hf + 1])};
          x1[i][hf] = h * sd + mu;
          OPAQUE(x1[i][hf]);
        }
      }
    }
    BAR();
    ST(16);
    for (int sp = 0; sp < SPIN_CAP; ++sp) {
      asm volatile("" ::: "memory");
      seal = ld_seal(1);
      ldw(wv, 1, 0, 6, 0);
      if (sealed(wv, seal, l < 18, tag)) break;
    }
    ST(12);
#pragma unroll 1
    for (bool exact = false;; exact = true) {
      BAR();
      ST(13);
      u32x4 p[3][2][2];
#pragma unroll
      for (int i = 0; i < 3; ++i)
#pragma unroll
        for (int hf = 0; hf < 2; ++hf) { p[i][hf][0] = lds_r128(pr[0] + i * 1024 + hf * 256); p[i][hf][1] = lds_r128(pr[1] + i * 1024 + hf * 256); }
      const f32x4 l0 = lds_r128f(la), l1 = lds_r128f(la + 16);
      SBAR();
#pragma unroll
      for (int i = 0; i < 3; ++i)
#pragma unroll
        for (int hf = 0; hf < 2; ++hf) lds_w64(dp[hf] + (L_FI - L_X) + i * 512, pack4(mfma16(pick, p[i][hf][1], mfma16(pick, p[i][hf][0], f32x4{0, 0, 0, 0}))));
      if (__builtin_expect(!low_sum(l0, l1, lt) || exact, 1)) break;
      BAR();
      BAR();
    }
    ST(14);
    const float linv[2] = {__builtin_amdgcn_rcpf(lt[0]), __builtin_amdgcn_rcpf(lt[1])};
    BAR();
    ST(15);
    u32x4 bf[6][2];
#pragma unroll
    for (int ks = 0; ks < 6; ++ks) { bf[ks][0] = lds_r128(fr[0] + (L_FI - L_X) + ks * 1024); bf[ks][1] = lds_r128(fr[1] + (L_FI - L_X) + ks * 1024); }
    SBAR();
    f32x4 d[3][2];
#pragma unroll
    for (int ks = 0; ks < 6; ++ks)
#pragma unroll
      for (int i = 0; i < 3; ++i) {
        d[i][0] = mfma16(wv[ks * 3 + i], bf[ks][0], ks ? d[i][0] : f32x4{0, 0, 0, 0});
        d[i][1] = mfma16(wv[ks * 3 + i], bf[ks][1], ks ? d[i][1] : f32x4{0, 0, 0, 0});
      }
#pragma unroll
    for (int i = 0; i < 3; ++i) {
      const u32x2 y0 = pack4(d[i][0] * linv[0] + x1[i][0]), y1 = pack4(d[i][1] * linv[1] + x1[i][1]);
      auto s0 = __builtin_amdgcn_permlane16_swap(y0[0], y1[0], false, false);
      auto s1 = __builtin_amdgcn_permlane16_swap(y0[1], y1[1], false, false);
      const u32x4 o = {s0[0], s1[0], s0[1], s1[1]};
      *(__attribute__((address_space(1))) u32x4*)(kc + oo + i * 32) = o;
    }
    ST(17);
    ST_OUT(ws, 12, 20);
  }
}

extern "C" void kernel_launch(void* const* d_in, const int* in_sizes, int n_in, void* d_out, int out_size, void* d_ws, size_t ws_size, hipStream_t stream) {
  (void)hipFuncSetAttribute((const void*)k_timesformer, hipFuncAttributeMaxDynamicSharedMemorySize, LDS_BYTES);
  k_timesformer<<<256, 512, LDS_BYTES, stream>>>((const char*)d_in[0], (const char*)d_in[1], (const char*)d_in[2], (const char*)d_in[3], (const char*)d_in[4],
                                                 (char*)d_ws, (char*)d_out);
}
```

```cpp
#include <hip/hip_runtime.h>
#include <hip/hip_bf16.h>
#include <type_traits>

typedef short s16x4 __attribute__((ext_vector_type(4)));
typedef __bf16 b16x8 __attribute__((ext_vector_type(8)));
typedef __bf16 b16x2 __attribute__((ext_vector_type(2)));
typedef float f32x2 __attribute__((ext_vector_type(2)));
typedef float f32x4 __attribute__((ext_vector_type(4)));
typedef float f32x16 __attribute__((ext_vector_type(16)));
typedef unsigned u32x2 __attribute__((ext_vector_type(2)));
typedef unsigned u32x4 __attribute__((ext_vector_type(4)));
typedef unsigned long long u64;
typedef u64 u64x4 __attribute__((ext_vector_type(4)));
typedef u64 u64x2 __attribute__((ext_vector_type(2)));
#define LAS __attribute__((address_space(3)))
#define DI __device__ __forceinline__
#define SBAR() __builtin_amdgcn_sched_barrier(0)

constexpr int TILE_B = 12288;
constexpr int FRAME_B = 393216;
constexpr unsigned WS_H2 = 0, WS_ST = 8 * FRAME_B, WS_WM = 16 * FRAME_B, WS_SEAL = WS_WM + 3 * 73728, WS_FLAG = WS_SEAL + 216 * 128, WS_FLAG2 = WS_FLAG + 8 * 2048, WS_EPOCH = WS_FLAG2 + 2048, WS_END = WS_EPOCH + 256;
constexpr float QSCALE = 0.07216878364870322f * 1.4426950408889634f;
constexpr int SC1 = 16;
constexpr unsigned POISON = 0xffffffffu;
constexpr unsigned CODE_BYTES = 26112;
constexpr unsigned CODE_SHARE = 32;
constexpr int TILE0_DELAY = 2, LOAD_DELAY = 2, WQ_SPLIT = 4, B9_DELAY = 2, VO_1 = 1, VO_2 = 3, MM_EARLY = 0, PQ = 12, PV = 9, PI = 23;
static_assert(9 * PQ + 18 * PV + 5 * PI >= 384, "the whole frame is poisoned");
constexpr int SPIN_CAP = 1 << 16;

#define ST_DECL
#define ST(k)
#define ST_OUT(ws, k0, k1)

DI float asf(unsigned u) { return __builtin_bit_cast(float, u); }
DI unsigned asu(float f) { return __builtin_bit_cast(unsigned, f); }
DI unsigned cvtpk(float lo, float hi) { f32x2 v = {lo, hi}; return __builtin_bit_cast(unsigned, __builtin_convertvector(v, b16x2)); }
DI float bflo(unsigned u) { return asf(u << 16); }
DI float bfhi(unsigned u) { return asf(u & 0xffff0000u); }
DI f32x16 mfma32(u32x4 a, u32x4 b, f32x16 c) { return __builtin_amdgcn_mfma_f32_32x32x16_bf16(__builtin_bit_cast(b16x8, a), __builtin_bit_cast(b16x8, b), c, 0, 0, 0); }
DI f32x4 mfma16(u32x4 a, u32x4 b, f32x4 c) { return __builtin_amdgcn_mfma_f32_16x16x32_bf16(__builtin_bit_cast(b16x8, a), __builtin_bit_cast(b16x8, b), c, 0, 0, 0); }
DI f32x4 mfma16k16(u32x2 a, u32x2 b, f32x4 c) { return __builtin_amdgcn_mfma_f32_16x16x16bf16_1k(__builtin_bit_cast(s16x4, a), __builtin_bit_cast(s16x4, b), c, 0, 0, 0); }
DI u32x2 pack4(f32x4 v) { u32x2 r = {cvtpk(v[0], v[1]), cvtpk(v[2], v[3])}; return r; }
DI u32x4 lds_r128(unsigned a) { return *(LAS u32x4*)(size_t)a; }
DI f32x4 lds_r128f(unsigned a) { return *(LAS f32x4*)(size_t)a; }
DI u32x2 lds_r64(unsigned a) { return *(LAS u32x2*)(size_t)a; }
DI void lds_w128(unsigned a, u32x4 v) { *(LAS u32x4*)(size_t)a = v; }
DI void lds_w128f(unsigned a, f32x4 v) { *(LAS f32x4*)(size_t)a = v; }
DI void lds_w64(unsigned a, u32x2 v) { *(LAS u32x2*)(size_t)a = v; }
DI void lds_w32(unsigned a, float v) { *(LAS float*)(size_t)a = v; }
DI u32x2 lds_tr(unsigned a) { return __builtin_bit_cast(u32x2, __builtin_amdgcn_ds_read_tr16_b64_v4i16((LAS s16x4*)(size_t)a)); }
DI u32x4 lds_tr2(unsigned a0, unsigned a1) { u32x2 x = lds_tr(a0), y = lds_tr(a1); u32x4 r = {x[0], x[1], y[0], y[1]}; return r; }
#define BAR() asm volatile("s_waitcnt lgkmcnt(0)\n\ts_barrier" ::: "memory")
#define BARV(n) asm volatile("s_waitcnt vmcnt(" #n ") lgkmcnt(0)\n\ts_barrier" ::: "memory")
template <int CTRL> DI float dpp_add(float v) { return v + asf(__builtin_amdgcn_update_dpp(0u, asu(v), CTRL, 0xf, 0xf, true)); }
DI float xor16_add(float v) { auto r = __builtin_amdgcn_permlane16_swap(asu(v), asu(v), false, false); return asf(r[0]) + asf(r[1]); }
DI float xor16_max(float v) { auto r = __builtin_amdgcn_permlane16_swap(asu(v), asu(v), false, false); return __builtin_amdgcn_fmed3f(asf(r[0]), asf(r[1]), __builtin_inff()); }
DI unsigned umax3(unsigned a, unsigned b, unsigned c) { return max(max(a, b), c); }
template <int N> DI bool poisoned(const u32x4 (&v)[N]) {
  unsigned m = 0;
#pragma unroll
  for (int i = 0; i < N; ++i) { m = umax3(m, v[i][0], v[i][1]); m = umax3(m, v[i][2], v[i][3]); }
  return __any(m == POISON);
}
DI u32x4 unpoison(u32x4 v) { u32x4 r = {min(v[0], POISON - 1), min(v[1], POISON - 1), min(v[2], POISON - 1), min(v[3], POISON - 1)}; return r; }
DI unsigned fi_pos(unsigned r, unsigned c8) {
  return (((r >> 4) * 6 + (c8 >> 2)) * 64 + (c8 & 3) * 16 + ((r & 15) ^ ((c8 & 1) | (((r >> 3) & 1) << 1) | ((r >> 4) << 2)))) * 16;
}
DI float row8_sum(float s) { s = dpp_add<0x124>(s); s = dpp_add<0x128>(s); return xor16_add(s); }
DI float sum24(const f32x2 (&v)[12]) {
  f32x2 s0 = v[0] + v[1], s1 = v[2] + v[3];
#pragma unroll
  for (int i = 4; i < 12; i += 2) { s0 += v[i]; s1 += v[i + 1]; }
  s0 += s1;
  return s0[0] + s0[1];
}
DI float sumsq24(const f32x2 (&v)[12]) {
  f32x2 q0 = v[0] * v[0], q1 = v[1] * v[1];
#pragma unroll
  for (int i = 2; i < 12; i += 2) { q0 = __builtin_elementwise_fma(v[i], v[i], q0); q1 = __builtin_elementwise_fma(v[i + 1], v[i + 1], q1); }
  q0 += q1;
  return q0[0] + q0[1];
}
DI f32x2 unpack2(unsigned u) { f32x2 r = {bflo(u), bfhi(u)}; return r; }

constexpr unsigned SLOT = 36864, L_X = 3 * SLOT, L_H = 4 * SLOT, L_N = L_H + TILE_B, L_L = L_N + 1024, L_PT = L_L + 1024, LDS_BYTES = 163840;
constexpr unsigned QK = 24576, L_JOB = 4 * QK;
constexpr unsigned L_VI = L_X, L_SP = L_X + TILE_B, L_YI = L_X + 20480;
constexpr unsigned L_Q = L_H, L_FI = L_H;

#define DMA3(v0, v1, v2, sbase, ldsa) \
  asm volatile("s_mov_b32 m0, %4\n\ts_nop 0\n\tglobal_load_lds_dwordx4 %0, %3\n\tglobal_load_lds_dwordx4 %1, %3 offset:1024\n\tglobal_load_lds_dwordx4 %2, %3 offset:2048" \
               :: "v"(v0), "v"(v1), "v"(v2), "s"(sbase), "s"(ldsa) : "memory")
#define DMA1(v0, sbase, ldsa) \
  asm volatile("s_mov_b32 m0, %2\n\ts_nop 0\n\tglobal_load_lds_dwordx4 %0, %1" :: "v"(v0), "s"(sbase), "s"(ldsa) : "memory")

template <int OFF> DI void bdma(unsigned voff, u32x4 rs, unsigned soff) { asm volatile("buffer_load_dwordx4 %0, %1, %2 offen offset:%3 sc1 lds" :: "v"(voff), "s"(rs), "s"(soff), "i"(OFF) : "memory"); }
template <int OFF> DI void setm0(unsigned base) { asm volatile("s_add_u32 m0, %0, %1\n\ts_nop 0" :: "s"(base), "i"(OFF) : "memory", "scc"); }
template <int N> DI void waitvm() { asm volatile("s_waitcnt vmcnt(%0)" :: "i"(N) : "memory"); }
#define IC(x) std::integral_constant<int, (x)>{}
#define OPAQUE(v) asm volatile("" : "+v"(v))
template <class R> DI void st128(u32x4 v, R rs, unsigned voff, unsigned soff) { __builtin_amdgcn_raw_buffer_store_b128(v, rs, voff, soff, SC1); asm volatile("s_nop 1" :: "v"(v)); }
template <int CTRL> DI unsigned dpp_xor(unsigned v) { return v ^ __builtin_amdgcn_update_dpp(0u, v, CTRL, 0xf, 0xf, true); }
DI unsigned fold_xor(unsigned e, unsigned o) {
  auto r = __builtin_amdgcn_permlane32_swap(e, o, false, false);
  unsigned u = r[0] ^ r[1];
  auto q = __builtin_amdgcn_permlane16_swap(u, u, false, false);
  u = q[0] ^ q[1];
  u = dpp_xor<0x128>(u); u = dpp_xor<0x124>(u); u = dpp_xor<0x4e>(u); return dpp_xor<0xb1>(u);
}
DI bool seal_ok(unsigned e, unsigned o, u32x4 seal, bool has, u32x2 tag) {
  const unsigned bad = has ? (seal[1] ^ tag[0]) | (seal[3] ^ tag[1]) : 0u;
  return __builtin_amdgcn_ballot_w64((fold_xor(e ^ seal[0], o ^ seal[2]) | bad) != 0) == 0;
}
DI unsigned xor3(unsigned a, unsigned b, unsigned c) { return __builtin_amdgcn_bitop3_b32(a, b, c, 0x96); }
#define XOR_IN(e, o, v) do { e = xor3(e, (v)[0], (v)[2]); o = xor3(o, (v)[1], (v)[3]); } while (0)
#define MIXV6() _Pragma("unroll") for (int q_ = 0; q_ < 6; ++q_) { __builtin_amdgcn_sched_group_barrier(0x8, 1, 0); __builtin_amdgcn_sched_group_barrier(0x2, 1, 0); } SBAR()
template <int N> DI bool sealed(const u32x4 (&v)[N], u32x4 seal, bool has, u32x2 tag) {
  unsigned e = 0, o = 0;
#pragma unroll
  for (int i = 0; i < N; ++i) XOR_IN(e, o, v[i]);
  return seal_ok(e, o, seal, has, tag);
}
DI bool tags_ok(u32x4 f, u32x2 tag) { return f[0] == tag[0] && f[1] == tag[1] && f[2] == tag[0] && f[3] == tag[1]; }

__global__ __launch_bounds__(512, 2) void k_timesformer(const char* x_, const char* wqkv_t_, const char* wo_t_, const char* wqkv_s_, const char* wo_s_, char* ws_, char* out_) {
  unsigned dc;
  u64x4 ka; u64x2 kb; u64 kc;
  {
    u64 pc;
    asm volatile("s_load_dwordx8 %2, %6, 0x0\n\ts_load_dwordx4 %3, %6, 0x20\n\ts_load_dwordx2 %4, %6, 0x30\n\ts_getpc_b64 %1\n\tglobal_load_dword %0, %5, %1"
                 : "=v"(dc), "=&s"(pc), "=&s"(ka), "=&s"(kb), "=&s"(kc) : "v"(min((threadIdx.x * CODE_SHARE + ((blockIdx.x >> 3) & (CODE_SHARE - 1))) * 128, CODE_BYTES)), "s"(__builtin_amdgcn_kernarg_segment_ptr()) : "memory");
  }
  constexpr unsigned lds0 = 0;
  const unsigned b = blockIdx.x;
  unsigned w8 = __builtin_amdgcn_readfirstlane(threadIdx.x >> 6);
  unsigned l = threadIdx.x & 63;
  const unsigned t = b & 7, qb = b >> 3;
  w8 = (w8 & 4) | ((w8 + qb / 6) & 3);
  ST_DECL;
  ST(0);
  const bool jqk = b < 72, job = b < 216;
  const unsigned jp = b % 72, jw = jp / 18, jks = (jp - jw * 18) / 3, jtau = jp - jw * 15 - jks * 3;
  const unsigned rot = qb % 6;
  unsigned fo[2][3];
#pragma unroll
  for (int i = 0; i < 3; ++i) {
    const unsigned p = i * 64 + l;
    if (w8 < 4) { fo[0][i] = p * 16 + (p >= 96 ? FRAME_B - 1536 : 0) + w8 * 2 * FRAME_B + b * 1536 - i * 1024; fo[1][i] = 0; }
    else if (w8 == 7) {
      const unsigned row = (p * 1366) >> 16;
      fo[0][i] = row * 1152 + (((p - row * 48) ^ (row << 1)) * 16) - i * 1024; fo[1][i] = row * 1152 + (((p - row * 48) ^ (row << 1) ^ 8) * 16) - i * 1024;
    } else {
      const unsigned row = (p * 2731) >> 16, o = row * 1152 + (((p - row * 24) ^ row) * 16) - i * 1024;
      fo[0][i] = o + jks * (32 * 1152) + (jqk ? 0 : 768);
      fo[1][i] = jqk ? o + jtau * (16 * 1152) + 384 : (l >> 1) * 384 + (l & 1) * 16 + jtau * 32 + i * (12288 - 1024);
    }
  }
  asm volatile("s_waitcnt lgkmcnt(0)" : "+s"(ka), "+s"(kb), "+s"(kc), "+v"(fo[0][0]), "+v"(fo[0][1]), "+v"(fo[0][2]), "+v"(fo[1][0]), "+v"(fo[1][1]), "+v"(fo[1][2]) :: "memory");
  char* const ws = (char*)kb[1];
  auto chunk = [&](const int n) {
    const u64 src = ka[1] + (n + rot >= 6 ? n + rot - 6 : n + rot) * (32 * 1152);
#pragma unroll
    for (int q = 0; q < 8; ++q) DMA3(fo[(q >> 1) & 1][0], fo[(q >> 1) & 1][1], fo[(q >> 1) & 1][2], src + q * 4608, (n & 3) * QK + q * 3072);
  };
  unsigned dw = 0, dx = 0;
  if (w8 < 4) {
    DMA3(fo[0][0], fo[0][1], fo[0][2], ka[0], L_YI + w8 * 3072);
  } else if (w8 == 4) {
    if (job) {
      __builtin_amdgcn_s_setprio(3);
      const u64 wqkv = b >= 144 ? ka[1] : ka[3], second = jqk ? wqkv : b >= 144 ? ka[2] : kb[0];
#pragma unroll
      for (int q = 0; q < 4; ++q) DMA3(fo[0][0], fo[0][1], fo[0][2], wqkv + q * (8 * 1152), L_JOB + q * 3072);
#pragma unroll
      for (int q = 0; q < 2; ++q) DMA3(fo[1][0], fo[1][1], fo[1][2], second + q * (jqk ? 8 * 1152 : 36864), L_JOB + 12288 + q * 3072);
    }
  } else if (w8 == 7) {
    __builtin_amdgcn_s_sleep(LOAD_DELAY);
    chunk(0); chunk(1);
  } else {
    if (w8 == 6) asm volatile("global_load_dword %0, %1, %2" : "=v"(dx) : "v"((qb * 6 + min(l, 35u) / 6) * 1152 + (min(l, 35u) % 6) * 128), "s"(ka[1]) : "memory");
    asm volatile("global_load_dword %0, %1, %2" : "=v"(dw) : "v"((qb * 18 + min(l, 17u)) * 128), "s"(w8 == 6 ? ka[2] : kb[0]) : "memory");
  }
  const auto rsWS = __builtin_amdgcn_make_buffer_rsrc((void*)ws, 0, WS_END, 0x00020000);
  const u32x4 pz = {POISON, POISON, POISON, POISON};
  u64 epoch;
  asm volatile("s_load_dwordx2 %0, %1, 0x0" : "=&s"(epoch) : "s"(ws + WS_EPOCH) : "memory");
  asm volatile("s_waitcnt lgkmcnt(0)" : "+s"(epoch) :: "memory");
  epoch += 1;
  const u32x2 tag = {(unsigned)epoch, (unsigned)(epoch >> 32)};
  asm volatile("" : "+s"(w8));
  const unsigned w = w8 & 3;
#define LANE_IDS const unsigned i16 = l & 15, g = l >> 4, r32 = l & 31, hh = l >> 5, tq = i16 >> 2, tp = l & 3
#define FRAG_POS unsigned fr[2], dp[2];                                                                       \
  fr[0] = L_X + fi_pos(i16, g); fr[1] = (fr[0] ^ 64) + 6144; dp[0] = L_X + fi_pos(i16, hh) + 8 * (g & 1) + w * 1536; dp[1] = (dp[0] ^ 64) + 6144; \
  OPAQUE(fr[0]); OPAQUE(fr[1]); OPAQUE(dp[0]); OPAQUE(dp[1])
  auto ldw = [&](u32x4 (&W)[18], const int m, const int k0, const int k1, const unsigned rot) {
#pragma unroll
    for (int kk = k0; kk < k1; ++kk) {
      const unsigned ks = kk + rot >= 6 ? kk + rot - 6 : kk + rot;
#pragma unroll
      for (int i = 0; i < 3; ++i) { SBAR(); W[kk * 3 + i] = __builtin_amdgcn_raw_buffer_load_b128(rsWS, l * 16 + i * 1024, WS_WM + m * 73728 + w * 18432 + ks * 3072, SC1); }
    }
  };
  auto ld_seal = [&](const int m) {
    return __builtin_amdgcn_raw_buffer_load_b128(rsWS, l < 18 ? l * 128 : WS_END, WS_SEAL + (m * 72 + 18 * w) * 128, SC1);
  };
  auto h_frags = [&](u32x4 (&H)[6][2], const unsigned (&fr)[2], const int k0, const int k1) {
#pragma unroll
    for (int kk = k0; kk < k1; ++kk) {
      const unsigned ks = kk + rot >= 6 ? kk + rot - 6 : kk + rot;
      H[kk][0] = lds_r128(fr[0] + ks * 1024 + (L_H - L_X)); H[kk][1] = lds_r128(fr[1] + ks * 1024 + (L_H - L_X));
    }
  };
  auto low_sum = [&](const f32x4 a, const f32x4 c, float (&lt)[2]) {
    lt[0] = (a[0] + a[2]) + (c[0] + c[2]);
    lt[1] = (a[1] + a[3]) + (c[1] + c[3]);
    return (bool)__any(lt[0] < 8.7e-19f || lt[1] < 8.7e-19f);
  };

  if (w8 < 4) {
    const unsigned nl = l & 3, j = (l >> 2) & 7, tt = 2 * w + (l >> 5), r = nl * 8 + tt;
    unsigned rp = L_X + fi_pos(r, j);
    OPAQUE(rp);
    waitvm<0>();
    asm volatile("" :: "v"(dc));
    f32x2 xv[12];
    float mu1;
    {
      u32x4 h[3], xr[3];
#pragma unroll
      for (int i = 0; i < 3; ++i) xr[i] = lds_r128(lds0 + L_YI + tt * 1536 + nl * 384 + j * 16 + i * 128);
#pragma unroll
      for (int i = 0; i < 12; ++i) xv[i] = unpack2(xr[i >> 2][i & 3]);
      mu1 = row8_sum(sum24(xv)) * (1.0f / 192.0f);
#pragma unroll
      for (int i = 0; i < 12; ++i) xv[i] -= mu1;
      const float rs = __builtin_amdgcn_rsqf(row8_sum(sumsq24(xv)) * (1.0f / 192.0f) + 1e-5f);
#pragma unroll
      for (int i = 0; i < 12; ++i) { const f32x2 n = xv[i] * rs; h[i >> 2][i & 3] = cvtpk(n[0], n[1]); }
#pragma unroll
      for (int i = 0; i < 3; ++i) lds_w128(rp + (L_H - L_X) + i * 2048, h[i]);
    }
    SBAR();
    OPAQUE(l);
    unsigned so = tt * FRAME_B + (j >> 1) * 1024 + ((t ^ ((j >> 1) & 1)) * 128) + (((j & 1) ^ (t >> 2)) * 64) + nl * 16;
    const unsigned sso = qb * TILE_B;
    LANE_IDS;
    FRAG_POS;
    unsigned sto = j == 0 ? (tt * 1024 + nl) * 8 : WS_END;
    unsigned sa = L_SP + l * 16;
    unsigned keep = (hh == (i16 >> 3)) ? ~0u : 0u;
    OPAQUE(sto); OPAQUE(so); OPAQUE(sa); OPAQUE(keep);
    ST(1);
    BAR();
    ST(6);
    {
      u32x4 H[6][2], W[18];
      BAR();
      h_frags(H, fr, 0, 6);
      asm volatile("s_barrier" ::: "memory");
      ST(2);
#pragma unroll 1
      for (int sp = 0;; ++sp) {
        const u32x4 seal = ld_seal(2);
        ldw(W, 2, 0, VO_1, rot);
        if (sp == 0) asm volatile("s_barrier" ::: "memory");
        ldw(W, 2, VO_1, VO_2, rot);
        if (sp == 0) asm volatile("s_barrier" ::: "memory");
        ldw(W, 2, VO_2, 6, rot);
        SBAR();
        f32x4 vp[3][2];
        unsigned he = 0, ho = 0;
        auto mm = [&](const int kk) {
#pragma unroll
          for (int i = 0; i < 3; ++i) {
            vp[i][0] = mfma16(H[kk][0], W[kk * 3 + i], kk ? vp[i][0] : f32x4{0, 0, 0, 0});
            vp[i][1] = mfma16(H[kk][1], W[kk * 3 + i], kk ? vp[i][1] : f32x4{0, 0, 0, 0});
            XOR_IN(he, ho, W[kk * 3 + i]);
          }
          MIXV6();
        };
        ST(3);
#pragma unroll
        for (int kk = 0; kk < MM_EARLY; ++kk) mm(kk);
        OPAQUE(he); OPAQUE(ho);
        ST(8);
        if (sp == 0) asm volatile("s_barrier" ::: "memory");
        u32x2 pt[2];
        {
          f32x4 e = (lds_r128f(sa) + lds_r128f(sa + 1024)) + lds_r128f(sa + 2048);
          const float mq = xor16_max(fmaxf(fmaxf(e[0], e[1]), fmaxf(e[2], e[3]))) * -QSCALE;
#pragma unroll
          for (int k = 0; k < 4; ++k) e[k] = __builtin_amdgcn_exp2f(fmaf(e[k], QSCALE, mq));
          pt[0] = pack4(e * __builtin_amdgcn_rcpf(xor16_add((e[0] + e[1]) + (e[2] + e[3]))));
#pragma unroll
          for (int k = 0; k < 2; ++k) {
            auto q = __builtin_amdgcn_permlane32_swap(pt[0][k], pt[0][k], false, false);
            pt[1][k] = (q[0] ^ q[1] ^ pt[0][k]) & keep;
            pt[0][k] &= keep;
          }
          OPAQUE(pt[0]); OPAQUE(pt[1]);
        }
        SBAR();
        ST(4);
#pragma unroll
        for (int kk = MM_EARLY; kk < 6; ++kk) mm(kk);
        OPAQUE(he); OPAQUE(ho);
        ST(7);
        f32x4 y[3][2];
#pragma unroll
        for (int hf = 0; hf < 2; ++hf)
#pragma unroll
          for (int i = 0; i < 3; ++i) y[i][hf] = mfma16k16(pack4(vp[i][hf]), pt[hf], f32x4{0, 0, 0, 0});
        SBAR();
#pragma unroll
        for (int hf = 0; hf < 2; ++hf)
#pragma unroll
          for (int i = 0; i < 3; ++i) lds_w64(dp[hf] + (L_YI - L_X) + i * 512, pack4(y[i][hf]));
        if (__builtin_expect(seal_ok(he, ho, seal, l < 18, tag), 1) || sp >= SPIN_CAP) break;
        asm volatile("" ::: "memory");
      }
    }
    ST(9);
    BAR();
    {
      u32x4 y[3], h[3];
#pragma unroll
      for (int i = 0; i < 3; ++i) y[i] = lds_r128(rp + (L_YI - L_X) + i * 2048);
#pragma unroll
      for (int i = 0; i < 12; ++i) xv[i] += unpack2(y[i >> 2][i & 3]);
      const float mu = row8_sum(sum24(xv)) * (1.0f / 192.0f), sq = row8_sum(sumsq24(xv));
      const float var = fmaxf(fmaf(sq, 1.0f / 192.0f, -mu * mu), 0.f) + 1e-5f;
      const float rs = __builtin_amdgcn_rsqf(var), nb = -mu * rs;
#pragma unroll
      for (int i = 0; i < 3; ++i) {
#pragma unroll
        for (int e = 0; e < 4; ++e) { const f32x2 n = xv[4 * i + e] * rs + nb; h[i][e] = cvtpk(n[0], n[1]); }
        if (i == 0) { OPAQUE(h[0]); ST(5); asm volatile("s_barrier" ::: "memory"); }
        st128(h[i], rsWS, so, WS_H2 + sso + i * 4096);
        SBAR();
      }
      const u32x2 stat = {asu(mu + mu1), asu(var)};
      __builtin_amdgcn_raw_buffer_store_b64(stat, rsWS, sto, WS_ST + b * 32, SC1);
    }
    ST(10);
    const unsigned ring = lds0 + w * SLOT;
    unsigned kr0 = ring + (r32 >> 2) * 128 + (hh ^ (r32 >> 4)) * 64 + (r32 & 3) * 16, kr1 = kr0 ^ 128;
    unsigned vr0 = ring + (g & 1) * 1024 + (hh ^ (g & 1)) * 128 + (tp >> 1) * 64 + tq * 16 + 8 * (tp & 1), vr1 = (vr0 ^ 64) + 512;
    unsigned lo[3] = {l * 16, l * 16 + 4096, l * 16 + 8192}, pw = ring + 2 * TILE_B + l * 16, lw = l < 16 ? L_L + (l * 4 + w) * 8 : L_L + 512 + l * 8;
    OPAQUE(kr0); OPAQUE(kr1); OPAQUE(vr0); OPAQUE(vr1); OPAQUE(lo[0]); OPAQUE(lo[1]); OPAQUE(lo[2]); OPAQUE(pw); OPAQUE(lw);
    const u64 hbu = (u64)(ws + WS_H2 + (size_t)t * FRAME_B);
    const u32x4 rs = {(unsigned)hbu, (unsigned)(hbu >> 32) & 0xffffu, (unsigned)FRAME_B, 0x00020000u};
    unsigned toff[8];
#pragma unroll
    for (int i = 0; i < 8; ++i) { toff[i] = (8 * w + ((i + qb) & 7)) * TILE_B; asm volatile("" : "+s"(toff[i])); }
    u32x4 sel;
    { const unsigned one = (tp == (g & 1)) ? 0x3f803f80u : 0u; sel = u32x4{one, one, one, one}; OPAQUE(sel); }
    auto piece = [&](auto Tc, auto Pc) {
      constexpr int T = decltype(Tc)::value, p = decltype(Pc)::value;
      if constexpr (T < 8) {
        if constexpr (p % 4 == 0) setm0<(T % 3) * TILE_B + p * 1024>(ring);
        bdma<(p % 4) * 1024>(lo[p / 4], rs, toff[T]);
      }
    };
#define P6(T, a) piece(IC(T), IC(a)); piece(IC(T), IC(a + 1)); piece(IC(T), IC(a + 2)); piece(IC(T), IC(a + 3)); piece(IC(T), IC(a + 4)); piece(IC(T), IC(a + 5));
    auto read_valid = [&](u32x4 (&KK)[12], const unsigned dst, unsigned off) {
      for (int sp = 0; sp < SPIN_CAP; ++sp) {
#pragma unroll
        for (int k = 0; k < 12; ++k) KK[k] = lds_r128(((k & 1) ? kr1 : kr0) + dst + k * 1024);
        if (__builtin_expect(!poisoned(KK), 1)) break;
#pragma unroll 1
        for (int k = 0; k < 12; ++k) { setm0<0>(ring + dst + k * 1024); bdma<0>(lo[0], rs, off + k * 1024); }
        waitvm<0>();
      }
    };
    unsigned qa = lds0 + L_Q + lo[0], na = lds0 + L_N + r32 * 16;
    OPAQUE(qa); OPAQUE(na);
    waitvm<0>();
    __builtin_amdgcn_s_sleep(B9_DELAY);
    BAR();
    __builtin_amdgcn_s_sleep(TILE0_DELAY);
    if (w != (qb >> 3)) { P6(0, 0) P6(0, 6) }
    else if (l == 0) __builtin_amdgcn_raw_buffer_store_b64(tag, rsWS, 0, WS_FLAG2 + b * 8, SC1);
    ST(11);
    BAR();
    u32x4 K0[12];
    if (w != (qb >> 3)) waitvm<0>();
    ST(12);
    read_valid(K0, 0, toff[0]);
    ST(13);
    piece(IC(1), IC(0)); piece(IC(1), IC(1)); piece(IC(1), IC(2)); piece(IC(1), IC(3));
    BAR();
    ST(14);
    u32x4 Q[12];
    float mref;
    {
      const f32x4 n0 = lds_r128f(na), n1 = lds_r128f(na + 512);
#pragma unroll
      for (int s = 0; s < 6; ++s) Q[s] = lds_r128(qa + s * 1024);
      SBAR();
      const f32x4 s = n0 + n1;
      mref = __builtin_amdgcn_sqrtf(((s[0] + s[1]) + (s[2] + s[3])) * (192.0f * 1.02f));
      OPAQUE(mref);
      SBAR();
#pragma unroll
      for (int s = 6; s < 12; ++s) Q[s] = lds_r128(qa + s * 1024);
      SBAR();
    }
    constexpr int KW = 4, VW = 4;
    bool safe = false, exact = false, late = false;
#pragma unroll 1
    for (;;) {
      f32x16 S[2], O[6], negm;
      u32x4 K[12], V[12], P[2][2];
      f32x4 lsum = {0, 0, 0, 0};
      float pe[2][2];
#pragma unroll
      for (int i = 0; i < 16; ++i) negm[i] = -mref;
#define RK(k, T) K[k] = lds_r128((((k) & 1) ? kr1 : kr0) + ((T) % 3) * TILE_B + (k) * 1024);
#define RV(u, T) { const unsigned a_ = (((u) & 1) ? vr1 : vr0) + ((T) % 3) * TILE_B + ((u) >> 1) * 2048; V[u] = lds_tr2(a_, a_ + 256); }
      auto sm = [&](auto Tc, auto Hc, auto Gc) {
        constexpr int T = decltype(Tc)::value, H = decltype(Hc)::value, G = decltype(Gc)::value;
        if constexpr (G < 8) pe[(G >> 1) & 1][G & 1] = __builtin_amdgcn_exp2f(S[T & 1][8 * H + G]);
        if constexpr (G >= 2 && G <= 8 && G % 2 == 0) { constexpr int q = G / 2 - 1; P[T & 1][H][q] = cvtpk(pe[q & 1][0], pe[q & 1][1]); }
        if constexpr (G == 10) lsum = mfma16(sel, P[T & 1][H], lsum);
      };
      auto put_o = [&](const int un) {
        u32x4 pk;
#pragma unroll
        for (int e = 0; e < 4; ++e) pk[e] = cvtpk(O[un >> 1][8 * (un & 1) + 2 * e], O[un >> 1][8 * (un & 1) + 2 * e + 1]);
        lds_w128(pw + un * 1024, pk);
      };
      auto qkphase = [&](auto Ic) {
        constexpr int I = decltype(Ic)::value;
        auto step = [&](auto Kc) {
          constexpr int k = decltype(Kc)::value;
          if constexpr (I + 1 < 8) {
            S[(I + 1) & 1] = mfma32(I < 0 ? K0[k] : K[k], Q[k], k == 0 ? negm : S[(I + 1) & 1]);
            if constexpr (k + KW < 12 && I >= 0) RK(k + KW, I + 1)
          }
          if constexpr (I < 0 && k < 8) piece(IC(1), IC(4 + k));
          if constexpr (I >= 0) { if constexpr (k & 1) piece(IC(I + 2), IC(6 + k / 2)); if constexpr (k < 11) sm(IC(I), IC(1), IC(k)); }
          if constexpr (I >= 0 && I < 7 && k >= 12 - VW) RV(k - (12 - VW), I)
          SBAR();
        };
        step(IC(0)); step(IC(1)); step(IC(2)); step(IC(3)); step(IC(4)); step(IC(5)); step(IC(6)); step(IC(7)); step(IC(8)); step(IC(9)); step(IC(10)); step(IC(11));
      };
      auto pvphase = [&](auto Ic) {
        constexpr int I = decltype(Ic)::value;
        auto step = [&](auto Uc) {
          constexpr int u = decltype(Uc)::value;
          if constexpr (I >= 0) {
            f32x16 z;
#pragma unroll
            for (int e = 0; e < 16; ++e) z[e] = 0.f;
            O[u >> 1] = mfma32(V[u], P[I & 1][u & 1], (I == 0 && !(u & 1)) ? z : O[u >> 1]);
            if constexpr (u & 1) piece(IC(I + 3), IC(u / 2));
            if constexpr (u + VW < 12) RV(u + VW, I)
          }
          if constexpr (I + 1 < 8 && u >= 1) sm(IC(I + 1), IC(0), IC(u - 1));
          if constexpr (I < 0 && (u & 1)) piece(IC(2), IC(u / 2));
          if constexpr (I + 2 < 8) {
            if constexpr (u == 12 - KW) waitvm<(I < 0 ? 12 : I + 3 < 8 ? 6 + (12 - KW) / 2 : 6)>();
            if constexpr (u >= 12 - KW) RK(u - (12 - KW), I + 2)
            if constexpr (u == 11) waitvm<(I + 3 < 8 ? 6 : 0)>();
          }
          SBAR();
        };
        step(IC(0)); step(IC(1)); step(IC(2)); step(IC(3)); step(IC(4)); step(IC(5)); step(IC(6)); step(IC(7)); step(IC(8)); step(IC(9)); step(IC(10)); step(IC(11));
      };
      auto drain = [&]() {
        constexpr int tl[24] = {6, 6, 6, 6, 6, 6, 6, 6, 7, 7, 7, 7, 6, 6, 7, 7, 6, 6, 7, 7, 7, 7, 7, 7};
        constexpr int fu[24] = {0, 1, 2, 3, 4, 5, 6, 7, 0, 1, 2, 3, 8, 9, 4, 5, 10, 11, 6, 7, 8, 9, 10, 11};
        constexpr int wr[24] = {-1, -1, -1, -1, -1, -1, -1, -1, -1, -1, -1, 0, 1, 2, -1, 3, -1, 4, 5, -1, -1, 6, 7, 8};
        u32x4 X[24];
#pragma unroll
        for (int n = 0; n < VW; ++n) X[n] = V[n];
        auto step = [&](auto Nc) {
          constexpr int n = decltype(Nc)::value;
          O[fu[n] >> 1] = mfma32(X[n], P[tl[n] & 1][fu[n] & 1], O[fu[n] >> 1]);
          if constexpr (n + VW < 24) {
            const unsigned a_ = ((fu[n + VW] & 1) ? vr1 : vr0) + (tl[n + VW] % 3) * TILE_B + (fu[n + VW] >> 1) * 2048;
            X[n + VW] = lds_tr2(a_, a_ + 256);
          }
          if constexpr (n >= 1 && n <= 3) { sm(IC(7), IC(0), IC(3 * n - 3)); sm(IC(7), IC(0), IC(3 * n - 2)); sm(IC(7), IC(0), IC(3 * n - 1)); }
          if constexpr (n >= 4 && n <= 6) { sm(IC(7), IC(1), IC(3 * n - 12)); sm(IC(7), IC(1), IC(3 * n - 11)); sm(IC(7), IC(1), IC(3 * n - 10)); }
          if constexpr (n == 7) sm(IC(7), IC(0), IC(10));
          if constexpr (n == 9) sm(IC(7), IC(1), IC(10));
          if constexpr (wr[n] >= 0) put_o(wr[n]);
          SBAR();
        };
        step(IC(0)); step(IC(1)); step(IC(2)); step(IC(3)); step(IC(4)); step(IC(5)); step(IC(6)); step(IC(7)); step(IC(8)); step(IC(9)); step(IC(10)); step(IC(11));
        step(IC(12)); step(IC(13)); step(IC(14)); step(IC(15)); step(IC(16)); step(IC(17)); step(IC(18)); step(IC(19)); step(IC(20)); step(IC(21)); step(IC(22)); step(IC(23));
      };
#define STALE(v) (__builtin_expect(!safe && __any((v) != (v)), 0))
      bool done = false;
      do {
        qkphase(IC(-1));
        ST(15);
        pvphase(IC(-1));
        ST(16);
        qkphase(IC(0)); pvphase(IC(0)); qkphase(IC(1)); pvphase(IC(1)); qkphase(IC(2));
        const float seen = lsum[0];
        pvphase(IC(2));
        if (STALE(seen)) break;
        qkphase(IC(3)); pvphase(IC(3)); qkphase(IC(4)); pvphase(IC(4)); qkphase(IC(5));
        if (!late) asm volatile("s_barrier" ::: "memory");
        late = true;
        pvphase(IC(5)); qkphase(IC(6)); drain();
        if (STALE(lsum[0])) break;
        { const u32x2 ls = {asu(lsum[0]), asu(lsum[1])}; lds_w64(lw, ls); }
        put_o(9); put_o(10); put_o(11);
        done = true;
      } while (false);
#pragma unroll
      for (int k = 0; k < 12; ++k) asm volatile("" : "=v"(K0[k]));
      if (__builtin_expect(done, 1)) {
        ST(17);
        BAR();
        ST(18);
        float lt[2];
        unsigned ll = lo[0];
        OPAQUE(ll);
        ll >>= 4;
        if (__builtin_expect(!low_sum(lds_r128f(L_L + (ll & 15) * 32), lds_r128f(L_L + (ll & 15) * 32 + 16), lt) || exact, 1)) break;
        BAR();
        float mx = -3.0e38f;
#pragma unroll 1
        for (unsigned i = 0; i < 8; ++i) {
#pragma unroll 1
          for (int k = 0; k < 12; ++k) { setm0<0>(ring + k * 1024); bdma<0>(lo[0], rs, (8 * w + i) * TILE_B + k * 1024); }
          waitvm<0>();
          f32x16 s;
#pragma unroll
          for (int e = 0; e < 16; ++e) s[e] = 0.f;
#pragma unroll
          for (int k = 0; k < 12; ++k) s = mfma32(lds_r128(((k & 1) ? kr1 : kr0) + k * 1024), Q[k], s);
#pragma unroll
          for (int e = 0; e < 16; ++e) mx = fmaxf(mx, s[e]);
        }
        lds_w32(L_N + ((ll & 31) * 8 + w * 2 + (ll >> 5)) * 4, mx);
        BAR();
        const f32x4 c = lds_r128f(L_N + (ll & 31) * 32), d = lds_r128f(L_N + (ll & 31) * 32 + 16);
        mref = fmaxf(fmaxf(fmaxf(c[0], c[1]), fmaxf(c[2], c[3])), fmaxf(fmaxf(d[0], d[1]), fmaxf(d[2], d[3])));
        exact = true;
      } else {
        unsigned ll = lo[0];
        OPAQUE(ll);
        for (int sp = 0; sp < SPIN_CAP; ++sp) {
          asm volatile("" ::: "memory");
          const u32x2 f = __builtin_amdgcn_raw_buffer_load_b64(rsWS, ll >> 1, WS_FLAG2 + w * 512, SC1);
          if (__all(f[0] == tag[0] && f[1] == tag[1])) break;
        }
      }
      safe = true;
      asm volatile("s_waitcnt vmcnt(0) lgkmcnt(0)" ::: "memory");
      P6(0, 0) P6(0, 6)
      waitvm<0>();
      piece(IC(1), IC(0)); piece(IC(1), IC(1)); piece(IC(1), IC(2)); piece(IC(1), IC(3));
#pragma unroll
      for (int k = 0; k < 12; ++k) K0[k] = lds_r128(((k & 1) ? kr1 : kr0) + k * 1024);
    }
    ST(19);
    BAR();
    ST_OUT(ws, 0, 12);
  } else {
    LANE_IDS;
    FRAG_POS;
    u32x4 wq[18], wv[18];
    if (w == 3) {
      {
      __builtin_amdgcn_raw_buffer_store_b32(POISON, rsWS, l * 4, WS_ST + (t * 1024 + qb * 32) * 8, SC1);
      const unsigned p0 = WS_H2 + t * FRAME_B + (jqk ? qb * PQ : job ? 9 * (PQ - PV) + qb * PV : 384 + (qb - 32) * PI) * 1024, np = jqk ? PQ : job ? PV : PI;
#pragma unroll 1
      for (unsigned i = 0; i < np; ++i) __builtin_amdgcn_raw_buffer_store_b128(pz, rsWS, l * 16, p0 + i * 1024, SC1);
      asm volatile("s_nop 1" :: "v"(pz));
    }
      chunk(2);
      ST(1);
      if (jqk) waitvm<25 + PQ>(); else if (job) waitvm<25 + PV>(); else waitvm<25 + PI>();
      BAR();
      chunk(3);
      ST(2);
      BARV(24);
      __builtin_amdgcn_raw_buffer_store_b64(tag, rsWS, l < 8 ? l * 2048 : WS_END, WS_FLAG + b * 8, SC1);
      chunk(4); chunk(5);
      ST(3);
      BARV(49);
      BARV(24);
      BARV(0);
    } else {
      unsigned ta[2][4][2];
#pragma unroll
      for (int m = 0; m < 2; ++m)
#pragma unroll
        for (int i = 0; i < 4; ++i) {
          ta[m][i][0] = (8 * g + tq) * 768 + (((12 * m + 4 * w + i) ^ (tq | ((g & 1) << 2))) * 2 + (tp >> 1)) * 16 + 8 * (tp & 1);
          ta[m][i][1] = ta[m][i][0] + 2 * QK;
          OPAQUE(ta[m][i][0]); OPAQUE(ta[m][i][1]);
        }
      unsigned spa = hh == (i16 >> 3) ? L_SP + w * 1024 + l * 16 : L_SP + 4096 + l * 16;
      OPAQUE(spa);
      if (w == 0 && job) {
        unsigned ja[2] = {L_JOB + i16 * 384 + ((g ^ (l & 7)) * 16), L_JOB + i16 * 384 + (((4 + g) ^ (l & 7)) * 16)}, jb = L_JOB + 12288 + (8 * g + tq) * 32 + tp * 8;
        unsigned jo = (g & 1) * 512 + (hh * 16 + i16) * 16;
        OPAQUE(ja[0]); OPAQUE(ja[1]); OPAQUE(jb); OPAQUE(jo);
        waitvm<0>();
        ST(2);
        u32x4 bb[6];
        if (jqk) {
#pragma unroll
          for (int kk = 0; kk < 6; ++kk) bb[kk] = lds_r128(ja[kk & 1] + (kk >> 1) * 128 + 12288);
        } else {
#pragma unroll
          for (int kk = 0; kk < 6; ++kk) bb[kk] = lds_tr2(jb + kk * 1024, jb + kk * 1024 + 128);
        }
        f32x4 d = {0, 0, 0, 0}, e = {0, 0, 0, 0};
#pragma unroll
        for (int kk = 0; kk < 6; ++kk) {
          d = mfma16(lds_r128(ja[kk & 1] + (kk >> 1) * 128), bb[kk], d);
          e = mfma16(lds_r128(ja[kk & 1] + (kk >> 1) * 128 + 6144), bb[kk], e);
        }
        const float sc = jqk ? QSCALE : 1.0f;
        const u32x2 r0 = pack4(d * sc), r1 = pack4(e * sc);
        auto s0 = __builtin_amdgcn_permlane16_swap(r0[0], r1[0], false, false);
        auto s1 = __builtin_amdgcn_permlane16_swap(r0[1], r1[1], false, false);
        st128(u32x4{s0[0], s1[0], s0[1], s1[1]}, rsWS, jo, WS_WM + b * 1024);
        const u32x2 half = {fold_xor(r0[0] ^ r1[0], r0[1] ^ r1[1]), hh ? tag[1] : tag[0]};
        __builtin_amdgcn_raw_buffer_store_b64(half, rsWS, r32 == 0 ? hh * 8 : WS_END, WS_SEAL + b * 128, SC1);
        ST(3);
      }
      __builtin_amdgcn_s_setprio(3);
      u32x4 H[6][2];
      f32x4 acc[2][4][2];
      auto step = [&](const int n) {
        u32x4 a[8];
#pragma unroll
        for (int u = 0; u < 8; ++u) { const unsigned at = ta[u >> 2][u & 3][(n >> 1) & 1] + (n & 1) * QK; a[u] = lds_tr2(at, at + 3072); }
#pragma unroll
        for (int u = 0; u < 8; ++u) {
          acc[u >> 2][u & 3][0] = mfma16(a[u], H[n][0], n ? acc[u >> 2][u & 3][0] : f32x4{0, 0, 0, 0});
          acc[u >> 2][u & 3][1] = mfma16(a[u], H[n][1], n ? acc[u >> 2][u & 3][1] : f32x4{0, 0, 0, 0});
        }
        __builtin_amdgcn_sched_group_barrier(0x100, 8, 0);
#pragma unroll
        for (int q = 0; q < 4; ++q) { __builtin_amdgcn_sched_group_barrier(0x8, 2, 0); __builtin_amdgcn_sched_group_barrier(0x100, 2, 0); }
        SBAR();
      };
      ST(4);
      BAR();
      h_frags(H, fr, 0, 2);
      SBAR();
      step(0); step(1);
      BAR();
      h_frags(H, fr, 2, 6);
      SBAR();
      step(2);
      BAR();
      step(3);
      BAR();
      step(4);
      BAR();
      step(5);
      ST(19);
#pragma unroll
      for (int hf = 0; hf < 2; ++hf) {
        f32x4 st = {0, 0, 0, 0};
#pragma unroll
        for (int i = 0; i < 4; ++i) st = mfma16k16(pack4(acc[1][i][hf]), pack4(acc[0][i][hf]), st);
        lds_w128f(spa ^ (hf * 512), st);
      }
      waitvm<0>();
    }
    asm volatile("" :: "v"(dc), "v"(dw), "v"(dx));
    ST(10);
    BAR();
    __builtin_amdgcn_s_setprio(0);
    u32x4 seal = ld_seal(0);
    {
      ldw(wq, 0, 0, WQ_SPLIT, 0);
      u32x2 fl = __builtin_amdgcn_raw_buffer_load_b64(rsWS, l * 8, WS_FLAG + t * 2048 + w * 512, SC1);
      ST(5);
      BAR();
      ldw(wq, 0, WQ_SPLIT, 6, 0);
      if (__builtin_expect(!__all(fl[0] == tag[0] && fl[1] == tag[1]), 0)) {
        for (int sp = 0; sp < SPIN_CAP; ++sp) {
          asm volatile("" ::: "memory");
          const u32x2 f2 = __builtin_amdgcn_raw_buffer_load_b64(rsWS, l * 8, WS_FLAG + t * 2048 + w * 512, SC1);
          if (__all(f2[0] == tag[0] && f2[1] == tag[1])) break;
        }
      }
      ST(18);
      BAR();
    }
    const unsigned qt = (qb >> 3) * SLOT;
    unsigned hqa[2], own[2][2];
    hqa[0] = qt + hh * 1024 + (tq ^ hh) * 128 + (g & 1) * 64 + tp * 16; hqa[1] = (hqa[0] ^ 64) + 512;
    OPAQUE(hqa[0]); OPAQUE(hqa[1]);
    const unsigned tok = 16 * (g & 1) + i16;
    unsigned nw = lds0 + L_N + l * 16 + w * 4;
    OPAQUE(nw);
    unsigned qw = L_Q + (3 * w * 64 + hh * 32 + i16) * 16 + 8 * (g & 1);
    own[0][0] = qt + w * 3072 + (tq ^ (w & 1)) * 128 + hh * 64 + tp * 16 + 8 * (g & 1); own[0][1] = (own[0][0] ^ 64) + 512;
    own[1][0] = own[0][0] ^ 128; own[1][1] = own[0][1] ^ 128;
    OPAQUE(qw); OPAQUE(own[0][0]); OPAQUE(own[0][1]); OPAQUE(own[1][0]); OPAQUE(own[1][1]);
    if (b == 0 && w == 3 && l == 0) __builtin_amdgcn_raw_buffer_store_b64(tag, rsWS, 0, WS_EPOCH, SC1);
    for (int sp = 0; sp < SPIN_CAP && !sealed(wq, seal, l < 18, tag); ++sp) {
      asm volatile("" ::: "memory");
      seal = ld_seal(0);
      ldw(wq, 0, 0, 6, 0);
    }
    u32x4 hq[3];
    {
      const u64 hbu = (u64)(ws + WS_H2 + (size_t)t * FRAME_B);
      u32x4 rs = {(unsigned)hbu, (unsigned)(hbu >> 32) & 0xffffu, (unsigned)FRAME_B, 0x00020000u};
      asm volatile("" : "+s"(rs));
      __builtin_amdgcn_s_setprio(3);
      ST(6);
      BAR();
      ST(7);
      for (int sp = 0; sp < SPIN_CAP; ++sp) {
        setm0<0>(qt + w * 3072);
        bdma<0>(l * 16, rs, qb * TILE_B + w * 3072); bdma<1024>(l * 16, rs, qb * TILE_B + w * 3072); bdma<2048>(l * 16, rs, qb * TILE_B + w * 3072);
        waitvm<0>();
#pragma unroll
        for (int i = 0; i < 3; ++i) { const u32x2 u0 = lds_r64(own[i & 1][0] + i * 1024), u1 = lds_r64(own[i & 1][1] + i * 1024); hq[i] = u32x4{u0[0], u0[1], u1[0], u1[1]}; }
        if (!poisoned(hq)) break;
      }
    }
    ST(8);
    BAR();
    ST(9);
    {
      u32x4 bf[6][2];
      f32x4 d[3][2];
      f32x2 n[2] = {{0.f, 0.f}, {0.f, 0.f}};
      auto fin = [&](const int i) {
#pragma unroll
        for (int hf = 0; hf < 2; ++hf) {
          lds_w64(qw + i * 1024 + hf * 256, pack4(d[i][hf]));
          const f32x2 a = {d[i][hf][0], d[i][hf][1]}, c = {d[i][hf][2], d[i][hf][3]};
          n[hf] = __builtin_elementwise_fma(a, a, n[hf]); n[hf] = __builtin_elementwise_fma(c, c, n[hf]);
        }
      };
      auto mm = [&](const int i, const int ks) {
        d[i][0] = mfma16(wq[ks * 3 + i], bf[ks][0], ks ? d[i][0] : f32x4{0, 0, 0, 0});
        d[i][1] = mfma16(wq[ks * 3 + i], bf[ks][1], ks ? d[i][1] : f32x4{0, 0, 0, 0});
      };
#pragma unroll
      for (int ks = 0; ks < 3; ++ks) { bf[ks][0] = lds_r128(hqa[0] + ks * 2048); bf[ks][1] = lds_r128(hqa[1] + ks * 2048); SBAR(); }
#pragma unroll
      for (int ks = 0; ks < 3; ++ks) {
        mm(0, ks);
        bf[ks + 3][0] = lds_r128(hqa[0] + (ks + 3) * 2048); bf[ks + 3][1] = lds_r128(hqa[1] + (ks + 3) * 2048);
        SBAR();
        mm(1, ks);
        SBAR();
      }
      mm(0, 3); mm(0, 4); mm(0, 5); mm(1, 3);
      SBAR();
      mm(1, 4); mm(1, 5); mm(2, 0); fin(0);
      SBAR();
      mm(2, 1); mm(2, 2); mm(2, 3); mm(2, 4); mm(2, 5); fin(1);
      SBAR();
      fin(2);
      auto rr = __builtin_amdgcn_permlane16_swap(asu(n[0][0] + n[0][1]), asu(n[1][0] + n[1][1]), false, false);
      lds_w32(nw, asf(rr[0]) + asf(rr[1]));
    }
    SBAR();
    ST(11);
    BAR();
    __builtin_amdgcn_s_setprio(0);
    unsigned oo = (t * 1024 + qb * 32 + tok) * 384 + (6 * w + hh) * 16;
    OPAQUE(oo);
    float lt[2];
    u32x4 pick;
#pragma unroll
    for (int e = 0; e < 4; ++e) {
      const unsigned d = 8 * (e >> 1) + 4 * (g & 1) + 2 * (e & 1);
      pick[e] = (i16 == d ? 0x3f80u : 0u) | (i16 == d + 1 ? 0x3f800000u : 0u);
    }
    unsigned pr[2], la = lds0 + L_L + i16 * 32;
    pr[0] = lds0 + 2 * TILE_B + hh * SLOT + 3 * w * 1024 + ((g & 1) * 32 + i16) * 16; pr[1] = pr[0] + 2 * SLOT;
    OPAQUE(pick); OPAQUE(pr[0]); OPAQUE(pr[1]); OPAQUE(la);
    f32x4 x1[3][2];
    {
      __builtin_amdgcn_s_sleep(40);
      u32x2 st[2];
      for (int sp = 0; sp < SPIN_CAP; ++sp) {
        asm volatile("" ::: "memory");
        st[0] = __builtin_amdgcn_raw_buffer_load_b64(rsWS, i16 * 8, WS_ST + (t * 1024 + qb * 32) * 8, SC1);
        st[1] = __builtin_amdgcn_raw_buffer_load_b64(rsWS, i16 * 8 + 128, WS_ST + (t * 1024 + qb * 32) * 8, SC1);
        if (!__any(max(max(st[0][0], st[0][1]), max(st[1][0], st[1][1])) == POISON)) break;
      }
#pragma unroll
      for (int hf = 0; hf < 2; ++hf) {
        const float mu = asf(st[hf][0]), sd = __builtin_amdgcn_sqrtf(asf(st[hf][1]));
#pragma unroll
        for (int i = 0; i < 3; ++i) {
          const f32x4 h = {bflo(hq[i][2 * hf]), bfhi(hq[i][2 * hf]), bflo(hq[i][2 * hf + 1]), bfhi(hq[i][2 * hf + 1])};
          x1[i][hf] = h * sd + mu;
          OPAQUE(x1[i][hf]);
        }
      }
    }
    BAR();
    ST(16);
    for (int sp = 0; sp < SPIN_CAP; ++sp) {
      asm volatile("" ::: "memory");
      seal = ld_seal(1);
      ldw(wv, 1, 0, 6, 0);
      if (sealed(wv, seal, l < 18, tag)) break;
    }
    ST(12);
#pragma unroll 1
    for (bool exact = false;; exact = true) {
      BAR();
      ST(13);
      u32x4 p[3][2][2];
#pragma unroll
      for (int i = 0; i < 3; ++i)
#pragma unroll
        for (int hf = 0; hf < 2; ++hf) { p[i][hf][0] = lds_r128(pr[0] + i * 1024 + hf * 256); p[i][hf][1] = lds_r128(pr[1] + i * 1024 + hf * 256); }
      const f32x4 l0 = lds_r128f(la), l1 = lds_r128f(la + 16);
      SBAR();
#pragma unroll
      for (int i = 0; i < 3; ++i)
#pragma unroll
        for (int hf = 0; hf < 2; ++hf) lds_w64(dp[hf] + (L_FI - L_X) + i * 512, pack4(mfma16(pick, p[i][hf][1], mfma16(pick, p[i][hf][0], f32x4{0, 0, 0, 0}))));
      if (__builtin_expect(!low_sum(l0, l1, lt) || exact, 1)) break;
      BAR();
      BAR();
    }
    ST(14);
    const float linv[2] = {__builtin_amdgcn_rcpf(lt[0]), __builtin_amdgcn_rcpf(lt[1])};
    BAR();
    ST(15);
    u32x4 bf[6][2];
#pragma unroll
    for (int ks = 0; ks < 6; ++ks) { bf[ks][0] = lds_r128(fr[0] + (L_FI - L_X) + ks * 1024); bf[ks][1] = lds_r128(fr[1] + (L_FI - L_X) + ks * 1024); }
    SBAR();
    f32x4 d[3][2];
#pragma unroll
    for (int ks = 0; ks < 6; ++ks)
#pragma unroll
      for (int i = 0; i < 3; ++i) {
        d[i][0] = mfma16(wv[ks * 3 + i], bf[ks][0], ks ? d[i][0] : f32x4{0, 0, 0, 0});
        d[i][1] = mfma16(wv[ks * 3 + i], bf[ks][1], ks ? d[i][1] : f32x4{0, 0, 0, 0});
      }
#pragma unroll
    for (int i = 0; i < 3; ++i) {
      const u32x2 y0 = pack4(d[i][0] * linv[0] + x1[i][0]), y1 = pack4(d[i][1] * linv[1] + x1[i][1]);
      auto s0 = __builtin_amdgcn_permlane16_swap(y0[0], y1[0], false, false);
      auto s1 = __builtin_amdgcn_permlane16_swap(y0[1], y1[1], false, false);
      const u32x4 o = {s0[0], s1[0], s0[1], s1[1]};
      *(__attribute__((address_space(1))) u32x4*)(kc + oo + i * 32) = o;
    }
    ST(17);
    ST_OUT(ws, 12, 20);
  }
}

extern "C" void kernel_launch(void* const* d_in, const int* in_sizes, int n_in, void* d_out, int out_size, void* d_ws, size_t ws_size, hipStream_t stream) {
  (void)hipFuncSetAttribute((const void*)k_timesformer, hipFuncAttributeMaxDynamicSharedMemorySize, LDS_BYTES);
  k_timesformer<<<256, 512, LDS_BYTES, stream>>>((const char*)d_in[0], (const char*)d_in[1], (const char*)d_in[2], (const char*)d_in[3], (const char*)d_in[4],
                                                 (char*)d_ws, (char*)d_out);
}
```

```cpp
#include <hip/hip_runtime.h>
#include <hip/hip_bf16.h>
#include <type_traits>

typedef short s16x4 __attribute__((ext_vector_type(4)));
typedef __bf16 b16x8 __attribute__((ext_vector_type(8)));
typedef __bf16 b16x2 __attribute__((ext_vector_type(2)));
typedef float f32x2 __attribute__((ext_vector_type(2)));
typedef float f32x4 __attribute__((ext_vector_type(4)));
typedef float f32x16 __attribute__((ext_vector_type(16)));
typedef unsigned u32x2 __attribute__((ext_vector_type(2)));
typedef unsigned u32x4 __attribute__((ext_vector_type(4)));
typedef unsigned long long u64;
typedef u64 u64x4 __attribute__((ext_vector_type(4)));
typedef u64 u64x2 __attribute__((ext_vector_type(2)));
#define LAS __attribute__((address_space(3)))
#define DI __device__ __forceinline__
#define SBAR() __builtin_amdgcn_sched_barrier(0)

constexpr int TILE_B = 12288;
constexpr int FRAME_B = 393216;
constexpr unsigned WS_H2 = 0, WS_ST = 8 * FRAME_B, WS_WM = 16 * FRAME_B, WS_SEAL = WS_WM + 3 * 73728, WS_FLAG = WS_SEAL + 216 * 128, WS_FLAG2 = WS_FLAG + 8 * 2048, WS_EPOCH = WS_FLAG2 + 2048, WS_END = WS_EPOCH + 256;
constexpr float QSCALE = 0.07216878364870322f * 1.4426950408889634f;
constexpr int SC1 = 16;
constexpr unsigned POISON = 0xffffffffu;
constexpr unsigned CODE_BYTES = 26112;
constexpr unsigned CODE_SHARE = 32;
constexpr int OUT_AUX = 16;
constexpr int TILE0_DELAY = 2, LOAD_DELAY = 2, WQ_SPLIT = 4, B9_DELAY = 2, VO_1 = 1, VO_2 = 3, MM_EARLY = 0, PQ = 12, PV = 9, PI = 23;
static_assert(9 * PQ + 18 * PV + 5 * PI >= 384, "the whole frame is poisoned");
constexpr int SPIN_CAP = 1 << 16;

#define ST_DECL
#define ST(k)
#define ST_OUT(ws, k0, k1)

DI float asf(unsigned u) { return __builtin_bit_cast(float, u); }
DI unsigned asu(float f) { return __builtin_bit_cast(unsigned, f); }
DI unsigned cvtpk(float lo, float hi) { f32x2 v = {lo, hi}; return __builtin_bit_cast(unsigned, __builtin_convertvector(v, b16x2)); }
DI float bflo(unsigned u) { return asf(u << 16); }
DI float bfhi(unsigned u) { return asf(u & 0xffff0000u); }
DI f32x16 mfma32(u32x4 a, u32x4 b, f32x16 c) { return __builtin_amdgcn_mfma_f32_32x32x16_bf16(__builtin_bit_cast(b16x8, a), __builtin_bit_cast(b16x8, b), c, 0, 0, 0); }
DI f32x4 mfma16(u32x4 a, u32x4 b, f32x4 c) { return __builtin_amdgcn_mfma_f32_16x16x32_bf16(__builtin_bit_cast(b16x8, a), __builtin_bit_cast(b16x8, b), c, 0, 0, 0); }
DI f32x4 mfma16k16(u32x2 a, u32x2 b, f32x4 c) { return __builtin_amdgcn_mfma_f32_16x16x16bf16_1k(__builtin_bit_cast(s16x4, a), __builtin_bit_cast(s16x4, b), c, 0, 0, 0); }
DI u32x2 pack4(f32x4 v) { u32x2 r = {cvtpk(v[0], v[1]), cvtpk(v[2], v[3])}; return r; }
DI u32x4 lds_r128(unsigned a) { return *(LAS u32x4*)(size_t)a; }
DI f32x4 lds_r128f(unsigned a) { return *(LAS f32x4*)(size_t)a; }
DI u32x2 lds_r64(unsigned a) { return *(LAS u32x2*)(size_t)a; }
DI void lds_w128(unsigned a, u32x4 v) { *(LAS u32x4*)(size_t)a = v; }
DI void lds_w128f(unsigned a, f32x4 v) { *(LAS f32x4*)(size_t)a = v; }
DI void lds_w64(unsigned a, u32x2 v) { *(LAS u32x2*)(size_t)a = v; }
DI void lds_w32(unsigned a, float v) { *(LAS float*)(size_t)a = v; }
DI u32x2 lds_tr(unsigned a) { return __builtin_bit_cast(u32x2, __builtin_amdgcn_ds_read_tr16_b64_v4i16((LAS s16x4*)(size_t)a)); }
DI u32x4 lds_tr2(unsigned a0, unsigned a1) { u32x2 x = lds_tr(a0), y = lds_tr(a1); u32x4 r = {x[0], x[1], y[0], y[1]}; return r; }
#define BAR() asm volatile("s_waitcnt lgkmcnt(0)\n\ts_barrier" ::: "memory")
#define BARV(n) asm volatile("s_waitcnt vmcnt(" #n ") lgkmcnt(0)\n\ts_barrier" ::: "memory")
template <int CTRL> DI float dpp_add(float v) { return v + asf(__builtin_amdgcn_update_dpp(0u, asu(v), CTRL, 0xf, 0xf, true)); }
DI float xor16_add(float v) { auto r = __builtin_amdgcn_permlane16_swap(asu(v), asu(v), false, false); return asf(r[0]) + asf(r[1]); }
DI float xor16_max(float v) { auto r = __builtin_amdgcn_permlane16_swap(asu(v), asu(v), false, false); return __builtin_amdgcn_fmed3f(asf(r[0]), asf(r[1]), __builtin_inff()); }
DI unsigned umax3(unsigned a, unsigned b, unsigned c) { return max(max(a, b), c); }
template <int N> DI bool poisoned(const u32x4 (&v)[N]) {
  unsigned m = 0;
#pragma unroll
  for (int i = 0; i < N; ++i) { m = umax3(m, v[i][0], v[i][1]); m = umax3(m, v[i][2], v[i][3]); }
  return __any(m == POISON);
}
DI u32x4 unpoison(u32x4 v) { u32x4 r = {min(v[0], POISON - 1), min(v[1], POISON - 1), min(v[2], POISON - 1), min(v[3], POISON - 1)}; return r; }
DI unsigned fi_pos(unsigned r, unsigned c8) {
  return (((r >> 4) * 6 + (c8 >> 2)) * 64 + (c8 & 3) * 16 + ((r & 15) ^ ((c8 & 1) | (((r >> 3) & 1) << 1) | ((r >> 4) << 2)))) * 16;
}
DI float row8_sum(float s) { s = dpp_add<0x124>(s); s = dpp_add<0x128>(s); return xor16_add(s); }
DI float sum24(const f32x2 (&v)[12]) {
  f32x2 s0 = v[0] + v[1], s1 = v[2] + v[3];
#pragma unroll
  for (int i = 4; i < 12; i += 2) { s0 += v[i]; s1 += v[i + 1]; }
  s0 += s1;
  return s0[0] + s0[1];
}
DI float sumsq24(const f32x2 (&v)[12]) {
  f32x2 q0 = v[0] * v[0], q1 = v[1] * v[1];
#pragma unroll
  for (int i = 2; i < 12; i += 2) { q0 = __builtin_elementwise_fma(v[i], v[i], q0); q1 = __builtin_elementwise_fma(v[i + 1], v[i + 1], q1); }
  q0 += q1;
  return q0[0] + q0[1];
}
DI f32x2 unpack2(unsigned u) { f32x2 r = {bflo(u), bfhi(u)}; return r; }

constexpr unsigned SLOT = 36864, L_X = 3 * SLOT, L_H = 4 * SLOT, L_N = L_H + TILE_B, L_L = L_N + 1024, L_PT = L_L + 1024, LDS_BYTES = 163840;
constexpr unsigned QK = 24576, L_JOB = 4 * QK;
constexpr unsigned L_VI = L_X, L_SP = L_X + TILE_B, L_YI = L_X + 20480;
constexpr unsigned L_Q = L_H, L_FI = L_H;

#define DMA3(v0, v1, v2, sbase, ldsa) \
  asm volatile("s_mov_b32 m0, %4\n\ts_nop 0\n\tglobal_load_lds_dwordx4 %0, %3\n\tglobal_load_lds_dwordx4 %1, %3 offset:1024\n\tglobal_load_lds_dwordx4 %2, %3 offset:2048" \
               :: "v"(v0), "v"(v1), "v"(v2), "s"(sbase), "s"(ldsa) : "memory")
#define DMA1(v0, sbase, ldsa) \
  asm volatile("s_mov_b32 m0, %2\n\ts_nop 0\n\tglobal_load_lds_dwordx4 %0, %1" :: "v"(v0), "s"(sbase), "s"(ldsa) : "memory")

template <int OFF> DI void bdma(unsigned voff, u32x4 rs, unsigned soff) { asm volatile("buffer_load_dwordx4 %0, %1, %2 offen offset:%3 sc1 lds" :: "v"(voff), "s"(rs), "s"(soff), "i"(OFF) : "memory"); }
template <int OFF> DI void setm0(unsigned base) { asm volatile("s_add_u32 m0, %0, %1\n\ts_nop 0" :: "s"(base), "i"(OFF) : "memory", "scc"); }
template <int N> DI void waitvm() { asm volatile("s_waitcnt vmcnt(%0)" :: "i"(N) : "memory"); }
#define IC(x) std::integral_constant<int, (x)>{}
#define OPAQUE(v) asm volatile("" : "+v"(v))
template <class R> DI void st128(u32x4 v, R rs, unsigned voff, unsigned soff) { __builtin_amdgcn_raw_buffer_store_b128(v, rs, voff, soff, SC1); asm volatile("s_nop 1" :: "v"(v)); }
template <int CTRL> DI unsigned dpp_xor(unsigned v) { return v ^ __builtin_amdgcn_update_dpp(0u, v, CTRL, 0xf, 0xf, true); }
DI unsigned fold_xor(unsigned e, unsigned o) {
  auto r = __builtin_amdgcn_permlane32_swap(e, o, false, false);
  unsigned u = r[0] ^ r[1];
  auto q = __builtin_amdgcn_permlane16_swap(u, u, false, false);
  u = q[0] ^ q[1];
  u = dpp_xor<0x128>(u); u = dpp_xor<0x124>(u); u = dpp_xor<0x4e>(u); return dpp_xor<0xb1>(u);
}
DI bool seal_ok(unsigned e, unsigned o, u32x4 seal, bool has, u32x2 tag) {
  const unsigned bad = has ? (seal[1] ^ tag[0]) | (seal[3] ^ tag[1]) : 0u;
  return __builtin_amdgcn_ballot_w64((fold_xor(e ^ seal[0], o ^ seal[2]) | bad) != 0) == 0;
}
DI unsigned xor3(unsigned a, unsigned b, unsigned c) { return __builtin_amdgcn_bitop3_b32(a, b, c, 0x96); }
#define XOR_IN(e, o, v) do { e = xor3(e, (v)[0], (v)[2]); o = xor3(o, (v)[1], (v)[3]); } while (0)
#define MIXV6() _Pragma("unroll") for (int q_ = 0; q_ < 6; ++q_) { __builtin_amdgcn_sched_group_barrier(0x8, 1, 0); __builtin_amdgcn_sched_group_barrier(0x2, 1, 0); } SBAR()
template <int N> DI bool sealed(const u32x4 (&v)[N], u32x4 seal, bool has, u32x2 tag) {
  unsigned e = 0, o = 0;
#pragma unroll
  for (int i = 0; i < N; ++i) XOR_IN(e, o, v[i]);
  return seal_ok(e, o, seal, has, tag);
}
DI bool tags_ok(u32x4 f, u32x2 tag) { return f[0] == tag[0] && f[1] == tag[1] && f[2] == tag[0] && f[3] == tag[1]; }

__global__ __launch_bounds__(512, 2) void k_timesformer(const char* x_, const char* wqkv_t_, const char* wo_t_, const char* wqkv_s_, const char* wo_s_, char* ws_, char* out_) {
  unsigned dc;
  u64x4 ka; u64x2 kb; u64 kc;
  {
    u64 pc;
    asm volatile("s_load_dwordx8 %2, %6, 0x0\n\ts_load_dwordx4 %3, %6, 0x20\n\ts_load_dwordx2 %4, %6, 0x30\n\ts_getpc_b64 %1\n\tglobal_load_dword %0, %5, %1"
                 : "=v"(dc), "=&s"(pc), "=&s"(ka), "=&s"(kb), "=&s"(kc) : "v"(min((threadIdx.x * CODE_SHARE + ((blockIdx.x >> 3) & (CODE_SHARE - 1))) * 128, CODE_BYTES)), "s"(__builtin_amdgcn_kernarg_segment_ptr()) : "memory");
  }
  constexpr unsigned lds0 = 0;
  const unsigned b = blockIdx.x;
  unsigned w8 = __builtin_amdgcn_readfirstlane(threadIdx.x >> 6);
  unsigned l = threadIdx.x & 63;
  const unsigned t = b & 7, qb = b >> 3;
  w8 = (w8 & 4) | ((w8 + qb / 6) & 3);
  ST_DECL;
  ST(0);
  const bool jqk = b < 72, job = b < 216;
  const unsigned jp = b % 72, jw = jp / 18, jks = (jp - jw * 18) / 3, jtau = jp - jw * 15 - jks * 3;
  const unsigned rot = qb % 6;
  unsigned fo[2][3];
#pragma unroll
  for (int i = 0; i < 3; ++i) {
    const unsigned p = i * 64 + l;
    if (w8 < 4) { fo[0][i] = p * 16 + (p >= 96 ? FRAME_B - 1536 : 0) + w8 * 2 * FRAME_B + b * 1536 - i * 1024; fo[1][i] = 0; }
    else if (w8 == 7) {
      const unsigned row = (p * 1366) >> 16;
      fo[0][i] = row * 1152 + (((p - row * 48) ^ (row << 1)) * 16) - i * 1024; fo[1][i] = row * 1152 + (((p - row * 48) ^ (row << 1) ^ 8) * 16) - i * 1024;
    } else {
      const unsigned row = (p * 2731) >> 16, o = row * 1152 + (((p - row * 24) ^ row) * 16) - i * 1024;
      fo[0][i] = o + jks * (32 * 1152) + (jqk ? 0 : 768);
      fo[1][i] = jqk ? o + jtau * (16 * 1152) + 384 : (l >> 1) * 384 + (l & 1) * 16 + jtau * 32 + i * (12288 - 1024);
    }
  }
  asm volatile("s_waitcnt lgkmcnt(0)" : "+s"(ka), "+s"(kb), "+s"(kc), "+v"(fo[0][0]), "+v"(fo[0][1]), "+v"(fo[0][2]), "+v"(fo[1][0]), "+v"(fo[1][1]), "+v"(fo[1][2]) :: "memory");
  char* const ws = (char*)kb[1];
  auto chunk = [&](const int n) {
    const u64 src = ka[1] + (n + rot >= 6 ? n + rot - 6 : n + rot) * (32 * 1152);
#pragma unroll
    for (int q = 0; q < 8; ++q) DMA3(fo[(q >> 1) & 1][0], fo[(q >> 1) & 1][1], fo[(q >> 1) & 1][2], src + q * 4608, (n & 3) * QK + q * 3072);
  };
  unsigned dw = 0, dx = 0;
  if (w8 < 4) {
    DMA3(fo[0][0], fo[0][1], fo[0][2], ka[0], L_YI + w8 * 3072);
  } else if (w8 == 4) {
    if (job) {
      __builtin_amdgcn_s_setprio(3);
      const u64 wqkv = b >= 144 ? ka[1] : ka[3], second = jqk ? wqkv : b >= 144 ? ka[2] : kb[0];
#pragma unroll
      for (int q = 0; q < 4; ++q) DMA3(fo[0][0], fo[0][1], fo[0][2], wqkv + q * (8 * 1152), L_JOB + q * 3072);
#pragma unroll
      for (int q = 0; q < 2; ++q) DMA3(fo[1][0], fo[1][1], fo[1][2], second + q * (jqk ? 8 * 1152 : 36864), L_JOB + 12288 + q * 3072);
    }
  } else if (w8 == 7) {
    __builtin_amdgcn_s_sleep(LOAD_DELAY);
    chunk(0); chunk(1);
  } else {
    if (w8 == 6) asm volatile("global_load_dword %0, %1, %2" : "=v"(dx) : "v"((qb * 6 + min(l, 35u) / 6) * 1152 + (min(l, 35u) % 6) * 128), "s"(ka[1]) : "memory");
    asm volatile("global_load_dword %0, %1, %2" : "=v"(dw) : "v"((qb * 18 + min(l, 17u)) * 128), "s"(w8 == 6 ? ka[2] : kb[0]) : "memory");
  }
  const auto rsWS = __builtin_amdgcn_make_buffer_rsrc((void*)ws, 0, WS_END, 0x00020000);
  const u32x4 pz = {POISON, POISON, POISON, POISON};
  u64 epoch;
  asm volatile("s_load_dwordx2 %0, %1, 0x0" : "=&s"(epoch) : "s"(ws + WS_EPOCH) : "memory");
  asm volatile("s_waitcnt lgkmcnt(0)" : "+s"(epoch) :: "memory");
  epoch += 1;
  const u32x2 tag = {(unsigned)epoch, (unsigned)(epoch >> 32)};
  asm volatile("" : "+s"(w8));
  const unsigned w = w8 & 3;
#define LANE_IDS const unsigned i16 = l & 15, g = l >> 4, r32 = l & 31, hh = l >> 5, tq = i16 >> 2, tp = l & 3
#define FRAG_POS unsigned fr[2], dp[2];                                                                       \
  fr[0] = L_X + fi_pos(i16, g); fr[1] = (fr[0] ^ 64) + 6144; dp[0] = L_X + fi_pos(i16, hh) + 8 * (g & 1) + w * 1536; dp[1] = (dp[0] ^ 64) + 6144; \
  OPAQUE(fr[0]); OPAQUE(fr[1]); OPAQUE(dp[0]); OPAQUE(dp[1])
  auto ldw = [&](u32x4 (&W)[18], const int m, const int k0, const int k1, const unsigned rot) {
#pragma unroll
    for (int kk = k0; kk < k1; ++kk) {
      const unsigned ks = kk + rot >= 6 ? kk + rot - 6 : kk + rot;
#pragma unroll
      for (int i = 0; i < 3; ++i) { SBAR(); W[kk * 3 + i] = __builtin_amdgcn_raw_buffer_load_b128(rsWS, l * 16 + i * 1024, WS_WM + m * 73728 + w * 18432 + ks * 3072, SC1); }
    }
  };
  auto ld_seal = [&](const int m) {
    return __builtin_amdgcn_raw_buffer_load_b128(rsWS, l < 18 ? l * 128 : WS_END, WS_SEAL + (m * 72 + 18 * w) * 128, SC1);
  };
  auto h_frags = [&](u32x4 (&H)[6][2], const unsigned (&fr)[2], const int k0, const int k1) {
#pragma unroll
    for (int kk = k0; kk < k1; ++kk) {
      const unsigned ks = kk + rot >= 6 ? kk + rot - 6 : kk + rot;
      H[kk][0] = lds_r128(fr[0] + ks * 1024 + (L_H - L_X)); H[kk][1] = lds_r128(fr[1] + ks * 1024 + (L_H - L_X));
    }
  };
  auto low_sum = [&](const f32x4 a, const f32x4 c, float (&lt)[2]) {
    lt[0] = (a[0] + a[2]) + (c[0] + c[2]);
    lt[1] = (a[1] + a[3]) + (c[1] + c[3]);
    return (bool)__any(lt[0] < 8.7e-19f || lt[1] < 8.7e-19f);
  };

  if (w8 < 4) {
    const unsigned nl = l & 3, j = (l >> 2) & 7, tt = 2 * w + (l >> 5), r = nl * 8 + tt;
    unsigned rp = L_X + fi_pos(r, j);
    OPAQUE(rp);
    waitvm<0>();
    asm volatile("" :: "v"(dc));
    f32x2 xv[12];
    float mu1;
    {
      u32x4 h[3], xr[3];
#pragma unroll
      for (int i = 0; i < 3; ++i) xr[i] = lds_r128(lds0 + L_YI + tt * 1536 + nl * 384 + j * 16 + i * 128);
#pragma unroll
      for (int i = 0; i < 12; ++i) xv[i] = unpack2(xr[i >> 2][i & 3]);
      mu1 = row8_sum(sum24(xv)) * (1.0f / 192.0f);
#pragma unroll
      for (int i = 0; i < 12; ++i) xv[i] -= mu1;
      const float rs = __builtin_amdgcn_rsqf(row8_sum(sumsq24(xv)) * (1.0f / 192.0f) + 1e-5f);
#pragma unroll
      for (int i = 0; i < 12; ++i) { const f32x2 n = xv[i] * rs; h[i >> 2][i & 3] = cvtpk(n[0], n[1]); }
#pragma unroll
      for (int i = 0; i < 3; ++i) lds_w128(rp + (L_H - L_X) + i * 2048, h[i]);
    }
    SBAR();
    OPAQUE(l);
    unsigned so = tt * FRAME_B + (j >> 1) * 1024 + ((t ^ ((j >> 1) & 1)) * 128) + (((j & 1) ^ (t >> 2)) * 64) + nl * 16;
    const unsigned sso = qb * TILE_B;
    LANE_IDS;
    FRAG_POS;
    unsigned sto = j == 0 ? (tt * 1024 + nl) * 8 : WS_END;
    unsigned sa = L_SP + l * 16;
    unsigned keep = (hh == (i16 >> 3)) ? ~0u : 0u;
    OPAQUE(sto); OPAQUE(so); OPAQUE(sa); OPAQUE(keep);
    ST(1);
    BAR();
    ST(6);
    {
      u32x4 H[6][2], W[18];
      BAR();
      h_frags(H, fr, 0, 6);
      asm volatile("s_barrier" ::: "memory");
      ST(2);
#pragma unroll 1
      for (int sp = 0;; ++sp) {
        const u32x4 seal = ld_seal(2);
        ldw(W, 2, 0, VO_1, rot);
        if (sp == 0) asm volatile("s_barrier" ::: "memory");
        ldw(W, 2, VO_1, VO_2, rot);
        if (sp == 0) asm volatile("s_barrier" ::: "memory");
        ldw(W, 2, VO_2, 6, rot);
        SBAR();
        f32x4 vp[3][2];
        unsigned he = 0, ho = 0;
        auto mm = [&](const int kk) {
#pragma unroll
          for (int i = 0; i < 3; ++i) {
            vp[i][0] = mfma16(H[kk][0], W[kk * 3 + i], kk ? vp[i][0] : f32x4{0, 0, 0, 0});
            vp[i][1] = mfma16(H[kk][1], W[kk * 3 + i], kk ? vp[i][1] : f32x4{0, 0, 0, 0});
            XOR_IN(he, ho, W[kk * 3 + i]);
          }
          MIXV6();
        };
        ST(3);
#pragma unroll
        for (int kk = 0; kk < MM_EARLY; ++kk) mm(kk);
        OPAQUE(he); OPAQUE(ho);
        ST(8);
        if (sp == 0) asm volatile("s_barrier" ::: "memory");
        u32x2 pt[2];
        {
          f32x4 e = (lds_r128f(sa) + lds_r128f(sa + 1024)) + lds_r128f(sa + 2048);
          const float mq = xor16_max(fmaxf(fmaxf(e[0], e[1]), fmaxf(e[2], e[3]))) * -QSCALE;
#pragma unroll
          for (int k = 0; k < 4; ++k) e[k] = __builtin_amdgcn_exp2f(fmaf(e[k], QSCALE, mq));
          pt[0] = pack4(e * __builtin_amdgcn_rcpf(xor16_add((e[0] + e[1]) + (e[2] + e[3]))));
#pragma unroll
          for (int k = 0; k < 2; ++k) {
            auto q = __builtin_amdgcn_permlane32_swap(pt[0][k], pt[0][k], false, false);
            pt[1][k] = (q[0] ^ q[1] ^ pt[0][k]) & keep;
            pt[0][k] &= keep;
          }
          OPAQUE(pt[0]); OPAQUE(pt[1]);
        }
        SBAR();
        ST(4);
#pragma unroll
        for (int kk = MM_EARLY; kk < 6; ++kk) mm(kk);
        OPAQUE(he); OPAQUE(ho);
        ST(7);
        f32x4 y[3][2];
#pragma unroll
        for (int hf = 0; hf < 2; ++hf)
#pragma unroll
          for (int i = 0; i < 3; ++i) y[i][hf] = mfma16k16(pack4(vp[i][hf]), pt[hf], f32x4{0, 0, 0, 0});
        SBAR();
#pragma unroll
        for (int hf = 0; hf < 2; ++hf)
#pragma unroll
          for (int i = 0; i < 3; ++i) lds_w64(dp[hf] + (L_YI - L_X) + i * 512, pack4(y[i][hf]));
        if (__builtin_expect(seal_ok(he, ho, seal, l < 18, tag), 1) || sp >= SPIN_CAP) break;
        asm volatile("" ::: "memory");
      }
    }
    ST(9);
    BAR();
    {
      u32x4 y[3], h[3];
#pragma unroll
      for (int i = 0; i < 3; ++i) y[i] = lds_r128(rp + (L_YI - L_X) + i * 2048);
#pragma unroll
      for (int i = 0; i < 12; ++i) xv[i] += unpack2(y[i >> 2][i & 3]);
      const float mu = row8_sum(sum24(xv)) * (1.0f / 192.0f), sq = row8_sum(sumsq24(xv));
      const float var = fmaxf(fmaf(sq, 1.0f / 192.0f, -mu * mu), 0.f) + 1e-5f;
      const float rs = __builtin_amdgcn_rsqf(var), nb = -mu * rs;
#pragma unroll
      for (int i = 0; i < 3; ++i) {
#pragma unroll
        for (int e = 0; e < 4; ++e) { const f32x2 n = xv[4 * i + e] * rs + nb; h[i][e] = cvtpk(n[0], n[1]); }
        if (i == 0) { OPAQUE(h[0]); ST(5); asm volatile("s_barrier" ::: "memory"); }
        st128(h[i], rsWS, so, WS_H2 + sso + i * 4096);
        SBAR();
      }
      const u32x2 stat = {asu(mu + mu1), asu(var)};
      __builtin_amdgcn_raw_buffer_store_b64(stat, rsWS, sto, WS_ST + b * 32, SC1);
    }
    ST(10);
    const unsigned ring = lds0 + w * SLOT;
    unsigned kr0 = ring + (r32 >> 2) * 128 + (hh ^ (r32 >> 4)) * 64 + (r32 & 3) * 16, kr1 = kr0 ^ 128;
    unsigned vr0 = ring + (g & 1) * 1024 + (hh ^ (g & 1)) * 128 + (tp >> 1) * 64 + tq * 16 + 8 * (tp & 1), vr1 = (vr0 ^ 64) + 512;
    unsigned lo[3] = {l * 16, l * 16 + 4096, l * 16 + 8192}, pw = ring + 2 * TILE_B + l * 16, lw = l < 16 ? L_L + (l * 4 + w) * 8 : L_L + 512 + l * 8;
    OPAQUE(kr0); OPAQUE(kr1); OPAQUE(vr0); OPAQUE(vr1); OPAQUE(lo[0]); OPAQUE(lo[1]); OPAQUE(lo[2]); OPAQUE(pw); OPAQUE(lw);
    const u64 hbu = (u64)(ws + WS_H2 + (size_t)t * FRAME_B);
    const u32x4 rs = {(unsigned)hbu, (unsigned)(hbu >> 32) & 0xffffu, (unsigned)FRAME_B, 0x00020000u};
    unsigned toff[8];
#pragma unroll
    for (int i = 0; i < 8; ++i) { toff[i] = (8 * w + ((i + qb) & 7)) * TILE_B; asm volatile("" : "+s"(toff[i])); }
    u32x4 sel;
    { const unsigned one = (tp == (g & 1)) ? 0x3f803f80u : 0u; sel = u32x4{one, one, one, one}; OPAQUE(sel); }
    auto piece = [&](auto Tc, auto Pc) {
      constexpr int T = decltype(Tc)::value, p = decltype(Pc)::value;
      if constexpr (T < 8) {
        if constexpr (p % 4 == 0) setm0<(T % 3) * TILE_B + p * 1024>(ring);
        bdma<(p % 4) * 1024>(lo[p / 4], rs, toff[T]);
      }
    };
#define P6(T, a) piece(IC(T), IC(a)); piece(IC(T), IC(a + 1)); piece(IC(T), IC(a + 2)); piece(IC(T), IC(a + 3)); piece(IC(T), IC(a + 4)); piece(IC(T), IC(a + 5));
    auto read_valid = [&](u32x4 (&KK)[12], const unsigned dst, unsigned off) {
      for (int sp = 0; sp < SPIN_CAP; ++sp) {
#pragma unroll
        for (int k = 0; k < 12; ++k) KK[k] = lds_r128(((k & 1) ? kr1 : kr0) + dst + k * 1024);
        if (__builtin_expect(!poisoned(KK), 1)) break;
#pragma unroll 1
        for (int k = 0; k < 12; ++k) { setm0<0>(ring + dst + k * 1024); bdma<0>(lo[0], rs, off + k * 1024); }
        waitvm<0>();
      }
    };
    unsigned qa = lds0 + L_Q + lo[0], na = lds0 + L_N + r32 * 16;
    OPAQUE(qa); OPAQUE(na);
    waitvm<0>();
    __builtin_amdgcn_s_sleep(B9_DELAY);
    BAR();
    __builtin_amdgcn_s_sleep(TILE0_DELAY);
    if (w != (qb >> 3)) { P6(0, 0) P6(0, 6) }
    else if (l == 0) __builtin_amdgcn_raw_buffer_store_b64(tag, rsWS, 0, WS_FLAG2 + b * 8, SC1);
    ST(11);
    BAR();
    u32x4 K0[12];
    if (w != (qb >> 3)) waitvm<0>();
    ST(12);
    read_valid(K0, 0, toff[0]);
    ST(13);
    piece(IC(1), IC(0)); piece(IC(1), IC(1)); piece(IC(1), IC(2)); piece(IC(1), IC(3));
    BAR();
    ST(14);
    u32x4 Q[12];
    float mref;
    {
      const f32x4 n0 = lds_r128f(na), n1 = lds_r128f(na + 512);
#pragma unroll
      for (int s = 0; s < 6; ++s) Q[s] = lds_r128(qa + s * 1024);
      SBAR();
      const f32x4 s = n0 + n1;
      mref = __builtin_amdgcn_sqrtf(((s[0] + s[1]) + (s[2] + s[3])) * (192.0f * 1.02f));
      OPAQUE(mref);
      SBAR();
#pragma unroll
      for (int s = 6; s < 12; ++s) Q[s] = lds_r128(qa + s * 1024);
      SBAR();
    }
    constexpr int KW = 4, VW = 4;
    bool safe = false, exact = false, late = false;
#pragma unroll 1
    for (;;) {
      f32x16 S[2], O[6], negm;
      u32x4 K[12], V[12], P[2][2];
      f32x4 lsum = {0, 0, 0, 0};
      float pe[2][2];
#pragma unroll
      for (int i = 0; i < 16; ++i) negm[i] = -mref;
#define RK(k, T) K[k] = lds_r128((((k) & 1) ? kr1 : kr0) + ((T) % 3) * TILE_B + (k) * 1024);
#define RV(u, T) { const unsigned a_ = (((u) & 1) ? vr1 : vr0) + ((T) % 3) * TILE_B + ((u) >> 1) * 2048; V[u] = lds_tr2(a_, a_ + 256); }
      auto sm = [&](auto Tc, auto Hc, auto Gc) {
        constexpr int T = decltype(Tc)::value, H = decltype(Hc)::value, G = decltype(Gc)::value;
        if constexpr (G < 8) pe[(G >> 1) & 1][G & 1] = __builtin_amdgcn_exp2f(S[T & 1][8 * H + G]);
        if constexpr (G >= 2 && G <= 8 && G % 2 == 0) { constexpr int q = G / 2 - 1; P[T & 1][H][q] = cvtpk(pe[q & 1][0], pe[q & 1][1]); }
        if constexpr (G == 10) lsum = mfma16(sel, P[T & 1][H], lsum);
      };
      auto put_o = [&](const int un) {
        u32x4 pk;
#pragma unroll
        for (int e = 0; e < 4; ++e) pk[e] = cvtpk(O[un >> 1][8 * (un & 1) + 2 * e], O[un >> 1][8 * (un & 1) + 2 * e + 1]);
        lds_w128(pw + un * 1024, pk);
      };
      auto qkphase = [&](auto Ic) {
        constexpr int I = decltype(Ic)::value;
        auto step = [&](auto Kc) {
          constexpr int k = decltype(Kc)::value;
          if constexpr (I + 1 < 8) {
            S[(I + 1) & 1] = mfma32(I < 0 ? K0[k] : K[k], Q[k], k == 0 ? negm : S[(I + 1) & 1]);
            if constexpr (k + KW < 12 && I >= 0) RK(k + KW, I + 1)
          }
          if constexpr (I < 0 && k < 8) piece(IC(1), IC(4 + k));
          if constexpr (I >= 0) { if constexpr (k & 1) piece(IC(I + 2), IC(6 + k / 2)); if constexpr (k < 11) sm(IC(I), IC(1), IC(k)); }
          if constexpr (I >= 0 && I < 7 && k >= 12 - VW) RV(k - (12 - VW), I)
          SBAR();
        };
        step(IC(0)); step(IC(1)); step(IC(2)); step(IC(3)); step(IC(4)); step(IC(5)); step(IC(6)); step(IC(7)); step(IC(8)); step(IC(9)); step(IC(10)); step(IC(11));
      };
      auto pvphase = [&](auto Ic) {
        constexpr int I = decltype(Ic)::value;
        auto step = [&](auto Uc) {
          constexpr int u = decltype(Uc)::value;
          if constexpr (I >= 0) {
            f32x16 z;
#pragma unroll
            for (int e = 0; e < 16; ++e) z[e] = 0.f;
            O[u >> 1] = mfma32(V[u], P[I & 1][u & 1], (I == 0 && !(u & 1)) ? z : O[u >> 1]);
            if constexpr (u & 1) piece(IC(I + 3), IC(u / 2));
            if constexpr (u + VW < 12) RV(u + VW, I)
          }
          if constexpr (I + 1 < 8 && u >= 1) sm(IC(I + 1), IC(0), IC(u - 1));
          if constexpr (I < 0 && (u & 1)) piece(IC(2), IC(u / 2));
          if constexpr (I + 2 < 8) {
            if constexpr (u == 12 - KW) waitvm<(I < 0 ? 12 : I + 3 < 8 ? 6 + (12 - KW) / 2 : 6)>();
            if constexpr (u >= 12 - KW) RK(u - (12 - KW), I + 2)
            if constexpr (u == 11) waitvm<(I + 3 < 8 ? 6 : 0)>();
          }
          SBAR();
        };
        step(IC(0)); step(IC(1)); step(IC(2)); step(IC(3)); step(IC(4)); step(IC(5)); step(IC(6)); step(IC(7)); step(IC(8)); step(IC(9)); step(IC(10)); step(IC(11));
      };
      auto drain = [&]() {
        constexpr int tl[24] = {6, 6, 6, 6, 6, 6, 6, 6, 7, 7, 7, 7, 6, 6, 7, 7, 6, 6, 7, 7, 7, 7, 7, 7};
        constexpr int fu[24] = {0, 1, 2, 3, 4, 5, 6, 7, 0, 1, 2, 3, 8, 9, 4, 5, 10, 11, 6, 7, 8, 9, 10, 11};
        constexpr int wr[24] = {-1, -1, -1, -1, -1, -1, -1, -1, -1, -1, -1, 0, 1, 2, -1, 3, -1, 4, 5, -1, -1, 6, 7, 8};
        u32x4 X[24];
#pragma unroll
        for (int n = 0; n < VW; ++n) X[n] = V[n];
        auto step = [&](auto Nc) {
          constexpr int n = decltype(Nc)::value;
          O[fu[n] >> 1] = mfma32(X[n], P[tl[n] & 1][fu[n] & 1], O[fu[n] >> 1]);
          if constexpr (n + VW < 24) {
            const unsigned a_ = ((fu[n + VW] & 1) ? vr1 : vr0) + (tl[n + VW] % 3) * TILE_B + (fu[n + VW] >> 1) * 2048;
            X[n + VW] = lds_tr2(a_, a_ + 256);
          }
          if constexpr (n >= 1 && n <= 3) { sm(IC(7), IC(0), IC(3 * n - 3)); sm(IC(7), IC(0), IC(3 * n - 2)); sm(IC(7), IC(0), IC(3 * n - 1)); }
          if constexpr (n >= 4 && n <= 6) { sm(IC(7), IC(1), IC(3 * n - 12)); sm(IC(7), IC(1), IC(3 * n - 11)); sm(IC(7), IC(1), IC(3 * n - 10)); }
          if constexpr (n == 7) sm(IC(7), IC(0), IC(10));
          if constexpr (n == 9) sm(IC(7), IC(1), IC(10));
          if constexpr (wr[n] >= 0) put_o(wr[n]);
          SBAR();
        };
        step(IC(0)); step(IC(1)); step(IC(2)); step(IC(3)); step(IC(4)); step(IC(5)); step(IC(6)); step(IC(7)); step(IC(8)); step(IC(9)); step(IC(10)); step(IC(11));
        step(IC(12)); step(IC(13)); step(IC(14)); step(IC(15)); step(IC(16)); step(IC(17)); step(IC(18)); step(IC(19)); step(IC(20)); step(IC(21)); step(IC(22)); step(IC(23));
      };
#define STALE(v) (__builtin_expect(!safe && __any((v) != (v)), 0))
      bool done = false;
      do {
        qkphase(IC(-1));
        ST(15);
        pvphase(IC(-1));
        ST(16);
        qkphase(IC(0)); pvphase(IC(0)); qkphase(IC(1)); pvphase(IC(1)); qkphase(IC(2));
        const float seen = lsum[0];
        pvphase(IC(2));
        if (STALE(seen)) break;
        qkphase(IC(3)); pvphase(IC(3)); qkphase(IC(4)); pvphase(IC(4)); qkphase(IC(5));
        if (!late) asm volatile("s_barrier" ::: "memory");
        late = true;
        pvphase(IC(5)); qkphase(IC(6)); drain();
        if (STALE(lsum[0])) break;
        { const u32x2 ls = {asu(lsum[0]), asu(lsum[1])}; lds_w64(lw, ls); }
        put_o(9); put_o(10); put_o(11);
        done = true;
      } while (false);
#pragma unroll
      for (int k = 0; k < 12; ++k) asm volatile("" : "=v"(K0[k]));
      if (__builtin_expect(done, 1)) {
        ST(17);
        BAR();
        ST(18);
        float lt[2];
        unsigned ll = lo[0];
        OPAQUE(ll);
        ll >>= 4;
        if (__builtin_expect(!low_sum(lds_r128f(L_L + (ll & 15) * 32), lds_r128f(L_L + (ll & 15) * 32 + 16), lt) || exact, 1)) break;
        BAR();
        float mx = -3.0e38f;
#pragma unroll 1
        for (unsigned i = 0; i < 8; ++i) {
#pragma unroll 1
          for (int k = 0; k < 12; ++k) { setm0<0>(ring + k * 1024); bdma<0>(lo[0], rs, (8 * w + i) * TILE_B + k * 1024); }
          waitvm<0>();
          f32x16 s;
#pragma unroll
          for (int e = 0; e < 16; ++e) s[e] = 0.f;
#pragma unroll
          for (int k = 0; k < 12; ++k) s = mfma32(lds_r128(((k & 1) ? kr1 : kr0) + k * 1024), Q[k], s);
#pragma unroll
          for (int e = 0; e < 16; ++e) mx = fmaxf(mx, s[e]);
        }
        lds_w32(L_N + ((ll & 31) * 8 + w * 2 + (ll >> 5)) * 4, mx);
        BAR();
        const f32x4 c = lds_r128f(L_N + (ll & 31) * 32), d = lds_r128f(L_N + (ll & 31) * 32 + 16);
        mref = fmaxf(fmaxf(fmaxf(c[0], c[1]), fmaxf(c[2], c[3])), fmaxf(fmaxf(d[0], d[1]), fmaxf(d[2], d[3])));
        exact = true;
      } else {
        unsigned ll = lo[0];
        OPAQUE(ll);
        for (int sp = 0; sp < SPIN_CAP; ++sp) {
          asm volatile("" ::: "memory");
          const u32x2 f = __builtin_amdgcn_raw_buffer_load_b64(rsWS, ll >> 1, WS_FLAG2 + w * 512, SC1);
          if (__all(f[0] == tag[0] && f[1] == tag[1])) break;
        }
      }
      safe = true;
      asm volatile("s_waitcnt vmcnt(0) lgkmcnt(0)" ::: "memory");
      P6(0, 0) P6(0, 6)
      waitvm<0>();
      piece(IC(1), IC(0)); piece(IC(1), IC(1)); piece(IC(1), IC(2)); piece(IC(1), IC(3));
#pragma unroll
      for (int k = 0; k < 12; ++k) K0[k] = lds_r128(((k & 1) ? kr1 : kr0) + k * 1024);
    }
    BAR();
    {
      const auto rsO = __builtin_amdgcn_make_buffer_rsrc((void*)kc, 0, 8 * FRAME_B, 0x00020000);
      unsigned oa[3], og = lo[0] + w * 3072 + t * FRAME_B + qb * TILE_B;
#pragma unroll
      for (int i = 0; i < 3; ++i) {
        const unsigned p = i * 64 + (lo[0] >> 4), row = (p * 2731) >> 16;
        oa[i] = L_X + fi_pos(8 * w + row, p - row * 24);
        OPAQUE(oa[i]);
      }
      OPAQUE(og);
      BAR();
#pragma unroll
      for (int i = 0; i < 3; ++i) __builtin_amdgcn_raw_buffer_store_b128(lds_r128(oa[i]), rsO, og + i * 1024, 0, OUT_AUX);
    }
    ST(19);
    ST_OUT(ws, 0, 12);
  } else {
    LANE_IDS;
    FRAG_POS;
    u32x4 wq[18], wv[18];
    if (w == 3) {
      {
      __builtin_amdgcn_raw_buffer_store_b32(POISON, rsWS, l * 4, WS_ST + (t * 1024 + qb * 32) * 8, SC1);
      const unsigned p0 = WS_H2 + t * FRAME_B + (jqk ? qb * PQ : job ? 9 * (PQ - PV) + qb * PV : 384 + (qb - 32) * PI) * 1024, np = jqk ? PQ : job ? PV : PI;
#pragma unroll 1
      for (unsigned i = 0; i < np; ++i) __builtin_amdgcn_raw_buffer_store_b128(pz, rsWS, l * 16, p0 + i * 1024, SC1);
      asm volatile("s_nop 1" :: "v"(pz));
    }
      chunk(2);
      ST(1);
      if (jqk) waitvm<25 + PQ>(); else if (job) waitvm<25 + PV>(); else waitvm<25 + PI>();
      BAR();
      chunk(3);
      ST(2);
      BARV(24);
      __builtin_amdgcn_raw_buffer_store_b64(tag, rsWS, l < 8 ? l * 2048 : WS_END, WS_FLAG + b * 8, SC1);
      chunk(4); chunk(5);
      ST(3);
      BARV(49);
      BARV(24);
      BARV(0);
    } else {
      unsigned ta[2][4][2];
#pragma unroll
      for (int m = 0; m < 2; ++m)
#pragma unroll
        for (int i = 0; i < 4; ++i) {
          ta[m][i][0] = (8 * g + tq) * 768 + (((12 * m + 4 * w + i) ^ (tq | ((g & 1) << 2))) * 2 + (tp >> 1)) * 16 + 8 * (tp & 1);
          ta[m][i][1] = ta[m][i][0] + 2 * QK;
          OPAQUE(ta[m][i][0]); OPAQUE(ta[m][i][1]);
        }
      unsigned spa = hh == (i16 >> 3) ? L_SP + w * 1024 + l * 16 : L_SP + 4096 + l * 16;
      OPAQUE(spa);
      if (w == 0 && job) {
        unsigned ja[2] = {L_JOB + i16 * 384 + ((g ^ (l & 7)) * 16), L_JOB + i16 * 384 + (((4 + g) ^ (l & 7)) * 16)}, jb = L_JOB + 12288 + (8 * g + tq) * 32 + tp * 8;
        unsigned jo = (g & 1) * 512 + (hh * 16 + i16) * 16;
        OPAQUE(ja[0]); OPAQUE(ja[1]); OPAQUE(jb); OPAQUE(jo);
        waitvm<0>();
        ST(2);
        u32x4 bb[6];
        if (jqk) {
#pragma unroll
          for (int kk = 0; kk < 6; ++kk) bb[kk] = lds_r128(ja[kk & 1] + (kk >> 1) * 128 + 12288);
        } else {
#pragma unroll
          for (int kk = 0; kk < 6; ++kk) bb[kk] = lds_tr2(jb + kk * 1024, jb + kk * 1024 + 128);
        }
        f32x4 d = {0, 0, 0, 0}, e = {0, 0, 0, 0};
#pragma unroll
        for (int kk = 0; kk < 6; ++kk) {
          d = mfma16(lds_r128(ja[kk & 1] + (kk >> 1) * 128), bb[kk], d);
          e = mfma16(lds_r128(ja[kk & 1] + (kk >> 1) * 128 + 6144), bb[kk], e);
        }
        const float sc = jqk ? QSCALE : 1.0f;
        const u32x2 r0 = pack4(d * sc), r1 = pack4(e * sc);
        auto s0 = __builtin_amdgcn_permlane16_swap(r0[0], r1[0], false, false);
        auto s1 = __builtin_amdgcn_permlane16_swap(r0[1], r1[1], false, false);
        st128(u32x4{s0[0], s1[0], s0[1], s1[1]}, rsWS, jo, WS_WM + b * 1024);
        const u32x2 half = {fold_xor(r0[0] ^ r1[0], r0[1] ^ r1[1]), hh ? tag[1] : tag[0]};
        __builtin_amdgcn_raw_buffer_store_b64(half, rsWS, r32 == 0 ? hh * 8 : WS_END, WS_SEAL + b * 128, SC1);
        ST(3);
      }
      __builtin_amdgcn_s_setprio(3);
      u32x4 H[6][2];
      f32x4 acc[2][4][2];
      auto step = [&](const int n) {
        u32x4 a[8];
#pragma unroll
        for (int u = 0; u < 8; ++u) { const unsigned at = ta[u >> 2][u & 3][(n >> 1) & 1] + (n & 1) * QK; a[u] = lds_tr2(at, at + 3072); }
#pragma unroll
        for (int u = 0; u < 8; ++u) {
          acc[u >> 2][u & 3][0] = mfma16(a[u], H[n][0], n ? acc[u >> 2][u & 3][0] : f32x4{0, 0, 0, 0});
          acc[u >> 2][u & 3][1] = mfma16(a[u], H[n][1], n ? acc[u >> 2][u & 3][1] : f32x4{0, 0, 0, 0});
        }
        __builtin_amdgcn_sched_group_barrier(0x100, 8, 0);
#pragma unroll
        for (int q = 0; q < 4; ++q) { __builtin_amdgcn_sched_group_barrier(0x8, 2, 0); __builtin_amdgcn_sched_group_barrier(0x100, 2, 0); }
        SBAR();
      };
      ST(4);
      BAR();
      h_frags(H, fr, 0, 2);
      SBAR();
      step(0); step(1);
      BAR();
      h_frags(H, fr, 2, 6);
      SBAR();
      step(2);
      BAR();
      step(3);
      BAR();
      step(4);
      BAR();
      step(5);
      ST(19);
#pragma unroll
      for (int hf = 0; hf < 2; ++hf) {
        f32x4 st = {0, 0, 0, 0};
#pragma unroll
        for (int i = 0; i < 4; ++i) st = mfma16k16(pack4(acc[1][i][hf]), pack4(acc[0][i][hf]), st);
        lds_w128f(spa ^ (hf * 512), st);
      }
      waitvm<0>();
    }
    asm volatile("" :: "v"(dc), "v"(dw), "v"(dx));
    ST(10);
    BAR();
    __builtin_amdgcn_s_setprio(0);
    u32x4 seal = ld_seal(0);
    {
      ldw(wq, 0, 0, WQ_SPLIT, 0);
      u32x2 fl = __builtin_amdgcn_raw_buffer_load_b64(rsWS, l * 8, WS_FLAG + t * 2048 + w * 512, SC1);
      ST(5);
      BAR();
      ldw(wq, 0, WQ_SPLIT, 6, 0);
      if (__builtin_expect(!__all(fl[0] == tag[0] && fl[1] == tag[1]), 0)) {
        for (int sp = 0; sp < SPIN_CAP; ++sp) {
          asm volatile("" ::: "memory");
          const u32x2 f2 = __builtin_amdgcn_raw_buffer_load_b64(rsWS, l * 8, WS_FLAG + t * 2048 + w * 512, SC1);
          if (__all(f2[0] == tag[0] && f2[1] == tag[1])) break;
        }
      }
      ST(18);
      BAR();
    }
    const unsigned qt = (qb >> 3) * SLOT;
    unsigned hqa[2], own[2][2];
    hqa[0] = qt + hh * 1024 + (tq ^ hh) * 128 + (g & 1) * 64 + tp * 16; hqa[1] = (hqa[0] ^ 64) + 512;
    OPAQUE(hqa[0]); OPAQUE(hqa[1]);
    unsigned nw = lds0 + L_N + l * 16 + w * 4;
    OPAQUE(nw);
    unsigned qw = L_Q + (3 * w * 64 + hh * 32 + i16) * 16 + 8 * (g & 1);
    own[0][0] = qt + w * 3072 + (tq ^ (w & 1)) * 128 + hh * 64 + tp * 16 + 8 * (g & 1); own[0][1] = (own[0][0] ^ 64) + 512;
    own[1][0] = own[0][0] ^ 128; own[1][1] = own[0][1] ^ 128;
    OPAQUE(qw); OPAQUE(own[0][0]); OPAQUE(own[0][1]); OPAQUE(own[1][0]); OPAQUE(own[1][1]);
    if (b == 0 && w == 3 && l == 0) __builtin_amdgcn_raw_buffer_store_b64(tag, rsWS, 0, WS_EPOCH, SC1);
    for (int sp = 0; sp < SPIN_CAP && !sealed(wq, seal, l < 18, tag); ++sp) {
      asm volatile("" ::: "memory");
      seal = ld_seal(0);
      ldw(wq, 0, 0, 6, 0);
    }
    u32x4 hq[3];
    {
      const u64 hbu = (u64)(ws + WS_H2 + (size_t)t * FRAME_B);
      u32x4 rs = {(unsigned)hbu, (unsigned)(hbu >> 32) & 0xffffu, (unsigned)FRAME_B, 0x00020000u};
      asm volatile("" : "+s"(rs));
      __builtin_amdgcn_s_setprio(3);
      ST(6);
      BAR();
      ST(7);
      for (int sp = 0; sp < SPIN_CAP; ++sp) {
        setm0<0>(qt + w * 3072);
        bdma<0>(l * 16, rs, qb * TILE_B + w * 3072); bdma<1024>(l * 16, rs, qb * TILE_B + w * 3072); bdma<2048>(l * 16, rs, qb * TILE_B + w * 3072);
        waitvm<0>();
#pragma unroll
        for (int i = 0; i < 3; ++i) { const u32x2 u0 = lds_r64(own[i & 1][0] + i * 1024), u1 = lds_r64(own[i & 1][1] + i * 1024); hq[i] = u32x4{u0[0], u0[1], u1[0], u1[1]}; }
        if (!poisoned(hq)) break;
      }
    }
    ST(8);
    BAR();
    ST(9);
    {
      u32x4 bf[6][2];
      f32x4 d[3][2];
      f32x2 n[2] = {{0.f, 0.f}, {0.f, 0.f}};
      auto fin = [&](const int i) {
#pragma unroll
        for (int hf = 0; hf < 2; ++hf) {
          lds_w64(qw + i * 1024 + hf * 256, pack4(d[i][hf]));
          const f32x2 a = {d[i][hf][0], d[i][hf][1]}, c = {d[i][hf][2], d[i][hf][3]};
          n[hf] = __builtin_elementwise_fma(a, a, n[hf]); n[hf] = __builtin_elementwise_fma(c, c, n[hf]);
        }
      };
      auto mm = [&](const int i, const int ks) {
        d[i][0] = mfma16(wq[ks * 3 + i], bf[ks][0], ks ? d[i][0] : f32x4{0, 0, 0, 0});
        d[i][1] = mfma16(wq[ks * 3 + i], bf[ks][1], ks ? d[i][1] : f32x4{0, 0, 0, 0});
      };
#pragma unroll
      for (int ks = 0; ks < 3; ++ks) { bf[ks][0] = lds_r128(hqa[0] + ks * 2048); bf[ks][1] = lds_r128(hqa[1] + ks * 2048); SBAR(); }
#pragma unroll
      for (int ks = 0; ks < 3; ++ks) {
        mm(0, ks);
        bf[ks + 3][0] = lds_r128(hqa[0] + (ks + 3) * 2048); bf[ks + 3][1] = lds_r128(hqa[1] + (ks + 3) * 2048);
        SBAR();
        mm(1, ks);
        SBAR();
      }
      mm(0, 3); mm(0, 4); mm(0, 5); mm(1, 3);
      SBAR();
      mm(1, 4); mm(1, 5); mm(2, 0); fin(0);
      SBAR();
      mm(2, 1); mm(2, 2); mm(2, 3); mm(2, 4); mm(2, 5); fin(1);
      SBAR();
      fin(2);
      auto rr = __builtin_amdgcn_permlane16_swap(asu(n[0][0] + n[0][1]), asu(n[1][0] + n[1][1]), false, false);
      lds_w32(nw, asf(rr[0]) + asf(rr[1]));
    }
    SBAR();
    ST(11);
    BAR();
    __builtin_amdgcn_s_setprio(0);
    float lt[2];
    u32x4 pick;
#pragma unroll
    for (int e = 0; e < 4; ++e) {
      const unsigned d = 8 * (e >> 1) + 4 * (g & 1) + 2 * (e & 1);
      pick[e] = (i16 == d ? 0x3f80u : 0u) | (i16 == d + 1 ? 0x3f800000u : 0u);
    }
    unsigned pr[2], la = lds0 + L_L + i16 * 32;
    pr[0] = lds0 + 2 * TILE_B + hh * SLOT + 3 * w * 1024 + ((g & 1) * 32 + i16) * 16; pr[1] = pr[0] + 2 * SLOT;
    OPAQUE(pick); OPAQUE(pr[0]); OPAQUE(pr[1]); OPAQUE(la);
    f32x4 x1[3][2];
    {
      __builtin_amdgcn_s_sleep(40);
      u32x2 st[2];
      for (int sp = 0; sp < SPIN_CAP; ++sp) {
        asm volatile("" ::: "memory");
        st[0] = __builtin_amdgcn_raw_buffer_load_b64(rsWS, i16 * 8, WS_ST + (t * 1024 + qb * 32) * 8, SC1);
        st[1] = __builtin_amdgcn_raw_buffer_load_b64(rsWS, i16 * 8 + 128, WS_ST + (t * 1024 + qb * 32) * 8, SC1);
        if (!__any(max(max(st[0][0], st[0][1]), max(st[1][0], st[1][1])) == POISON)) break;
      }
#pragma unroll
      for (int hf = 0; hf < 2; ++hf) {
        const float mu = asf(st[hf][0]), sd = __builtin_amdgcn_sqrtf(asf(st[hf][1]));
#pragma unroll
        for (int i = 0; i < 3; ++i) {
          const f32x4 h = {bflo(hq[i][2 * hf]), bfhi(hq[i][2 * hf]), bflo(hq[i][2 * hf + 1]), bfhi(hq[i][2 * hf + 1])};
          x1[i][hf] = h * sd + mu;
          OPAQUE(x1[i][hf]);
        }
      }
    }
    BAR();
    ST(16);
    for (int sp = 0; sp < SPIN_CAP; ++sp) {
      asm volatile("" ::: "memory");
      seal = ld_seal(1);
      ldw(wv, 1, 0, 6, 0);
      if (sealed(wv, seal, l < 18, tag)) break;
    }
    ST(12);
#pragma unroll 1
    for (bool exact = false;; exact = true) {
      BAR();
      ST(13);
      u32x4 p[3][2][2];
#pragma unroll
      for (int i = 0; i < 3; ++i)
#pragma unroll
        for (int hf = 0; hf < 2; ++hf) { p[i][hf][0] = lds_r128(pr[0] + i * 1024 + hf * 256); p[i][hf][1] = lds_r128(pr[1] + i * 1024 + hf * 256); }
      const f32x4 l0 = lds_r128f(la), l1 = lds_r128f(la + 16);
      SBAR();
#pragma unroll
      for (int i = 0; i < 3; ++i)
#pragma unroll
        for (int hf = 0; hf < 2; ++hf) lds_w64(dp[hf] + (L_FI - L_X) + i * 512, pack4(mfma16(pick, p[i][hf][1], mfma16(pick, p[i][hf][0], f32x4{0, 0, 0, 0}))));
      if (__builtin_expect(!low_sum(l0, l1, lt) || exact, 1)) break;
      BAR();
      BAR();
    }
    ST(14);
    const float linv[2] = {__builtin_amdgcn_rcpf(lt[0]), __builtin_amdgcn_rcpf(lt[1])};
    BAR();
    ST(15);
    {
      u32x4 bf[6][2];
      f32x4 d[3][2];
      auto fin = [&](const int i) {
#pragma unroll
        for (int hf = 0; hf < 2; ++hf) lds_w64(dp[hf] + i * 512, pack4(d[i][hf] * linv[hf] + x1[i][hf]));
      };
      auto mm = [&](const int i, const int ks) {
        d[i][0] = mfma16(wv[ks * 3 + i], bf[ks][0], ks ? d[i][0] : f32x4{0, 0, 0, 0});
        d[i][1] = mfma16(wv[ks * 3 + i], bf[ks][1], ks ? d[i][1] : f32x4{0, 0, 0, 0});
      };
#pragma unroll
      for (int ks = 0; ks < 3; ++ks) { bf[ks][0] = lds_r128(fr[0] + (L_FI - L_X) + ks * 1024); bf[ks][1] = lds_r128(fr[1] + (L_FI - L_X) + ks * 1024); SBAR(); }
#pragma unroll
      for (int ks = 0; ks < 3; ++ks) {
        mm(0, ks);
        bf[ks + 3][0] = lds_r128(fr[0] + (L_FI - L_X) + (ks + 3) * 1024); bf[ks + 3][1] = lds_r128(fr[1] + (L_FI - L_X) + (ks + 3) * 1024);
        SBAR();
        mm(1, ks);
        SBAR();
      }
      mm(0, 3); mm(0, 4); mm(0, 5); mm(1, 3);
      SBAR();
      mm(1, 4); mm(1, 5); mm(2, 0); fin(0);
      SBAR();
      mm(2, 1); mm(2, 2); mm(2, 3); mm(2, 4); mm(2, 5); fin(1);
      SBAR();
      fin(2);
    }
    BAR();
    ST(17);
    ST_OUT(ws, 12, 20);
  }
}

extern "C" void kernel_launch(void* const* d_in, const int* in_sizes, int n_in, void* d_out, int out_size, void* d_ws, size_t ws_size, hipStream_t stream) {
  (void)hipFuncSetAttribute((const void*)k_timesformer, hipFuncAttributeMaxDynamicSharedMemorySize, LDS_BYTES);
  k_timesformer<<<256, 512, LDS_BYTES, stream>>>((const char*)d_in[0], (const char*)d_in[1], (const char*)d_in[2], (const char*)d_in[3], (const char*)d_in[4],
                                                 (char*)d_ws, (char*)d_out);
}
```

```cpp
#include <hip/hip_runtime.h>
#include <hip/hip_bf16.h>
#include <type_traits>

typedef short s16x4 __attribute__((ext_vector_type(4)));
typedef __bf16 b16x8 __attribute__((ext_vector_type(8)));
typedef __bf16 b16x2 __attribute__((ext_vector_type(2)));
typedef float f32x2 __attribute__((ext_vector_type(2)));
typedef float f32x4 __attribute__((ext_vector_type(4)));
typedef float f32x16 __attribute__((ext_vector_type(16)));
typedef unsigned u32x2 __attribute__((ext_vector_type(2)));
typedef unsigned u32x4 __attribute__((ext_vector_type(4)));
typedef unsigned long long u64;
typedef u64 u64x4 __attribute__((ext_vector_type(4)));
typedef u64 u64x2 __attribute__((ext_vector_type(2)));
#define LAS __attribute__((address_space(3)))
#define DI __device__ __forceinline__
#define SBAR() __builtin_amdgcn_sched_barrier(0)

constexpr int TILE_B = 12288;
constexpr int FRAME_B = 393216;
constexpr unsigned WS_H2 = 0, WS_ST = 8 * FRAME_B, WS_WM = 16 * FRAME_B, WS_SEAL = WS_WM + 3 * 73728, WS_FLAG = WS_SEAL + 216 * 128, WS_FLAG2 = WS_FLAG + 8 * 2048, WS_EPOCH = WS_FLAG2 + 2048, WS_END = WS_EPOCH + 256;
constexpr float QSCALE = 0.07216878364870322f * 1.4426950408889634f;
constexpr int SC1 = 16;
constexpr unsigned POISON = 0xffffffffu;
constexpr unsigned CODE_BYTES = 26112;
constexpr unsigned CODE_SHARE = 32;
constexpr int OUT_AUX = 16;
constexpr int TILE0_DELAY = 2, LOAD_DELAY = 2, WQ_SPLIT = 4, B9_DELAY = 2, VO_1 = 1, VO_2 = 3, MM_EARLY = 0, PQ = 12, PV = 9, PI = 23;
static_assert(9 * PQ + 18 * PV + 5 * PI >= 384, "the whole frame is poisoned");
constexpr int SPIN_CAP = 1 << 16;

#define ST_DECL
#define ST(k)
#define ST_OUT(ws, k0, k1)

DI float asf(unsigned u) { return __builtin_bit_cast(float, u); }
DI unsigned asu(float f) { return __builtin_bit_cast(unsigned, f); }
DI unsigned cvtpk(float lo, float hi) { f32x2 v = {lo, hi}; return __builtin_bit_cast(unsigned, __builtin_convertvector(v, b16x2)); }
DI float bflo(unsigned u) { return asf(u << 16); }
DI float bfhi(unsigned u) { return asf(u & 0xffff0000u); }
DI f32x16 mfma32(u32x4 a, u32x4 b, f32x16 c) { return __builtin_amdgcn_mfma_f32_32x32x16_bf16(__builtin_bit_cast(b16x8, a), __builtin_bit_cast(b16x8, b), c, 0, 0, 0); }
DI f32x4 mfma16(u32x4 a, u32x4 b, f32x4 c) { return __builtin_amdgcn_mfma_f32_16x16x32_bf16(__builtin_bit_cast(b16x8, a), __builtin_bit_cast(b16x8, b), c, 0, 0, 0); }
DI f32x4 mfma16k16(u32x2 a, u32x2 b, f32x4 c) { return __builtin_amdgcn_mfma_f32_16x16x16bf16_1k(__builtin_bit_cast(s16x4, a), __builtin_bit_cast(s16x4, b), c, 0, 0, 0); }
DI u32x2 pack4(f32x4 v) { u32x2 r = {cvtpk(v[0], v[1]), cvtpk(v[2], v[3])}; return r; }
DI u32x4 lds_r128(unsigned a) { return *(LAS u32x4*)(size_t)a; }
DI f32x4 lds_r128f(unsigned a) { return *(LAS f32x4*)(size_t)a; }
DI u32x2 lds_r64(unsigned a) { return *(LAS u32x2*)(size_t)a; }
DI void lds_w128(unsigned a, u32x4 v) { *(LAS u32x4*)(size_t)a = v; }
DI void lds_w128f(unsigned a, f32x4 v) { *(LAS f32x4*)(size_t)a = v; }
DI void lds_w64(unsigned a, u32x2 v) { *(LAS u32x2*)(size_t)a = v; }
DI void lds_w32(unsigned a, float v) { *(LAS float*)(size_t)a = v; }
DI u32x2 lds_tr(unsigned a) { return __builtin_bit_cast(u32x2, __builtin_amdgcn_ds_read_tr16_b64_v4i16((LAS s16x4*)(size_t)a)); }
DI u32x4 lds_tr2(unsigned a0, unsigned a1) { u32x2 x = lds_tr(a0), y = lds_tr(a1); u32x4 r = {x[0], x[1], y[0], y[1]}; return r; }
#define BAR() asm volatile("s_waitcnt lgkmcnt(0)\n\ts_barrier" ::: "memory")
#define BARV(n) asm volatile("s_waitcnt vmcnt(" #n ") lgkmcnt(0)\n\ts_barrier" ::: "memory")
template <int CTRL> DI float dpp_add(float v) { return v + asf(__builtin_amdgcn_update_dpp(0u, asu(v), CTRL, 0xf, 0xf, true)); }
DI float xor16_add(float v) { auto r = __builtin_amdgcn_permlane16_swap(asu(v), asu(v), false, false); return asf(r[0]) + asf(r[1]); }
DI float xor16_max(float v) { auto r = __builtin_amdgcn_permlane16_swap(asu(v), asu(v), false, false); return __builtin_amdgcn_fmed3f(asf(r[0]), asf(r[1]), __builtin_inff()); }
DI unsigned umax3(unsigned a, unsigned b, unsigned c) { return max(max(a, b), c); }
template <int N> DI bool poisoned(const u32x4 (&v)[N]) {
  unsigned m = 0;
#pragma unroll
  for (int i = 0; i < N; ++i) { m = umax3(m, v[i][0], v[i][1]); m = umax3(m, v[i][2], v[i][3]); }
  return __any(m == POISON);
}
DI u32x4 unpoison(u32x4 v) { u32x4 r = {min(v[0], POISON - 1), min(v[1], POISON - 1), min(v[2], POISON - 1), min(v[3], POISON - 1)}; return r; }
DI unsigned fi_pos(unsigned r, unsigned c8) {
  return (((r >> 4) * 6 + (c8 >> 2)) * 64 + (c8 & 3) * 16 + ((r & 15) ^ ((c8 & 1) | (((r >> 3) & 1) << 1) | ((r >> 4) << 2)))) * 16;
}
DI float row8_sum(float s) { s = dpp_add<0x124>(s); s = dpp_add<0x128>(s); return xor16_add(s); }
DI float sum24(const f32x2 (&v)[12]) {
  f32x2 s0 = v[0] + v[1], s1 = v[2] + v[3];
#pragma unroll
  for (int i = 4; i < 12; i += 2) { s0 += v[i]; s1 += v[i + 1]; }
  s0 += s1;
  return s0[0] + s0[1];
}
DI float sumsq24(const f32x2 (&v)[12]) {
  f32x2 q0 = v[0] * v[0], q1 = v[1] * v[1];
#pragma unroll
  for (int i = 2; i < 12; i += 2) { q0 = __builtin_elementwise_fma(v[i], v[i], q0); q1 = __builtin_elementwise_fma(v[i + 1], v[i + 1], q1); }
  q0 += q1;
  return q0[0] + q0[1];
}
DI f32x2 unpack2(unsigned u) { f32x2 r = {bflo(u), bfhi(u)}; return r; }

constexpr unsigned SLOT = 36864, L_X = 3 * SLOT, L_H = 4 * SLOT, L_N = L_H + TILE_B, L_L = L_N + 1024, L_PT = L_L + 1024, LDS_BYTES = 163840;
constexpr unsigned QK = 24576, L_JOB = 4 * QK;
constexpr unsigned L_VI = L_X, L_SP = L_X + TILE_B, L_YI = L_X + 20480;
constexpr unsigned L_Q = L_H, L_FI = L_H;

#define DMA3(v0, v1, v2, sbase, ldsa) \
  asm volatile("s_mov_b32 m0, %4\n\ts_nop 0\n\tglobal_load_lds_dwordx4 %0, %3\n\tglobal_load_lds_dwordx4 %1, %3 offset:1024\n\tglobal_load_lds_dwordx4 %2, %3 offset:2048" \
               :: "v"(v0), "v"(v1), "v"(v2), "s"(sbase), "s"(ldsa) : "memory")
#define DMA1(v0, sbase, ldsa) \
  asm volatile("s_mov_b32 m0, %2\n\ts_nop 0\n\tglobal_load_lds_dwordx4 %0, %1" :: "v"(v0), "s"(sbase), "s"(ldsa) : "memory")

template <int OFF> DI void bdma(unsigned voff, u32x4 rs, unsigned soff) { asm volatile("buffer_load_dwordx4 %0, %1, %2 offen offset:%3 sc1 lds" :: "v"(voff), "s"(rs), "s"(soff), "i"(OFF) : "memory"); }
template <int OFF> DI void setm0(unsigned base) { asm volatile("s_add_u32 m0, %0, %1\n\ts_nop 0" :: "s"(base), "i"(OFF) : "memory", "scc"); }
template <int N> DI void waitvm() { asm volatile("s_waitcnt vmcnt(%0)" :: "i"(N) : "memory"); }
#define IC(x) std::integral_constant<int, (x)>{}
#define OPAQUE(v) asm volatile("" : "+v"(v))
template <class R> DI void st128(u32x4 v, R rs, unsigned voff, unsigned soff) { __builtin_amdgcn_raw_buffer_store_b128(v, rs, voff, soff, SC1); asm volatile("s_nop 1" :: "v"(v)); }
template <int CTRL> DI unsigned dpp_xor(unsigned v) { return v ^ __builtin_amdgcn_update_dpp(0u, v, CTRL, 0xf, 0xf, true); }
DI unsigned fold_xor(unsigned e, unsigned o) {
  auto r = __builtin_amdgcn_permlane32_swap(e, o, false, false);
  unsigned u = r[0] ^ r[1];
  auto q = __builtin_amdgcn_permlane16_swap(u, u, false, false);
  u = q[0] ^ q[1];
  u = dpp_xor<0x128>(u); u = dpp_xor<0x124>(u); u = dpp_xor<0x4e>(u); return dpp_xor<0xb1>(u);
}
DI bool seal_ok(unsigned e, unsigned o, u32x4 seal, bool has, u32x2 tag) {
  const unsigned bad = has ? (seal[1] ^ tag[0]) | (seal[3] ^ tag[1]) : 0u;
  return __builtin_amdgcn_ballot_w64((fold_xor(e ^ seal[0], o ^ seal[2]) | bad) != 0) == 0;
}
DI unsigned xor3(unsigned a, unsigned b, unsigned c) { return __builtin_amdgcn_bitop3_b32(a, b, c, 0x96); }
#define XOR_IN(e, o, v) do { e = xor3(e, (v)[0], (v)[2]); o = xor3(o, (v)[1], (v)[3]); } while (0)
#define MIXV6() _Pragma("unroll") for (int q_ = 0; q_ < 6; ++q_) { __builtin_amdgcn_sched_group_barrier(0x8, 1, 0); __builtin_amdgcn_sched_group_barrier(0x2, 1, 0); } SBAR()
template <int N> DI bool sealed(const u32x4 (&v)[N], u32x4 seal, bool has, u32x2 tag) {
  unsigned e = 0, o = 0;
#pragma unroll
  for (int i = 0; i < N; ++i) XOR_IN(e, o, v[i]);
  return seal_ok(e, o, seal, has, tag);
}
DI bool tags_ok(u32x4 f, u32x2 tag) { return f[0] == tag[0] && f[1] == tag[1] && f[2] == tag[0] && f[3] == tag[1]; }

__global__ __launch_bounds__(512, 2) void k_timesformer(const char* x_, const char* wqkv_t_, const char* wo_t_, const char* wqkv_s_, const char* wo_s_, char* ws_, char* out_) {
  unsigned dc;
  u64x4 ka; u64x2 kb; u64 kc;
  {
    u64 pc;
    asm volatile("s_load_dwordx8 %2, %6, 0x0\n\ts_load_dwordx4 %3, %6, 0x20\n\ts_load_dwordx2 %4, %6, 0x30\n\ts_getpc_b64 %1\n\tglobal_load_dword %0, %5, %1"
                 : "=v"(dc), "=&s"(pc), "=&s"(ka), "=&s"(kb), "=&s"(kc) : "v"(min((threadIdx.x * CODE_SHARE + ((blockIdx.x >> 3) & (CODE_SHARE - 1))) * 128, CODE_BYTES)), "s"(__builtin_amdgcn_kernarg_segment_ptr()) : "memory");
  }
  constexpr unsigned lds0 = 0;
  const unsigned b = blockIdx.x;
  unsigned w8 = __builtin_amdgcn_readfirstlane(threadIdx.x >> 6);
  unsigned l = threadIdx.x & 63;
  const unsigned t = b & 7, qb = b >> 3;
  w8 = (w8 & 4) | ((w8 + qb / 6) & 3);
  ST_DECL;
  ST(0);
  const bool jqk = b < 72, job = b < 216;
  const unsigned jp = b % 72, jw = jp / 18, jks = (jp - jw * 18) / 3, jtau = jp - jw * 15 - jks * 3;
  const unsigned rot = qb % 6;
  unsigned fo[2][3];
#pragma unroll
  for (int i = 0; i < 3; ++i) {
    const unsigned p = i * 64 + l;
    if (w8 < 4) { fo[0][i] = p * 16 + (p >= 96 ? FRAME_B - 1536 : 0) + w8 * 2 * FRAME_B + b * 1536 - i * 1024; fo[1][i] = 0; }
    else if (w8 == 7) {
      const unsigned row = (p * 1366) >> 16;
      fo[0][i] = row * 1152 + (((p - row * 48) ^ (row << 1)) * 16) - i * 1024; fo[1][i] = row * 1152 + (((p - row * 48) ^ (row << 1) ^ 8) * 16) - i * 1024;
    } else {
      const unsigned row = (p * 2731) >> 16, o = row * 1152 + (((p - row * 24) ^ row) * 16) - i * 1024;
      fo[0][i] = o + jks * (32 * 1152) + (jqk ? 0 : 768);
      fo[1][i] = jqk ? o + jtau * (16 * 1152) + 384 : (l >> 1) * 384 + (l & 1) * 16 + jtau * 32 + i * (12288 - 1024);
    }
  }
  asm volatile("s_waitcnt lgkmcnt(0)" : "+s"(ka), "+s"(kb), "+s"(kc), "+v"(fo[0][0]), "+v"(fo[0][1]), "+v"(fo[0][2]), "+v"(fo[1][0]), "+v"(fo[1][1]), "+v"(fo[1][2]) :: "memory");
  char* const ws = (char*)kb[1];
  auto chunk = [&](const int n) {
    const u64 src = ka[1] + (n + rot >= 6 ? n + rot - 6 : n + rot) * (32 * 1152);
#pragma unroll
    for (int q = 0; q < 8; ++q) DMA3(fo[(q >> 1) & 1][0], fo[(q >> 1) & 1][1], fo[(q >> 1) & 1][2], src + q * 4608, (n & 3) * QK + q * 3072);
  };
  unsigned dw = 0, dx = 0;
  if (w8 < 4) {
    DMA3(fo[0][0], fo[0][1], fo[0][2], ka[0], L_YI + w8 * 3072);
  } else if (w8 == 4) {
    if (job) {
      __builtin_amdgcn_s_setprio(3);
      const u64 wqkv = b >= 144 ? ka[1] : ka[3], second = jqk ? wqkv : b >= 144 ? ka[2] : kb[0];
#pragma unroll
      for (int q = 0; q < 4; ++q) DMA3(fo[0][0], fo[0][1], fo[0][2], wqkv + q * (8 * 1152), L_JOB + q * 3072);
#pragma unroll
      for (int q = 0; q < 2; ++q) DMA3(fo[1][0], fo[1][1], fo[1][2], second + q * (jqk ? 8 * 1152 : 36864), L_JOB + 12288 + q * 3072);
    }
  } else if (w8 == 7) {
    __builtin_amdgcn_s_sleep(LOAD_DELAY);
    chunk(0); chunk(1);
  } else {
    if (w8 == 6) asm volatile("global_load_dword %0, %1, %2" : "=v"(dx) : "v"((qb * 6 + min(l, 35u) / 6) * 1152 + (min(l, 35u) % 6) * 128), "s"(ka[1]) : "memory");
    asm volatile("global_load_dword %0, %1, %2" : "=v"(dw) : "v"((qb * 18 + min(l, 17u)) * 128), "s"(w8 == 6 ? ka[2] : kb[0]) : "memory");
  }
  const auto rsWS = __builtin_amdgcn_make_buffer_rsrc((void*)ws, 0, WS_END, 0x00020000);
  const u32x4 pz = {POISON, POISON, POISON, POISON};
  u64 epoch;
  asm volatile("s_load_dwordx2 %0, %1, 0x0" : "=&s"(epoch) : "s"(ws + WS_EPOCH) : "memory");
  asm volatile("s_waitcnt lgkmcnt(0)" : "+s"(epoch) :: "memory");
  epoch += 1;
  const u32x2 tag = {(unsigned)epoch, (unsigned)(epoch >> 32)};
  asm volatile("" : "+s"(w8));
  const unsigned w = w8 & 3;
#define LANE_IDS const unsigned i16 = l & 15, g = l >> 4, r32 = l & 31, hh = l >> 5, tq = i16 >> 2, tp = l & 3
#define FRAG_POS unsigned fr[2], dp[2];                                                                       \
  fr[0] = L_X + fi_pos(i16, g); fr[1] = (fr[0] ^ 64) + 6144; dp[0] = L_X + fi_pos(i16, hh) + 8 * (g & 1) + w * 1536; dp[1] = (dp[0] ^ 64) + 6144; \
  OPAQUE(fr[0]); OPAQUE(fr[1]); OPAQUE(dp[0]); OPAQUE(dp[1])
  auto ldw = [&](u32x4 (&W)[18], const int m, const int k0, const int k1, const unsigned rot) {
#pragma unroll
    for (int kk = k0; kk < k1; ++kk) {
      const unsigned ks = kk + rot >= 6 ? kk + rot - 6 : kk + rot;
#pragma unroll
      for (int i = 0; i < 3; ++i) { SBAR(); W[kk * 3 + i] = __builtin_amdgcn_raw_buffer_load_b128(rsWS, l * 16 + i * 1024, WS_WM + m * 73728 + w * 18432 + ks * 3072, SC1); }
    }
  };
  auto ld_seal = [&](const int m) {
    return __builtin_amdgcn_raw_buffer_load_b128(rsWS, l < 18 ? l * 128 : WS_END, WS_SEAL + (m * 72 + 18 * w) * 128, SC1);
  };
  auto h_frags = [&](u32x4 (&H)[6][2], const unsigned (&fr)[2], const int k0, const int k1) {
#pragma unroll
    for (int kk = k0; kk < k1; ++kk) {
      const unsigned ks = kk + rot >= 6 ? kk + rot - 6 : kk + rot;
      H[kk][0] = lds_r128(fr[0] + ks * 1024 + (L_H - L_X)); H[kk][1] = lds_r128(fr[1] + ks * 1024 + (L_H - L_X));
    }
  };
  auto low_sum = [&](const f32x4 a, const f32x4 c, float (&lt)[2]) {
    lt[0] = (a[0] + a[2]) + (c[0] + c[2]);
    lt[1] = (a[1] + a[3]) + (c[1] + c[3]);
    return (bool)__any(lt[0] < 8.7e-19f || lt[1] < 8.7e-19f);
  };

  if (w8 < 4) {
    const unsigned nl = l & 3, j = (l >> 2) & 7, tt = 2 * w + (l >> 5), r = nl * 8 + tt;
    unsigned rp = L_X + fi_pos(r, j);
    OPAQUE(rp);
    waitvm<0>();
    asm volatile("" :: "v"(dc));
    f32x2 xv[12];
    float mu1;
    {
      u32x4 h[3], xr[3];
#pragma unroll
      for (int i = 0; i < 3; ++i) xr[i] = lds_r128(lds0 + L_YI + tt * 1536 + nl * 384 + j * 16 + i * 128);
#pragma unroll
      for (int i = 0; i < 12; ++i) xv[i] = unpack2(xr[i >> 2][i & 3]);
      mu1 = row8_sum(sum24(xv)) * (1.0f / 192.0f);
#pragma unroll
      for (int i = 0; i < 12; ++i) xv[i] -= mu1;
      const float rs = __builtin_amdgcn_rsqf(row8_sum(sumsq24(xv)) * (1.0f / 192.0f) + 1e-5f);
#pragma unroll
      for (int i = 0; i < 12; ++i) { const f32x2 n = xv[i] * rs; h[i >> 2][i & 3] = cvtpk(n[0], n[1]); }
#pragma unroll
      for (int i = 0; i < 3; ++i) lds_w128(rp + (L_H - L_X) + i * 2048, h[i]);
    }
    SBAR();
    OPAQUE(l);
    unsigned so = tt * FRAME_B + (j >> 1) * 1024 + ((t ^ ((j >> 1) & 1)) * 128) + (((j & 1) ^ (t >> 2)) * 64) + nl * 16;
    const unsigned sso = qb * TILE_B;
    LANE_IDS;
    FRAG_POS;
    unsigned sto = j == 0 ? (tt * 1024 + nl) * 8 : WS_END;
    unsigned sa = L_SP + l * 16;
    unsigned keep = (hh == (i16 >> 3)) ? ~0u : 0u;
    OPAQUE(sto); OPAQUE(so); OPAQUE(sa); OPAQUE(keep);
    ST(1);
    BAR();
    ST(6);
    {
      u32x4 H[6][2], W[18];
      BAR();
      h_frags(H, fr, 0, 6);
      asm volatile("s_barrier" ::: "memory");
      ST(2);
#pragma unroll 1
      for (int sp = 0;; ++sp) {
        const u32x4 seal = ld_seal(2);
        ldw(W, 2, 0, VO_1, rot);
        if (sp == 0) asm volatile("s_barrier" ::: "memory");
        ldw(W, 2, VO_1, VO_2, rot);
        if (sp == 0) asm volatile("s_barrier" ::: "memory");
        ldw(W, 2, VO_2, 6, rot);
        SBAR();
        f32x4 vp[3][2];
        unsigned he = 0, ho = 0;
        auto mm = [&](const int kk) {
#pragma unroll
          for (int i = 0; i < 3; ++i) {
            vp[i][0] = mfma16(H[kk][0], W[kk * 3 + i], kk ? vp[i][0] : f32x4{0, 0, 0, 0});
            vp[i][1] = mfma16(H[kk][1], W[kk * 3 + i], kk ? vp[i][1] : f32x4{0, 0, 0, 0});
            XOR_IN(he, ho, W[kk * 3 + i]);
          }
          MIXV6();
        };
        ST(3);
#pragma unroll
        for (int kk = 0; kk < MM_EARLY; ++kk) mm(kk);
        OPAQUE(he); OPAQUE(ho);
        ST(8);
        if (sp == 0) asm volatile("s_barrier" ::: "memory");
        u32x2 pt[2];
        {
          f32x4 e = (lds_r128f(sa) + lds_r128f(sa + 1024)) + lds_r128f(sa + 2048);
          const float mq = xor16_max(fmaxf(fmaxf(e[0], e[1]), fmaxf(e[2], e[3]))) * -QSCALE;
#pragma unroll
          for (int k = 0; k < 4; ++k) e[k] = __builtin_amdgcn_exp2f(fmaf(e[k], QSCALE, mq));
          pt[0] = pack4(e * __builtin_amdgcn_rcpf(xor16_add((e[0] + e[1]) + (e[2] + e[3]))));
#pragma unroll
          for (int k = 0; k < 2; ++k) {
            auto q = __builtin_amdgcn_permlane32_swap(pt[0][k], pt[0][k], false, false);
            pt[1][k] = (q[0] ^ q[1] ^ pt[0][k]) & keep;
            pt[0][k] &= keep;
          }
          OPAQUE(pt[0]); OPAQUE(pt[1]);
        }
        SBAR();
        ST(4);
#pragma unroll
        for (int kk = MM_EARLY; kk < 6; ++kk) mm(kk);
        OPAQUE(he); OPAQUE(ho);
        ST(7);
        f32x4 y[3][2];
#pragma unroll
        for (int hf = 0; hf < 2; ++hf)
#pragma unroll
          for (int i = 0; i < 3; ++i) y[i][hf] = mfma16k16(pack4(vp[i][hf]), pt[hf], f32x4{0, 0, 0, 0});
        SBAR();
#pragma unroll
        for (int hf = 0; hf < 2; ++hf)
#pragma unroll
          for (int i = 0; i < 3; ++i) lds_w64(dp[hf] + (L_YI - L_X) + i * 512, pack4(y[i][hf]));
        if (__builtin_expect(seal_ok(he, ho, seal, l < 18, tag), 1) || sp >= SPIN_CAP) break;
        asm volatile("" ::: "memory");
      }
    }
    ST(9);
    BAR();
    {
      u32x4 y[3], h[3];
#pragma unroll
      for (int i = 0; i < 3; ++i) y[i] = lds_r128(rp + (L_YI - L_X) + i * 2048);
#pragma unroll
      for (int i = 0; i < 12; ++i) xv[i] += unpack2(y[i >> 2][i & 3]);
      const float mu = row8_sum(sum24(xv)) * (1.0f / 192.0f), sq = row8_sum(sumsq24(xv));
      const float var = fmaxf(fmaf(sq, 1.0f / 192.0f, -mu * mu), 0.f) + 1e-5f;
      const float rs = __builtin_amdgcn_rsqf(var), nb = -mu * rs;
#pragma unroll
      for (int i = 0; i < 3; ++i) {
#pragma unroll
        for (int e = 0; e < 4; ++e) { const f32x2 n = xv[4 * i + e] * rs + nb; h[i][e] = cvtpk(n[0], n[1]); }
        if (i == 0) { OPAQUE(h[0]); ST(5); asm volatile("s_barrier" ::: "memory"); }
        st128(h[i], rsWS, so, WS_H2 + sso + i * 4096);
        SBAR();
      }
      const u32x2 stat = {asu(mu + mu1), asu(var)};
      __builtin_amdgcn_raw_buffer_store_b64(stat, rsWS, sto, WS_ST + b * 32, SC1);
    }
    ST(10);
    const unsigned ring = lds0 + w * SLOT;
    unsigned kr0 = ring + (r32 >> 2) * 128 + (hh ^ (r32 >> 4)) * 64 + (r32 & 3) * 16, kr1 = kr0 ^ 128;
    unsigned vr0 = ring + (g & 1) * 1024 + (hh ^ (g & 1)) * 128 + (tp >> 1) * 64 + tq * 16 + 8 * (tp & 1), vr1 = (vr0 ^ 64) + 512;
    unsigned lo[3] = {l * 16, l * 16 + 4096, l * 16 + 8192}, pw = ring + 2 * TILE_B + l * 16, lw = l < 16 ? L_L + (l * 4 + w) * 8 : L_L + 512 + l * 8;
    OPAQUE(kr0); OPAQUE(kr1); OPAQUE(vr0); OPAQUE(vr1); OPAQUE(lo[0]); OPAQUE(lo[1]); OPAQUE(lo[2]); OPAQUE(pw); OPAQUE(lw);
    const u64 hbu = (u64)(ws + WS_H2 + (size_t)t * FRAME_B);
    const u32x4 rs = {(unsigned)hbu, (unsigned)(hbu >> 32) & 0xffffu, (unsigned)FRAME_B, 0x00020000u};
    unsigned toff[8];
#pragma unroll
    for (int i = 0; i < 8; ++i) { toff[i] = (8 * w + ((i + qb) & 7)) * TILE_B; asm volatile("" : "+s"(toff[i])); }
    u32x4 sel;
    { const unsigned one = (tp == (g & 1)) ? 0x3f803f80u : 0u; sel = u32x4{one, one, one, one}; OPAQUE(sel); }
    auto piece = [&](auto Tc, auto Pc) {
      constexpr int T = decltype(Tc)::value, p = decltype(Pc)::value;
      if constexpr (T < 8) {
        if constexpr (p % 4 == 0) setm0<(T % 3) * TILE_B + p * 1024>(ring);
        bdma<(p % 4) * 1024>(lo[p / 4], rs, toff[T]);
      }
    };
#define P6(T, a) piece(IC(T), IC(a)); piece(IC(T), IC(a + 1)); piece(IC(T), IC(a + 2)); piece(IC(T), IC(a + 3)); piece(IC(T), IC(a + 4)); piece(IC(T), IC(a + 5));
    auto read_valid = [&](u32x4 (&KK)[12], const unsigned dst, unsigned off) {
      for (int sp = 0; sp < SPIN_CAP; ++sp) {
#pragma unroll
        for (int k = 0; k < 12; ++k) KK[k] = lds_r128(((k & 1) ? kr1 : kr0) + dst + k * 1024);
        if (__builtin_expect(!poisoned(KK), 1)) break;
#pragma unroll 1
        for (int k = 0; k < 12; ++k) { setm0<0>(ring + dst + k * 1024); bdma<0>(lo[0], rs, off + k * 1024); }
        waitvm<0>();
      }
    };
    unsigned qa = lds0 + L_Q + lo[0], na = lds0 + L_N + r32 * 16;
    OPAQUE(qa); OPAQUE(na);
    waitvm<0>();
    __builtin_amdgcn_s_sleep(B9_DELAY);
    BAR();
    __builtin_amdgcn_s_sleep(TILE0_DELAY);
    if (w != (qb >> 3)) { P6(0, 0) P6(0, 6) }
    else if (l == 0) __builtin_amdgcn_raw_buffer_store_b64(tag, rsWS, 0, WS_FLAG2 + b * 8, SC1);
    ST(11);
    BAR();
    u32x4 K0[12];
    if (w != (qb >> 3)) waitvm<0>();
    ST(12);
    read_valid(K0, 0, toff[0]);
    ST(13);
    piece(IC(1), IC(0)); piece(IC(1), IC(1)); piece(IC(1), IC(2)); piece(IC(1), IC(3));
    BAR();
    ST(14);
    u32x4 Q[12];
    float mref;
    {
      const f32x4 n0 = lds_r128f(na), n1 = lds_r128f(na + 512);
#pragma unroll
      for (int s = 0; s < 6; ++s) Q[s] = lds_r128(qa + s * 1024);
      SBAR();
      const f32x4 s = n0 + n1;
      mref = __builtin_amdgcn_sqrtf(((s[0] + s[1]) + (s[2] + s[3])) * (192.0f * 1.02f));
      OPAQUE(mref);
      SBAR();
#pragma unroll
      for (int s = 6; s < 12; ++s) Q[s] = lds_r128(qa + s * 1024);
      SBAR();
    }
    constexpr int KW = 4, VW = 4;
    bool safe = false, exact = false, late = false;
#pragma unroll 1
    for (;;) {
      f32x16 S[2], O[6], negm;
      u32x4 K[12], V[12], P[2][2];
      f32x4 lsum = {0, 0, 0, 0};
      float pe[2][2];
#pragma unroll
      for (int i = 0; i < 16; ++i) negm[i] = -mref;
#define RK(k, T) K[k] = lds_r128((((k) & 1) ? kr1 : kr0) + ((T) % 3) * TILE_B + (k) * 1024);
#define RV(u, T) { const unsigned a_ = (((u) & 1) ? vr1 : vr0) + ((T) % 3) * TILE_B + ((u) >> 1) * 2048; V[u] = lds_tr2(a_, a_ + 256); }
      auto sm = [&](auto Tc, auto Hc, auto Gc) {
        constexpr int T = decltype(Tc)::value, H = decltype(Hc)::value, G = decltype(Gc)::value;
        if constexpr (G < 8) pe[(G >> 1) & 1][G & 1] = __builtin_amdgcn_exp2f(S[T & 1][8 * H + G]);
        if constexpr (G >= 2 && G <= 8 && G % 2 == 0) { constexpr int q = G / 2 - 1; P[T & 1][H][q] = cvtpk(pe[q & 1][0], pe[q & 1][1]); }
        if constexpr (G == 10) lsum = mfma16(sel, P[T & 1][H], lsum);
      };
      auto put_o = [&](const int un) {
        u32x4 pk;
#pragma unroll
        for (int e = 0; e < 4; ++e) pk[e] = cvtpk(O[un >> 1][8 * (un & 1) + 2 * e], O[un >> 1][8 * (un & 1) + 2 * e + 1]);
        lds_w128(pw + un * 1024, pk);
      };
      auto qkphase = [&](auto Ic) {
        constexpr int I = decltype(Ic)::value;
        auto step = [&](auto Kc) {
          constexpr int k = decltype(Kc)::value;
          if constexpr (I + 1 < 8) {
            S[(I + 1) & 1] = mfma32(I < 0 ? K0[k] : K[k], Q[k], k == 0 ? negm : S[(I + 1) & 1]);
            if constexpr (k + KW < 12 && I >= 0) RK(k + KW, I + 1)
          }
          if constexpr (I < 0 && k < 8) piece(IC(1), IC(4 + k));
          if constexpr (I >= 0) { if constexpr (k & 1) piece(IC(I + 2), IC(6 + k / 2)); if constexpr (k < 11) sm(IC(I), IC(1), IC(k)); }
          if constexpr (I >= 0 && I < 7 && k >= 12 - VW) RV(k - (12 - VW), I)
          SBAR();
        };
        step(IC(0)); step(IC(1)); step(IC(2)); step(IC(3)); step(IC(4)); step(IC(5)); step(IC(6)); step(IC(7)); step(IC(8)); step(IC(9)); step(IC(10)); step(IC(11));
      };
      auto pvphase = [&](auto Ic) {
        constexpr int I = decltype(Ic)::value;
        auto step = [&](auto Uc) {
          constexpr int u = decltype(Uc)::value;
          if constexpr (I >= 0) {
            f32x16 z;
#pragma unroll
            for (int e = 0; e < 16; ++e) z[e] = 0.f;
            O[u >> 1] = mfma32(V[u], P[I & 1][u & 1], (I == 0 && !(u & 1)) ? z : O[u >> 1]);
            if constexpr (u & 1) piece(IC(I + 3), IC(u / 2));
            if constexpr (u + VW < 12) RV(u + VW, I)
          }
          if constexpr (I + 1 < 8 && u >= 1) sm(IC(I + 1), IC(0), IC(u - 1));
          if constexpr (I < 0 && (u & 1)) piece(IC(2), IC(u / 2));
          if constexpr (I + 2 < 8) {
            if constexpr (u == 12 - KW) waitvm<(I < 0 ? 12 : I + 3 < 8 ? 6 + (12 - KW) / 2 : 6)>();
            if constexpr (u >= 12 - KW) RK(u - (12 - KW), I + 2)
            if constexpr (u == 11) waitvm<(I + 3 < 8 ? 6 : 0)>();
          }
          SBAR();
        };
        step(IC(0)); step(IC(1)); step(IC(2)); step(IC(3)); step(IC(4)); step(IC(5)); step(IC(6)); step(IC(7)); step(IC(8)); step(IC(9)); step(IC(10)); step(IC(11));
      };
      auto drain = [&]() {
        constexpr int tl[24] = {6, 6, 6, 6, 6, 6, 6, 6, 7, 7, 7, 7, 6, 6, 7, 7, 6, 6, 7, 7, 7, 7, 7, 7};
        constexpr int fu[24] = {0, 1, 2, 3, 4, 5, 6, 7, 0, 1, 2, 3, 8, 9, 4, 5, 10, 11, 6, 7, 8, 9, 10, 11};
        constexpr int wr[24] = {-1, -1, -1, -1, -1, -1, -1, -1, -1, -1, -1, 0, 1, 2, -1, 3, -1, 4, 5, -1, -1, 6, 7, 8};
        u32x4 X[24];
#pragma unroll
        for (int n = 0; n < VW; ++n) X[n] = V[n];
        auto step = [&](auto Nc) {
          constexpr int n = decltype(Nc)::value;
          O[fu[n] >> 1] = mfma32(X[n], P[tl[n] & 1][fu[n] & 1], O[fu[n] >> 1]);
          if constexpr (n + VW < 24) {
            const unsigned a_ = ((fu[n + VW] & 1) ? vr1 : vr0) + (tl[n + VW] % 3) * TILE_B + (fu[n + VW] >> 1) * 2048;
            X[n + VW] = lds_tr2(a_, a_ + 256);
          }
          if constexpr (n >= 1 && n <= 3) { sm(IC(7), IC(0), IC(3 * n - 3)); sm(IC(7), IC(0), IC(3 * n - 2)); sm(IC(7), IC(0), IC(3 * n - 1)); }
          if constexpr (n >= 4 && n <= 6) { sm(IC(7), IC(1), IC(3 * n - 12)); sm(IC(7), IC(1), IC(3 * n - 11)); sm(IC(7), IC(1), IC(3 * n - 10)); }
          if constexpr (n == 7) sm(IC(7), IC(0), IC(10));
          if constexpr (n == 9) sm(IC(7), IC(1), IC(10));
          if constexpr (wr[n] >= 0) put_o(wr[n]);
          SBAR();
        };
        step(IC(0)); step(IC(1)); step(IC(2)); step(IC(3)); step(IC(4)); step(IC(5)); step(IC(6)); step(IC(7)); step(IC(8)); step(IC(9)); step(IC(10)); step(IC(11));
        step(IC(12)); step(IC(13)); step(IC(14)); step(IC(15)); step(IC(16)); step(IC(17)); step(IC(18)); step(IC(19)); step(IC(20)); step(IC(21)); step(IC(22)); step(IC(23));
      };
#define STALE(v) (__builtin_expect(!safe && __any((v) != (v)), 0))
      bool done = false;
      do {
        qkphase(IC(-1));
        ST(15);
        pvphase(IC(-1));
        ST(16);
        qkphase(IC(0)); pvphase(IC(0)); qkphase(IC(1)); pvphase(IC(1)); qkphase(IC(2));
        const float seen = lsum[0];
        pvphase(IC(2));
        if (STALE(seen)) break;
        qkphase(IC(3)); pvphase(IC(3)); qkphase(IC(4)); pvphase(IC(4)); qkphase(IC(5));
        if (!late) asm volatile("s_barrier" ::: "memory");
        late = true;
        pvphase(IC(5)); qkphase(IC(6)); drain();
        if (STALE(lsum[0])) break;
        { const u32x2 ls = {asu(lsum[0]), asu(lsum[1])}; lds_w64(lw, ls); }
        put_o(9); put_o(10); put_o(11);
        done = true;
      } while (false);
#pragma unroll
      for (int k = 0; k < 12; ++k) asm volatile("" : "=v"(K0[k]));
      if (__builtin_expect(done, 1)) {
        ST(17);
        BAR();
        ST(18);
        float lt[2];
        unsigned ll = lo[0];
        OPAQUE(ll);
        ll >>= 4;
        if (__builtin_expect(!low_sum(lds_r128f(L_L + (ll & 15) * 32), lds_r128f(L_L + (ll & 15) * 32 + 16), lt) || exact, 1)) break;
        BAR();
        float mx = -3.0e38f;
#pragma unroll 1
        for (unsigned i = 0; i < 8; ++i) {
#pragma unroll 1
          for (int k = 0; k < 12; ++k) { setm0<0>(ring + k * 1024); bdma<0>(lo[0], rs, (8 * w + i) * TILE_B + k * 1024); }
          waitvm<0>();
          f32x16 s;
#pragma unroll
          for (int e = 0; e < 16; ++e) s[e] = 0.f;
#pragma unroll
          for (int k = 0; k < 12; ++k) s = mfma32(lds_r128(((k & 1) ? kr1 : kr0) + k * 1024), Q[k], s);
#pragma unroll
          for (int e = 0; e < 16; ++e) mx = fmaxf(mx, s[e]);
        }
        lds_w32(L_N + ((ll & 31) * 8 + w * 2 + (ll >> 5)) * 4, mx);
        BAR();
        const f32x4 c = lds_r128f(L_N + (ll & 31) * 32), d = lds_r128f(L_N + (ll & 31) * 32 + 16);
        mref = fmaxf(fmaxf(fmaxf(c[0], c[1]), fmaxf(c[2], c[3])), fmaxf(fmaxf(d[0], d[1]), fmaxf(d[2], d[3])));
        exact = true;
      } else {
        unsigned ll = lo[0];
        OPAQUE(ll);
        for (int sp = 0; sp < SPIN_CAP; ++sp) {
          asm volatile("" ::: "memory");
          const u32x2 f = __builtin_amdgcn_raw_buffer_load_b64(rsWS, ll >> 1, WS_FLAG2 + w * 512, SC1);
          if (__all(f[0] == tag[0] && f[1] == tag[1])) break;
        }
      }
      safe = true;
      asm volatile("s_waitcnt vmcnt(0) lgkmcnt(0)" ::: "memory");
      P6(0, 0) P6(0, 6)
      waitvm<0>();
      piece(IC(1), IC(0)); piece(IC(1), IC(1)); piece(IC(1), IC(2)); piece(IC(1), IC(3));
#pragma unroll
      for (int k = 0; k < 12; ++k) K0[k] = lds_r128(((k & 1) ? kr1 : kr0) + k * 1024);
    }
    BAR();
    {
      const auto rsO = __builtin_amdgcn_make_buffer_rsrc((void*)kc, 0, 8 * FRAME_B, 0x00020000);
      unsigned oa[3], og = lo[0] + w * 3072 + t * FRAME_B + qb * TILE_B;
#pragma unroll
      for (int i = 0; i < 3; ++i) {
        const unsigned p = i * 64 + (lo[0] >> 4), row = (p * 2731) >> 16;
        oa[i] = w * 3072 + (p ^ row) * 16;
        OPAQUE(oa[i]);
      }
      OPAQUE(og);
      BAR();
#pragma unroll
      for (int i = 0; i < 3; ++i) __builtin_amdgcn_raw_buffer_store_b128(lds_r128(oa[i]), rsO, og + i * 1024, 0, OUT_AUX);
    }
    ST(19);
    ST_OUT(ws, 0, 12);
  } else {
    LANE_IDS;
    FRAG_POS;
    u32x4 wq[18], wv[18];
    if (w == 3) {
      {
      __builtin_amdgcn_raw_buffer_store_b32(POISON, rsWS, l * 4, WS_ST + (t * 1024 + qb * 32) * 8, SC1);
      const unsigned p0 = WS_H2 + t * FRAME_B + (jqk ? qb * PQ : job ? 9 * (PQ - PV) + qb * PV : 384 + (qb - 32) * PI) * 1024, np = jqk ? PQ : job ? PV : PI;
#pragma unroll 1
      for (unsigned i = 0; i < np; ++i) __builtin_amdgcn_raw_buffer_store_b128(pz, rsWS, l * 16, p0 + i * 1024, SC1);
      asm volatile("s_nop 1" :: "v"(pz));
    }
      chunk(2);
      ST(1);
      if (jqk) waitvm<25 + PQ>(); else if (job) waitvm<25 + PV>(); else waitvm<25 + PI>();
      BAR();
      chunk(3);
      ST(2);
      BARV(24);
      __builtin_amdgcn_raw_buffer_store_b64(tag, rsWS, l < 8 ? l * 2048 : WS_END, WS_FLAG + b * 8, SC1);
      chunk(4); chunk(5);
      ST(3);
      BARV(49);
      BARV(24);
      BARV(0);
    } else {
      unsigned ta[2][4][2];
#pragma unroll
      for (int m = 0; m < 2; ++m)
#pragma unroll
        for (int i = 0; i < 4; ++i) {
          ta[m][i][0] = (8 * g + tq) * 768 + (((12 * m + 4 * w + i) ^ (tq | ((g & 1) << 2))) * 2 + (tp >> 1)) * 16 + 8 * (tp & 1);
          ta[m][i][1] = ta[m][i][0] + 2 * QK;
          OPAQUE(ta[m][i][0]); OPAQUE(ta[m][i][1]);
        }
      unsigned spa = hh == (i16 >> 3) ? L_SP + w * 1024 + l * 16 : L_SP + 4096 + l * 16;
      OPAQUE(spa);
      if (w == 0 && job) {
        unsigned ja[2] = {L_JOB + i16 * 384 + ((g ^ (l & 7)) * 16), L_JOB + i16 * 384 + (((4 + g) ^ (l & 7)) * 16)}, jb = L_JOB + 12288 + (8 * g + tq) * 32 + tp * 8;
        unsigned jo = (g & 1) * 512 + (hh * 16 + i16) * 16;
        OPAQUE(ja[0]); OPAQUE(ja[1]); OPAQUE(jb); OPAQUE(jo);
        waitvm<0>();
        ST(2);
        u32x4 bb[6];
        if (jqk) {
#pragma unroll
          for (int kk = 0; kk < 6; ++kk) bb[kk] = lds_r128(ja[kk & 1] + (kk >> 1) * 128 + 12288);
        } else {
#pragma unroll
          for (int kk = 0; kk < 6; ++kk) bb[kk] = lds_tr2(jb + kk * 1024, jb + kk * 1024 + 128);
        }
        f32x4 d = {0, 0, 0, 0}, e = {0, 0, 0, 0};
#pragma unroll
        for (int kk = 0; kk < 6; ++kk) {
          d = mfma16(lds_r128(ja[kk & 1] + (kk >> 1) * 128), bb[kk], d);
          e = mfma16(lds_r128(ja[kk & 1] + (kk >> 1) * 128 + 6144), bb[kk], e);
        }
        const float sc = jqk ? QSCALE : 1.0f;
        const u32x2 r0 = pack4(d * sc), r1 = pack4(e * sc);
        auto s0 = __builtin_amdgcn_permlane16_swap(r0[0], r1[0], false, false);
        auto s1 = __builtin_amdgcn_permlane16_swap(r0[1], r1[1], false, false);
        st128(u32x4{s0[0], s1[0], s0[1], s1[1]}, rsWS, jo, WS_WM + b * 1024);
        const u32x2 half = {fold_xor(r0[0] ^ r1[0], r0[1] ^ r1[1]), hh ? tag[1] : tag[0]};
        __builtin_amdgcn_raw_buffer_store_b64(half, rsWS, r32 == 0 ? hh * 8 : WS_END, WS_SEAL + b * 128, SC1);
        ST(3);
      }
      __builtin_amdgcn_s_setprio(3);
      u32x4 H[6][2];
      f32x4 acc[2][4][2];
      auto step = [&](const int n) {
        u32x4 a[8];
#pragma unroll
        for (int u = 0; u < 8; ++u) { const unsigned at = ta[u >> 2][u & 3][(n >> 1) & 1] + (n & 1) * QK; a[u] = lds_tr2(at, at + 3072); }
#pragma unroll
        for (int u = 0; u < 8; ++u) {
          acc[u >> 2][u & 3][0] = mfma16(a[u], H[n][0], n ? acc[u >> 2][u & 3][0] : f32x4{0, 0, 0, 0});
          acc[u >> 2][u & 3][1] = mfma16(a[u], H[n][1], n ? acc[u >> 2][u & 3][1] : f32x4{0, 0, 0, 0});
        }
        __builtin_amdgcn_sched_group_barrier(0x100, 8, 0);
#pragma unroll
        for (int q = 0; q < 4; ++q) { __builtin_amdgcn_sched_group_barrier(0x8, 2, 0); __builtin_amdgcn_sched_group_barrier(0x100, 2, 0); }
        SBAR();
      };
      ST(4);
      BAR();
      h_frags(H, fr, 0, 2);
      SBAR();
      step(0); step(1);
      BAR();
      h_frags(H, fr, 2, 6);
      SBAR();
      step(2);
      BAR();
      step(3);
      BAR();
      step(4);
      BAR();
      step(5);
      ST(19);
#pragma unroll
      for (int hf = 0; hf < 2; ++hf) {
        f32x4 st = {0, 0, 0, 0};
#pragma unroll
        for (int i = 0; i < 4; ++i) st = mfma16k16(pack4(acc[1][i][hf]), pack4(acc[0][i][hf]), st);
        lds_w128f(spa ^ (hf * 512), st);
      }
      waitvm<0>();
    }
    asm volatile("" :: "v"(dc), "v"(dw), "v"(dx));
    ST(10);
    BAR();
    __builtin_amdgcn_s_setprio(0);
    u32x4 seal = ld_seal(0);
    {
      ldw(wq, 0, 0, WQ_SPLIT, 0);
      u32x2 fl = __builtin_amdgcn_raw_buffer_load_b64(rsWS, l * 8, WS_FLAG + t * 2048 + w * 512, SC1);
      ST(5);
      BAR();
      ldw(wq, 0, WQ_SPLIT, 6, 0);
      if (__builtin_expect(!__all(fl[0] == tag[0] && fl[1] == tag[1]), 0)) {
        for (int sp = 0; sp < SPIN_CAP; ++sp) {
          asm volatile("" ::: "memory");
          const u32x2 f2 = __builtin_amdgcn_raw_buffer_load_b64(rsWS, l * 8, WS_FLAG + t * 2048 + w * 512, SC1);
          if (__all(f2[0] == tag[0] && f2[1] == tag[1])) break;
        }
      }
      ST(18);
      BAR();
    }
    const unsigned qt = (qb >> 3) * SLOT;
    unsigned hqa[2], own[2][2];
    hqa[0] = qt + hh * 1024 + (tq ^ hh) * 128 + (g & 1) * 64 + tp * 16; hqa[1] = (hqa[0] ^ 64) + 512;
    OPAQUE(hqa[0]); OPAQUE(hqa[1]);
    unsigned nw = lds0 + L_N + l * 16 + w * 4;
    OPAQUE(nw);
    unsigned qw = L_Q + (3 * w * 64 + hh * 32 + i16) * 16 + 8 * (g & 1);
    own[0][0] = qt + w * 3072 + (tq ^ (w & 1)) * 128 + hh * 64 + tp * 16 + 8 * (g & 1); own[0][1] = (own[0][0] ^ 64) + 512;
    own[1][0] = own[0][0] ^ 128; own[1][1] = own[0][1] ^ 128;
    OPAQUE(qw); OPAQUE(own[0][0]); OPAQUE(own[0][1]); OPAQUE(own[1][0]); OPAQUE(own[1][1]);
    if (b == 0 && w == 3 && l == 0) __builtin_amdgcn_raw_buffer_store_b64(tag, rsWS, 0, WS_EPOCH, SC1);
    for (int sp = 0; sp < SPIN_CAP && !sealed(wq, seal, l < 18, tag); ++sp) {
      asm volatile("" ::: "memory");
      seal = ld_seal(0);
      ldw(wq, 0, 0, 6, 0);
    }
    u32x4 hq[3];
    {
      const u64 hbu = (u64)(ws + WS_H2 + (size_t)t * FRAME_B);
      u32x4 rs = {(unsigned)hbu, (unsigned)(hbu >> 32) & 0xffffu, (unsigned)FRAME_B, 0x00020000u};
      asm volatile("" : "+s"(rs));
      __builtin_amdgcn_s_setprio(3);
      ST(6);
      BAR();
      ST(7);
      for (int sp = 0; sp < SPIN_CAP; ++sp) {
        setm0<0>(qt + w * 3072);
        bdma<0>(l * 16, rs, qb * TILE_B + w * 3072); bdma<1024>(l * 16, rs, qb * TILE_B + w * 3072); bdma<2048>(l * 16, rs, qb * TILE_B + w * 3072);
        waitvm<0>();
#pragma unroll
        for (int i = 0; i < 3; ++i) { const u32x2 u0 = lds_r64(own[i & 1][0] + i * 1024), u1 = lds_r64(own[i & 1][1] + i * 1024); hq[i] = u32x4{u0[0], u0[1], u1[0], u1[1]}; }
        if (!poisoned(hq)) break;
      }
    }
    ST(8);
    BAR();
    ST(9);
    {
      u32x4 bf[6][2];
      f32x4 d[3][2];
      f32x2 n[2] = {{0.f, 0.f}, {0.f, 0.f}};
      auto fin = [&](const int i) {
#pragma unroll
        for (int hf = 0; hf < 2; ++hf) {
          lds_w64(qw + i * 1024 + hf * 256, pack4(d[i][hf]));
          const f32x2 a = {d[i][hf][0], d[i][hf][1]}, c = {d[i][hf][2], d[i][hf][3]};
          n[hf] = __builtin_elementwise_fma(a, a, n[hf]); n[hf] = __builtin_elementwise_fma(c, c, n[hf]);
        }
      };
      auto mm = [&](const int i, const int ks) {
        d[i][0] = mfma16(wq[ks * 3 + i], bf[ks][0], ks ? d[i][0] : f32x4{0, 0, 0, 0});
        d[i][1] = mfma16(wq[ks * 3 + i], bf[ks][1], ks ? d[i][1] : f32x4{0, 0, 0, 0});
      };
#pragma unroll
      for (int ks = 0; ks < 3; ++ks) { bf[ks][0] = lds_r128(hqa[0] + ks * 2048); bf[ks][1] = lds_r128(hqa[1] + ks * 2048); SBAR(); }
#pragma unroll
      for (int ks = 0; ks < 3; ++ks) {
        mm(0, ks);
        bf[ks + 3][0] = lds_r128(hqa[0] + (ks + 3) * 2048); bf[ks + 3][1] = lds_r128(hqa[1] + (ks + 3) * 2048);
        SBAR();
        mm(1, ks);
        SBAR();
      }
      mm(0, 3); mm(0, 4); mm(0, 5); mm(1, 3);
      SBAR();
      mm(1, 4); mm(1, 5); mm(2, 0); fin(0);
      SBAR();
      mm(2, 1); mm(2, 2); mm(2, 3); mm(2, 4); mm(2, 5); fin(1);
      SBAR();
      fin(2);
      auto rr = __builtin_amdgcn_permlane16_swap(asu(n[0][0] + n[0][1]), asu(n[1][0] + n[1][1]), false, false);
      lds_w32(nw, asf(rr[0]) + asf(rr[1]));
    }
    SBAR();
    ST(11);
    BAR();
    __builtin_amdgcn_s_setprio(0);
    unsigned ow[3];
#pragma unroll
    for (int i = 0; i < 3; ++i) { ow[i] = i16 * 384 + (((6 * w + 2 * i + hh) ^ (i16 & 7)) * 16) + 8 * (g & 1); OPAQUE(ow[i]); }
    float lt[2];
    u32x4 pick;
#pragma unroll
    for (int e = 0; e < 4; ++e) {
      const unsigned d = 8 * (e >> 1) + 4 * (g & 1) + 2 * (e & 1);
      pick[e] = (i16 == d ? 0x3f80u : 0u) | (i16 == d + 1 ? 0x3f800000u : 0u);
    }
    unsigned pr[2], la = lds0 + L_L + i16 * 32;
    pr[0] = lds0 + 2 * TILE_B + hh * SLOT + 3 * w * 1024 + ((g & 1) * 32 + i16) * 16; pr[1] = pr[0] + 2 * SLOT;
    OPAQUE(pick); OPAQUE(pr[0]); OPAQUE(pr[1]); OPAQUE(la);
    f32x4 x1[3][2];
    {
      __builtin_amdgcn_s_sleep(40);
      u32x2 st[2];
      for (int sp = 0; sp < SPIN_CAP; ++sp) {
        asm volatile("" ::: "memory");
        st[0] = __builtin_amdgcn_raw_buffer_load_b64(rsWS, i16 * 8, WS_ST + (t * 1024 + qb * 32) * 8, SC1);
        st[1] = __builtin_amdgcn_raw_buffer_load_b64(rsWS, i16 * 8 + 128, WS_ST + (t * 1024 + qb * 32) * 8, SC1);
        if (!__any(max(max(st[0][0], st[0][1]), max(st[1][0], st[1][1])) == POISON)) break;
      }
#pragma unroll
      for (int hf = 0; hf < 2; ++hf) {
        const float mu = asf(st[hf][0]), sd = __builtin_amdgcn_sqrtf(asf(st[hf][1]));
#pragma unroll
        for (int i = 0; i < 3; ++i) {
          const f32x4 h = {bflo(hq[i][2 * hf]), bfhi(hq[i][2 * hf]), bflo(hq[i][2 * hf + 1]), bfhi(hq[i][2 * hf + 1])};
          x1[i][hf] = h * sd + mu;
          OPAQUE(x1[i][hf]);
        }
      }
    }
    BAR();
    ST(16);
    for (int sp = 0; sp < SPIN_CAP; ++sp) {
      asm volatile("" ::: "memory");
      seal = ld_seal(1);
      ldw(wv, 1, 0, 6, 0);
      if (sealed(wv, seal, l < 18, tag)) break;
    }
    ST(12);
#pragma unroll 1
    for (bool exact = false;; exact = true) {
      BAR();
      ST(13);
      u32x4 p[3][2][2];
#pragma unroll
      for (int i = 0; i < 3; ++i)
#pragma unroll
        for (int hf = 0; hf < 2; ++hf) { p[i][hf][0] = lds_r128(pr[0] + i * 1024 + hf * 256); p[i][hf][1] = lds_r128(pr[1] + i * 1024 + hf * 256); }
      const f32x4 l0 = lds_r128f(la), l1 = lds_r128f(la + 16);
      SBAR();
#pragma unroll
      for (int i = 0; i < 3; ++i)
#pragma unroll
        for (int hf = 0; hf < 2; ++hf) lds_w64(dp[hf] + (L_FI - L_X) + i * 512, pack4(mfma16(pick, p[i][hf][1], mfma16(pick, p[i][hf][0], f32x4{0, 0, 0, 0}))));
      if (__builtin_expect(!low_sum(l0, l1, lt) || exact, 1)) break;
      BAR();
      BAR();
    }
    ST(14);
    const float linv[2] = {__builtin_amdgcn_rcpf(lt[0]), __builtin_amdgcn_rcpf(lt[1])};
    BAR();
    ST(15);
    {
      u32x4 bf[6][2];
      f32x4 d[3][2];
      auto fin = [&](const int i) {
#pragma unroll
        for (int hf = 0; hf < 2; ++hf) lds_w64(ow[i] + hf * 6144, pack4(d[i][hf] * linv[hf] + x1[i][hf]));
      };
      auto mm = [&](const int i, const int ks) {
        d[i][0] = mfma16(wv[ks * 3 + i], bf[ks][0], ks ? d[i][0] : f32x4{0, 0, 0, 0});
        d[i][1] = mfma16(wv[ks * 3 + i], bf[ks][1], ks ? d[i][1] : f32x4{0, 0, 0, 0});
      };
#pragma unroll
      for (int ks = 0; ks < 3; ++ks) { bf[ks][0] = lds_r128(fr[0] + (L_FI - L_X) + ks * 1024); bf[ks][1] = lds_r128(fr[1] + (L_FI - L_X) + ks * 1024); SBAR(); }
#pragma unroll
      for (int ks = 0; ks < 3; ++ks) {
        mm(0, ks);
        bf[ks + 3][0] = lds_r128(fr[0] + (L_FI - L_X) + (ks + 3) * 1024); bf[ks + 3][1] = lds_r128(fr[1] + (L_FI - L_X) + (ks + 3) * 1024);
        SBAR();
        mm(1, ks);
        SBAR();
      }
      mm(0, 3); mm(0, 4); mm(0, 5); mm(1, 3);
      SBAR();
      mm(1, 4); mm(1, 5); mm(2, 0); fin(0);
      SBAR();
      mm(2, 1); mm(2, 2); mm(2, 3); mm(2, 4); mm(2, 5); fin(1);
      SBAR();
      fin(2);
    }
    BAR();
    ST(17);
    ST_OUT(ws, 12, 20);
  }
}

extern "C" void kernel_launch(void* const* d_in, const int* in_sizes, int n_in, void* d_out, int out_size, void* d_ws, size_t ws_size, hipStream_t stream) {
  (void)hipFuncSetAttribute((const void*)k_timesformer, hipFuncAttributeMaxDynamicSharedMemorySize, LDS_BYTES);
  k_timesformer<<<256, 512, LDS_BYTES, stream>>>((const char*)d_in[0], (const char*)d_in[1], (const char*)d_in[2], (const char*)d_in[3], (const char*)d_in[4],
                                                 (char*)d_ws, (char*)d_out);
}
```

```cpp
#include <hip/hip_runtime.h>
#include <hip/hip_bf16.h>
#include <type_traits>

typedef short s16x4 __attribute__((ext_vector_type(4)));
typedef __bf16 b16x8 __attribute__((ext_vector_type(8)));
typedef __bf16 b16x2 __attribute__((ext_vector_type(2)));
typedef float f32x2 __attribute__((ext_vector_type(2)));
typedef float f32x4 __attribute__((ext_vector_type(4)));
typedef float f32x16 __attribute__((ext_vector_type(16)));
typedef unsigned u32x2 __attribute__((ext_vector_type(2)));
typedef unsigned u32x4 __attribute__((ext_vector_type(4)));
typedef unsigned long long u64;
typedef u64 u64x4 __attribute__((ext_vector_type(4)));
typedef u64 u64x2 __attribute__((ext_vector_type(2)));
#define LAS __attribute__((address_space(3)))
#define DI __device__ __forceinline__
#define SBAR() __builtin_amdgcn_sched_barrier(0)

constexpr int TILE_B = 12288;
constexpr int FRAME_B = 393216;
constexpr unsigned WS_H2 = 0, WS_ST = 8 * FRAME_B, WS_WM = 16 * FRAME_B, WS_SEAL = WS_WM + 3 * 73728, WS_FLAG = WS_SEAL + 216 * 128, WS_FLAG2 = WS_FLAG + 8 * 2048, WS_EPOCH = WS_FLAG2 + 2048, WS_END = WS_EPOCH + 256;
constexpr float QSCALE = 0.07216878364870322f * 1.4426950408889634f;
constexpr int SC1 = 16;
constexpr unsigned POISON = 0xffffffffu;
constexpr unsigned CODE_BYTES = 26112;
constexpr unsigned CODE_SHARE = 32;
constexpr int OUT_AUX = 16;
constexpr int TILE0_DELAY = 2, LOAD_DELAY = 2, WQ_SPLIT = 4, B9_DELAY = 2, VO_1 = 1, VO_2 = 3, MM_EARLY = 0, PQ = 12, PV = 9, PI = 23;
static_assert(9 * PQ + 18 * PV + 5 * PI >= 384, "the whole frame is poisoned");
constexpr int SPIN_CAP = 1 << 16;


constexpr int PA = 0, PB = 2, ROLE = 1; constexpr size_t WS_T0 = 32u << 20;
#define ST_DECL u64 sa_ = 0, sb_ = 0, t0_ = 0
#define ST(k) do { if ((k) == 0) { SBAR(); u64 t_; asm volatile("s_memrealtime %0\n\ts_waitcnt lgkmcnt(0)" : "=s"(t_) :: "memory"); SBAR(); \
    t0_ = t_; } STO_##k \
    if ((k) == PA || (k) == PB) { SBAR(); u64 t_; asm volatile("s_memrealtime %0\n\ts_waitcnt lgkmcnt(0)" : "=s"(t_) :: "memory"); SBAR(); if ((k) == PA) sa_ = t_; else if (sb_ == 0) sb_ = t_; } } while (0)
#define STO_(k)
#define STO_0
#define STO_1
#define STO_2
#define STO_3
#define STO_4
#define STO_5
#define STO_6
#define STO_7
#define STO_8
#define STO_9
#define STO_10
#define STO_11
#define STO_12
#define STO_13
#define STO_14
#define STO_15
#define STO_16 if (threadIdx.x == 0) __hip_atomic_store((__attribute__((address_space(1))) u64*)(ws + WS_T0) + blockIdx.x, t0_, __ATOMIC_RELAXED, __HIP_MEMORY_SCOPE_AGENT);
#define STO_17
#define STO_18
#define STO_19
#define ST_OUT(ws, k0, k1) do { if (ROLE == ((k0) != 0)) { u64 m_ = ~0ull; \
    for (int q_ = 0; q_ < 4; ++q_) { u64 v_ = __hip_atomic_load((__attribute__((address_space(1))) u64*)(ws + WS_T0) + (threadIdx.x & 63) * 4 + q_, __ATOMIC_RELAXED, __HIP_MEMORY_SCOPE_AGENT); m_ = v_ < m_ ? v_ : m_; } \
    for (int o_ = 32; o_; o_ >>= 1) { u64 v_ = __shfl_xor(m_, o_); m_ = v_ < m_ ? v_ : m_; } \
    int d_ = sb_ ? (int)(sb_ - sa_) / 1 : 0; if (d_ > 600 || !(w==0)) d_ = 0; if (d_ < 0) d_ = 0; if (d_ > 5000) d_ = 5000; \
    const u64 e_ = m_ + 4000 + 4 * d_; while (__builtin_amdgcn_s_memrealtime() < e_) __builtin_amdgcn_s_sleep(1); } } while (0)


DI float asf(unsigned u) { return __builtin_bit_cast(float, u); }
DI unsigned asu(float f) { return __builtin_bit_cast(unsigned, f); }
DI unsigned cvtpk(float lo, float hi) { f32x2 v = {lo, hi}; return __builtin_bit_cast(unsigned, __builtin_convertvector(v, b16x2)); }
DI float bflo(unsigned u) { return asf(u << 16); }
DI float bfhi(unsigned u) { return asf(u & 0xffff0000u); }
DI f32x16 mfma32(u32x4 a, u32x4 b, f32x16 c) { return __builtin_amdgcn_mfma_f32_32x32x16_bf16(__builtin_bit_cast(b16x8, a), __builtin_bit_cast(b16x8, b), c, 0, 0, 0); }
DI f32x4 mfma16(u32x4 a, u32x4 b, f32x4 c) { return __builtin_amdgcn_mfma_f32_16x16x32_bf16(__builtin_bit_cast(b16x8, a), __builtin_bit_cast(b16x8, b), c, 0, 0, 0); }
DI f32x4 mfma16k16(u32x2 a, u32x2 b, f32x4 c) { return __builtin_amdgcn_mfma_f32_16x16x16bf16_1k(__builtin_bit_cast(s16x4, a), __builtin_bit_cast(s16x4, b), c, 0, 0, 0); }
DI u32x2 pack4(f32x4 v) { u32x2 r = {cvtpk(v[0], v[1]), cvtpk(v[2], v[3])}; return r; }
DI u32x4 lds_r128(unsigned a) { return *(LAS u32x4*)(size_t)a; }
DI f32x4 lds_r128f(unsigned a) { return *(LAS f32x4*)(size_t)a; }
DI u32x2 lds_r64(unsigned a) { return *(LAS u32x2*)(size_t)a; }
DI void lds_w128(unsigned a, u32x4 v) { *(LAS u32x4*)(size_t)a = v; }
DI void lds_w128f(unsigned a, f32x4 v) { *(LAS f32x4*)(size_t)a = v; }
DI void lds_w64(unsigned a, u32x2 v) { *(LAS u32x2*)(size_t)a = v; }
DI void lds_w32(unsigned a, float v) { *(LAS float*)(size_t)a = v; }
DI u32x2 lds_tr(unsigned a) { return __builtin_bit_cast(u32x2, __builtin_amdgcn_ds_read_tr16_b64_v4i16((LAS s16x4*)(size_t)a)); }
DI u32x4 lds_tr2(unsigned a0, unsigned a1) { u32x2 x = lds_tr(a0), y = lds_tr(a1); u32x4 r = {x[0], x[1], y[0], y[1]}; return r; }
#define BAR() asm volatile("s_waitcnt lgkmcnt(0)\n\ts_barrier" ::: "memory")
#define BARV(n) asm volatile("s_waitcnt vmcnt(" #n ") lgkmcnt(0)\n\ts_barrier" ::: "memory")
template <int CTRL> DI float dpp_add(float v) { return v + asf(__builtin_amdgcn_update_dpp(0u, asu(v), CTRL, 0xf, 0xf, true)); }
DI float xor16_add(float v) { auto r = __builtin_amdgcn_permlane16_swap(asu(v), asu(v), false, false); return asf(r[0]) + asf(r[1]); }
DI float xor16_max(float v) { auto r = __builtin_amdgcn_permlane16_swap(asu(v), asu(v), false, false); return __builtin_amdgcn_fmed3f(asf(r[0]), asf(r[1]), __builtin_inff()); }
DI unsigned umax3(unsigned a, unsigned b, unsigned c) { return max(max(a, b), c); }
template <int N> DI bool poisoned(const u32x4 (&v)[N]) {
  unsigned m = 0;
#pragma unroll
  for (int i = 0; i < N; ++i) { m = umax3(m, v[i][0], v[i][1]); m = umax3(m, v[i][2], v[i][3]); }
  return __any(m == POISON);
}
DI unsigned fi_pos(unsigned r, unsigned c8) {
  return (((r >> 4) * 6 + (c8 >> 2)) * 64 + (c8 & 3) * 16 + ((r & 15) ^ ((c8 & 1) | (((r >> 3) & 1) << 1) | ((r >> 4) << 2)))) * 16;
}
DI float row8_sum(float s) { s = dpp_add<0x124>(s); s = dpp_add<0x128>(s); return xor16_add(s); }
DI float sum24(const f32x2 (&v)[12]) {
  f32x2 s0 = v[0] + v[1], s1 = v[2] + v[3];
#pragma unroll
  for (int i = 4; i < 12; i += 2) { s0 += v[i]; s1 += v[i + 1]; }
  s0 += s1;
  return s0[0] + s0[1];
}
DI float sumsq24(const f32x2 (&v)[12]) {
  f32x2 q0 = v[0] * v[0], q1 = v[1] * v[1];
#pragma unroll
  for (int i = 2; i < 12; i += 2) { q0 = __builtin_elementwise_fma(v[i], v[i], q0); q1 = __builtin_elementwise_fma(v[i + 1], v[i + 1], q1); }
  q0 += q1;
  return q0[0] + q0[1];
}
DI f32x2 unpack2(unsigned u) { f32x2 r = {bflo(u), bfhi(u)}; return r; }

constexpr unsigned SLOT = 36864, L_X = 3 * SLOT, L_H = 4 * SLOT, L_N = L_H + TILE_B, L_L = L_N + 1024, L_PT = L_L + 1024, LDS_BYTES = 163840;
constexpr unsigned QK = 24576, L_JOB = 4 * QK;
constexpr unsigned L_VI = L_X, L_SP = L_X + TILE_B, L_YI = L_X + 20480;
constexpr unsigned L_Q = L_H, L_FI = L_H;

#define DMA3(v0, v1, v2, sbase, ldsa) \
  asm volatile("s_mov_b32 m0, %4\n\ts_nop 0\n\tglobal_load_lds_dwordx4 %0, %3\n\tglobal_load_lds_dwordx4 %1, %3 offset:1024\n\tglobal_load_lds_dwordx4 %2, %3 offset:2048" \
               :: "v"(v0), "v"(v1), "v"(v2), "s"(sbase), "s"(ldsa) : "memory")

template <int OFF> DI void bdma(unsigned voff, u32x4 rs, unsigned soff) { asm volatile("buffer_load_dwordx4 %0, %1, %2 offen offset:%3 sc1 lds" :: "v"(voff), "s"(rs), "s"(soff), "i"(OFF) : "memory"); }
template <int OFF> DI void setm0(unsigned base) { asm volatile("s_add_u32 m0, %0, %1\n\ts_nop 0" :: "s"(base), "i"(OFF) : "memory", "scc"); }
template <int N> DI void waitvm() { asm volatile("s_waitcnt vmcnt(%0)" :: "i"(N) : "memory"); }
#define IC(x) std::integral_constant<int, (x)>{}
#define OPAQUE(v) asm volatile("" : "+v"(v))
template <class R> DI void st128(u32x4 v, R rs, unsigned voff, unsigned soff) { __builtin_amdgcn_raw_buffer_store_b128(v, rs, voff, soff, SC1); asm volatile("s_nop 1" :: "v"(v)); }
template <int CTRL> DI unsigned dpp_addu(unsigned v) { return v + __builtin_amdgcn_update_dpp(0u, v, CTRL, 0xf, 0xf, true); }
DI unsigned fold_sum(unsigned e, unsigned o) {
  auto r = __builtin_amdgcn_permlane32_swap(e, o, false, false);
  unsigned u = r[0] + r[1];
  auto q = __builtin_amdgcn_permlane16_swap(u, u, false, false);
  u = q[0] + q[1];
  u = dpp_addu<0x128>(u); u = dpp_addu<0x124>(u); u = dpp_addu<0x4e>(u); return dpp_addu<0xb1>(u);
}
DI bool seal_ok(unsigned e, unsigned o, u32x4 seal, bool has, u32x2 tag) {
  const unsigned bad = has ? (seal[1] ^ tag[0]) | (seal[3] ^ tag[1]) : 0u;
  return __builtin_amdgcn_ballot_w64((fold_sum(e - seal[0], o - seal[2]) | bad) != 0) == 0;
}
DI void add2(unsigned& s, unsigned a, unsigned b) { asm("v_add3_u32 %0, %0, %1, %2" : "+v"(s) : "v"(a), "v"(b)); }
#define SUM_IN(e, o, v) do { add2(e, (v)[0], (v)[2]); add2(o, (v)[1], (v)[3]); } while (0)
template <int N> DI bool sealed(const u32x4 (&v)[N], u32x4 seal, bool has, u32x2 tag) {
  unsigned e = 0, o = 0;
#pragma unroll
  for (int i = 0; i < N; ++i) SUM_IN(e, o, v[i]);
  return seal_ok(e, o, seal, has, tag);
}

__global__ __launch_bounds__(512, 2) void k_timesformer(const char* x_, const char* wqkv_t_, const char* wo_t_, const char* wqkv_s_, const char* wo_s_, char* ws_, char* out_) {
  unsigned dc;
  u64x4 ka; u64x2 kb; u64 kc;
  {
    u64 pc;
    asm volatile("s_load_dwordx8 %2, %6, 0x0\n\ts_load_dwordx4 %3, %6, 0x20\n\ts_load_dwordx2 %4, %6, 0x30\n\ts_getpc_b64 %1\n\tglobal_load_dword %0, %5, %1"
                 : "=v"(dc), "=&s"(pc), "=&s"(ka), "=&s"(kb), "=&s"(kc) : "v"(min((threadIdx.x * CODE_SHARE + ((blockIdx.x >> 3) & (CODE_SHARE - 1))) * 128, CODE_BYTES)), "s"(__builtin_amdgcn_kernarg_segment_ptr()) : "memory");
  }
  constexpr unsigned lds0 = 0;
  const unsigned b = blockIdx.x;
  unsigned w8 = __builtin_amdgcn_readfirstlane(threadIdx.x >> 6);
  unsigned l = threadIdx.x & 63;
  const unsigned t = b & 7, qb = b >> 3;
  w8 = (w8 & 4) | ((w8 + qb / 6) & 3);
  ST_DECL;
  ST(0);
  const bool jqk = b < 72, job = b < 216;
  const unsigned jm = qb / 9, jj = t * 9 + qb - jm * 9, jks = jj / 12, jtau = jj - jks * 12, jpos = jm * 72 + jtau + (jtau / 3) * 15 + jks * 3;
  const unsigned rot = qb % 6;
  unsigned fo[2][3];
#pragma unroll
  for (int i = 0; i < 3; ++i) {
    const unsigned p = i * 64 + l;
    if (w8 < 4) { fo[0][i] = p * 16 + (p >= 96 ? FRAME_B - 1536 : 0) + w8 * 2 * FRAME_B + b * 1536 - i * 1024; fo[1][i] = 0; }
    else if (w8 == 7) {
      const unsigned row = (p * 1366) >> 16;
      fo[0][i] = row * 1152 + (((p - row * 48) ^ (row << 1)) * 16) - i * 1024; fo[1][i] = row * 1152 + (((p - row * 48) ^ (row << 1) ^ 8) * 16) - i * 1024;
    } else {
      const unsigned row = (p * 2731) >> 16, o = row * 1152 + (((p - row * 24) ^ row) * 16) - i * 1024;
      fo[0][i] = o + jks * (32 * 1152) + (jqk ? 0 : 768);
      fo[1][i] = jqk ? o + jtau * (16 * 1152) + 384 : (l >> 1) * 384 + (l & 1) * 16 + jtau * 32 + i * (12288 - 1024);
    }
  }
  asm volatile("s_waitcnt lgkmcnt(0)" : "+s"(ka), "+s"(kb), "+s"(kc), "+v"(fo[0][0]), "+v"(fo[0][1]), "+v"(fo[0][2]), "+v"(fo[1][0]), "+v"(fo[1][1]), "+v"(fo[1][2]) :: "memory");
  char* const ws = (char*)kb[1];
  auto chunk = [&](const int n) {
    const u64 src = ka[1] + (n + rot >= 6 ? n + rot - 6 : n + rot) * (32 * 1152);
#pragma unroll
    for (int q = 0; q < 8; ++q) DMA3(fo[(q >> 1) & 1][0], fo[(q >> 1) & 1][1], fo[(q >> 1) & 1][2], src + q * 4608, (n & 3) * QK + q * 3072);
  };
  unsigned dw = 0, dx = 0;
  if (w8 < 4) {
    DMA3(fo[0][0], fo[0][1], fo[0][2], ka[0], L_YI + w8 * 3072);
  } else if (w8 == 4) {
    if (job) {
      __builtin_amdgcn_s_setprio(3);
      const u64 wqkv = b >= 144 ? ka[1] : ka[3], second = jqk ? wqkv : b >= 144 ? ka[2] : kb[0];
#pragma unroll
      for (int q = 0; q < 4; ++q) DMA3(fo[0][0], fo[0][1], fo[0][2], wqkv + ((q + qb) & 3) * (8 * 1152), L_JOB + ((q + qb) & 3) * 3072);
#pragma unroll
      for (int q = 0; q < 2; ++q) DMA3(fo[1][0], fo[1][1], fo[1][2], second + q * (jqk ? 8 * 1152 : 36864), L_JOB + 12288 + q * 3072);
    }
  } else if (w8 == 7) {
    __builtin_amdgcn_s_sleep(LOAD_DELAY);
    chunk(0); chunk(1);
  } else {
    if (w8 == 6) asm volatile("global_load_dword %0, %1, %2" : "=v"(dx) : "v"((qb * 6 + min(l, 35u) / 6) * 1152 + (min(l, 35u) % 6) * 128), "s"(ka[1]) : "memory");
    asm volatile("global_load_dword %0, %1, %2" : "=v"(dw) : "v"((qb * 18 + min(l, 17u)) * 128), "s"(w8 == 6 ? ka[2] : kb[0]) : "memory");
  }
  const auto rsWS = __builtin_amdgcn_make_buffer_rsrc((void*)ws, 0, WS_END, 0x00020000);
  const u32x4 pz = {POISON, POISON, POISON, POISON};
  u64 epoch;
  asm volatile("s_load_dwordx2 %0, %1, 0x0" : "=&s"(epoch) : "s"(ws + WS_EPOCH) : "memory");
  asm volatile("s_waitcnt lgkmcnt(0)" : "+s"(epoch) :: "memory");
  epoch += 1;
  const u32x2 tag = {(unsigned)epoch, (unsigned)(epoch >> 32) ^ ((unsigned)epoch * 0x9e3779b1u)};
  asm volatile("" : "+s"(w8));
  const unsigned w = w8 & 3;
#define LANE_IDS const unsigned i16 = l & 15, g = l >> 4, r32 = l & 31, hh = l >> 5, tq = i16 >> 2, tp = l & 3
#define FRAG_POS unsigned fr[2], dp[2];                                                                       \
  fr[0] = L_X + fi_pos(i16, g); fr[1] = (fr[0] ^ 64) + 6144; dp[0] = L_X + fi_pos(i16, hh) + 8 * (g & 1) + w * 1536; dp[1] = (dp[0] ^ 64) + 6144; \
  OPAQUE(fr[0]); OPAQUE(fr[1]); OPAQUE(dp[0]); OPAQUE(dp[1])
  auto ldw = [&](u32x4 (&W)[18], const int m, const int k0, const int k1, const unsigned rot) {
#pragma unroll
    for (int kk = k0; kk < k1; ++kk) {
      const unsigned ks = kk + rot >= 6 ? kk + rot - 6 : kk + rot;
#pragma unroll
      for (int i = 0; i < 3; ++i) { SBAR(); W[kk * 3 + i] = __builtin_amdgcn_raw_buffer_load_b128(rsWS, l * 16 + i * 1024, WS_WM + m * 73728 + w * 18432 + ks * 3072, SC1); }
    }
  };
  auto ld_seal = [&](const int m) {
    return __builtin_amdgcn_raw_buffer_load_b128(rsWS, l < 18 ? l * 128 : WS_END, WS_SEAL + (m * 72 + 18 * w) * 128, SC1);
  };
  auto h_frags = [&](u32x4 (&H)[6][2], const unsigned (&fr)[2], const int k0, const int k1) {
#pragma unroll
    for (int kk = k0; kk < k1; ++kk) {
      const unsigned ks = kk + rot >= 6 ? kk + rot - 6 : kk + rot;
      H[kk][0] = lds_r128(fr[0] + ks * 1024 + (L_H - L_X)); H[kk][1] = lds_r128(fr[1] + ks * 1024 + (L_H - L_X));
    }
  };
  auto low_sum = [&](const f32x4 a, const f32x4 c, float (&lt)[2]) {
    lt[0] = (a[0] + a[2]) + (c[0] + c[2]);
    lt[1] = (a[1] + a[3]) + (c[1] + c[3]);
    return (bool)__any(lt[0] < 8.7e-19f || lt[1] < 8.7e-19f);
  };

  if (w8 < 4) {
    const unsigned nl = l & 3, j = (l >> 2) & 7, tt = 2 * w + (l >> 5), r = nl * 8 + tt;
    unsigned rp = L_X + fi_pos(r, j);
    OPAQUE(rp);
    waitvm<0>();
    asm volatile("" :: "v"(dc));
    f32x2 xv[12];
    float mu1;
    {
      u32x4 h[3], xr[3];
#pragma unroll
      for (int i = 0; i < 3; ++i) xr[i] = lds_r128(lds0 + L_YI + tt * 1536 + nl * 384 + j * 16 + i * 128);
#pragma unroll
      for (int i = 0; i < 12; ++i) xv[i] = unpack2(xr[i >> 2][i & 3]);
      mu1 = row8_sum(sum24(xv)) * (1.0f / 192.0f);
#pragma unroll
      for (int i = 0; i < 12; ++i) xv[i] -= mu1;
      const float rs = __builtin_amdgcn_rsqf(row8_sum(sumsq24(xv)) * (1.0f / 192.0f) + 1e-5f);
#pragma unroll
      for (int i = 0; i < 12; ++i) { const f32x2 n = xv[i] * rs; h[i >> 2][i & 3] = cvtpk(n[0], n[1]); }
#pragma unroll
      for (int i = 0; i < 3; ++i) lds_w128(rp + (L_H - L_X) + i * 2048, h[i]);
    }
    SBAR();
    OPAQUE(l);
    unsigned so = tt * FRAME_B + (j >> 1) * 1024 + ((t ^ ((j >> 1) & 1)) * 128) + (((j & 1) ^ (t >> 2)) * 64) + nl * 16;
    const unsigned sso = qb * TILE_B;
    LANE_IDS;
    FRAG_POS;
    unsigned sto = j == 0 ? (tt * 1024 + nl) * 8 : WS_END;
    unsigned sa = L_SP + l * 16;
    unsigned keep = (hh == (i16 >> 3)) ? ~0u : 0u;
    OPAQUE(sto); OPAQUE(so); OPAQUE(sa); OPAQUE(keep);
    ST(1);
    BAR();
    ST(6);
    {
      u32x4 H[6][2], W[18];
      BAR();
      h_frags(H, fr, 0, 6);
      asm volatile("s_barrier" ::: "memory");
      ST(2);
#pragma unroll 1
      for (int sp = 0;; ++sp) {
        const u32x4 seal = ld_seal(2);
        ldw(W, 2, 0, VO_1, rot);
        if (sp == 0) asm volatile("s_barrier" ::: "memory");
        ldw(W, 2, VO_1, VO_2, rot);
        if (sp == 0) asm volatile("s_barrier" ::: "memory");
        ldw(W, 2, VO_2, 6, rot);
        SBAR();
        f32x4 vp[3][2];
        unsigned he = 0, ho = 0;
        auto mm = [&](const int kk) {
#pragma unroll
          for (int i = 0; i < 3; ++i) {
            vp[i][0] = mfma16(H[kk][0], W[kk * 3 + i], kk ? vp[i][0] : f32x4{0, 0, 0, 0});
            add2(he, W[kk * 3 + i][0], W[kk * 3 + i][2]);
            SBAR();
            vp[i][1] = mfma16(H[kk][1], W[kk * 3 + i], kk ? vp[i][1] : f32x4{0, 0, 0, 0});
            add2(ho, W[kk * 3 + i][1], W[kk * 3 + i][3]);
            SBAR();
          }
        };
        ST(3);
#pragma unroll
        for (int kk = 0; kk < MM_EARLY; ++kk) mm(kk);
        OPAQUE(he); OPAQUE(ho);
        ST(8);
        if (sp == 0) asm volatile("s_barrier" ::: "memory");
        u32x2 pt[2];
        {
          f32x4 e = (lds_r128f(sa) + lds_r128f(sa + 1024)) + lds_r128f(sa + 2048);
          const float mq = xor16_max(fmaxf(fmaxf(e[0], e[1]), fmaxf(e[2], e[3]))) * -QSCALE;
#pragma unroll
          for (int k = 0; k < 4; ++k) e[k] = __builtin_amdgcn_exp2f(fmaf(e[k], QSCALE, mq));
          pt[0] = pack4(e * __builtin_amdgcn_rcpf(xor16_add((e[0] + e[1]) + (e[2] + e[3]))));
#pragma unroll
          for (int k = 0; k < 2; ++k) {
            auto q = __builtin_amdgcn_permlane32_swap(pt[0][k], pt[0][k], false, false);
            pt[1][k] = (q[0] ^ q[1] ^ pt[0][k]) & keep;
            pt[0][k] &= keep;
          }
          OPAQUE(pt[0]); OPAQUE(pt[1]);
        }
        SBAR();
        ST(4);
#pragma unroll
        for (int kk = MM_EARLY; kk < 6; ++kk) mm(kk);
        OPAQUE(he); OPAQUE(ho);
        ST(7);
        f32x4 y[3][2];
#pragma unroll
        for (int hf = 0; hf < 2; ++hf)
#pragma unroll
          for (int i = 0; i < 3; ++i) y[i][hf] = mfma16k16(pack4(vp[i][hf]), pt[hf], f32x4{0, 0, 0, 0});
        SBAR();
#pragma unroll
        for (int hf = 0; hf < 2; ++hf)
#pragma unroll
          for (int i = 0; i < 3; ++i) lds_w64(dp[hf] + (L_YI - L_X) + i * 512, pack4(y[i][hf]));
        if (__builtin_expect(seal_ok(he, ho, seal, l < 18, tag), 1) || sp >= SPIN_CAP) break;
        asm volatile("" ::: "memory");
      }
    }
    ST(9);
    BAR();
    {
      u32x4 y[3], h[3];
#pragma unroll
      for (int i = 0; i < 3; ++i) y[i] = lds_r128(rp + (L_YI - L_X) + i * 2048);
#pragma unroll
      for (int i = 0; i < 12; ++i) xv[i] += unpack2(y[i >> 2][i & 3]);
      const float mu = row8_sum(sum24(xv)) * (1.0f / 192.0f), sq = row8_sum(sumsq24(xv));
      const float var = fmaxf(fmaf(sq, 1.0f / 192.0f, -mu * mu), 0.f) + 1e-5f;
      const float rs = __builtin_amdgcn_rsqf(var), nb = -mu * rs;
#pragma unroll
      for (int i = 0; i < 3; ++i) {
#pragma unroll
        for (int e = 0; e < 4; ++e) { const f32x2 n = xv[4 * i + e] * rs + nb; h[i][e] = cvtpk(n[0], n[1]); }
        if (i == 0) { OPAQUE(h[0]); ST(5); asm volatile("s_barrier" ::: "memory"); }
        st128(h[i], rsWS, so, WS_H2 + sso + i * 4096);
        SBAR();
      }
      const u32x2 stat = {asu(mu + mu1), asu(var)};
      __builtin_amdgcn_raw_buffer_store_b64(stat, rsWS, sto, WS_ST + b * 32, SC1);
    }
    ST(10);
    const unsigned ring = lds0 + w * SLOT;
    unsigned kr0 = ring + (r32 >> 2) * 128 + (hh ^ (r32 >> 4)) * 64 + (r32 & 3) * 16, kr1 = kr0 ^ 128;
    unsigned vr0 = ring + (g & 1) * 1024 + (hh ^ (g & 1)) * 128 + (tp >> 1) * 64 + tq * 16 + 8 * (tp & 1), vr1 = (vr0 ^ 64) + 512;
    unsigned lo[3] = {l * 16, l * 16 + 4096, l * 16 + 8192}, pw = ring + 2 * TILE_B + l * 16, lw = l < 16 ? L_L + (l * 4 + w) * 8 : L_L + 512 + l * 8;
    OPAQUE(kr0); OPAQUE(kr1); OPAQUE(vr0); OPAQUE(vr1); OPAQUE(lo[0]); OPAQUE(lo[1]); OPAQUE(lo[2]); OPAQUE(pw); OPAQUE(lw);
    const u64 hbu = (u64)(ws + WS_H2 + (size_t)t * FRAME_B);
    const u32x4 rs = {(unsigned)hbu, (unsigned)(hbu >> 32) & 0xffffu, (unsigned)FRAME_B, 0x00020000u};
    unsigned toff[8];
#pragma unroll
    for (int i = 0; i < 8; ++i) { toff[i] = (8 * w + ((i + qb) & 7)) * TILE_B; asm volatile("" : "+s"(toff[i])); }
    u32x4 sel;
    { const unsigned one = (tp == (g & 1)) ? 0x3f803f80u : 0u; sel = u32x4{one, one, one, one}; OPAQUE(sel); }
    auto piece = [&](auto Tc, auto Pc) {
      constexpr int T = decltype(Tc)::value, p = decltype(Pc)::value;
      if constexpr (T < 8) {
        if constexpr (p % 4 == 0) setm0<(T % 3) * TILE_B + p * 1024>(ring);
        bdma<(p % 4) * 1024>(lo[p / 4], rs, toff[T]);
      }
    };
#define P6(T, a) piece(IC(T), IC(a)); piece(IC(T), IC(a + 1)); piece(IC(T), IC(a + 2)); piece(IC(T), IC(a + 3)); piece(IC(T), IC(a + 4)); piece(IC(T), IC(a + 5));
    auto read_valid = [&](u32x4 (&KK)[12], const unsigned dst, unsigned off) {
      for (int sp = 0; sp < SPIN_CAP; ++sp) {
#pragma unroll
        for (int k = 0; k < 12; ++k) KK[k] = lds_r128(((k & 1) ? kr1 : kr0) + dst + k * 1024);
        if (__builtin_expect(!poisoned(KK), 1)) break;
#pragma unroll 1
        for (int k = 0; k < 12; ++k) { setm0<0>(ring + dst + k * 1024); bdma<0>(lo[0], rs, off + k * 1024); }
        waitvm<0>();
      }
    };
    unsigned qa = lds0 + L_Q + lo[0], na = lds0 + L_N + r32 * 16;
    OPAQUE(qa); OPAQUE(na);
    waitvm<0>();
    __builtin_amdgcn_s_sleep(B9_DELAY);
    BAR();
    __builtin_amdgcn_s_sleep(TILE0_DELAY);
    if (w != (qb >> 3)) { P6(0, 0) P6(0, 6) }
    else if (l == 0) __builtin_amdgcn_raw_buffer_store_b64(tag, rsWS, 0, WS_FLAG2 + b * 8, SC1);
    ST(11);
    BAR();
    u32x4 K0[12];
    if (w != (qb >> 3)) waitvm<0>();
    ST(12);
    read_valid(K0, 0, toff[0]);
    ST(13);
    piece(IC(1), IC(0)); piece(IC(1), IC(1)); piece(IC(1), IC(2)); piece(IC(1), IC(3));
    BAR();
    ST(14);
    u32x4 Q[12];
    float mref;
    {
      const f32x4 n0 = lds_r128f(na), n1 = lds_r128f(na + 512);
#pragma unroll
      for (int s = 0; s < 6; ++s) Q[s] = lds_r128(qa + s * 1024);
      SBAR();
      const f32x4 s = n0 + n1;
      mref = __builtin_amdgcn_sqrtf(((s[0] + s[1]) + (s[2] + s[3])) * (192.0f * 1.02f));
      OPAQUE(mref);
      SBAR();
#pragma unroll
      for (int s = 6; s < 12; ++s) Q[s] = lds_r128(qa + s * 1024);
      SBAR();
    }
    constexpr int KW = 4, VW = 4;
    bool safe = false, exact = false, late = false;
#pragma unroll 1
    for (;;) {
      f32x16 S[2], O[6], negm;
      u32x4 K[12], V[12], P[2][2];
      f32x4 lsum = {0, 0, 0, 0};
      float pe[2][2];
#pragma unroll
      for (int i = 0; i < 16; ++i) negm[i] = -mref;
#define RK(k, T) K[k] = lds_r128((((k) & 1) ? kr1 : kr0) + ((T) % 3) * TILE_B + (k) * 1024);
#define RV(u, T) { const unsigned a_ = (((u) & 1) ? vr1 : vr0) + ((T) % 3) * TILE_B + ((u) >> 1) * 2048; V[u] = lds_tr2(a_, a_ + 256); }
      auto sm = [&](auto Tc, auto Hc, auto Gc) {
        constexpr int T = decltype(Tc)::value, H = decltype(Hc)::value, G = decltype(Gc)::value;
        if constexpr (G < 8) pe[(G >> 1) & 1][G & 1] = __builtin_amdgcn_exp2f(S[T & 1][8 * H + G]);
        if constexpr (G >= 2 && G <= 8 && G % 2 == 0) { constexpr int q = G / 2 - 1; P[T & 1][H][q] = cvtpk(pe[q & 1][0], pe[q & 1][1]); }
        if constexpr (G == 10) lsum = mfma16(sel, P[T & 1][H], lsum);
      };
      auto put_o = [&](const int un) {
        u32x4 pk;
#pragma unroll
        for (int e = 0; e < 4; ++e) pk[e] = cvtpk(O[un >> 1][8 * (un & 1) + 2 * e], O[un >> 1][8 * (un & 1) + 2 * e + 1]);
        lds_w128(pw + un * 1024, pk);
      };
      auto qkphase = [&](auto Ic) {
        constexpr int I = decltype(Ic)::value;
        auto step = [&](auto Kc) {
          constexpr int k = decltype(Kc)::value;
          if constexpr (I + 1 < 8) {
            S[(I + 1) & 1] = mfma32(I < 0 ? K0[k] : K[k], Q[k], k == 0 ? negm : S[(I + 1) & 1]);
            if constexpr (k + KW < 12 && I >= 0) RK(k + KW, I + 1)
          }
          if constexpr (I < 0 && k < 8) piece(IC(1), IC(4 + k));
          if constexpr (I >= 0) { if constexpr (k & 1) piece(IC(I + 2), IC(6 + k / 2)); if constexpr (k < 11) sm(IC(I), IC(1), IC(k)); }
          if constexpr (I >= 0 && I < 7 && k >= 12 - VW) RV(k - (12 - VW), I)
          SBAR();
        };
        step(IC(0)); step(IC(1)); step(IC(2)); step(IC(3)); step(IC(4)); step(IC(5)); step(IC(6)); step(IC(7)); step(IC(8)); step(IC(9)); step(IC(10)); step(IC(11));
      };
      auto pvphase = [&](auto Ic) {
        constexpr int I = decltype(Ic)::value;
        auto step = [&](auto Uc) {
          constexpr int u = decltype(Uc)::value;
          if constexpr (I >= 0) {
            f32x16 z;
#pragma unroll
            for (int e = 0; e < 16; ++e) z[e] = 0.f;
            O[u >> 1] = mfma32(V[u], P[I & 1][u & 1], (I == 0 && !(u & 1)) ? z : O[u >> 1]);
            if constexpr (u & 1) piece(IC(I + 3), IC(u / 2));
            if constexpr (u + VW < 12) RV(u + VW, I)
          }
          if constexpr (I + 1 < 8 && u >= 1) sm(IC(I + 1), IC(0), IC(u - 1));
          if constexpr (I < 0 && (u & 1)) piece(IC(2), IC(u / 2));
          if constexpr (I + 2 < 8) {
            if constexpr (u == 12 - KW) waitvm<(I < 0 ? 12 : I + 3 < 8 ? 6 + (12 - KW) / 2 : 6)>();
            if constexpr (u >= 12 - KW) RK(u - (12 - KW), I + 2)
            if constexpr (u == 11) waitvm<(I + 3 < 8 ? 6 : 0)>();
          }
          SBAR();
        };
        step(IC(0)); step(IC(1)); step(IC(2)); step(IC(3)); step(IC(4)); step(IC(5)); step(IC(6)); step(IC(7)); step(IC(8)); step(IC(9)); step(IC(10)); step(IC(11));
      };
      auto drain = [&]() {
        constexpr int tl[24] = {6, 6, 6, 6, 6, 6, 6, 6, 7, 7, 7, 7, 6, 6, 7, 7, 6, 6, 7, 7, 7, 7, 7, 7};
        constexpr int fu[24] = {0, 1, 2, 3, 4, 5, 6, 7, 0, 1, 2, 3, 8, 9, 4, 5, 10, 11, 6, 7, 8, 9, 10, 11};
        constexpr int wr[24] = {-1, -1, -1, -1, -1, -1, -1, -1, -1, -1, -1, 0, 1, 2, -1, 3, -1, 4, 5, -1, -1, 6, 7, 8};
        u32x4 X[24];
#pragma unroll
        for (int n = 0; n < VW; ++n) X[n] = V[n];
        auto step = [&](auto Nc) {
          constexpr int n = decltype(Nc)::value;
          O[fu[n] >> 1] = mfma32(X[n], P[tl[n] & 1][fu[n] & 1], O[fu[n] >> 1]);
          if constexpr (n + VW < 24) {
            const unsigned a_ = ((fu[n + VW] & 1) ? vr1 : vr0) + (tl[n + VW] % 3) * TILE_B + (fu[n + VW] >> 1) * 2048;
            X[n + VW] = lds_tr2(a_, a_ + 256);
          }
          if constexpr (n >= 1 && n <= 3) { sm(IC(7), IC(0), IC(3 * n - 3)); sm(IC(7), IC(0), IC(3 * n - 2)); sm(IC(7), IC(0), IC(3 * n - 1)); }
          if constexpr (n >= 4 && n <= 6) { sm(IC(7), IC(1), IC(3 * n - 12)); sm(IC(7), IC(1), IC(3 * n - 11)); sm(IC(7), IC(1), IC(3 * n - 10)); }
          if constexpr (n == 7) sm(IC(7), IC(0), IC(10));
          if constexpr (n == 9) sm(IC(7), IC(1), IC(10));
          if constexpr (wr[n] >= 0) put_o(wr[n]);
          SBAR();
        };
        step(IC(0)); step(IC(1)); step(IC(2)); step(IC(3)); step(IC(4)); step(IC(5)); step(IC(6)); step(IC(7)); step(IC(8)); step(IC(9)); step(IC(10)); step(IC(11));
        step(IC(12)); step(IC(13)); step(IC(14)); step(IC(15)); step(IC(16)); step(IC(17)); step(IC(18)); step(IC(19)); step(IC(20)); step(IC(21)); step(IC(22)); step(IC(23));
      };
#define STALE(v) (__builtin_expect(!safe && __any((v) != (v)), 0))
      bool done = false;
      do {
        qkphase(IC(-1));
        ST(15);
        pvphase(IC(-1));
        ST(16);
        qkphase(IC(0)); pvphase(IC(0)); qkphase(IC(1)); pvphase(IC(1)); qkphase(IC(2));
        const float seen = lsum[0];
        pvphase(IC(2));
        if (STALE(seen)) break;
        qkphase(IC(3)); pvphase(IC(3)); qkphase(IC(4)); pvphase(IC(4)); qkphase(IC(5));
        if (!late) asm volatile("s_barrier" ::: "memory");
        late = true;
        pvphase(IC(5)); qkphase(IC(6)); drain();
        if (STALE(lsum[0])) break;
        { const u32x2 ls = {asu(lsum[0]), asu(lsum[1])}; lds_w64(lw, ls); }
        put_o(9); put_o(10); put_o(11);
        done = true;
      } while (false);
#pragma unroll
      for (int k = 0; k < 12; ++k) asm volatile("" : "=v"(K0[k]));
      if (__builtin_expect(done, 1)) {
        ST(17);
        BAR();
        ST(18);
        float lt[2];
        unsigned ll = lo[0];
        OPAQUE(ll);
        ll >>= 4;
        if (__builtin_expect(!low_sum(lds_r128f(L_L + (ll & 15) * 32), lds_r128f(L_L + (ll & 15) * 32 + 16), lt) || exact, 1)) break;
        BAR();
        float mx = -3.0e38f;
#pragma unroll 1
        for (unsigned i = 0; i < 8; ++i) {
#pragma unroll 1
          for (int k = 0; k < 12; ++k) { setm0<0>(ring + k * 1024); bdma<0>(lo[0], rs, (8 * w + i) * TILE_B + k * 1024); }
          waitvm<0>();
          f32x16 s;
#pragma unroll
          for (int e = 0; e < 16; ++e) s[e] = 0.f;
#pragma unroll
          for (int k = 0; k < 12; ++k) s = mfma32(lds_r128(((k & 1) ? kr1 : kr0) + k * 1024), Q[k], s);
#pragma unroll
          for (int e = 0; e < 16; ++e) mx = fmaxf(mx, s[e]);
        }
        lds_w32(L_N + ((ll & 31) * 8 + w * 2 + (ll >> 5)) * 4, mx);
        BAR();
        const f32x4 c = lds_r128f(L_N + (ll & 31) * 32), d = lds_r128f(L_N + (ll & 31) * 32 + 16);
        mref = fmaxf(fmaxf(fmaxf(c[0], c[1]), fmaxf(c[2], c[3])), fmaxf(fmaxf(d[0], d[1]), fmaxf(d[2], d[3])));
        exact = true;
      } else {
        unsigned ll = lo[0];
        OPAQUE(ll);
        for (int sp = 0; sp < SPIN_CAP; ++sp) {
          asm volatile("" ::: "memory");
          const u32x2 f = __builtin_amdgcn_raw_buffer_load_b64(rsWS, ll >> 1, WS_FLAG2 + w * 512, SC1);
          if (__all(f[0] == tag[0] && f[1] == tag[1])) break;
        }
      }
      safe = true;
      asm volatile("s_waitcnt vmcnt(0) lgkmcnt(0)" ::: "memory");
      P6(0, 0) P6(0, 6)
      waitvm<0>();
      piece(IC(1), IC(0)); piece(IC(1), IC(1)); piece(IC(1), IC(2)); piece(IC(1), IC(3));
#pragma unroll
      for (int k = 0; k < 12; ++k) K0[k] = lds_r128(((k & 1) ? kr1 : kr0) + k * 1024);
    }
    BAR();
    {
      const auto rsO = __builtin_amdgcn_make_buffer_rsrc((void*)kc, 0, 8 * FRAME_B, 0x00020000);
      unsigned oa[3], og = lo[0] + w * 3072 + t * FRAME_B + qb * TILE_B;
#pragma unroll
      for (int i = 0; i < 3; ++i) {
        const unsigned p = i * 64 + (lo[0] >> 4), row = (p * 2731) >> 16;
        oa[i] = w * 3072 + (p ^ row) * 16;
        OPAQUE(oa[i]);
      }
      OPAQUE(og);
      BAR();
#pragma unroll
      for (int i = 0; i < 3; ++i) __builtin_amdgcn_raw_buffer_store_b128(lds_r128(oa[i]), rsO, og + i * 1024, 0, OUT_AUX);
    }
    ST(19);
    ST_OUT(ws, 0, 12);
  } else {
    LANE_IDS;
    FRAG_POS;
    u32x4 wq[18], wv[18];
    if (w == 3) {
      {
      __builtin_amdgcn_raw_buffer_store_b32(POISON, rsWS, l * 4, WS_ST + (t * 1024 + qb * 32) * 8, SC1);
      const unsigned p0 = WS_H2 + t * FRAME_B + (jqk ? qb * PQ : job ? 9 * (PQ - PV) + qb * PV : 384 + (qb - 32) * PI) * 1024, np = jqk ? PQ : job ? PV : PI;
#pragma unroll 1
      for (unsigned i = 0; i < np; ++i) __builtin_amdgcn_raw_buffer_store_b128(pz, rsWS, l * 16, p0 + i * 1024, SC1);
      asm volatile("s_nop 1" :: "v"(pz));
    }
      chunk(2);
      ST(1);
      if (jqk) waitvm<25 + PQ>(); else if (job) waitvm<25 + PV>(); else waitvm<25 + PI>();
      BAR();
      chunk(3);
      ST(2);
      BARV(24);
      __builtin_amdgcn_raw_buffer_store_b64(tag, rsWS, l < 8 ? l * 2048 : WS_END, WS_FLAG + b * 8, SC1);
      chunk(4); chunk(5);
      ST(3);
      BARV(49);
      BARV(24);
      BARV(0);
    } else {
      unsigned ta[2][4][2];
#pragma unroll
      for (int m = 0; m < 2; ++m)
#pragma unroll
        for (int i = 0; i < 4; ++i) {
          ta[m][i][0] = (8 * g + tq) * 768 + (((12 * m + 4 * w + i) ^ (tq | ((g & 1) << 2))) * 2 + (tp >> 1)) * 16 + 8 * (tp & 1);
          ta[m][i][1] = ta[m][i][0] + 2 * QK;
          OPAQUE(ta[m][i][0]); OPAQUE(ta[m][i][1]);
        }
      unsigned spa = hh == (i16 >> 3) ? L_SP + w * 1024 + l * 16 : L_SP + 4096 + l * 16;
      OPAQUE(spa);
      if (w == 0 && job) {
        unsigned ja[2] = {L_JOB + i16 * 384 + ((g ^ (l & 7)) * 16), L_JOB + i16 * 384 + (((4 + g) ^ (l & 7)) * 16)}, jb = L_JOB + 12288 + (8 * g + tq) * 32 + tp * 8;
        unsigned jo = (g & 1) * 512 + (hh * 16 + i16) * 16;
        OPAQUE(ja[0]); OPAQUE(ja[1]); OPAQUE(jb); OPAQUE(jo);
        waitvm<0>();
        ST(2);
        u32x4 bb[6];
        if (jqk) {
#pragma unroll
          for (int kk = 0; kk < 6; ++kk) bb[kk] = lds_r128(ja[kk & 1] + (kk >> 1) * 128 + 12288);
        } else {
#pragma unroll
          for (int kk = 0; kk < 6; ++kk) bb[kk] = lds_tr2(jb + kk * 1024, jb + kk * 1024 + 128);
        }
        f32x4 d = {0, 0, 0, 0}, e = {0, 0, 0, 0};
#pragma unroll
        for (int kk = 0; kk < 6; ++kk) {
          d = mfma16(lds_r128(ja[kk & 1] + (kk >> 1) * 128), bb[kk], d);
          e = mfma16(lds_r128(ja[kk & 1] + (kk >> 1) * 128 + 6144), bb[kk], e);
        }
        const float sc = jqk ? QSCALE : 1.0f;
        const u32x2 r0 = pack4(d * sc), r1 = pack4(e * sc);
        auto s0 = __builtin_amdgcn_permlane16_swap(r0[0], r1[0], false, false);
        auto s1 = __builtin_amdgcn_permlane16_swap(r0[1], r1[1], false, false);
        st128(u32x4{s0[0], s1[0], s0[1], s1[1]}, rsWS, jo, WS_WM + jpos * 1024);
        const u32x2 half = {fold_sum(r0[0] + r1[0], r0[1] + r1[1]), hh ? tag[1] : tag[0]};
        __builtin_amdgcn_raw_buffer_store_b64(half, rsWS, r32 == 0 ? hh * 8 : WS_END, WS_SEAL + jpos * 128, SC1);
        ST(3);
      }
      __builtin_amdgcn_s_setprio(3);
      u32x4 H[6][2];
      f32x4 acc[2][4][2];
      auto step = [&](const int n) {
        u32x4 a[8];
#pragma unroll
        for (int u = 0; u < 8; ++u) { const unsigned at = ta[u >> 2][u & 3][(n >> 1) & 1] + (n & 1) * QK; a[u] = lds_tr2(at, at + 3072); }
#pragma unroll
        for (int u = 0; u < 8; ++u) {
          acc[u >> 2][u & 3][0] = mfma16(a[u], H[n][0], n ? acc[u >> 2][u & 3][0] : f32x4{0, 0, 0, 0});
          acc[u >> 2][u & 3][1] = mfma16(a[u], H[n][1], n ? acc[u >> 2][u & 3][1] : f32x4{0, 0, 0, 0});
        }
        __builtin_amdgcn_sched_group_barrier(0x100, 8, 0);
#pragma unroll
        for (int q = 0; q < 4; ++q) { __builtin_amdgcn_sched_group_barrier(0x8, 2, 0); __builtin_amdgcn_sched_group_barrier(0x100, 2, 0); }
        SBAR();
      };
      ST(4);
      BAR();
      h_frags(H, fr, 0, 2);
      SBAR();
      step(0); step(1);
      BAR();
      h_frags(H, fr, 2, 6);
      SBAR();
      step(2);
      BAR();
      step(3);
      BAR();
      step(4);
      BAR();
      step(5);
      ST(19);
#pragma unroll
      for (int hf = 0; hf < 2; ++hf) {
        f32x4 st = {0, 0, 0, 0};
#pragma unroll
        for (int i = 0; i < 4; ++i) st = mfma16k16(pack4(acc[1][i][hf]), pack4(acc[0][i][hf]), st);
        lds_w128f(spa ^ (hf * 512), st);
      }
      waitvm<0>();
    }
    asm volatile("" :: "v"(dc), "v"(dw), "v"(dx));
    ST(10);
    BAR();
    __builtin_amdgcn_s_setprio(0);
    u32x4 seal = ld_seal(0);
    {
      ldw(wq, 0, 0, WQ_SPLIT, 0);
      u32x2 fl = __builtin_amdgcn_raw_buffer_load_b64(rsWS, l * 8, WS_FLAG + t * 2048 + w * 512, SC1);
      ST(5);
      BAR();
      ldw(wq, 0, WQ_SPLIT, 6, 0);
      if (__builtin_expect(!__all(fl[0] == tag[0] && fl[1] == tag[1]), 0)) {
        for (int sp = 0; sp < SPIN_CAP; ++sp) {
          asm volatile("" ::: "memory");
          const u32x2 f2 = __builtin_amdgcn_raw_buffer_load_b64(rsWS, l * 8, WS_FLAG + t * 2048 + w * 512, SC1);
          if (__all(f2[0] == tag[0] && f2[1] == tag[1])) break;
        }
      }
      ST(18);
      BAR();
    }
    const unsigned qt = (qb >> 3) * SLOT;
    unsigned hqa[2], own[2][2];
    hqa[0] = qt + hh * 1024 + (tq ^ hh) * 128 + (g & 1) * 64 + tp * 16; hqa[1] = (hqa[0] ^ 64) + 512;
    OPAQUE(hqa[0]); OPAQUE(hqa[1]);
    unsigned nw = lds0 + L_N + l * 16 + w * 4;
    OPAQUE(nw);
    unsigned qw = L_Q + (3 * w * 64 + hh * 32 + i16) * 16 + 8 * (g & 1);
    own[0][0] = qt + w * 3072 + (tq ^ (w & 1)) * 128 + hh * 64 + tp * 16 + 8 * (g & 1); own[0][1] = (own[0][0] ^ 64) + 512;
    own[1][0] = own[0][0] ^ 128; own[1][1] = own[0][1] ^ 128;
    OPAQUE(qw); OPAQUE(own[0][0]); OPAQUE(own[0][1]); OPAQUE(own[1][0]); OPAQUE(own[1][1]);
    if (b == 0 && w == 3 && l == 0) __builtin_amdgcn_raw_buffer_store_b64(u32x2{(unsigned)epoch, (unsigned)(epoch >> 32)}, rsWS, 0, WS_EPOCH, SC1);
    for (int sp = 0; sp < SPIN_CAP && !sealed(wq, seal, l < 18, tag); ++sp) {
      asm volatile("" ::: "memory");
      seal = ld_seal(0);
      ldw(wq, 0, 0, 6, 0);
    }
    u32x4 hq[3];
    {
      const u64 hbu = (u64)(ws + WS_H2 + (size_t)t * FRAME_B);
      u32x4 rs = {(unsigned)hbu, (unsigned)(hbu >> 32) & 0xffffu, (unsigned)FRAME_B, 0x00020000u};
      asm volatile("" : "+s"(rs));
      __builtin_amdgcn_s_setprio(3);
      ST(6);
      BAR();
      ST(7);
      for (int sp = 0; sp < SPIN_CAP; ++sp) {
        setm0<0>(qt + w * 3072);
        bdma<0>(l * 16, rs, qb * TILE_B + w * 3072); bdma<1024>(l * 16, rs, qb * TILE_B + w * 3072); bdma<2048>(l * 16, rs, qb * TILE_B + w * 3072);
        waitvm<0>();
#pragma unroll
        for (int i = 0; i < 3; ++i) { const u32x2 u0 = lds_r64(own[i & 1][0] + i * 1024), u1 = lds_r64(own[i & 1][1] + i * 1024); hq[i] = u32x4{u0[0], u0[1], u1[0], u1[1]}; }
        if (!poisoned(hq)) break;
      }
    }
    ST(8);
    BAR();
    ST(9);
    {
      u32x4 bf[6][2];
      f32x4 d[3][2];
      f32x2 n[2] = {{0.f, 0.f}, {0.f, 0.f}};
      auto fin = [&](const int i) {
#pragma unroll
        for (int hf = 0; hf < 2; ++hf) {
          lds_w64(qw + i * 1024 + hf * 256, pack4(d[i][hf]));
          const f32x2 a = {d[i][hf][0], d[i][hf][1]}, c = {d[i][hf][2], d[i][hf][3]};
          n[hf] = __builtin_elementwise_fma(a, a, n[hf]); n[hf] = __builtin_elementwise_fma(c, c, n[hf]);
        }
      };
      auto mm = [&](const int i, const int ks) {
        d[i][0] = mfma16(wq[ks * 3 + i], bf[ks][0], ks ? d[i][0] : f32x4{0, 0, 0, 0});
        d[i][1] = mfma16(wq[ks * 3 + i], bf[ks][1], ks ? d[i][1] : f32x4{0, 0, 0, 0});
      };
#pragma unroll
      for (int ks = 0; ks < 3; ++ks) { bf[ks][0] = lds_r128(hqa[0] + ks * 2048); bf[ks][1] = lds_r128(hqa[1] + ks * 2048); SBAR(); }
#pragma unroll
      for (int ks = 0; ks < 3; ++ks) {
        mm(0, ks);
        bf[ks + 3][0] = lds_r128(hqa[0] + (ks + 3) * 2048); bf[ks + 3][1] = lds_r128(hqa[1] + (ks + 3) * 2048);
        SBAR();
        mm(1, ks);
        SBAR();
      }
      mm(0, 3); mm(0, 4); mm(0, 5); mm(1, 3);
      SBAR();
      mm(1, 4); mm(1, 5); mm(2, 0); fin(0);
      SBAR();
      mm(2, 1); mm(2, 2); mm(2, 3); mm(2, 4); mm(2, 5); fin(1);
      SBAR();
      fin(2);
      auto rr = __builtin_amdgcn_permlane16_swap(asu(n[0][0] + n[0][1]), asu(n[1][0] + n[1][1]), false, false);
      lds_w32(nw, asf(rr[0]) + asf(rr[1]));
    }
    SBAR();
    ST(11);
    BAR();
    __builtin_amdgcn_s_setprio(0);
    unsigned ow[3];
#pragma unroll
    for (int i = 0; i < 3; ++i) { ow[i] = i16 * 384 + (((6 * w + 2 * i + hh) ^ (i16 & 7)) * 16) + 8 * (g & 1); OPAQUE(ow[i]); }
    float lt[2];
    u32x4 pick;
#pragma unroll
    for (int e = 0; e < 4; ++e) {
      const unsigned d = 8 * (e >> 1) + 4 * (g & 1) + 2 * (e & 1);
      pick[e] = (i16 == d ? 0x3f80u : 0u) | (i16 == d + 1 ? 0x3f800000u : 0u);
    }
    unsigned pr[2], la = lds0 + L_L + i16 * 32;
    pr[0] = lds0 + 2 * TILE_B + hh * SLOT + 3 * w * 1024 + ((g & 1) * 32 + i16) * 16; pr[1] = pr[0] + 2 * SLOT;
    OPAQUE(pick); OPAQUE(pr[0]); OPAQUE(pr[1]); OPAQUE(la);
    f32x4 x1[3][2];
    {
      __builtin_amdgcn_s_sleep(40);
      u32x2 st[2];
      for (int sp = 0; sp < SPIN_CAP; ++sp) {
        asm volatile("" ::: "memory");
        st[0] = __builtin_amdgcn_raw_buffer_load_b64(rsWS, i16 * 8, WS_ST + (t * 1024 + qb * 32) * 8, SC1);
        st[1] = __builtin_amdgcn_raw_buffer_load_b64(rsWS, i16 * 8 + 128, WS_ST + (t * 1024 + qb * 32) * 8, SC1);
        if (!__any(max(max(st[0][0], st[0][1]), max(st[1][0], st[1][1])) == POISON)) break;
      }
#pragma unroll
      for (int hf = 0; hf < 2; ++hf) {
        const float mu = asf(st[hf][0]), sd = __builtin_amdgcn_sqrtf(asf(st[hf][1]));
#pragma unroll
        for (int i = 0; i < 3; ++i) {
          const f32x4 h = {bflo(hq[i][2 * hf]), bfhi(hq[i][2 * hf]), bflo(hq[i][2 * hf + 1]), bfhi(hq[i][2 * hf + 1])};
          x1[i][hf] = h * sd + mu;
          OPAQUE(x1[i][hf]);
        }
      }
    }
    BAR();
    ST(16);
    for (int sp = 0; sp < SPIN_CAP; ++sp) {
      asm volatile("" ::: "memory");
      seal = ld_seal(1);
      ldw(wv, 1, 0, 6, 0);
      if (sealed(wv, seal, l < 18, tag)) break;
    }
    ST(12);
#pragma unroll 1
    for (bool exact = false;; exact = true) {
      BAR();
      ST(13);
      u32x4 p[3][2][2];
#pragma unroll
      for (int i = 0; i < 3; ++i)
#pragma unroll
        for (int hf = 0; hf < 2; ++hf) { p[i][hf][0] = lds_r128(pr[0] + i * 1024 + hf * 256); p[i][hf][1] = lds_r128(pr[1] + i * 1024 + hf * 256); }
      const f32x4 l0 = lds_r128f(la), l1 = lds_r128f(la + 16);
      SBAR();
#pragma unroll
      for (int i = 0; i < 3; ++i)
#pragma unroll
        for (int hf = 0; hf < 2; ++hf) lds_w64(dp[hf] + (L_FI - L_X) + i * 512, pack4(mfma16(pick, p[i][hf][1], mfma16(pick, p[i][hf][0], f32x4{0, 0, 0, 0}))));
      if (__builtin_expect(!low_sum(l0, l1, lt) || exact, 1)) break;
      BAR();
      BAR();
    }
    ST(14);
    const float linv[2] = {__builtin_amdgcn_rcpf(lt[0]), __builtin_amdgcn_rcpf(lt[1])};
    BAR();
    ST(15);
    {
      u32x4 bf[6][2];
      f32x4 d[3][2];
      auto fin = [&](const int i) {
#pragma unroll
        for (int hf = 0; hf < 2; ++hf) lds_w64(ow[i] + hf * 6144, pack4(d[i][hf] * linv[hf] + x1[i][hf]));
      };
      auto mm = [&](const int i, const int ks) {
        d[i][0] = mfma16(wv[ks * 3 + i], bf[ks][0], ks ? d[i][0] : f32x4{0, 0, 0, 0});
        d[i][1] = mfma16(wv[ks * 3 + i], bf[ks][1], ks ? d[i][1] : f32x4{0, 0, 0, 0});
      };
#pragma unroll
      for (int ks = 0; ks < 3; ++ks) { bf[ks][0] = lds_r128(fr[0] + (L_FI - L_X) + ks * 1024); bf[ks][1] = lds_r128(fr[1] + (L_FI - L_X) + ks * 1024); SBAR(); }
#pragma unroll
      for (int ks = 0; ks < 3; ++ks) {
        mm(0, ks);
        bf[ks + 3][0] = lds_r128(fr[0] + (L_FI - L_X) + (ks + 3) * 1024); bf[ks + 3][1] = lds_r128(fr[1] + (L_FI - L_X) + (ks + 3) * 1024);
        SBAR();
        mm(1, ks);
        SBAR();
      }
      mm(0, 3); mm(0, 4); mm(0, 5); mm(1, 3);
      SBAR();
      mm(1, 4); mm(1, 5); mm(2, 0); fin(0);
      SBAR();
      mm(2, 1); mm(2, 2); mm(2, 3); mm(2, 4); mm(2, 5); fin(1);
      SBAR();
      fin(2);
    }
    BAR();
    ST(17);
    ST_OUT(ws, 12, 20);
  }
}

extern "C" void kernel_launch(void* const* d_in, const int* in_sizes, int n_in, void* d_out, int out_size, void* d_ws, size_t ws_size, hipStream_t stream) {
  (void)hipFuncSetAttribute((const void*)k_timesformer, hipFuncAttributeMaxDynamicSharedMemorySize, LDS_BYTES);
  k_timesformer<<<256, 512, LDS_BYTES, stream>>>((const char*)d_in[0], (const char*)d_in[1], (const char*)d_in[2], (const char*)d_in[3], (const char*)d_in[4],
                                                 (char*)d_ws, (char*)d_out);
}
```

```cpp
#include <hip/hip_runtime.h>
#include <hip/hip_bf16.h>
#include <type_traits>

typedef short s16x4 __attribute__((ext_vector_type(4)));
typedef __bf16 b16x8 __attribute__((ext_vector_type(8)));
typedef __bf16 b16x2 __attribute__((ext_vector_type(2)));
typedef float f32x2 __attribute__((ext_vector_type(2)));
typedef float f32x4 __attribute__((ext_vector_type(4)));
typedef float f32x16 __attribute__((ext_vector_type(16)));
typedef unsigned u32x2 __attribute__((ext_vector_type(2)));
typedef unsigned u32x4 __attribute__((ext_vector_type(4)));
typedef unsigned long long u64;
typedef u64 u64x4 __attribute__((ext_vector_type(4)));
typedef u64 u64x2 __attribute__((ext_vector_type(2)));
#define LAS __attribute__((address_space(3)))
#define DI __device__ __forceinline__
#define SBAR() __builtin_amdgcn_sched_barrier(0)

constexpr int TILE_B = 12288;
constexpr int FRAME_B = 393216;
constexpr unsigned WS_H2 = 0, WS_ST = 8 * FRAME_B, WS_WM = 16 * FRAME_B, WS_SEAL = WS_WM + 3 * 73728, WS_FLAG = WS_SEAL + 216 * 128, WS_FLAG2 = WS_FLAG + 8 * 2048, WS_EPOCH = WS_FLAG2 + 2048, WS_END = WS_EPOCH + 256;
constexpr float QSCALE = 0.07216878364870322f * 1.4426950408889634f;
constexpr int SC1 = 16;
constexpr unsigned POISON = 0xffffffffu;
constexpr unsigned CODE_BYTES = 26112;
constexpr unsigned CODE_SHARE = 32;
constexpr int OUT_AUX = 16;
constexpr int TILE0_DELAY = 2, LOAD_DELAY = 2, WQ_SPLIT = 4, B9_DELAY = 2, VO_1 = 1, VO_2 = 3, MM_EARLY = 0, PQ = 12, PV = 9, PI = 23;
static_assert(9 * PQ + 18 * PV + 5 * PI >= 384, "the whole frame is poisoned");
constexpr int SPIN_CAP = 1 << 16;


constexpr int PROBE = 10, ROLE = 0; constexpr size_t WS_T0 = 32u << 20;
#define ST_DECL u64 st_ = 0, t0_ = 0
#define ST(k) do { if ((k) == PROBE || (k) == 0) { SBAR(); u64 t_; asm volatile("s_memrealtime %0\n\ts_waitcnt lgkmcnt(0)" : "=s"(t_) :: "memory"); SBAR(); \
    if ((k) == PROBE) st_ = t_; \
    if ((k) == 0) t0_ = t_; } STO_##k } while (0)
#define STO_(k)
#define STO_0
#define STO_1
#define STO_2
#define STO_3
#define STO_4
#define STO_5
#define STO_6
#define STO_7
#define STO_8
#define STO_9
#define STO_10
#define STO_11
#define STO_12
#define STO_13
#define STO_14
#define STO_15
#define STO_16 if (threadIdx.x == 0) __hip_atomic_store((__attribute__((address_space(1))) u64*)(ws + WS_T0) + blockIdx.x, t0_, __ATOMIC_RELAXED, __HIP_MEMORY_SCOPE_AGENT);
#define STO_17
#define STO_18
#define STO_19
#define ST_OUT(ws, k0, k1) do { if (ROLE == ((k0) != 0)) { u64 m_ = ~0ull; \
    for (int q_ = 0; q_ < 4; ++q_) { u64 v_ = __hip_atomic_load((__attribute__((address_space(1))) u64*)(ws + WS_T0) + (threadIdx.x & 63) * 4 + q_, __ATOMIC_RELAXED, __HIP_MEMORY_SCOPE_AGENT); m_ = v_ < m_ ? v_ : m_; } \
    for (int o_ = 32; o_; o_ >>= 1) { u64 v_ = __shfl_xor(m_, o_); m_ = v_ < m_ ? v_ : m_; } \
    long long d_ = (long long)(st_ - m_); if (d_ < 0) d_ = 0; if (d_ > 5000) d_ = 5000; \
    const u64 e_ = m_ + 4000 + 4 * d_; while (__builtin_amdgcn_s_memrealtime() < e_) __builtin_amdgcn_s_sleep(1); } } while (0)


DI float asf(unsigned u) { return __builtin_bit_cast(float, u); }
DI unsigned asu(float f) { return __builtin_bit_cast(unsigned, f); }
DI unsigned cvtpk(float lo, float hi) { f32x2 v = {lo, hi}; return __builtin_bit_cast(unsigned, __builtin_convertvector(v, b16x2)); }
DI float bflo(unsigned u) { return asf(u << 16); }
DI float bfhi(unsigned u) { return asf(u & 0xffff0000u); }
DI f32x16 mfma32(u32x4 a, u32x4 b, f32x16 c) { return __builtin_amdgcn_mfma_f32_32x32x16_bf16(__builtin_bit_cast(b16x8, a), __builtin_bit_cast(b16x8, b), c, 0, 0, 0); }
DI f32x4 mfma16(u32x4 a, u32x4 b, f32x4 c) { return __builtin_amdgcn_mfma_f32_16x16x32_bf16(__builtin_bit_cast(b16x8, a), __builtin_bit_cast(b16x8, b), c, 0, 0, 0); }
DI f32x4 mfma16k16(u32x2 a, u32x2 b, f32x4 c) { return __builtin_amdgcn_mfma_f32_16x16x16bf16_1k(__builtin_bit_cast(s16x4, a), __builtin_bit_cast(s16x4, b), c, 0, 0, 0); }
DI u32x2 pack4(f32x4 v) { u32x2 r = {cvtpk(v[0], v[1]), cvtpk(v[2], v[3])}; return r; }
DI u32x4 lds_r128(unsigned a) { return *(LAS u32x4*)(size_t)a; }
DI f32x4 lds_r128f(unsigned a) { return *(LAS f32x4*)(size_t)a; }
DI u32x2 lds_r64(unsigned a) { return *(LAS u32x2*)(size_t)a; }
DI void lds_w128(unsigned a, u32x4 v) { *(LAS u32x4*)(size_t)a = v; }
DI void lds_w128f(unsigned a, f32x4 v) { *(LAS f32x4*)(size_t)a = v; }
DI void lds_w64(unsigned a, u32x2 v) { *(LAS u32x2*)(size_t)a = v; }
DI void lds_w32(unsigned a, float v) { *(LAS float*)(size_t)a = v; }
DI u32x2 lds_tr(unsigned a) { return __builtin_bit_cast(u32x2, __builtin_amdgcn_ds_read_tr16_b64_v4i16((LAS s16x4*)(size_t)a)); }
DI u32x4 lds_tr2(unsigned a0, unsigned a1) { u32x2 x = lds_tr(a0), y = lds_tr(a1); u32x4 r = {x[0], x[1], y[0], y[1]}; return r; }
#define BAR() asm volatile("s_waitcnt lgkmcnt(0)\n\ts_barrier" ::: "memory")
#define BARV(n) asm volatile("s_waitcnt vmcnt(" #n ") lgkmcnt(0)\n\ts_barrier" ::: "memory")
template <int CTRL> DI float dpp_add(float v) { return v + asf(__builtin_amdgcn_update_dpp(0u, asu(v), CTRL, 0xf, 0xf, true)); }
DI float xor16_add(float v) { auto r = __builtin_amdgcn_permlane16_swap(asu(v), asu(v), false, false); return asf(r[0]) + asf(r[1]); }
DI float xor16_max(float v) { auto r = __builtin_amdgcn_permlane16_swap(asu(v), asu(v), false, false); return __builtin_amdgcn_fmed3f(asf(r[0]), asf(r[1]), __builtin_inff()); }
DI unsigned umax3(unsigned a, unsigned b, unsigned c) { return max(max(a, b), c); }
template <int N> DI bool poisoned(const u32x4 (&v)[N]) {
  unsigned m = 0;
#pragma unroll
  for (int i = 0; i < N; ++i) { m = umax3(m, v[i][0], v[i][1]); m = umax3(m, v[i][2], v[i][3]); }
  return __any(m == POISON);
}
DI unsigned fi_pos(unsigned r, unsigned c8) {
  return (((r >> 4) * 6 + (c8 >> 2)) * 64 + (c8 & 3) * 16 + ((r & 15) ^ ((c8 & 1) | (((r >> 3) & 1) << 1) | ((r >> 4) << 2)))) * 16;
}
DI float row8_sum(float s) { s = dpp_add<0x124>(s); s = dpp_add<0x128>(s); return xor16_add(s); }
DI float sum24(const f32x2 (&v)[12]) {
  f32x2 s0 = v[0] + v[1], s1 = v[2] + v[3];
#pragma unroll
  for (int i = 4; i < 12; i += 2) { s0 += v[i]; s1 += v[i + 1]; }
  s0 += s1;
  return s0[0] + s0[1];
}
DI float sumsq24(const f32x2 (&v)[12]) {
  f32x2 q0 = v[0] * v[0], q1 = v[1] * v[1];
#pragma unroll
  for (int i = 2; i < 12; i += 2) { q0 = __builtin_elementwise_fma(v[i], v[i], q0); q1 = __builtin_elementwise_fma(v[i + 1], v[i + 1], q1); }
  q0 += q1;
  return q0[0] + q0[1];
}
DI f32x2 unpack2(unsigned u) { f32x2 r = {bflo(u), bfhi(u)}; return r; }

constexpr unsigned SLOT = 36864, L_X = 3 * SLOT, L_H = 4 * SLOT, L_N = L_H + TILE_B, L_L = L_N + 1024, L_PT = L_L + 1024, LDS_BYTES = 163840;
constexpr unsigned QK = 24576, L_JOB = 4 * QK;
constexpr unsigned L_VI = L_X, L_SP = L_X + TILE_B, L_YI = L_X + 20480;
constexpr unsigned L_Q = L_H, L_FI = L_H;

#define DMA3(v0, v1, v2, sbase, ldsa) \
  asm volatile("s_mov_b32 m0, %4\n\ts_nop 0\n\tglobal_load_lds_dwordx4 %0, %3\n\tglobal_load_lds_dwordx4 %1, %3 offset:1024\n\tglobal_load_lds_dwordx4 %2, %3 offset:2048" \
               :: "v"(v0), "v"(v1), "v"(v2), "s"(sbase), "s"(ldsa) : "memory")

template <int OFF> DI void bdma(unsigned voff, u32x4 rs, unsigned soff) { asm volatile("buffer_load_dwordx4 %0, %1, %2 offen offset:%3 sc1 lds" :: "v"(voff), "s"(rs), "s"(soff), "i"(OFF) : "memory"); }
template <int OFF> DI void setm0(unsigned base) { asm volatile("s_add_u32 m0, %0, %1\n\ts_nop 0" :: "s"(base), "i"(OFF) : "memory", "scc"); }
template <int N> DI void waitvm() { asm volatile("s_waitcnt vmcnt(%0)" :: "i"(N) : "memory"); }
#define IC(x) std::integral_constant<int, (x)>{}
#define OPAQUE(v) asm volatile("" : "+v"(v))
template <class R> DI void st128(u32x4 v, R rs, unsigned voff, unsigned soff) { __builtin_amdgcn_raw_buffer_store_b128(v, rs, voff, soff, SC1); asm volatile("s_nop 1" :: "v"(v)); }
template <int CTRL> DI unsigned dpp_addu(unsigned v) { return v + __builtin_amdgcn_update_dpp(0u, v, CTRL, 0xf, 0xf, true); }
DI unsigned fold_sum(unsigned e, unsigned o) {
  auto r = __builtin_amdgcn_permlane32_swap(e, o, false, false);
  unsigned u = r[0] + r[1];
  auto q = __builtin_amdgcn_permlane16_swap(u, u, false, false);
  u = q[0] + q[1];
  u = dpp_addu<0x128>(u); u = dpp_addu<0x124>(u); u = dpp_addu<0x4e>(u); return dpp_addu<0xb1>(u);
}
DI bool seal_ok(unsigned e, unsigned o, u32x4 seal, bool has, u32x2 tag) {
  const unsigned bad = has ? (seal[1] ^ tag[0]) | (seal[3] ^ tag[1]) : 0u;
  return __builtin_amdgcn_ballot_w64((fold_sum(e - seal[0], o - seal[2]) | bad) != 0) == 0;
}
DI void add2(unsigned& s, unsigned a, unsigned b) { asm("v_add3_u32 %0, %0, %1, %2" : "+v"(s) : "v"(a), "v"(b)); }
#define SUM_IN(e, o, v) do { add2(e, (v)[0], (v)[2]); add2(o, (v)[1], (v)[3]); } while (0)
template <int N> DI bool sealed(const u32x4 (&v)[N], u32x4 seal, bool has, u32x2 tag) {
  unsigned e = 0, o = 0;
#pragma unroll
  for (int i = 0; i < N; ++i) SUM_IN(e, o, v[i]);
  return seal_ok(e, o, seal, has, tag);
}

__global__ __launch_bounds__(512, 2) void k_timesformer(const char* x_, const char* wqkv_t_, const char* wo_t_, const char* wqkv_s_, const char* wo_s_, char* ws_, char* out_) {
  unsigned dc;
  u64x4 ka; u64x2 kb; u64 kc;
  {
    u64 pc;
    asm volatile("s_load_dwordx8 %2, %6, 0x0\n\ts_load_dwordx4 %3, %6, 0x20\n\ts_load_dwordx2 %4, %6, 0x30\n\ts_getpc_b64 %1\n\tglobal_load_dword %0, %5, %1"
                 : "=v"(dc), "=&s"(pc), "=&s"(ka), "=&s"(kb), "=&s"(kc) : "v"(min((threadIdx.x * CODE_SHARE + ((blockIdx.x >> 3) & (CODE_SHARE - 1))) * 128, CODE_BYTES)), "s"(__builtin_amdgcn_kernarg_segment_ptr()) : "memory");
  }
  constexpr unsigned lds0 = 0;
  const unsigned b = blockIdx.x;
  unsigned w8 = __builtin_amdgcn_readfirstlane(threadIdx.x >> 6);
  unsigned l = threadIdx.x & 63;
  const unsigned t = b & 7, qb = b >> 3;
  w8 = (w8 & 4) | ((w8 + qb / 6) & 3);
  ST_DECL;
  ST(0);
  const bool jqk = b < 72, job = b < 216;
  const unsigned jm = qb / 9, jj = t * 9 + qb - jm * 9, jks = jj / 12, jtau = jj - jks * 12, jpos = jm * 72 + jtau + (jtau / 3) * 15 + jks * 3;
  const unsigned rot = qb % 6;
  unsigned fo[2][3];
#pragma unroll
  for (int i = 0; i < 3; ++i) {
    const unsigned p = i * 64 + l;
    if (w8 < 4) { fo[0][i] = p * 16 + (p >= 96 ? FRAME_B - 1536 : 0) + w8 * 2 * FRAME_B + b * 1536 - i * 1024; fo[1][i] = 0; }
    else if (w8 == 7) {
      const unsigned row = (p * 1366) >> 16;
      fo[0][i] = row * 1152 + (((p - row * 48) ^ (row << 1)) * 16) - i * 1024; fo[1][i] = row * 1152 + (((p - row * 48) ^ (row << 1) ^ 8) * 16) - i * 1024;
    } else {
      const unsigned row = (p * 2731) >> 16, o = row * 1152 + (((p - row * 24) ^ row) * 16) - i * 1024;
      fo[0][i] = o + jks * (32 * 1152) + (jqk ? 0 : 768);
      fo[1][i] = jqk ? o + jtau * (16 * 1152) + 384 : (l >> 1) * 384 + (l & 1) * 16 + jtau * 32 + i * (12288 - 1024);
    }
  }
  asm volatile("s_waitcnt lgkmcnt(0)" : "+s"(ka), "+s"(kb), "+s"(kc), "+v"(fo[0][0]), "+v"(fo[0][1]), "+v"(fo[0][2]), "+v"(fo[1][0]), "+v"(fo[1][1]), "+v"(fo[1][2]) :: "memory");
  char* const ws = (char*)kb[1];
  auto chunk = [&](const int n) {
    const u64 src = ka[1] + (n + rot >= 6 ? n + rot - 6 : n + rot) * (32 * 1152);
#pragma unroll
    for (int q = 0; q < 8; ++q) DMA3(fo[(q >> 1) & 1][0], fo[(q >> 1) & 1][1], fo[(q >> 1) & 1][2], src + q * 4608, (n & 3) * QK + q * 3072);
  };
  unsigned dw = 0, dx = 0;
  if (w8 < 4) {
    DMA3(fo[0][0], fo[0][1], fo[0][2], ka[0], L_YI + w8 * 3072);
  } else if (w8 == 4) {
    if (job) {
      __builtin_amdgcn_s_setprio(3);
      const u64 wqkv = b >= 144 ? ka[1] : ka[3], second = jqk ? wqkv : b >= 144 ? ka[2] : kb[0];
#pragma unroll
      for (int q = 0; q < 4; ++q) DMA3(fo[0][0], fo[0][1], fo[0][2], wqkv + ((q + qb) & 3) * (8 * 1152), L_JOB + ((q + qb) & 3) * 3072);
#pragma unroll
      for (int q = 0; q < 2; ++q) DMA3(fo[1][0], fo[1][1], fo[1][2], second + q * (jqk ? 8 * 1152 : 36864), L_JOB + 12288 + q * 3072);
    }
  } else if (w8 == 7) {
    __builtin_amdgcn_s_sleep(LOAD_DELAY);
    chunk(0); chunk(1);
  } else {
    if (w8 == 6) asm volatile("global_load_dword %0, %1, %2" : "=v"(dx) : "v"((qb * 6 + min(l, 35u) / 6) * 1152 + (min(l, 35u) % 6) * 128), "s"(ka[1]) : "memory");
    asm volatile("global_load_dword %0, %1, %2" : "=v"(dw) : "v"((qb * 18 + min(l, 17u)) * 128), "s"(w8 == 6 ? ka[2] : kb[0]) : "memory");
  }
  const auto rsWS = __builtin_amdgcn_make_buffer_rsrc((void*)ws, 0, WS_END, 0x00020000);
  const u32x4 pz = {POISON, POISON, POISON, POISON};
  u64 epoch;
  asm volatile("s_load_dwordx2 %0, %1, 0x0" : "=&s"(epoch) : "s"(ws + WS_EPOCH) : "memory");
  asm volatile("s_waitcnt lgkmcnt(0)" : "+s"(epoch) :: "memory");
  epoch += 1;
  const u32x2 tag = {(unsigned)epoch, (unsigned)(epoch >> 32) ^ ((unsigned)epoch * 0x9e3779b1u)};
  asm volatile("" : "+s"(w8));
  const unsigned w = w8 & 3;
#define LANE_IDS const unsigned i16 = l & 15, g = l >> 4, r32 = l & 31, hh = l >> 5, tq = i16 >> 2, tp = l & 3
#define FRAG_POS unsigned fr[2], dp[2];                                                                       \
  fr[0] = L_X + fi_pos(i16, g); fr[1] = (fr[0] ^ 64) + 6144; dp[0] = L_X + fi_pos(i16, hh) + 8 * (g & 1) + w * 1536; dp[1] = (dp[0] ^ 64) + 6144; \
  OPAQUE(fr[0]); OPAQUE(fr[1]); OPAQUE(dp[0]); OPAQUE(dp[1])
  auto ldw = [&](u32x4 (&W)[18], const int m, const int k0, const int k1, const unsigned rot) {
#pragma unroll
    for (int kk = k0; kk < k1; ++kk) {
      const unsigned ks = kk + rot >= 6 ? kk + rot - 6 : kk + rot;
#pragma unroll
      for (int i = 0; i < 3; ++i) { SBAR(); W[kk * 3 + i] = __builtin_amdgcn_raw_buffer_load_b128(rsWS, l * 16 + i * 1024, WS_WM + m * 73728 + w * 18432 + ks * 3072, SC1); }
    }
  };
  auto ld_seal = [&](const int m) {
    return __builtin_amdgcn_raw_buffer_load_b128(rsWS, l < 18 ? l * 128 : WS_END, WS_SEAL + (m * 72 + 18 * w) * 128, SC1);
  };
  auto h_frags = [&](u32x4 (&H)[6][2], const unsigned (&fr)[2], const int k0, const int k1) {
#pragma unroll
    for (int kk = k0; kk < k1; ++kk) {
      const unsigned ks = kk + rot >= 6 ? kk + rot - 6 : kk + rot;
      H[kk][0] = lds_r128(fr[0] + ks * 1024 + (L_H - L_X)); H[kk][1] = lds_r128(fr[1] + ks * 1024 + (L_H - L_X));
    }
  };
  auto low_sum = [&](const f32x4 a, const f32x4 c, float (&lt)[2]) {
    lt[0] = (a[0] + a[2]) + (c[0] + c[2]);
    lt[1] = (a[1] + a[3]) + (c[1] + c[3]);
    return (bool)__any(lt[0] < 8.7e-19f || lt[1] < 8.7e-19f);
  };

  if (w8 < 4) {
    const unsigned nl = l & 3, j = (l >> 2) & 7, tt = 2 * w + (l >> 5), r = nl * 8 + tt;
    unsigned rp = L_X + fi_pos(r, j);
    OPAQUE(rp);
    waitvm<0>();
    asm volatile("" :: "v"(dc));
    f32x2 xv[12];
    float mu1;
    {
      u32x4 h[3], xr[3];
#pragma unroll
      for (int i = 0; i < 3; ++i) xr[i] = lds_r128(lds0 + L_YI + tt * 1536 + nl * 384 + j * 16 + i * 128);
#pragma unroll
      for (int i = 0; i < 12; ++i) xv[i] = unpack2(xr[i >> 2][i & 3]);
      mu1 = row8_sum(sum24(xv)) * (1.0f / 192.0f);
#pragma unroll
      for (int i = 0; i < 12; ++i) xv[i] -= mu1;
      const float rs = __builtin_amdgcn_rsqf(row8_sum(sumsq24(xv)) * (1.0f / 192.0f) + 1e-5f);
#pragma unroll
      for (int i = 0; i < 12; ++i) { const f32x2 n = xv[i] * rs; h[i >> 2][i & 3] = cvtpk(n[0], n[1]); }
#pragma unroll
      for (int i = 0; i < 3; ++i) lds_w128(rp + (L_H - L_X) + i * 2048, h[i]);
    }
    SBAR();
    OPAQUE(l);
    unsigned so = tt * FRAME_B + (j >> 1) * 1024 + ((t ^ ((j >> 1) & 1)) * 128) + (((j & 1) ^ (t >> 2)) * 64) + nl * 16;
    const unsigned sso = qb * TILE_B;
    LANE_IDS;
    FRAG_POS;
    unsigned sto = j == 0 ? (tt * 1024 + nl) * 8 : WS_END;
    unsigned sa = L_SP + l * 16;
    unsigned keep = (hh == (i16 >> 3)) ? ~0u : 0u;
    OPAQUE(sto); OPAQUE(so); OPAQUE(sa); OPAQUE(keep);
    ST(1);
    BAR();
    ST(6);
    {
      u32x4 H[6][2], W[18];
      BAR();
      h_frags(H, fr, 0, 6);
      asm volatile("s_barrier" ::: "memory");
      ST(2);
#pragma unroll 1
      for (int sp = 0;; ++sp) {
        const u32x4 seal = ld_seal(2);
        ldw(W, 2, 0, VO_1, rot);
        if (sp == 0) asm volatile("s_barrier" ::: "memory");
        ldw(W, 2, VO_1, VO_2, rot);
        if (sp == 0) asm volatile("s_barrier" ::: "memory");
        ldw(W, 2, VO_2, 6, rot);
        SBAR();
        f32x4 vp[3][2];
        unsigned he = 0, ho = 0;
        auto mm = [&](const int kk) {
#pragma unroll
          for (int i = 0; i < 3; ++i) {
            vp[i][0] = mfma16(H[kk][0], W[kk * 3 + i], kk ? vp[i][0] : f32x4{0, 0, 0, 0});
            add2(he, W[kk * 3 + i][0], W[kk * 3 + i][2]);
            SBAR();
            vp[i][1] = mfma16(H[kk][1], W[kk * 3 + i], kk ? vp[i][1] : f32x4{0, 0, 0, 0});
            add2(ho, W[kk * 3 + i][1], W[kk * 3 + i][3]);
            SBAR();
          }
        };
        ST(3);
#pragma unroll
        for (int kk = 0; kk < MM_EARLY; ++kk) mm(kk);
        OPAQUE(he); OPAQUE(ho);
        ST(8);
        if (sp == 0) asm volatile("s_barrier" ::: "memory");
        u32x2 pt[2];
        {
          f32x4 e = (lds_r128f(sa) + lds_r128f(sa + 1024)) + lds_r128f(sa + 2048);
          const float mq = xor16_max(fmaxf(fmaxf(e[0], e[1]), fmaxf(e[2], e[3]))) * -QSCALE;
#pragma unroll
          for (int k = 0; k < 4; ++k) e[k] = __builtin_amdgcn_exp2f(fmaf(e[k], QSCALE, mq));
          pt[0] = pack4(e * __builtin_amdgcn_rcpf(xor16_add((e[0] + e[1]) + (e[2] + e[3]))));
#pragma unroll
          for (int k = 0; k < 2; ++k) {
            auto q = __builtin_amdgcn_permlane32_swap(pt[0][k], pt[0][k], false, false);
            pt[1][k] = (q[0] ^ q[1] ^ pt[0][k]) & keep;
            pt[0][k] &= keep;
          }
          OPAQUE(pt[0]); OPAQUE(pt[1]);
        }
        SBAR();
        ST(4);
#pragma unroll
        for (int kk = MM_EARLY; kk < 6; ++kk) mm(kk);
        OPAQUE(he); OPAQUE(ho);
        ST(7);
        f32x4 y[3][2];
#pragma unroll
        for (int hf = 0; hf < 2; ++hf)
#pragma unroll
          for (int i = 0; i < 3; ++i) y[i][hf] = mfma16k16(pack4(vp[i][hf]), pt[hf], f32x4{0, 0, 0, 0});
        SBAR();
#pragma unroll
        for (int hf = 0; hf < 2; ++hf)
#pragma unroll
          for (int i = 0; i < 3; ++i) lds_w64(dp[hf] + (L_YI - L_X) + i * 512, pack4(y[i][hf]));
        if (__builtin_expect(seal_ok(he, ho, seal, l < 18, tag), 1) || sp >= SPIN_CAP) break;
        asm volatile("" ::: "memory");
      }
    }
    ST(9);
    BAR();
    {
      u32x4 y[3], h[3];
#pragma unroll
      for (int i = 0; i < 3; ++i) y[i] = lds_r128(rp + (L_YI - L_X) + i * 2048);
#pragma unroll
      for (int i = 0; i < 12; ++i) xv[i] += unpack2(y[i >> 2][i & 3]);
      const float mu = row8_sum(sum24(xv)) * (1.0f / 192.0f), sq = row8_sum(sumsq24(xv));
      const float var = fmaxf(fmaf(sq, 1.0f / 192.0f, -mu * mu), 0.f) + 1e-5f;
      const float rs = __builtin_amdgcn_rsqf(var), nb = -mu * rs;
#pragma unroll
      for (int i = 0; i < 3; ++i) {
#pragma unroll
        for (int e = 0; e < 4; ++e) { const f32x2 n = xv[4 * i + e] * rs + nb; h[i][e] = cvtpk(n[0], n[1]); }
        if (i == 0) { OPAQUE(h[0]); ST(5); asm volatile("s_barrier" ::: "memory"); }
        st128(h[i], rsWS, so, WS_H2 + sso + i * 4096);
        SBAR();
      }
      const u32x2 stat = {asu(mu + mu1), asu(var)};
      __builtin_amdgcn_raw_buffer_store_b64(stat, rsWS, sto, WS_ST + b * 32, SC1);
    }
    ST(10);
    const unsigned ring = lds0 + w * SLOT;
    unsigned kr0 = ring + (r32 >> 2) * 128 + (hh ^ (r32 >> 4)) * 64 + (r32 & 3) * 16, kr1 = kr0 ^ 128;
    unsigned vr0 = ring + (g & 1) * 1024 + (hh ^ (g & 1)) * 128 + (tp >> 1) * 64 + tq * 16 + 8 * (tp & 1), vr1 = (vr0 ^ 64) + 512;
    unsigned lo[3] = {l * 16, l * 16 + 4096, l * 16 + 8192}, pw = ring + 2 * TILE_B + l * 16, lw = l < 16 ? L_L + (l * 4 + w) * 8 : L_L + 512 + l * 8;
    OPAQUE(kr0); OPAQUE(kr1); OPAQUE(vr0); OPAQUE(vr1); OPAQUE(lo[0]); OPAQUE(lo[1]); OPAQUE(lo[2]); OPAQUE(pw); OPAQUE(lw);
    const u64 hbu = (u64)(ws + WS_H2 + (size_t)t * FRAME_B);
    const u32x4 rs = {(unsigned)hbu, (unsigned)(hbu >> 32) & 0xffffu, (unsigned)FRAME_B, 0x00020000u};
    unsigned toff[8];
#pragma unroll
    for (int i = 0; i < 8; ++i) { toff[i] = (8 * w + ((i + qb) & 7)) * TILE_B; asm volatile("" : "+s"(toff[i])); }
    u32x4 sel;
    { const unsigned one = (tp == (g & 1)) ? 0x3f803f80u : 0u; sel = u32x4{one, one, one, one}; OPAQUE(sel); }
    auto piece = [&](auto Tc, auto Pc) {
      constexpr int T = decltype(Tc)::value, p = decltype(Pc)::value;
      if constexpr (T < 8) {
        if constexpr (p % 4 == 0) setm0<(T % 3) * TILE_B + p * 1024>(ring);
        bdma<(p % 4) * 1024>(lo[p / 4], rs, toff[T]);
      }
    };
#define P6(T, a) piece(IC(T), IC(a)); piece(IC(T), IC(a + 1)); piece(IC(T), IC(a + 2)); piece(IC(T), IC(a + 3)); piece(IC(T), IC(a + 4)); piece(IC(T), IC(a + 5));
    auto read_valid = [&](u32x4 (&KK)[12], const unsigned dst, unsigned off) {
      for (int sp = 0; sp < SPIN_CAP; ++sp) {
#pragma unroll
        for (int k = 0; k < 12; ++k) KK[k] = lds_r128(((k & 1) ? kr1 : kr0) + dst + k * 1024);
        if (__builtin_expect(!poisoned(KK), 1)) break;
#pragma unroll 1
        for (int k = 0; k < 12; ++k) { setm0<0>(ring + dst + k * 1024); bdma<0>(lo[0], rs, off + k * 1024); }
        waitvm<0>();
      }
    };
    unsigned qa = lds0 + L_Q + lo[0], na = lds0 + L_N + r32 * 16;
    OPAQUE(qa); OPAQUE(na);
    waitvm<0>();
    __builtin_amdgcn_s_sleep(B9_DELAY);
    BAR();
    __builtin_amdgcn_s_sleep(TILE0_DELAY);
    if (w != (qb >> 3)) { P6(0, 0) P6(0, 6) }
    else if (l == 0) __builtin_amdgcn_raw_buffer_store_b64(tag, rsWS, 0, WS_FLAG2 + b * 8, SC1);
    ST(11);
    BAR();
    u32x4 K0[12];
    if (w != (qb >> 3)) waitvm<0>();
    ST(12);
    read_valid(K0, 0, toff[0]);
    ST(13);
    piece(IC(1), IC(0)); piece(IC(1), IC(1)); piece(IC(1), IC(2)); piece(IC(1), IC(3));
    BAR();
    ST(14);
    u32x4 Q[12];
    float mref;
    {
      const f32x4 n0 = lds_r128f(na), n1 = lds_r128f(na + 512);
#pragma unroll
      for (int s = 0; s < 6; ++s) Q[s] = lds_r128(qa + s * 1024);
      SBAR();
      const f32x4 s = n0 + n1;
      mref = __builtin_amdgcn_sqrtf(((s[0] + s[1]) + (s[2] + s[3])) * (192.0f * 1.02f));
      OPAQUE(mref);
      SBAR();
#pragma unroll
      for (int s = 6; s < 12; ++s) Q[s] = lds_r128(qa + s * 1024);
      SBAR();
    }
    constexpr int KW = 4, VW = 4;
    bool safe = false, exact = false, late = false;
#pragma unroll 1
    for (;;) {
      f32x16 S[2], O[6], negm;
      u32x4 K[12], V[12], P[2][2];
      f32x4 lsum = {0, 0, 0, 0};
      float pe[2][2];
#pragma unroll
      for (int i = 0; i < 16; ++i) negm[i] = -mref;
#define RK(k, T) K[k] = lds_r128((((k) & 1) ? kr1 : kr0) + ((T) % 3) * TILE_B + (k) * 1024);
#define RV(u, T) { const unsigned a_ = (((u) & 1) ? vr1 : vr0) + ((T) % 3) * TILE_B + ((u) >> 1) * 2048; V[u] = lds_tr2(a_, a_ + 256); }
      auto sm = [&](auto Tc, auto Hc, auto Gc) {
        constexpr int T = decltype(Tc)::value, H = decltype(Hc)::value, G = decltype(Gc)::value;
        if constexpr (G < 8) pe[(G >> 1) & 1][G & 1] = __builtin_amdgcn_exp2f(S[T & 1][8 * H + G]);
        if constexpr (G >= 2 && G <= 8 && G % 2 == 0) { constexpr int q = G / 2 - 1; P[T & 1][H][q] = cvtpk(pe[q & 1][0], pe[q & 1][1]); }
        if constexpr (G == 10) lsum = mfma16(sel, P[T & 1][H], lsum);
      };
      auto put_o = [&](const int un) {
        u32x4 pk;
#pragma unroll
        for (int e = 0; e < 4; ++e) pk[e] = cvtpk(O[un >> 1][8 * (un & 1) + 2 * e], O[un >> 1][8 * (un & 1) + 2 * e + 1]);
        lds_w128(pw + un * 1024, pk);
      };
      auto qkphase = [&](auto Ic) {
        constexpr int I = decltype(Ic)::value;
        auto step = [&](auto Kc) {
          constexpr int k = decltype(Kc)::value;
          if constexpr (I + 1 < 8) {
            S[(I + 1) & 1] = mfma32(I < 0 ? K0[k] : K[k], Q[k], k == 0 ? negm : S[(I + 1) & 1]);
            if constexpr (k + KW < 12 && I >= 0) RK(k + KW, I + 1)
          }
          if constexpr (I < 0 && k < 8) piece(IC(1), IC(4 + k));
          if constexpr (I >= 0) { if constexpr (k & 1) piece(IC(I + 2), IC(6 + k / 2)); if constexpr (k < 11) sm(IC(I), IC(1), IC(k)); }
          if constexpr (I >= 0 && I < 7 && k >= 12 - VW) RV(k - (12 - VW), I)
          SBAR();
        };
        step(IC(0)); step(IC(1)); step(IC(2)); step(IC(3)); step(IC(4)); step(IC(5)); step(IC(6)); step(IC(7)); step(IC(8)); step(IC(9)); step(IC(10)); step(IC(11));
      };
      auto pvphase = [&](auto Ic) {
        constexpr int I = decltype(Ic)::value;
        auto step = [&](auto Uc) {
          constexpr int u = decltype(Uc)::value;
          if constexpr (I >= 0) {
            f32x16 z;
#pragma unroll
            for (int e = 0; e < 16; ++e) z[e] = 0.f;
            O[u >> 1] = mfma32(V[u], P[I & 1][u & 1], (I == 0 && !(u & 1)) ? z : O[u >> 1]);
            if constexpr (u & 1) piece(IC(I + 3), IC(u / 2));
            if constexpr (u + VW < 12) RV(u + VW, I)
          }
          if constexpr (I + 1 < 8 && u >= 1) sm(IC(I + 1), IC(0), IC(u - 1));
          if constexpr (I < 0 && (u & 1)) piece(IC(2), IC(u / 2));
          if constexpr (I + 2 < 8) {
            if constexpr (u == 12 - KW) waitvm<(I < 0 ? 12 : I + 3 < 8 ? 6 + (12 - KW) / 2 : 6)>();
            if constexpr (u >= 12 - KW) RK(u - (12 - KW), I + 2)
            if constexpr (u == 11) waitvm<(I + 3 < 8 ? 6 : 0)>();
          }
          SBAR();
        };
        step(IC(0)); step(IC(1)); step(IC(2)); step(IC(3)); step(IC(4)); step(IC(5)); step(IC(6)); step(IC(7)); step(IC(8)); step(IC(9)); step(IC(10)); step(IC(11));
      };
      auto drain = [&]() {
        constexpr int tl[24] = {6, 6, 6, 6, 6, 6, 6, 6, 7, 7, 7, 7, 6, 6, 7, 7, 6, 6, 7, 7, 7, 7, 7, 7};
        constexpr int fu[24] = {0, 1, 2, 3, 4, 5, 6, 7, 0, 1, 2, 3, 8, 9, 4, 5, 10, 11, 6, 7, 8, 9, 10, 11};
        constexpr int wr[24] = {-1, -1, -1, -1, -1, -1, -1, -1, -1, -1, -1, 0, 1, 2, -1, 3, -1, 4, 5, -1, -1, 6, 7, 8};
        u32x4 X[24];
#pragma unroll
        for (int n = 0; n < VW; ++n) X[n] = V[n];
        auto step = [&](auto Nc) {
          constexpr int n = decltype(Nc)::value;
          O[fu[n] >> 1] = mfma32(X[n], P[tl[n] & 1][fu[n] & 1], O[fu[n] >> 1]);
          if constexpr (n + VW < 24) {
            const unsigned a_ = ((fu[n + VW] & 1) ? vr1 : vr0) + (tl[n + VW] % 3) * TILE_B + (fu[n + VW] >> 1) * 2048;
            X[n + VW] = lds_tr2(a_, a_ + 256);
          }
          if constexpr (n >= 1 && n <= 3) { sm(IC(7), IC(0), IC(3 * n - 3)); sm(IC(7), IC(0), IC(3 * n - 2)); sm(IC(7), IC(0), IC(3 * n - 1)); }
          if constexpr (n >= 4 && n <= 6) { sm(IC(7), IC(1), IC(3 * n - 12)); sm(IC(7), IC(1), IC(3 * n - 11)); sm(IC(7), IC(1), IC(3 * n - 10)); }
          if constexpr (n == 7) sm(IC(7), IC(0), IC(10));
          if constexpr (n == 9) sm(IC(7), IC(1), IC(10));
          if constexpr (wr[n] >= 0) put_o(wr[n]);
          SBAR();
        };
        step(IC(0)); step(IC(1)); step(IC(2)); step(IC(3)); step(IC(4)); step(IC(5)); step(IC(6)); step(IC(7)); step(IC(8)); step(IC(9)); step(IC(10)); step(IC(11));
        step(IC(12)); step(IC(13)); step(IC(14)); step(IC(15)); step(IC(16)); step(IC(17)); step(IC(18)); step(IC(19)); step(IC(20)); step(IC(21)); step(IC(22)); step(IC(23));
      };
#define STALE(v) (__builtin_expect(!safe && __any((v) != (v)), 0))
      bool done = false;
      do {
        qkphase(IC(-1));
        ST(15);
        pvphase(IC(-1));
        ST(16);
        qkphase(IC(0)); pvphase(IC(0)); qkphase(IC(1)); pvphase(IC(1)); qkphase(IC(2));
        const float seen = lsum[0];
        pvphase(IC(2));
        if (STALE(seen)) break;
        qkphase(IC(3)); pvphase(IC(3)); qkphase(IC(4)); pvphase(IC(4)); qkphase(IC(5));
        if (!late) asm volatile("s_barrier" ::: "memory");
        late = true;
        pvphase(IC(5)); qkphase(IC(6)); drain();
        if (STALE(lsum[0])) break;
        { const u32x2 ls = {asu(lsum[0]), asu(lsum[1])}; lds_w64(lw, ls); }
        put_o(9); put_o(10); put_o(11);
        done = true;
      } while (false);
#pragma unroll
      for (int k = 0; k < 12; ++k) asm volatile("" : "=v"(K0[k]));
      if (__builtin_expect(done, 1)) {
        ST(17);
        BAR();
        ST(18);
        float lt[2];
        unsigned ll = lo[0];
        OPAQUE(ll);
        ll >>= 4;
        if (__builtin_expect(!low_sum(lds_r128f(L_L + (ll & 15) * 32), lds_r128f(L_L + (ll & 15) * 32 + 16), lt) || exact, 1)) break;
        BAR();
        float mx = -3.0e38f;
#pragma unroll 1
        for (unsigned i = 0; i < 8; ++i) {
#pragma unroll 1
          for (int k = 0; k < 12; ++k) { setm0<0>(ring + k * 1024); bdma<0>(lo[0], rs, (8 * w + i) * TILE_B + k * 1024); }
          waitvm<0>();
          f32x16 s;
#pragma unroll
          for (int e = 0; e < 16; ++e) s[e] = 0.f;
#pragma unroll
          for (int k = 0; k < 12; ++k) s = mfma32(lds_r128(((k & 1) ? kr1 : kr0) + k * 1024), Q[k], s);
#pragma unroll
          for (int e = 0; e < 16; ++e) mx = fmaxf(mx, s[e]);
        }
        lds_w32(L_N + ((ll & 31) * 8 + w * 2 + (ll >> 5)) * 4, mx);
        BAR();
        const f32x4 c = lds_r128f(L_N + (ll & 31) * 32), d = lds_r128f(L_N + (ll & 31) * 32 + 16);
        mref = fmaxf(fmaxf(fmaxf(c[0], c[1]), fmaxf(c[2], c[3])), fmaxf(fmaxf(d[0], d[1]), fmaxf(d[2], d[3])));
        exact = true;
      } else {
        unsigned ll = lo[0];
        OPAQUE(ll);
        for (int sp = 0; sp < SPIN_CAP; ++sp) {
          asm volatile("" ::: "memory");
          const u32x2 f = __builtin_amdgcn_raw_buffer_load_b64(rsWS, ll >> 1, WS_FLAG2 + w * 512, SC1);
          if (__all(f[0] == tag[0] && f[1] == tag[1])) break;
        }
      }
      safe = true;
      asm volatile("s_waitcnt vmcnt(0) lgkmcnt(0)" ::: "memory");
      P6(0, 0) P6(0, 6)
      waitvm<0>();
      piece(IC(1), IC(0)); piece(IC(1), IC(1)); piece(IC(1), IC(2)); piece(IC(1), IC(3));
#pragma unroll
      for (int k = 0; k < 12; ++k) K0[k] = lds_r128(((k & 1) ? kr1 : kr0) + k * 1024);
    }
    BAR();
    {
      const auto rsO = __builtin_amdgcn_make_buffer_rsrc((void*)kc, 0, 8 * FRAME_B, 0x00020000);
      unsigned oa[3], og = lo[0] + w * 3072 + t * FRAME_B + qb * TILE_B;
#pragma unroll
      for (int i = 0; i < 3; ++i) {
        const unsigned p = i * 64 + (lo[0] >> 4), row = (p * 2731) >> 16;
        oa[i] = w * 3072 + (p ^ row) * 16;
        OPAQUE(oa[i]);
      }
      OPAQUE(og);
      BAR();
#pragma unroll
      for (int i = 0; i < 3; ++i) __builtin_amdgcn_raw_buffer_store_b128(lds_r128(oa[i]), rsO, og + i * 1024, 0, OUT_AUX);
    }
    ST(19);
    ST_OUT(ws, 0, 12);
  } else {
    LANE_IDS;
    FRAG_POS;
    u32x4 wq[18], wv[18];
    if (w == 3) {
      {
      __builtin_amdgcn_raw_buffer_store_b32(POISON, rsWS, l * 4, WS_ST + (t * 1024 + qb * 32) * 8, SC1);
      const unsigned p0 = WS_H2 + t * FRAME_B + (jqk ? qb * PQ : job ? 9 * (PQ - PV) + qb * PV : 384 + (qb - 32) * PI) * 1024, np = jqk ? PQ : job ? PV : PI;
#pragma unroll 1
      for (unsigned i = 0; i < np; ++i) __builtin_amdgcn_raw_buffer_store_b128(pz, rsWS, l * 16, p0 + i * 1024, SC1);
      asm volatile("s_nop 1" :: "v"(pz));
    }
      chunk(2);
      ST(1);
      if (jqk) waitvm<25 + PQ>(); else if (job) waitvm<25 + PV>(); else waitvm<25 + PI>();
      BAR();
      chunk(3);
      ST(2);
      BARV(24);
      __builtin_amdgcn_raw_buffer_store_b64(tag, rsWS, l < 8 ? l * 2048 : WS_END, WS_FLAG + b * 8, SC1);
      chunk(4); chunk(5);
      ST(3);
      BARV(49);
      BARV(24);
      BARV(0);
    } else {
      unsigned ta[2][4][2];
#pragma unroll
      for (int m = 0; m < 2; ++m)
#pragma unroll
        for (int i = 0; i < 4; ++i) {
          ta[m][i][0] = (8 * g + tq) * 768 + (((12 * m + 4 * w + i) ^ (tq | ((g & 1) << 2))) * 2 + (tp >> 1)) * 16 + 8 * (tp & 1);
          ta[m][i][1] = ta[m][i][0] + 2 * QK;
          OPAQUE(ta[m][i][0]); OPAQUE(ta[m][i][1]);
        }
      unsigned spa = hh == (i16 >> 3) ? L_SP + w * 1024 + l * 16 : L_SP + 4096 + l * 16;
      OPAQUE(spa);
      if (w == 0 && job) {
        unsigned ja[2] = {L_JOB + i16 * 384 + ((g ^ (l & 7)) * 16), L_JOB + i16 * 384 + (((4 + g) ^ (l & 7)) * 16)}, jb = L_JOB + 12288 + (8 * g + tq) * 32 + tp * 8;
        unsigned jo = (g & 1) * 512 + (hh * 16 + i16) * 16;
        OPAQUE(ja[0]); OPAQUE(ja[1]); OPAQUE(jb); OPAQUE(jo);
        waitvm<0>();
        ST(2);
        u32x4 bb[6];
        if (jqk) {
#pragma unroll
          for (int kk = 0; kk < 6; ++kk) bb[kk] = lds_r128(ja[kk & 1] + (kk >> 1) * 128 + 12288);
        } else {
#pragma unroll
          for (int kk = 0; kk < 6; ++kk) bb[kk] = lds_tr2(jb + kk * 1024, jb + kk * 1024 + 128);
        }
        f32x4 d = {0, 0, 0, 0}, e = {0, 0, 0, 0};
#pragma unroll
        for (int kk = 0; kk < 6; ++kk) {
          d = mfma16(lds_r128(ja[kk & 1] + (kk >> 1) * 128), bb[kk], d);
          e = mfma16(lds_r128(ja[kk & 1] + (kk >> 1) * 128 + 6144), bb[kk], e);
        }
        const float sc = jqk ? QSCALE : 1.0f;
        const u32x2 r0 = pack4(d * sc), r1 = pack4(e * sc);
        auto s0 = __builtin_amdgcn_permlane16_swap(r0[0], r1[0], false, false);
        auto s1 = __builtin_amdgcn_permlane16_swap(r0[1], r1[1], false, false);
        st128(u32x4{s0[0], s1[0], s0[1], s1[1]}, rsWS, jo, WS_WM + jpos * 1024);
        const u32x2 half = {fold_sum(r0[0] + r1[0], r0[1] + r1[1]), hh ? tag[1] : tag[0]};
        __builtin_amdgcn_raw_buffer_store_b64(half, rsWS, r32 == 0 ? hh * 8 : WS_END, WS_SEAL + jpos * 128, SC1);
        ST(3);
      }
      __builtin_amdgcn_s_setprio(3);
      u32x4 H[6][2];
      f32x4 acc[2][4][2];
      auto step = [&](const int n) {
        u32x4 a[8];
#pragma unroll
        for (int u = 0; u < 8; ++u) { const unsigned at = ta[u >> 2][u & 3][(n >> 1) & 1] + (n & 1) * QK; a[u] = lds_tr2(at, at + 3072); }
#pragma unroll
        for (int u = 0; u < 8; ++u) {
          acc[u >> 2][u & 3][0] = mfma16(a[u], H[n][0], n ? acc[u >> 2][u & 3][0] : f32x4{0, 0, 0, 0});
          acc[u >> 2][u & 3][1] = mfma16(a[u], H[n][1], n ? acc[u >> 2][u & 3][1] : f32x4{0, 0, 0, 0});
        }
        __builtin_amdgcn_sched_group_barrier(0x100, 8, 0);
#pragma unroll
        for (int q = 0; q < 4; ++q) { __builtin_amdgcn_sched_group_barrier(0x8, 2, 0); __builtin_amdgcn_sched_group_barrier(0x100, 2, 0); }
        SBAR();
      };
      ST(4);
      BAR();
      h_frags(H, fr, 0, 2);
      SBAR();
      step(0); step(1);
      BAR();
      h_frags(H, fr, 2, 6);
      SBAR();
      step(2);
      BAR();
      step(3);
      BAR();
      step(4);
      BAR();
      step(5);
      ST(19);
#pragma unroll
      for (int hf = 0; hf < 2; ++hf) {
        f32x4 st = {0, 0, 0, 0};
#pragma unroll
        for (int i = 0; i < 4; ++i) st = mfma16k16(pack4(acc[1][i][hf]), pack4(acc[0][i][hf]), st);
        lds_w128f(spa ^ (hf * 512), st);
      }
      waitvm<0>();
    }
    asm volatile("" :: "v"(dc), "v"(dw), "v"(dx));
    ST(10);
    BAR();
    __builtin_amdgcn_s_setprio(0);
    u32x4 seal = ld_seal(0);
    {
      ldw(wq, 0, 0, WQ_SPLIT, 0);
      u32x2 fl = __builtin_amdgcn_raw_buffer_load_b64(rsWS, l * 8, WS_FLAG + t * 2048 + w * 512, SC1);
      ST(5);
      BAR();
      ldw(wq, 0, WQ_SPLIT, 6, 0);
      if (__builtin_expect(!__all(fl[0] == tag[0] && fl[1] == tag[1]), 0)) {
        for (int sp = 0; sp < SPIN_CAP; ++sp) {
          asm volatile("" ::: "memory");
          const u32x2 f2 = __builtin_amdgcn_raw_buffer_load_b64(rsWS, l * 8, WS_FLAG + t * 2048 + w * 512, SC1);
          if (__all(f2[0] == tag[0] && f2[1] == tag[1])) break;
        }
      }
      ST(18);
      BAR();
    }
    const unsigned qt = (qb >> 3) * SLOT;
    unsigned hqa[2], own[2][2];
    hqa[0] = qt + hh * 1024 + (tq ^ hh) * 128 + (g & 1) * 64 + tp * 16; hqa[1] = (hqa[0] ^ 64) + 512;
    OPAQUE(hqa[0]); OPAQUE(hqa[1]);
    unsigned nw = lds0 + L_N + l * 16 + w * 4;
    OPAQUE(nw);
    unsigned qw = L_Q + (3 * w * 64 + hh * 32 + i16) * 16 + 8 * (g & 1);
    own[0][0] = qt + w * 3072 + (tq ^ (w & 1)) * 128 + hh * 64 + tp * 16 + 8 * (g & 1); own[0][1] = (own[0][0] ^ 64) + 512;
    own[1][0] = own[0][0] ^ 128; own[1][1] = own[0][1] ^ 128;
    OPAQUE(qw); OPAQUE(own[0][0]); OPAQUE(own[0][1]); OPAQUE(own[1][0]); OPAQUE(own[1][1]);
    if (b == 0 && w == 3 && l == 0) __builtin_amdgcn_raw_buffer_store_b64(u32x2{(unsigned)epoch, (unsigned)(epoch >> 32)}, rsWS, 0, WS_EPOCH, SC1);
    for (int sp = 0; sp < SPIN_CAP && !sealed(wq, seal, l < 18, tag); ++sp) {
      asm volatile("" ::: "memory");
      seal = ld_seal(0);
      ldw(wq, 0, 0, 6, 0);
    }
    u32x4 hq[3];
    {
      const u64 hbu = (u64)(ws + WS_H2 + (size_t)t * FRAME_B);
      u32x4 rs = {(unsigned)hbu, (unsigned)(hbu >> 32) & 0xffffu, (unsigned)FRAME_B, 0x00020000u};
      asm volatile("" : "+s"(rs));
      __builtin_amdgcn_s_setprio(3);
      ST(6);
      BAR();
      ST(7);
      for (int sp = 0; sp < SPIN_CAP; ++sp) {
        setm0<0>(qt + w * 3072);
        bdma<0>(l * 16, rs, qb * TILE_B + w * 3072); bdma<1024>(l * 16, rs, qb * TILE_B + w * 3072); bdma<2048>(l * 16, rs, qb * TILE_B + w * 3072);
        waitvm<0>();
#pragma unroll
        for (int i = 0; i < 3; ++i) { const u32x2 u0 = lds_r64(own[i & 1][0] + i * 1024), u1 = lds_r64(own[i & 1][1] + i * 1024); hq[i] = u32x4{u0[0], u0[1], u1[0], u1[1]}; }
        if (!poisoned(hq)) break;
      }
    }
    ST(8);
    BAR();
    ST(9);
    {
      u32x4 bf[6][2];
      f32x4 d[3][2];
      f32x2 n[2] = {{0.f, 0.f}, {0.f, 0.f}};
      auto fin = [&](const int i) {
#pragma unroll
        for (int hf = 0; hf < 2; ++hf) {
          lds_w64(qw + i * 1024 + hf * 256, pack4(d[i][hf]));
          const f32x2 a = {d[i][hf][0], d[i][hf][1]}, c = {d[i][hf][2], d[i][hf][3]};
          n[hf] = __builtin_elementwise_fma(a, a, n[hf]); n[hf] = __builtin_elementwise_fma(c, c, n[hf]);
        }
      };
      auto mm = [&](const int i, const int ks) {
        d[i][0] = mfma16(wq[ks * 3 + i], bf[ks][0], ks ? d[i][0] : f32x4{0, 0, 0, 0});
        d[i][1] = mfma16(wq[ks * 3 + i], bf[ks][1], ks ? d[i][1] : f32x4{0, 0, 0, 0});
      };
#pragma unroll
      for (int ks = 0; ks < 3; ++ks) { bf[ks][0] = lds_r128(hqa[0] + ks * 2048); bf[ks][1] = lds_r128(hqa[1] + ks * 2048); SBAR(); }
#pragma unroll
      for (int ks = 0; ks < 3; ++ks) {
        mm(0, ks);
        bf[ks + 3][0] = lds_r128(hqa[0] + (ks + 3) * 2048); bf[ks + 3][1] = lds_r128(hqa[1] + (ks + 3) * 2048);
        SBAR();
        mm(1, ks);
        SBAR();
      }
      mm(0, 3); mm(0, 4); mm(0, 5); mm(1, 3);
      SBAR();
      mm(1, 4); mm(1, 5); mm(2, 0); fin(0);
      SBAR();
      mm(2, 1); mm(2, 2); mm(2, 3); mm(2, 4); mm(2, 5); fin(1);
      SBAR();
      fin(2);
      auto rr = __builtin_amdgcn_permlane16_swap(asu(n[0][0] + n[0][1]), asu(n[1][0] + n[1][1]), false, false);
      lds_w32(nw, asf(rr[0]) + asf(rr[1]));
    }
    SBAR();
    ST(11);
    BAR();
    __builtin_amdgcn_s_setprio(0);
    unsigned ow[3];
#pragma unroll
    for (int i = 0; i < 3; ++i) { ow[i] = i16 * 384 + (((6 * w + 2 * i + hh) ^ (i16 & 7)) * 16) + 8 * (g & 1); OPAQUE(ow[i]); }
    float lt[2];
    u32x4 pick;
#pragma unroll
    for (int e = 0; e < 4; ++e) {
      const unsigned d = 8 * (e >> 1) + 4 * (g & 1) + 2 * (e & 1);
      pick[e] = (i16 == d ? 0x3f80u : 0u) | (i16 == d + 1 ? 0x3f800000u : 0u);
    }
    unsigned pr[2], la = lds0 + L_L + i16 * 32;
    pr[0] = lds0 + 2 * TILE_B + hh * SLOT + 3 * w * 1024 + ((g & 1) * 32 + i16) * 16; pr[1] = pr[0] + 2 * SLOT;
    OPAQUE(pick); OPAQUE(pr[0]); OPAQUE(pr[1]); OPAQUE(la);
    f32x4 x1[3][2];
    {
      __builtin_amdgcn_s_sleep(40);
      u32x2 st[2];
      for (int sp = 0; sp < SPIN_CAP; ++sp) {
        asm volatile("" ::: "memory");
        st[0] = __builtin_amdgcn_raw_buffer_load_b64(rsWS, i16 * 8, WS_ST + (t * 1024 + qb * 32) * 8, SC1);
        st[1] = __builtin_amdgcn_raw_buffer_load_b64(rsWS, i16 * 8 + 128, WS_ST + (t * 1024 + qb * 32) * 8, SC1);
        if (!__any(max(max(st[0][0], st[0][1]), max(st[1][0], st[1][1])) == POISON)) break;
      }
#pragma unroll
      for (int hf = 0; hf < 2; ++hf) {
        const float mu = asf(st[hf][0]), sd = __builtin_amdgcn_sqrtf(asf(st[hf][1]));
#pragma unroll
        for (int i = 0; i < 3; ++i) {
          const f32x4 h = {bflo(hq[i][2 * hf]), bfhi(hq[i][2 * hf]), bflo(hq[i][2 * hf + 1]), bfhi(hq[i][2 * hf + 1])};
          x1[i][hf] = h * sd + mu;
          OPAQUE(x1[i][hf]);
        }
      }
    }
    BAR();
    ST(16);
    for (int sp = 0; sp < SPIN_CAP; ++sp) {
      asm volatile("" ::: "memory");
      seal = ld_seal(1);
      ldw(wv, 1, 0, 6, 0);
      if (sealed(wv, seal, l < 18, tag)) break;
    }
    ST(12);
#pragma unroll 1
    for (bool exact = false;; exact = true) {
      BAR();
      ST(13);
      u32x4 p[3][2][2];
#pragma unroll
      for (int i = 0; i < 3; ++i)
#pragma unroll
        for (int hf = 0; hf < 2; ++hf) { p[i][hf][0] = lds_r128(pr[0] + i * 1024 + hf * 256); p[i][hf][1] = lds_r128(pr[1] + i * 1024 + hf * 256); }
      const f32x4 l0 = lds_r128f(la), l1 = lds_r128f(la + 16);
      SBAR();
#pragma unroll
      for (int i = 0; i < 3; ++i)
#pragma unroll
        for (int hf = 0; hf < 2; ++hf) lds_w64(dp[hf] + (L_FI - L_X) + i * 512, pack4(mfma16(pick, p[i][hf][1], mfma16(pick, p[i][hf][0], f32x4{0, 0, 0, 0}))));
      if (__builtin_expect(!low_sum(l0, l1, lt) || exact, 1)) break;
      BAR();
      BAR();
    }
    ST(14);
    const float linv[2] = {__builtin_amdgcn_rcpf(lt[0]), __builtin_amdgcn_rcpf(lt[1])};
    BAR();
    ST(15);
    {
      u32x4 bf[6][2];
      f32x4 d[3][2];
      auto fin = [&](const int i) {
#pragma unroll
        for (int hf = 0; hf < 2; ++hf) lds_w64(ow[i] + hf * 6144, pack4(d[i][hf] * linv[hf] + x1[i][hf]));
      };
      auto mm = [&](const int i, const int ks) {
        d[i][0] = mfma16(wv[ks * 3 + i], bf[ks][0], ks ? d[i][0] : f32x4{0, 0, 0, 0});
        d[i][1] = mfma16(wv[ks * 3 + i], bf[ks][1], ks ? d[i][1] : f32x4{0, 0, 0, 0});
      };
#pragma unroll
      for (int ks = 0; ks < 3; ++ks) { bf[ks][0] = lds_r128(fr[0] + (L_FI - L_X) + ks * 1024); bf[ks][1] = lds_r128(fr[1] + (L_FI - L_X) + ks * 1024); SBAR(); }
#pragma unroll
      for (int ks = 0; ks < 3; ++ks) {
        mm(0, ks);
        bf[ks + 3][0] = lds_r128(fr[0] + (L_FI - L_X) + (ks + 3) * 1024); bf[ks + 3][1] = lds_r128(fr[1] + (L_FI - L_X) + (ks + 3) * 1024);
        SBAR();
        mm(1, ks);
        SBAR();
      }
      mm(0, 3); mm(0, 4); mm(0, 5); mm(1, 3);
      SBAR();
      mm(1, 4); mm(1, 5); mm(2, 0); fin(0);
      SBAR();
      mm(2, 1); mm(2, 2); mm(2, 3); mm(2, 4); mm(2, 5); fin(1);
      SBAR();
      fin(2);
    }
    BAR();
    ST(17);
    ST_OUT(ws, 12, 20);
  }
}

extern "C" void kernel_launch(void* const* d_in, const int* in_sizes, int n_in, void* d_out, int out_size, void* d_ws, size_t ws_size, hipStream_t stream) {
  (void)hipFuncSetAttribute((const void*)k_timesformer, hipFuncAttributeMaxDynamicSharedMemorySize, LDS_BYTES);
  k_timesformer<<<256, 512, LDS_BYTES, stream>>>((const char*)d_in[0], (const char*)d_in[1], (const char*)d_in[2], (const char*)d_in[3], (const char*)d_in[4],
                                                 (char*)d_ws, (char*)d_out);
}
```

```cpp
#include <hip/hip_runtime.h>
#include <hip/hip_bf16.h>
#include <type_traits>

typedef short s16x4 __attribute__((ext_vector_type(4)));
typedef __bf16 b16x8 __attribute__((ext_vector_type(8)));
typedef __bf16 b16x2 __attribute__((ext_vector_type(2)));
typedef float f32x2 __attribute__((ext_vector_type(2)));
typedef float f32x4 __attribute__((ext_vector_type(4)));
typedef float f32x16 __attribute__((ext_vector_type(16)));
typedef unsigned u32x2 __attribute__((ext_vector_type(2)));
typedef unsigned u32x4 __attribute__((ext_vector_type(4)));
typedef unsigned long long u64;
typedef u64 u64x4 __attribute__((ext_vector_type(4)));
typedef u64 u64x2 __attribute__((ext_vector_type(2)));
#define LAS __attribute__((address_space(3)))
#define DI __device__ __forceinline__
#define SBAR() __builtin_amdgcn_sched_barrier(0)

constexpr int TILE_B = 12288;
constexpr int FRAME_B = 393216;
constexpr unsigned WS_H2 = 0, WS_ST = 8 * FRAME_B, WS_WM = 16 * FRAME_B, WS_SEAL = WS_WM + 3 * 73728, WS_FLAG = WS_SEAL + 216 * 128, WS_FLAG2 = WS_FLAG + 8 * 2048, WS_EPOCH = WS_FLAG2 + 2048, WS_END = WS_EPOCH + 256;
constexpr float QSCALE = 0.07216878364870322f * 1.4426950408889634f;
constexpr int SC1 = 16;
constexpr unsigned POISON = 0xffffffffu;
constexpr unsigned CODE_BYTES = 26112;
constexpr unsigned CODE_SHARE = 32;
constexpr int OUT_AUX = 16;
constexpr int TILE0_DELAY = 2, LOAD_DELAY = 2, WQ_SPLIT = 4, B9_DELAY = 2, VO_1 = 1, VO_2 = 3, MM_EARLY = 0, PQ = 12, PV = 9, PI = 23;
static_assert(9 * PQ + 18 * PV + 5 * PI >= 384, "the whole frame is poisoned");
constexpr int SPIN_CAP = 1 << 16;

#define ST_DECL
#define ST(k)
#define ST_OUT(ws, k0, k1)

DI float asf(unsigned u) { return __builtin_bit_cast(float, u); }
DI unsigned asu(float f) { return __builtin_bit_cast(unsigned, f); }
DI unsigned cvtpk(float lo, float hi) { f32x2 v = {lo, hi}; return __builtin_bit_cast(unsigned, __builtin_convertvector(v, b16x2)); }
DI float bflo(unsigned u) { return asf(u << 16); }
DI float bfhi(unsigned u) { return asf(u & 0xffff0000u); }
DI f32x16 mfma32(u32x4 a, u32x4 b, f32x16 c) { return __builtin_amdgcn_mfma_f32_32x32x16_bf16(__builtin_bit_cast(b16x8, a), __builtin_bit_cast(b16x8, b), c, 0, 0, 0); }
DI f32x4 mfma16(u32x4 a, u32x4 b, f32x4 c) { return __builtin_amdgcn_mfma_f32_16x16x32_bf16(__builtin_bit_cast(b16x8, a), __builtin_bit_cast(b16x8, b), c, 0, 0, 0); }
DI f32x4 mfma16k16(u32x2 a, u32x2 b, f32x4 c) { return __builtin_amdgcn_mfma_f32_16x16x16bf16_1k(__builtin_bit_cast(s16x4, a), __builtin_bit_cast(s16x4, b), c, 0, 0, 0); }
DI u32x2 pack4(f32x4 v) { u32x2 r = {cvtpk(v[0], v[1]), cvtpk(v[2], v[3])}; return r; }
DI u32x4 lds_r128(unsigned a) { return *(LAS u32x4*)(size_t)a; }
DI f32x4 lds_r128f(unsigned a) { return *(LAS f32x4*)(size_t)a; }
DI u32x2 lds_r64(unsigned a) { return *(LAS u32x2*)(size_t)a; }
DI void lds_w128(unsigned a, u32x4 v) { *(LAS u32x4*)(size_t)a = v; }
DI void lds_w128f(unsigned a, f32x4 v) { *(LAS f32x4*)(size_t)a = v; }
DI void lds_w64(unsigned a, u32x2 v) { *(LAS u32x2*)(size_t)a = v; }
DI void lds_w32(unsigned a, float v) { *(LAS float*)(size_t)a = v; }
DI u32x2 lds_tr(unsigned a) { return __builtin_bit_cast(u32x2, __builtin_amdgcn_ds_read_tr16_b64_v4i16((LAS s16x4*)(size_t)a)); }
DI u32x4 lds_tr2(unsigned a0, unsigned a1) { u32x2 x = lds_tr(a0), y = lds_tr(a1); u32x4 r = {x[0], x[1], y[0], y[1]}; return r; }
#define BAR() asm volatile("s_waitcnt lgkmcnt(0)\n\ts_barrier" ::: "memory")
#define BARV(n) asm volatile("s_waitcnt vmcnt(" #n ") lgkmcnt(0)\n\ts_barrier" ::: "memory")
template <int CTRL> DI float dpp_add(float v) { return v + asf(__builtin_amdgcn_update_dpp(0u, asu(v), CTRL, 0xf, 0xf, true)); }
DI float xor16_add(float v) { auto r = __builtin_amdgcn_permlane16_swap(asu(v), asu(v), false, false); return asf(r[0]) + asf(r[1]); }
DI float xor16_max(float v) { auto r = __builtin_amdgcn_permlane16_swap(asu(v), asu(v), false, false); return __builtin_amdgcn_fmed3f(asf(r[0]), asf(r[1]), __builtin_inff()); }
DI unsigned umax3(unsigned a, unsigned b, unsigned c) { return max(max(a, b), c); }
template <int N> DI bool poisoned(const u32x4 (&v)[N]) {
  unsigned m = 0;
#pragma unroll
  for (int i = 0; i < N; ++i) { m = umax3(m, v[i][0], v[i][1]); m = umax3(m, v[i][2], v[i][3]); }
  return __any(m == POISON);
}
DI unsigned fi_pos(unsigned r, unsigned c8) {
  return (((r >> 4) * 6 + (c8 >> 2)) * 64 + (c8 & 3) * 16 + ((r & 15) ^ ((c8 & 1) | (((r >> 3) & 1) << 1) | ((r >> 4) << 2)))) * 16;
}
DI float row8_sum(float s) { s = dpp_add<0x124>(s); s = dpp_add<0x128>(s); return xor16_add(s); }
DI float sum24(const f32x2 (&v)[12]) {
  f32x2 s0 = v[0] + v[1], s1 = v[2] + v[3];
#pragma unroll
  for (int i = 4; i < 12; i += 2) { s0 += v[i]; s1 += v[i + 1]; }
  s0 += s1;
  return s0[0] + s0[1];
}
DI float sumsq24(const f32x2 (&v)[12]) {
  f32x2 q0 = v[0] * v[0], q1 = v[1] * v[1];
#pragma unroll
  for (int i = 2; i < 12; i += 2) { q0 = __builtin_elementwise_fma(v[i], v[i], q0); q1 = __builtin_elementwise_fma(v[i + 1], v[i + 1], q1); }
  q0 += q1;
  return q0[0] + q0[1];
}
DI f32x2 unpack2(unsigned u) { f32x2 r = {bflo(u), bfhi(u)}; return r; }

constexpr unsigned SLOT = 36864, L_X = 3 * SLOT, L_H = 4 * SLOT, L_N = L_H + TILE_B, L_L = L_N + 1024, L_PT = L_L + 1024, LDS_BYTES = 163840;
constexpr unsigned QK = 24576, L_JOB = 4 * QK;
constexpr unsigned L_VI = L_X, L_SP = L_X + TILE_B, L_YI = L_X + 20480;
constexpr unsigned L_Q = L_H, L_FI = L_H;

#define DMA3(v0, v1, v2, sbase, ldsa) \
  asm volatile("s_mov_b32 m0, %4\n\ts_nop 0\n\tglobal_load_lds_dwordx4 %0, %3\n\tglobal_load_lds_dwordx4 %1, %3 offset:1024\n\tglobal_load_lds_dwordx4 %2, %3 offset:2048" \
               :: "v"(v0), "v"(v1), "v"(v2), "s"(sbase), "s"(ldsa) : "memory")

template <int OFF> DI void bdma(unsigned voff, u32x4 rs, unsigned soff) { asm volatile("buffer_load_dwordx4 %0, %1, %2 offen offset:%3 sc1 lds" :: "v"(voff), "s"(rs), "s"(soff), "i"(OFF) : "memory"); }
template <int OFF> DI void setm0(unsigned base) { asm volatile("s_add_u32 m0, %0, %1\n\ts_nop 0" :: "s"(base), "i"(OFF) : "memory", "scc"); }
template <int N> DI void waitvm() { asm volatile("s_waitcnt vmcnt(%0)" :: "i"(N) : "memory"); }
#define IC(x) std::integral_constant<int, (x)>{}
#define OPAQUE(v) asm volatile("" : "+v"(v))
template <class R> DI void st128(u32x4 v, R rs, unsigned voff, unsigned soff) { __builtin_amdgcn_raw_buffer_store_b128(v, rs, voff, soff, SC1); asm volatile("s_nop 1" :: "v"(v)); }
template <int CTRL> DI unsigned dpp_addu(unsigned v) { return v + __builtin_amdgcn_update_dpp(0u, v, CTRL, 0xf, 0xf, true); }
DI unsigned fold_sum(unsigned e, unsigned o) {
  auto r = __builtin_amdgcn_permlane32_swap(e, o, false, false);
  unsigned u = r[0] + r[1];
  auto q = __builtin_amdgcn_permlane16_swap(u, u, false, false);
  u = q[0] + q[1];
  u = dpp_addu<0x128>(u); u = dpp_addu<0x124>(u); u = dpp_addu<0x4e>(u); return dpp_addu<0xb1>(u);
}
DI bool seal_ok(unsigned e, unsigned o, u32x4 seal, bool has, u32x2 tag) {
  const unsigned bad = has ? (seal[1] ^ tag[0]) | (seal[3] ^ tag[1]) : 0u;
  return __builtin_amdgcn_ballot_w64((fold_sum(e - seal[0], o - seal[2]) | bad) != 0) == 0;
}
DI void add2(unsigned& s, unsigned a, unsigned b) { asm("v_add3_u32 %0, %0, %1, %2" : "+v"(s) : "v"(a), "v"(b)); }
#define SUM_IN(e, o, v) do { add2(e, (v)[0], (v)[2]); add2(o, (v)[1], (v)[3]); } while (0)
template <int N> DI bool sealed(const u32x4 (&v)[N], u32x4 seal, bool has, u32x2 tag) {
  unsigned e = 0, o = 0;
#pragma unroll
  for (int i = 0; i < N; ++i) SUM_IN(e, o, v[i]);
  return seal_ok(e, o, seal, has, tag);
}

__global__ __launch_bounds__(512, 2) void k_timesformer(const char* x_, const char* wqkv_t_, const char* wo_t_, const char* wqkv_s_, const char* wo_s_, char* ws_, char* out_) {
  unsigned dc;
  u64x4 ka; u64x2 kb; u64 kc;
  {
    u64 pc;
    asm volatile("s_load_dwordx8 %2, %6, 0x0\n\ts_load_dwordx4 %3, %6, 0x20\n\ts_load_dwordx2 %4, %6, 0x30\n\ts_getpc_b64 %1\n\tglobal_load_dword %0, %5, %1"
                 : "=v"(dc), "=&s"(pc), "=&s"(ka), "=&s"(kb), "=&s"(kc) : "v"(min((threadIdx.x * CODE_SHARE + ((blockIdx.x >> 3) & (CODE_SHARE - 1))) * 128, CODE_BYTES)), "s"(__builtin_amdgcn_kernarg_segment_ptr()) : "memory");
  }
  constexpr unsigned lds0 = 0;
  const unsigned b = blockIdx.x;
  unsigned w8 = __builtin_amdgcn_readfirstlane(threadIdx.x >> 6);
  unsigned l = threadIdx.x & 63;
  const unsigned t = b & 7, qb = b >> 3;
  w8 = (w8 & 4) | ((w8 + qb / 6) & 3);
  ST_DECL;
  ST(0);
  const bool jqk = b < 72, job = b < 216;
  const unsigned jp = b % 72, jw = jp / 18, jks = (jp - jw * 18) / 3, jtau = jp - jw * 15 - jks * 3;
  const unsigned rot = qb % 6;
  unsigned fo[2][3];
#pragma unroll
  for (int i = 0; i < 3; ++i) {
    const unsigned p = i * 64 + l;
    if (w8 < 4) { fo[0][i] = p * 16 + (p >= 96 ? FRAME_B - 1536 : 0) + w8 * 2 * FRAME_B + b * 1536 - i * 1024; fo[1][i] = 0; }
    else if (w8 == 7) {
      const unsigned row = (p * 1366) >> 16;
      fo[0][i] = row * 1152 + (((p - row * 48) ^ (row << 1)) * 16) - i * 1024; fo[1][i] = row * 1152 + (((p - row * 48) ^ (row << 1) ^ 8) * 16) - i * 1024;
    } else {
      const unsigned row = (p * 2731) >> 16, o = row * 1152 + (((p - row * 24) ^ row) * 16) - i * 1024;
      fo[0][i] = o + jks * (32 * 1152) + (jqk ? 0 : 768);
      fo[1][i] = jqk ? o + jtau * (16 * 1152) + 384 : (l >> 1) * 384 + (l & 1) * 16 + jtau * 32 + i * (12288 - 1024);
    }
  }
  asm volatile("s_waitcnt lgkmcnt(0)" : "+s"(ka), "+s"(kb), "+s"(kc), "+v"(fo[0][0]), "+v"(fo[0][1]), "+v"(fo[0][2]), "+v"(fo[1][0]), "+v"(fo[1][1]), "+v"(fo[1][2]) :: "memory");
  char* const ws = (char*)kb[1];
  auto chunk = [&](const int n) {
    const u64 src = ka[1] + (n + rot >= 6 ? n + rot - 6 : n + rot) * (32 * 1152);
#pragma unroll
    for (int q = 0; q < 8; ++q) DMA3(fo[(q >> 1) & 1][0], fo[(q >> 1) & 1][1], fo[(q >> 1) & 1][2], src + q * 4608, (n & 3) * QK + q * 3072);
  };
  unsigned dw = 0, dx = 0;
  if (w8 < 4) {
    DMA3(fo[0][0], fo[0][1], fo[0][2], ka[0], L_YI + w8 * 3072);
  } else if (w8 == 4) {
    if (job) {
      __builtin_amdgcn_s_setprio(3);
      const u64 wqkv = b >= 144 ? ka[1] : ka[3], second = jqk ? wqkv : b >= 144 ? ka[2] : kb[0];
#pragma unroll
      for (int q = 0; q < 4; ++q) DMA3(fo[0][0], fo[0][1], fo[0][2], wqkv + q * (8 * 1152), L_JOB + q * 3072);
#pragma unroll
      for (int q = 0; q < 2; ++q) DMA3(fo[1][0], fo[1][1], fo[1][2], second + q * (jqk ? 8 * 1152 : 36864), L_JOB + 12288 + q * 3072);
    }
  } else if (w8 == 7) {
    __builtin_amdgcn_s_sleep(LOAD_DELAY);
    chunk(0); chunk(1);
  } else {
    if (w8 == 6) asm volatile("global_load_dword %0, %1, %2" : "=v"(dx) : "v"((qb * 6 + min(l, 35u) / 6) * 1152 + (min(l, 35u) % 6) * 128), "s"(ka[1]) : "memory");
    asm volatile("global_load_dword %0, %1, %2" : "=v"(dw) : "v"((qb * 18 + min(l, 17u)) * 128), "s"(w8 == 6 ? ka[2] : kb[0]) : "memory");
  }
  const auto rsWS = __builtin_amdgcn_make_buffer_rsrc((void*)ws, 0, WS_END, 0x00020000);
  const u32x4 pz = {POISON, POISON, POISON, POISON};
  u64 epoch;
  asm volatile("s_load_dwordx2 %0, %1, 0x0" : "=&s"(epoch) : "s"(ws + WS_EPOCH) : "memory");
  asm volatile("s_waitcnt lgkmcnt(0)" : "+s"(epoch) :: "memory");
  epoch += 1;
  const u32x2 tag = {(unsigned)epoch, (unsigned)(epoch >> 32) ^ ((unsigned)epoch * 0x9e3779b1u)};
  asm volatile("" : "+s"(w8));
  const unsigned w = w8 & 3;
#define LANE_IDS const unsigned i16 = l & 15, g = l >> 4, r32 = l & 31, hh = l >> 5, tq = i16 >> 2, tp = l & 3
#define FRAG_POS unsigned fr[2], dp[2];                                                                       \
  fr[0] = L_X + fi_pos(i16, g); fr[1] = (fr[0] ^ 64) + 6144; dp[0] = L_X + fi_pos(i16, hh) + 8 * (g & 1) + w * 1536; dp[1] = (dp[0] ^ 64) + 6144; \
  OPAQUE(fr[0]); OPAQUE(fr[1]); OPAQUE(dp[0]); OPAQUE(dp[1])
  auto ldw = [&](u32x4 (&W)[18], const int m, const int k0, const int k1, const unsigned rot) {
#pragma unroll
    for (int kk = k0; kk < k1; ++kk) {
      const unsigned ks = kk + rot >= 6 ? kk + rot - 6 : kk + rot;
#pragma unroll
      for (int i = 0; i < 3; ++i) { SBAR(); W[kk * 3 + i] = __builtin_amdgcn_raw_buffer_load_b128(rsWS, l * 16 + i * 1024, WS_WM + m * 73728 + w * 18432 + ks * 3072, SC1); }
    }
  };
  auto ld_seal = [&](const int m) {
    return __builtin_amdgcn_raw_buffer_load_b128(rsWS, l < 18 ? l * 128 : WS_END, WS_SEAL + (m * 72 + 18 * w) * 128, SC1);
  };
  auto h_frags = [&](u32x4 (&H)[6][2], const unsigned (&fr)[2], const int k0, const int k1) {
#pragma unroll
    for (int kk = k0; kk < k1; ++kk) {
      const unsigned ks = kk + rot >= 6 ? kk + rot - 6 : kk + rot;
      H[kk][0] = lds_r128(fr[0] + ks * 1024 + (L_H - L_X)); H[kk][1] = lds_r128(fr[1] + ks * 1024 + (L_H - L_X));
    }
  };
  auto low_sum = [&](const f32x4 a, const f32x4 c, float (&lt)[2]) {
    lt[0] = (a[0] + a[2]) + (c[0] + c[2]);
    lt[1] = (a[1] + a[3]) + (c[1] + c[3]);
    return (bool)__any(lt[0] < 8.7e-19f || lt[1] < 8.7e-19f);
  };

  if (w8 < 4) {
    const unsigned nl = l & 3, j = (l >> 2) & 7, tt = 2 * w + (l >> 5), r = nl * 8 + tt;
    unsigned rp = L_X + fi_pos(r, j);
    OPAQUE(rp);
    waitvm<0>();
    asm volatile("" :: "v"(dc));
    f32x2 xv[12];
    float mu1;
    {
      u32x4 h[3], xr[3];
#pragma unroll
      for (int i = 0; i < 3; ++i) xr[i] = lds_r128(lds0 + L_YI + tt * 1536 + nl * 384 + j * 16 + i * 128);
#pragma unroll
      for (int i = 0; i < 12; ++i) xv[i] = unpack2(xr[i >> 2][i & 3]);
      mu1 = row8_sum(sum24(xv)) * (1.0f / 192.0f);
#pragma unroll
      for (int i = 0; i < 12; ++i) xv[i] -= mu1;
      const float rs = __builtin_amdgcn_rsqf(row8_sum(sumsq24(xv)) * (1.0f / 192.0f) + 1e-5f);
#pragma unroll
      for (int i = 0; i < 12; ++i) { const f32x2 n = xv[i] * rs; h[i >> 2][i & 3] = cvtpk(n[0], n[1]); }
#pragma unroll
      for (int i = 0; i < 3; ++i) lds_w128(rp + (L_H - L_X) + i * 2048, h[i]);
    }
    SBAR();
    OPAQUE(l);
    unsigned so = tt * FRAME_B + (j >> 1) * 1024 + ((t ^ ((j >> 1) & 1)) * 128) + (((j & 1) ^ (t >> 2)) * 64) + nl * 16;
    const unsigned sso = qb * TILE_B;
    LANE_IDS;
    FRAG_POS;
    unsigned sto = j == 0 ? (tt * 1024 + nl) * 8 : WS_END;
    unsigned sa = L_SP + l * 16;
    unsigned keep = (hh == (i16 >> 3)) ? ~0u : 0u;
    OPAQUE(sto); OPAQUE(so); OPAQUE(sa); OPAQUE(keep);
    ST(1);
    BAR();
    ST(6);
    {
      u32x4 H[6][2], W[18];
      BAR();
      h_frags(H, fr, 0, 6);
      asm volatile("s_barrier" ::: "memory");
      ST(2);
#pragma unroll 1
      for (int sp = 0;; ++sp) {
        const u32x4 seal = ld_seal(2);
        ldw(W, 2, 0, VO_1, rot);
        if (sp == 0) asm volatile("s_barrier" ::: "memory");
        ldw(W, 2, VO_1, VO_2, rot);
        if (sp == 0) asm volatile("s_barrier" ::: "memory");
        ldw(W, 2, VO_2, 6, rot);
        SBAR();
        f32x4 vp[3][2];
        unsigned he = 0, ho = 0;
        auto mm = [&](const int kk) {
#pragma unroll
          for (int i = 0; i < 3; ++i) {
            vp[i][0] = mfma16(H[kk][0], W[kk * 3 + i], kk ? vp[i][0] : f32x4{0, 0, 0, 0});
            add2(he, W[kk * 3 + i][0], W[kk * 3 + i][2]);
            SBAR();
            vp[i][1] = mfma16(H[kk][1], W[kk * 3 + i], kk ? vp[i][1] : f32x4{0, 0, 0, 0});
            add2(ho, W[kk * 3 + i][1], W[kk * 3 + i][3]);
            SBAR();
          }
        };
        ST(3);
#pragma unroll
        for (int kk = 0; kk < MM_EARLY; ++kk) mm(kk);
        OPAQUE(he); OPAQUE(ho);
        ST(8);
        if (sp == 0) asm volatile("s_barrier" ::: "memory");
        u32x2 pt[2];
        {
          f32x4 e = (lds_r128f(sa) + lds_r128f(sa + 1024)) + lds_r128f(sa + 2048);
          const float mq = xor16_max(fmaxf(fmaxf(e[0], e[1]), fmaxf(e[2], e[3]))) * -QSCALE;
#pragma unroll
          for (int k = 0; k < 4; ++k) e[k] = __builtin_amdgcn_exp2f(fmaf(e[k], QSCALE, mq));
          pt[0] = pack4(e * __builtin_amdgcn_rcpf(xor16_add((e[0] + e[1]) + (e[2] + e[3]))));
#pragma unroll
          for (int k = 0; k < 2; ++k) {
            auto q = __builtin_amdgcn_permlane32_swap(pt[0][k], pt[0][k], false, false);
            pt[1][k] = (q[0] ^ q[1] ^ pt[0][k]) & keep;
            pt[0][k] &= keep;
          }
          OPAQUE(pt[0]); OPAQUE(pt[1]);
        }
        SBAR();
        ST(4);
#pragma unroll
        for (int kk = MM_EARLY; kk < 6; ++kk) mm(kk);
        OPAQUE(he); OPAQUE(ho);
        ST(7);
        f32x4 y[3][2];
#pragma unroll
        for (int hf = 0; hf < 2; ++hf)
#pragma unroll
          for (int i = 0; i < 3; ++i) y[i][hf] = mfma16k16(pack4(vp[i][hf]), pt[hf], f32x4{0, 0, 0, 0});
        SBAR();
#pragma unroll
        for (int hf = 0; hf < 2; ++hf)
#pragma unroll
          for (int i = 0; i < 3; ++i) lds_w64(dp[hf] + (L_YI - L_X) + i * 512, pack4(y[i][hf]));
        if (__builtin_expect(seal_ok(he, ho, seal, l < 18, tag), 1) || sp >= SPIN_CAP) break;
        asm volatile("" ::: "memory");
      }
    }
    ST(9);
    BAR();
    {
      u32x4 y[3], h[3];
#pragma unroll
      for (int i = 0; i < 3; ++i) y[i] = lds_r128(rp + (L_YI - L_X) + i * 2048);
#pragma unroll
      for (int i = 0; i < 12; ++i) xv[i] += unpack2(y[i >> 2][i & 3]);
      const float mu = row8_sum(sum24(xv)) * (1.0f / 192.0f), sq = row8_sum(sumsq24(xv));
      const float var = fmaxf(fmaf(sq, 1.0f / 192.0f, -mu * mu), 0.f) + 1e-5f;
      const float rs = __builtin_amdgcn_rsqf(var), nb = -mu * rs;
#pragma unroll
      for (int i = 0; i < 3; ++i) {
#pragma unroll
        for (int e = 0; e < 4; ++e) { const f32x2 n = xv[4 * i + e] * rs + nb; h[i][e] = cvtpk(n[0], n[1]); }
        if (i == 0) { OPAQUE(h[0]); ST(5); asm volatile("s_barrier" ::: "memory"); }
        st128(h[i], rsWS, so, WS_H2 + sso + i * 4096);
        SBAR();
      }
      const u32x2 stat = {asu(mu + mu1), asu(var)};
      __builtin_amdgcn_raw_buffer_store_b64(stat, rsWS, sto, WS_ST + b * 32, SC1);
    }
    ST(10);
    const unsigned ring = lds0 + w * SLOT;
    unsigned kr0 = ring + (r32 >> 2) * 128 + (hh ^ (r32 >> 4)) * 64 + (r32 & 3) * 16, kr1 = kr0 ^ 128;
    unsigned vr0 = ring + (g & 1) * 1024 + (hh ^ (g & 1)) * 128 + (tp >> 1) * 64 + tq * 16 + 8 * (tp & 1), vr1 = (vr0 ^ 64) + 512;
    unsigned lo[3] = {l * 16, l * 16 + 4096, l * 16 + 8192}, pw = ring + 2 * TILE_B + l * 16, lw = l < 16 ? L_L + (l * 4 + w) * 8 : L_L + 512 + l * 8;
    OPAQUE(kr0); OPAQUE(kr1); OPAQUE(vr0); OPAQUE(vr1); OPAQUE(lo[0]); OPAQUE(lo[1]); OPAQUE(lo[2]); OPAQUE(pw); OPAQUE(lw);
    const u64 hbu = (u64)(ws + WS_H2 + (size_t)t * FRAME_B);
    const u32x4 rs = {(unsigned)hbu, (unsigned)(hbu >> 32) & 0xffffu, (unsigned)FRAME_B, 0x00020000u};
    unsigned toff[8];
#pragma unroll
    for (int i = 0; i < 8; ++i) { toff[i] = (8 * w + ((i + qb) & 7)) * TILE_B; asm volatile("" : "+s"(toff[i])); }
    u32x4 sel;
    { const unsigned one = (tp == (g & 1)) ? 0x3f803f80u : 0u; sel = u32x4{one, one, one, one}; OPAQUE(sel); }
    auto piece = [&](auto Tc, auto Pc) {
      constexpr int T = decltype(Tc)::value, p = decltype(Pc)::value;
      if constexpr (T < 8) {
        if constexpr (p % 4 == 0) setm0<(T % 3) * TILE_B + p * 1024>(ring);
        bdma<(p % 4) * 1024>(lo[p / 4], rs, toff[T]);
      }
    };
#define P6(T, a) piece(IC(T), IC(a)); piece(IC(T), IC(a + 1)); piece(IC(T), IC(a + 2)); piece(IC(T), IC(a + 3)); piece(IC(T), IC(a + 4)); piece(IC(T), IC(a + 5));
    auto read_valid = [&](u32x4 (&KK)[12], const unsigned dst, unsigned off) {
      for (int sp = 0; sp < SPIN_CAP; ++sp) {
#pragma unroll
        for (int k = 0; k < 12; ++k) KK[k] = lds_r128(((k & 1) ? kr1 : kr0) + dst + k * 1024);
        if (__builtin_expect(!poisoned(KK), 1)) break;
#pragma unroll 1
        for (int k = 0; k < 12; ++k) { setm0<0>(ring + dst + k * 1024); bdma<0>(lo[0], rs, off + k * 1024); }
        waitvm<0>();
      }
    };
    unsigned qa = lds0 + L_Q + lo[0], na = lds0 + L_N + r32 * 16;
    OPAQUE(qa); OPAQUE(na);
    waitvm<0>();
    __builtin_amdgcn_s_sleep(B9_DELAY);
    BAR();
    __builtin_amdgcn_s_sleep(TILE0_DELAY);
    if (w != (qb >> 3)) { P6(0, 0) P6(0, 6) }
    else if (l == 0) __builtin_amdgcn_raw_buffer_store_b64(tag, rsWS, 0, WS_FLAG2 + b * 8, SC1);
    ST(11);
    BAR();
    u32x4 K0[12];
    if (w != (qb >> 3)) waitvm<0>();
    ST(12);
    read_valid(K0, 0, toff[0]);
    ST(13);
    piece(IC(1), IC(0)); piece(IC(1), IC(1)); piece(IC(1), IC(2)); piece(IC(1), IC(3));
    BAR();
    ST(14);
    u32x4 Q[12];
    float mref;
    {
      const f32x4 n0 = lds_r128f(na), n1 = lds_r128f(na + 512);
#pragma unroll
      for (int s = 0; s < 6; ++s) Q[s] = lds_r128(qa + s * 1024);
      SBAR();
      const f32x4 s = n0 + n1;
      mref = __builtin_amdgcn_sqrtf(((s[0] + s[1]) + (s[2] + s[3])) * (192.0f * 1.02f));
      OPAQUE(mref);
      SBAR();
#pragma unroll
      for (int s = 6; s < 12; ++s) Q[s] = lds_r128(qa + s * 1024);
      SBAR();
    }
    constexpr int KW = 4, VW = 4;
    bool safe = false, exact = false, late = false;
#pragma unroll 1
    for (;;) {
      f32x16 S[2], O[6], negm;
      u32x4 K[12], V[12], P[2][2];
      f32x4 lsum = {0, 0, 0, 0};
      float pe[2][2];
#pragma unroll
      for (int i = 0; i < 16; ++i) negm[i] = -mref;
#define RK(k, T) K[k] = lds_r128((((k) & 1) ? kr1 : kr0) + ((T) % 3) * TILE_B + (k) * 1024);
#define RV(u, T) { const unsigned a_ = (((u) & 1) ? vr1 : vr0) + ((T) % 3) * TILE_B + ((u) >> 1) * 2048; V[u] = lds_tr2(a_, a_ + 256); }
      auto sm = [&](auto Tc, auto Hc, auto Gc) {
        constexpr int T = decltype(Tc)::value, H = decltype(Hc)::value, G = decltype(Gc)::value;
        if constexpr (G < 8) pe[(G >> 1) & 1][G & 1] = __builtin_amdgcn_exp2f(S[T & 1][8 * H + G]);
        if constexpr (G >= 2 && G <= 8 && G % 2 == 0) { constexpr int q = G / 2 - 1; P[T & 1][H][q] = cvtpk(pe[q & 1][0], pe[q & 1][1]); }
        if constexpr (G == 10) lsum = mfma16(sel, P[T & 1][H], lsum);
      };
      auto put_o = [&](const int un) {
        u32x4 pk;
#pragma unroll
        for (int e = 0; e < 4; ++e) pk[e] = cvtpk(O[un >> 1][8 * (un & 1) + 2 * e], O[un >> 1][8 * (un & 1) + 2 * e + 1]);
        lds_w128(pw + un * 1024, pk);
      };
      auto qkphase = [&](auto Ic) {
        constexpr int I = decltype(Ic)::value;
        auto step = [&](auto Kc) {
          constexpr int k = decltype(Kc)::value;
          if constexpr (I + 1 < 8) {
            S[(I + 1) & 1] = mfma32(I < 0 ? K0[k] : K[k], Q[k], k == 0 ? negm : S[(I + 1) & 1]);
            if constexpr (k + KW < 12 && I >= 0) RK(k + KW, I + 1)
          }
          if constexpr (I < 0 && k < 8) piece(IC(1), IC(4 + k));
          if constexpr (I >= 0) { if constexpr (k & 1) piece(IC(I + 2), IC(6 + k / 2)); if constexpr (k < 11) sm(IC(I), IC(1), IC(k)); }
          if constexpr (I >= 0 && I < 7 && k >= 12 - VW) RV(k - (12 - VW), I)
          SBAR();
        };
        step(IC(0)); step(IC(1)); step(IC(2)); step(IC(3)); step(IC(4)); step(IC(5)); step(IC(6)); step(IC(7)); step(IC(8)); step(IC(9)); step(IC(10)); step(IC(11));
      };
      auto pvphase = [&](auto Ic) {
        constexpr int I = decltype(Ic)::value;
        auto step = [&](auto Uc) {
          constexpr int u = decltype(Uc)::value;
          if constexpr (I >= 0) {
            f32x16 z;
#pragma unroll
            for (int e = 0; e < 16; ++e) z[e] = 0.f;
            O[u >> 1] = mfma32(V[u], P[I & 1][u & 1], (I == 0 && !(u & 1)) ? z : O[u >> 1]);
            if constexpr (u & 1) piece(IC(I + 3), IC(u / 2));
            if constexpr (u + VW < 12) RV(u + VW, I)
          }
          if constexpr (I + 1 < 8 && u >= 1) sm(IC(I + 1), IC(0), IC(u - 1));
          if constexpr (I < 0 && (u & 1)) piece(IC(2), IC(u / 2));
          if constexpr (I + 2 < 8) {
            if constexpr (u == 12 - KW) waitvm<(I < 0 ? 12 : I + 3 < 8 ? 6 + (12 - KW) / 2 : 6)>();
            if constexpr (u >= 12 - KW) RK(u - (12 - KW), I + 2)
            if constexpr (u == 11) waitvm<(I + 3 < 8 ? 6 : 0)>();
          }
          SBAR();
        };
        step(IC(0)); step(IC(1)); step(IC(2)); step(IC(3)); step(IC(4)); step(IC(5)); step(IC(6)); step(IC(7)); step(IC(8)); step(IC(9)); step(IC(10)); step(IC(11));
      };
      auto drain = [&]() {
        constexpr int tl[24] = {6, 6, 6, 6, 6, 6, 6, 6, 7, 7, 7, 7, 6, 6, 7, 7, 6, 6, 7, 7, 7, 7, 7, 7};
        constexpr int fu[24] = {0, 1, 2, 3, 4, 5, 6, 7, 0, 1, 2, 3, 8, 9, 4, 5, 10, 11, 6, 7, 8, 9, 10, 11};
        constexpr int wr[24] = {-1, -1, -1, -1, -1, -1, -1, -1, -1, -1, -1, 0, 1, 2, -1, 3, -1, 4, 5, -1, -1, 6, 7, 8};
        u32x4 X[24];
#pragma unroll
        for (int n = 0; n < VW; ++n) X[n] = V[n];
        auto step = [&](auto Nc) {
          constexpr int n = decltype(Nc)::value;
          O[fu[n] >> 1] = mfma32(X[n], P[tl[n] & 1][fu[n] & 1], O[fu[n] >> 1]);
          if constexpr (n + VW < 24) {
            const unsigned a_ = ((fu[n + VW] & 1) ? vr1 : vr0) + (tl[n + VW] % 3) * TILE_B + (fu[n + VW] >> 1) * 2048;
            X[n + VW] = lds_tr2(a_, a_ + 256);
          }
          if constexpr (n >= 1 && n <= 3) { sm(IC(7), IC(0), IC(3 * n - 3)); sm(IC(7), IC(0), IC(3 * n - 2)); sm(IC(7), IC(0), IC(3 * n - 1)); }
          if constexpr (n >= 4 && n <= 6) { sm(IC(7), IC(1), IC(3 * n - 12)); sm(IC(7), IC(1), IC(3 * n - 11)); sm(IC(7), IC(1), IC(3 * n - 10)); }
          if constexpr (n == 7) sm(IC(7), IC(0), IC(10));
          if constexpr (n == 9) sm(IC(7), IC(1), IC(10));
          if constexpr (wr[n] >= 0) put_o(wr[n]);
          SBAR();
        };
        step(IC(0)); step(IC(1)); step(IC(2)); step(IC(3)); step(IC(4)); step(IC(5)); step(IC(6)); step(IC(7)); step(IC(8)); step(IC(9)); step(IC(10)); step(IC(11));
        step(IC(12)); step(IC(13)); step(IC(14)); step(IC(15)); step(IC(16)); step(IC(17)); step(IC(18)); step(IC(19)); step(IC(20)); step(IC(21)); step(IC(22)); step(IC(23));
      };
#define STALE(v) (__builtin_expect(!safe && __any((v) != (v)), 0))
      bool done = false;
      do {
        qkphase(IC(-1));
        ST(15);
        pvphase(IC(-1));
        ST(16);
        qkphase(IC(0)); pvphase(IC(0)); qkphase(IC(1)); pvphase(IC(1)); qkphase(IC(2));
        const float seen = lsum[0];
        pvphase(IC(2));
        if (STALE(seen)) break;
        qkphase(IC(3)); pvphase(IC(3)); qkphase(IC(4)); pvphase(IC(4)); qkphase(IC(5));
        if (!late) asm volatile("s_barrier" ::: "memory");
        late = true;
        pvphase(IC(5)); qkphase(IC(6)); drain();
        if (STALE(lsum[0])) break;
        { const u32x2 ls = {asu(lsum[0]), asu(lsum[1])}; lds_w64(lw, ls); }
        put_o(9); put_o(10); put_o(11);
        done = true;
      } while (false);
#pragma unroll
      for (int k = 0; k < 12; ++k) asm volatile("" : "=v"(K0[k]));
      if (__builtin_expect(done, 1)) {
        ST(17);
        BAR();
        ST(18);
        float lt[2];
        unsigned ll = lo[0];
        OPAQUE(ll);
        ll >>= 4;
        if (__builtin_expect(!low_sum(lds_r128f(L_L + (ll & 15) * 32), lds_r128f(L_L + (ll & 15) * 32 + 16), lt) || exact, 1)) break;
        BAR();
        float mx = -3.0e38f;
#pragma unroll 1
        for (unsigned i = 0; i < 8; ++i) {
#pragma unroll 1
          for (int k = 0; k < 12; ++k) { setm0<0>(ring + k * 1024); bdma<0>(lo[0], rs, (8 * w + i) * TILE_B + k * 1024); }
          waitvm<0>();
          f32x16 s;
#pragma unroll
          for (int e = 0; e < 16; ++e) s[e] = 0.f;
#pragma unroll
          for (int k = 0; k < 12; ++k) s = mfma32(lds_r128(((k & 1) ? kr1 : kr0) + k * 1024), Q[k], s);
#pragma unroll
          for (int e = 0; e < 16; ++e) mx = fmaxf(mx, s[e]);
        }
        lds_w32(L_N + ((ll & 31) * 8 + w * 2 + (ll >> 5)) * 4, mx);
        BAR();
        const f32x4 c = lds_r128f(L_N + (ll & 31) * 32), d = lds_r128f(L_N + (ll & 31) * 32 + 16);
        mref = fmaxf(fmaxf(fmaxf(c[0], c[1]), fmaxf(c[2], c[3])), fmaxf(fmaxf(d[0], d[1]), fmaxf(d[2], d[3])));
        exact = true;
      } else {
        unsigned ll = lo[0];
        OPAQUE(ll);
        for (int sp = 0; sp < SPIN_CAP; ++sp) {
          asm volatile("" ::: "memory");
          const u32x2 f = __builtin_amdgcn_raw_buffer_load_b64(rsWS, ll >> 1, WS_FLAG2 + w * 512, SC1);
          if (__all(f[0] == tag[0] && f[1] == tag[1])) break;
        }
      }
      safe = true;
      asm volatile("s_waitcnt vmcnt(0) lgkmcnt(0)" ::: "memory");
      P6(0, 0) P6(0, 6)
      waitvm<0>();
      piece(IC(1), IC(0)); piece(IC(1), IC(1)); piece(IC(1), IC(2)); piece(IC(1), IC(3));
#pragma unroll
      for (int k = 0; k < 12; ++k) K0[k] = lds_r128(((k & 1) ? kr1 : kr0) + k * 1024);
    }
    BAR();
    {
      const auto rsO = __builtin_amdgcn_make_buffer_rsrc((void*)kc, 0, 8 * FRAME_B, 0x00020000);
      unsigned oa[3], og = lo[0] + w * 3072 + t * FRAME_B + qb * TILE_B;
#pragma unroll
      for (int i = 0; i < 3; ++i) {
        const unsigned p = i * 64 + (lo[0] >> 4), row = (p * 2731) >> 16;
        oa[i] = w * 3072 + (p ^ row) * 16;
        OPAQUE(oa[i]);
      }
      OPAQUE(og);
      BAR();
#pragma unroll
      for (int i = 0; i < 3; ++i) __builtin_amdgcn_raw_buffer_store_b128(lds_r128(oa[i]), rsO, og + i * 1024, 0, OUT_AUX);
    }
    ST(19);
    ST_OUT(ws, 0, 12);
  } else {
    LANE_IDS;
    FRAG_POS;
    u32x4 wq[18], wv[18];
    if (w == 3) {
      {
      __builtin_amdgcn_raw_buffer_store_b32(POISON, rsWS, l * 4, WS_ST + (t * 1024 + qb * 32) * 8, SC1);
      const unsigned p0 = WS_H2 + t * FRAME_B + (jqk ? qb * PQ : job ? 9 * (PQ - PV) + qb * PV : 384 + (qb - 32) * PI) * 1024, np = jqk ? PQ : job ? PV : PI;
#pragma unroll 1
      for (unsigned i = 0; i < np; ++i) __builtin_amdgcn_raw_buffer_store_b128(pz, rsWS, l * 16, p0 + i * 1024, SC1);
      asm volatile("s_nop 1" :: "v"(pz));
    }
      chunk(2);
      ST(1);
      if (jqk) waitvm<25 + PQ>(); else if (job) waitvm<25 + PV>(); else waitvm<25 + PI>();
      BAR();
      chunk(3);
      ST(2);
      BARV(24);
      __builtin_amdgcn_raw_buffer_store_b64(tag, rsWS, l < 8 ? l * 2048 : WS_END, WS_FLAG + b * 8, SC1);
      chunk(4); chunk(5);
      ST(3);
      BARV(49);
      BARV(24);
      BARV(0);
    } else {
      unsigned ta[2][4][2];
#pragma unroll
      for (int m = 0; m < 2; ++m)
#pragma unroll
        for (int i = 0; i < 4; ++i) {
          ta[m][i][0] = (8 * g + tq) * 768 + (((12 * m + 4 * w + i) ^ (tq | ((g & 1) << 2))) * 2 + (tp >> 1)) * 16 + 8 * (tp & 1);
          ta[m][i][1] = ta[m][i][0] + 2 * QK;
          OPAQUE(ta[m][i][0]); OPAQUE(ta[m][i][1]);
        }
      unsigned spa = hh == (i16 >> 3) ? L_SP + w * 1024 + l * 16 : L_SP + 4096 + l * 16;
      OPAQUE(spa);
      if (w == 0 && job) {
        unsigned ja[2] = {L_JOB + i16 * 384 + ((g ^ (l & 7)) * 16), L_JOB + i16 * 384 + (((4 + g) ^ (l & 7)) * 16)}, jb = L_JOB + 12288 + (8 * g + tq) * 32 + tp * 8;
        unsigned jo = (g & 1) * 512 + (hh * 16 + i16) * 16;
        OPAQUE(ja[0]); OPAQUE(ja[1]); OPAQUE(jb); OPAQUE(jo);
        waitvm<0>();
        ST(2);
        u32x4 bb[6];
        if (jqk) {
#pragma unroll
          for (int kk = 0; kk < 6; ++kk) bb[kk] = lds_r128(ja[kk & 1] + (kk >> 1) * 128 + 12288);
        } else {
#pragma unroll
          for (int kk = 0; kk < 6; ++kk) bb[kk] = lds_tr2(jb + kk * 1024, jb + kk * 1024 + 128);
        }
        f32x4 d = {0, 0, 0, 0}, e = {0, 0, 0, 0};
#pragma unroll
        for (int kk = 0; kk < 6; ++kk) {
          d = mfma16(lds_r128(ja[kk & 1] + (kk >> 1) * 128), bb[kk], d);
          e = mfma16(lds_r128(ja[kk & 1] + (kk >> 1) * 128 + 6144), bb[kk], e);
        }
        const float sc = jqk ? QSCALE : 1.0f;
        const u32x2 r0 = pack4(d * sc), r1 = pack4(e * sc);
        auto s0 = __builtin_amdgcn_permlane16_swap(r0[0], r1[0], false, false);
        auto s1 = __builtin_amdgcn_permlane16_swap(r0[1], r1[1], false, false);
        st128(u32x4{s0[0], s1[0], s0[1], s1[1]}, rsWS, jo, WS_WM + b * 1024);
        const u32x2 half = {fold_sum(r0[0] + r1[0], r0[1] + r1[1]), hh ? tag[1] : tag[0]};
        __builtin_amdgcn_raw_buffer_store_b64(half, rsWS, r32 == 0 ? hh * 8 : WS_END, WS_SEAL + b * 128, SC1);
        ST(3);
      }
      __builtin_amdgcn_s_setprio(3);
      u32x4 H[6][2];
      f32x4 acc[2][4][2];
      auto step = [&](const int n) {
        u32x4 a[8];
#pragma unroll
        for (int u = 0; u < 8; ++u) { const unsigned at = ta[u >> 2][u & 3][(n >> 1) & 1] + (n & 1) * QK; a[u] = lds_tr2(at, at + 3072); }
#pragma unroll
        for (int u = 0; u < 8; ++u) {
          acc[u >> 2][u & 3][0] = mfma16(a[u], H[n][0], n ? acc[u >> 2][u & 3][0] : f32x4{0, 0, 0, 0});
          acc[u >> 2][u & 3][1] = mfma16(a[u], H[n][1], n ? acc[u >> 2][u & 3][1] : f32x4{0, 0, 0, 0});
        }
        __builtin_amdgcn_sched_group_barrier(0x100, 8, 0);
#pragma unroll
        for (int q = 0; q < 4; ++q) { __builtin_amdgcn_sched_group_barrier(0x8, 2, 0); __builtin_amdgcn_sched_group_barrier(0x100, 2, 0); }
        SBAR();
      };
      ST(4);
      BAR();
      h_frags(H, fr, 0, 2);
      SBAR();
      step(0); step(1);
      BAR();
      h_frags(H, fr, 2, 6);
      SBAR();
      step(2);
      BAR();
      step(3);
      BAR();
      step(4);
      BAR();
      step(5);
      ST(19);
#pragma unroll
      for (int hf = 0; hf < 2; ++hf) {
        f32x4 st = {0, 0, 0, 0};
#pragma unroll
        for (int i = 0; i < 4; ++i) st = mfma16k16(pack4(acc[1][i][hf]), pack4(acc[0][i][hf]), st);
        lds_w128f(spa ^ (hf * 512), st);
      }
      waitvm<0>();
    }
    asm volatile("" :: "v"(dc), "v"(dw), "v"(dx));
    ST(10);
    BAR();
    __builtin_amdgcn_s_setprio(0);
    u32x4 seal = ld_seal(0);
    {
      ldw(wq, 0, 0, WQ_SPLIT, 0);
      u32x2 fl = __builtin_amdgcn_raw_buffer_load_b64(rsWS, l * 8, WS_FLAG + t * 2048 + w * 512, SC1);
      ST(5);
      BAR();
      ldw(wq, 0, WQ_SPLIT, 6, 0);
      if (__builtin_expect(!__all(fl[0] == tag[0] && fl[1] == tag[1]), 0)) {
        for (int sp = 0; sp < SPIN_CAP; ++sp) {
          asm volatile("" ::: "memory");
          const u32x2 f2 = __builtin_amdgcn_raw_buffer_load_b64(rsWS, l * 8, WS_FLAG + t * 2048 + w * 512, SC1);
          if (__all(f2[0] == tag[0] && f2[1] == tag[1])) break;
        }
      }
      ST(18);
      BAR();
    }
    const unsigned qt = (qb >> 3) * SLOT;
    unsigned hqa[2], own[2][2];
    hqa[0] = qt + hh * 1024 + (tq ^ hh) * 128 + (g & 1) * 64 + tp * 16; hqa[1] = (hqa[0] ^ 64) + 512;
    OPAQUE(hqa[0]); OPAQUE(hqa[1]);
    unsigned nw = lds0 + L_N + l * 16 + w * 4;
    OPAQUE(nw);
    unsigned qw = L_Q + (3 * w * 64 + hh * 32 + i16) * 16 + 8 * (g & 1);
    own[0][0] = qt + w * 3072 + (tq ^ (w & 1)) * 128 + hh * 64 + tp * 16 + 8 * (g & 1); own[0][1] = (own[0][0] ^ 64) + 512;
    own[1][0] = own[0][0] ^ 128; own[1][1] = own[0][1] ^ 128;
    OPAQUE(qw); OPAQUE(own[0][0]); OPAQUE(own[0][1]); OPAQUE(own[1][0]); OPAQUE(own[1][1]);
    if (b == 0 && w == 3 && l == 0) __builtin_amdgcn_raw_buffer_store_b64(u32x2{(unsigned)epoch, (unsigned)(epoch >> 32)}, rsWS, 0, WS_EPOCH, SC1);
    for (int sp = 0; sp < SPIN_CAP && !sealed(wq, seal, l < 18, tag); ++sp) {
      asm volatile("" ::: "memory");
      seal = ld_seal(0);
      ldw(wq, 0, 0, 6, 0);
    }
    u32x4 hq[3];
    {
      const u64 hbu = (u64)(ws + WS_H2 + (size_t)t * FRAME_B);
      u32x4 rs = {(unsigned)hbu, (unsigned)(hbu >> 32) & 0xffffu, (unsigned)FRAME_B, 0x00020000u};
      asm volatile("" : "+s"(rs));
      __builtin_amdgcn_s_setprio(3);
      ST(6);
      BAR();
      ST(7);
      for (int sp = 0; sp < SPIN_CAP; ++sp) {
        setm0<0>(qt + w * 3072);
        bdma<0>(l * 16, rs, qb * TILE_B + w * 3072); bdma<1024>(l * 16, rs, qb * TILE_B + w * 3072); bdma<2048>(l * 16, rs, qb * TILE_B + w * 3072);
        waitvm<0>();
#pragma unroll
        for (int i = 0; i < 3; ++i) { const u32x2 u0 = lds_r64(own[i & 1][0] + i * 1024), u1 = lds_r64(own[i & 1][1] + i * 1024); hq[i] = u32x4{u0[0], u0[1], u1[0], u1[1]}; }
        if (!poisoned(hq)) break;
      }
    }
    ST(8);
    BAR();
    ST(9);
    {
      u32x4 bf[6][2];
      f32x4 d[3][2];
      f32x2 n[2] = {{0.f, 0.f}, {0.f, 0.f}};
      auto fin = [&](const int i) {
#pragma unroll
        for (int hf = 0; hf < 2; ++hf) {
          lds_w64(qw + i * 1024 + hf * 256, pack4(d[i][hf]));
          const f32x2 a = {d[i][hf][0], d[i][hf][1]}, c = {d[i][hf][2], d[i][hf][3]};
          n[hf] = __builtin_elementwise_fma(a, a, n[hf]); n[hf] = __builtin_elementwise_fma(c, c, n[hf]);
        }
      };
      auto mm = [&](const int i, const int ks) {
        d[i][0] = mfma16(wq[ks * 3 + i], bf[ks][0], ks ? d[i][0] : f32x4{0, 0, 0, 0});
        d[i][1] = mfma16(wq[ks * 3 + i], bf[ks][1], ks ? d[i][1] : f32x4{0, 0, 0, 0});
      };
#pragma unroll
      for (int ks = 0; ks < 3; ++ks) { bf[ks][0] = lds_r128(hqa[0] + ks * 2048); bf[ks][1] = lds_r128(hqa[1] + ks * 2048); SBAR(); }
#pragma unroll
      for (int ks = 0; ks < 3; ++ks) {
        mm(0, ks);
        bf[ks + 3][0] = lds_r128(hqa[0] + (ks + 3) * 2048); bf[ks + 3][1] = lds_r128(hqa[1] + (ks + 3) * 2048);
        SBAR();
        mm(1, ks);
        SBAR();
      }
      mm(0, 3); mm(0, 4); mm(0, 5); mm(1, 3);
      SBAR();
      mm(1, 4); mm(1, 5); mm(2, 0); fin(0);
      SBAR();
      mm(2, 1); mm(2, 2); mm(2, 3); mm(2, 4); mm(2, 5); fin(1);
      SBAR();
      fin(2);
      auto rr = __builtin_amdgcn_permlane16_swap(asu(n[0][0] + n[0][1]), asu(n[1][0] + n[1][1]), false, false);
      lds_w32(nw, asf(rr[0]) + asf(rr[1]));
    }
    SBAR();
    ST(11);
    BAR();
    __builtin_amdgcn_s_setprio(0);
    unsigned ow[3];
#pragma unroll
    for (int i = 0; i < 3; ++i) { ow[i] = i16 * 384 + (((6 * w + 2 * i + hh) ^ (i16 & 7)) * 16) + 8 * (g & 1); OPAQUE(ow[i]); }
    float lt[2];
    u32x4 pick;
#pragma unroll
    for (int e = 0; e < 4; ++e) {
      const unsigned d = 8 * (e >> 1) + 4 * (g & 1) + 2 * (e & 1);
      pick[e] = (i16 == d ? 0x3f80u : 0u) | (i16 == d + 1 ? 0x3f800000u : 0u);
    }
    unsigned pr[2], la = lds0 + L_L + i16 * 32;
    pr[0] = lds0 + 2 * TILE_B + hh * SLOT + 3 * w * 1024 + ((g & 1) * 32 + i16) * 16; pr[1] = pr[0] + 2 * SLOT;
    OPAQUE(pick); OPAQUE(pr[0]); OPAQUE(pr[1]); OPAQUE(la);
    f32x4 x1[3][2];
    {
      __builtin_amdgcn_s_sleep(40);
      u32x2 st[2];
      for (int sp = 0; sp < SPIN_CAP; ++sp) {
        asm volatile("" ::: "memory");
        st[0] = __builtin_amdgcn_raw_buffer_load_b64(rsWS, i16 * 8, WS_ST + (t * 1024 + qb * 32) * 8, SC1);
        st[1] = __builtin_amdgcn_raw_buffer_load_b64(rsWS, i16 * 8 + 128, WS_ST + (t * 1024 + qb * 32) * 8, SC1);
        if (!__any(max(max(st[0][0], st[0][1]), max(st[1][0], st[1][1])) == POISON)) break;
      }
#pragma unroll
      for (int hf = 0; hf < 2; ++hf) {
        const float mu = asf(st[hf][0]), sd = __builtin_amdgcn_sqrtf(asf(st[hf][1]));
#pragma unroll
        for (int i = 0; i < 3; ++i) {
          const f32x4 h = {bflo(hq[i][2 * hf]), bfhi(hq[i][2 * hf]), bflo(hq[i][2 * hf + 1]), bfhi(hq[i][2 * hf + 1])};
          x1[i][hf] = h * sd + mu;
          OPAQUE(x1[i][hf]);
        }
      }
    }
    BAR();
    ST(16);
    for (int sp = 0; sp < SPIN_CAP; ++sp) {
      asm volatile("" ::: "memory");
      seal = ld_seal(1);
      ldw(wv, 1, 0, 6, 0);
      if (sealed(wv, seal, l < 18, tag)) break;
    }
    ST(12);
#pragma unroll 1
    for (bool exact = false;; exact = true) {
      BAR();
      ST(13);
      u32x4 p[3][2][2];
#pragma unroll
      for (int i = 0; i < 3; ++i)
#pragma unroll
        for (int hf = 0; hf < 2; ++hf) { p[i][hf][0] = lds_r128(pr[0] + i * 1024 + hf * 256); p[i][hf][1] = lds_r128(pr[1] + i * 1024 + hf * 256); }
      const f32x4 l0 = lds_r128f(la), l1 = lds_r128f(la + 16);
      SBAR();
#pragma unroll
      for (int i = 0; i < 3; ++i)
#pragma unroll
        for (int hf = 0; hf < 2; ++hf) lds_w64(dp[hf] + (L_FI - L_X) + i * 512, pack4(mfma16(pick, p[i][hf][1], mfma16(pick, p[i][hf][0], f32x4{0, 0, 0, 0}))));
      if (__builtin_expect(!low_sum(l0, l1, lt) || exact, 1)) break;
      BAR();
      BAR();
    }
    ST(14);
    const float linv[2] = {__builtin_amdgcn_rcpf(lt[0]), __builtin_amdgcn_rcpf(lt[1])};
    BAR();
    ST(15);
    {
      u32x4 bf[6][2];
      f32x4 d[3][2];
      auto fin = [&](const int i) {
#pragma unroll
        for (int hf = 0; hf < 2; ++hf) lds_w64(ow[i] + hf * 6144, pack4(d[i][hf] * linv[hf] + x1[i][hf]));
      };
      auto mm = [&](const int i, const int ks) {
        d[i][0] = mfma16(wv[ks * 3 + i], bf[ks][0], ks ? d[i][0] : f32x4{0, 0, 0, 0});
        d[i][1] = mfma16(wv[ks * 3 + i], bf[ks][1], ks ? d[i][1] : f32x4{0, 0, 0, 0});
      };
#pragma unroll
      for (int ks = 0; ks < 3; ++ks) { bf[ks][0] = lds_r128(fr[0] + (L_FI - L_X) + ks * 1024); bf[ks][1] = lds_r128(fr[1] + (L_FI - L_X) + ks * 1024); SBAR(); }
#pragma unroll
      for (int ks = 0; ks < 3; ++ks) {
        mm(0, ks);
        bf[ks + 3][0] = lds_r128(fr[0] + (L_FI - L_X) + (ks + 3) * 1024); bf[ks + 3][1] = lds_r128(fr[1] + (L_FI - L_X) + (ks + 3) * 1024);
        SBAR();
        mm(1, ks);
        SBAR();
      }
      mm(0, 3); mm(0, 4); mm(0, 5); mm(1, 3);
      SBAR();
      mm(1, 4); mm(1, 5); mm(2, 0); fin(0);
      SBAR();
      mm(2, 1); mm(2, 2); mm(2, 3); mm(2, 4); mm(2, 5); fin(1);
      SBAR();
      fin(2);
    }
    BAR();
    ST(17);
    ST_OUT(ws, 12, 20);
  }
}

extern "C" void kernel_launch(void* const* d_in, const int* in_sizes, int n_in, void* d_out, int out_size, void* d_ws, size_t ws_size, hipStream_t stream) {
  (void)hipFuncSetAttribute((const void*)k_timesformer, hipFuncAttributeMaxDynamicSharedMemorySize, LDS_BYTES);
  k_timesformer<<<256, 512, LDS_BYTES, stream>>>((const char*)d_in[0], (const char*)d_in[1], (const char*)d_in[2], (const char*)d_in[3], (const char*)d_in[4],
                                                 (char*)d_ws, (char*)d_out);
}
```
